```python
import torch

def _softmax(x, axis=-1):
    return torch.softmax(x, dim=axis)
def _gelu(x):
    return torch.nn.functional.gelu(x, approximate="tanh")
def _silu(x):
    return torch.nn.functional.silu(x)
def _sigmoid(x):
    return torch.sigmoid(x)
def _layernorm(x, axis=-1, eps=1e-5):
    m = x.mean(dim=axis, keepdim=True)
    v = x.var(dim=axis, keepdim=True, unbiased=False)
    return (x - m) / torch.sqrt(v + eps)
def _scatter_add(tgt, idx, src, axis=0):
    return tgt.clone().index_add_(axis, idx, src)
def _segment_sum(src, seg, n):
    out = torch.zeros((n,) + src.shape[1:], dtype=src.dtype)
    return out.index_add_(0, seg, src)
def _pad(x, pw):
    flat = tuple(v for pair in reversed(pw) for v in pair)
    return torch.nn.functional.pad(x, flat)

kernel_name = 'standalone__mha_block_cached__near_ridge'

def setup_inputs():

    _w = (lambda a: (lambda t: t.to(torch.bfloat16) if t.is_floating_point() else t)(torch.as_tensor(a)))
    B, Sq, D = 16, 192, 512
    H, Sk, Dh = 32, 512, 128
    return {
        "x": _w((lambda *s: torch.randn(*s, dtype=torch.bfloat16) * 0.8)(B, Sq, D)),
        "k_cache": _w((lambda *s: torch.randn(*s, dtype=torch.bfloat16) * 0.8)(B, H, Sk, Dh)),
        "v_cache": _w((lambda *s: torch.randn(*s, dtype=torch.bfloat16) * 0.05)(B, H, Sk, Dh)),
        "wq": _w((lambda *s: torch.randn(*s, dtype=torch.bfloat16) * 0.8)(D, H * Dh) * (D ** -0.5)),
        "wo": _w((lambda *s: torch.randn(*s, dtype=torch.bfloat16) * 0.05)(H * Dh, D) * ((H * Dh) ** -0.5)),
    }

def reference(x, k_cache, v_cache, wq, wo):
    B, Sq, D = x.shape
    H, Dh = 32, 128
    q = (lambda x, *d: x.permute(*d))((x @ wq).reshape(B, Sq, H, Dh), 0, 2, 1, 3)
    scores = torch.einsum("bhqd,bhkd->bhqk", q, k_cache) * (Dh ** -0.5)
    p = _softmax(scores, axis=-1)
    o = torch.einsum("bhqk,bhkd->bhqd", p, v_cache)
    return (lambda x, *d: x.permute(*d))(o, 0, 2, 1, 3).reshape(B, Sq, H * Dh) @ wo
```

```cpp
#include <hip/hip_runtime.h>
#include <hip/hip_bf16.h>
typedef __hip_bfloat16 bf16;
constexpr int B=16,SQ=192,D=512,H=32,SK=512,DH=128;
__device__ inline float b2f(unsigned short u){return __uint_as_float(((unsigned)u)<<16);}
__global__ void gemm_naive(const unsigned short* A,const unsigned short* Bm, bf16* C,int M,int N,int K){
  __shared__ float As[16][17], Bs[16][17];
  int tx=threadIdx.x&15, ty=threadIdx.x>>4;
  int row=blockIdx.y*16+ty, col=blockIdx.x*16+tx;
  float acc=0;
  for(int k0=0;k0<K;k0+=16){
    As[ty][tx]=b2f(A[(long)row*K+k0+tx]);
    Bs[ty][tx]=b2f(Bm[(long)(k0+ty)*N+col]);
    __syncthreads();
    for(int k=0;k<16;k++) acc+=As[ty][k]*Bs[k][tx];
    __syncthreads();
  }
  C[(long)row*N+col]=__float2bfloat16(acc);
}
__global__ void attn_naive(const unsigned short* Q,const unsigned short* Kc,const unsigned short* Vc,bf16* O){
  int row=blockIdx.x, h=blockIdx.y, b=blockIdx.z, t=threadIdx.x;
  __shared__ float q[DH], s[SK], red[128];
  q[t]=b2f(Q[((long)b*SQ+row)*(H*DH)+h*DH+t]);
  __syncthreads();
  const unsigned short* Kp=Kc+((long)(b*H+h)*SK)*DH;
  const unsigned short* Vp=Vc+((long)(b*H+h)*SK)*DH;
  float mx=-1e30f;
  for(int k=t;k<SK;k+=128){float a=0;for(int d=0;d<DH;d++)a+=q[d]*b2f(Kp[k*DH+d]);a*=0.08838834764f;s[k]=a;mx=fmaxf(mx,a);}
  red[t]=mx;__syncthreads();
  for(int o=64;o>0;o>>=1){if(t<o)red[t]=fmaxf(red[t],red[t+o]);__syncthreads();}
  mx=red[0];__syncthreads();
  float sum=0;for(int k=t;k<SK;k+=128){float e=__expf(s[k]-mx);s[k]=e;sum+=e;}
  red[t]=sum;__syncthreads();
  for(int o=64;o>0;o>>=1){if(t<o)red[t]+=red[t+o];__syncthreads();}
  sum=red[0];
  float a=0;for(int k=0;k<SK;k++)a+=s[k]*b2f(Vp[k*DH+t]);
  O[((long)b*SQ+row)*(H*DH)+h*DH+t]=__float2bfloat16(a/sum);
}
extern "C" void kernel_launch(void* const* d_in, const int* in_sizes, int n_in, void* d_out, int out_size, void* d_ws, size_t ws_size, hipStream_t stream){
  const unsigned short* x=(const unsigned short*)d_in[0];
  const unsigned short* k=(const unsigned short*)d_in[1];
  const unsigned short* v=(const unsigned short*)d_in[2];
  const unsigned short* wq=(const unsigned short*)d_in[3];
  const unsigned short* wo=(const unsigned short*)d_in[4];
  bf16* q=(bf16*)d_ws; bf16* o=q+(long)B*SQ*H*DH;
  gemm_naive<<<dim3(H*DH/16,B*SQ/16),256,0,stream>>>(x,wq,q,B*SQ,H*DH,D);
  attn_naive<<<dim3(SQ,H,B),128,0,stream>>>((const unsigned short*)q,k,v,o);
  gemm_naive<<<dim3(D/16,B*SQ/16),256,0,stream>>>((const unsigned short*)o,wo,(bf16*)d_out,B*SQ,D,H*DH);
}
```

```cpp
#include <hip/hip_runtime.h>
#include <hip/hip_bf16.h>

__global__ __launch_bounds__(256, 1) void k_attn(const void* x, const void* k, const void* v, const void* wq, void* ws, const void* wo, void* out, void* pad) {
  __shared__ __attribute__((aligned(16))) char lds[163840];
  unsigned ldsb = __builtin_amdgcn_readfirstlane((unsigned)(size_t)lds);
  asm volatile("" :: "s"(ldsb));
  asm volatile(
    "Lstart_%=:\n\t"
    "s_load_dwordx16 s[4:19], %0, 0\n\t"
    "v_mbcnt_lo_u32_b32 v0, -1, 0\n\t"
    "v_mbcnt_hi_u32_b32 v0, -1, v0\n\t"
    "s_mov_b32 s56, %2\n\t"
    "s_and_b32 s61, %1, 7\n\t"
    "s_lshr_b32 s62, %1, 3\n\t"
    "s_lshr_b32 s59, s61, 2\n\t"
    "s_lshl_b32 s59, s59, 3\n\t"
    "s_lshr_b32 s63, s62, 2\n\t"
    "s_add_u32 s59, s59, s63\n\t"
    "s_and_b32 s60, s61, 3\n\t"
    "s_lshl_b32 s60, s60, 2\n\t"
    "s_and_b32 s63, s62, 3\n\t"
    "s_add_u32 s60, s60, s63\n\t"
    "s_lshr_b32 s57, s56, 1\n\t"
    "s_and_b32 s58, s56, 1\n\t"
    "v_lshrrev_b32 v70, 4, v0\n\t"
    "v_and_b32 v71, 15, v0\n\t"
    "v_bfe_u32 v72, v0, 2, 2\n\t"
    "v_and_b32 v73, 3, v0\n\t"
    "v_lshlrev_b32 v64, 13, v72\n\t"
    "v_lshl_add_u32 v64, v70, 6, v64\n\t"
    "v_lshl_add_u32 v15, v73, 4, v64\n\t"
    "v_lshrrev_b32 v64, 3, v0\n\t"
    "v_and_b32 v65, 7, v0\n\t"
    "v_xor_b32 v65, v65, v70\n\t"
    "v_lshlrev_b32 v64, 10, v64\n\t"
    "v_lshl_add_u32 v13, v65, 4, v64\n\t"
    "v_xor_b32 v14, 64, v13\n\t"
    "s_lshl_b32 s61, s57, 13\n\t"
    "s_lshl_b32 s62, s58, 12\n\t"
    "s_add_u32 s65, s61, s62\n\t"
    "s_add_u32 s65, s65, 0\n\t"
    "s_lshl_b32 s68, s58, 17\n\t"
    "s_mul_i32 s64, s56, 0x1800\n\t"
    "s_add_u32 s64, s64, 0\n\t"
    "s_mul_i32 s67, s56, 0xc000\n\t"
    "s_lshr_b32 s61, %1, 3\n\t"
    "s_lshr_b32 s82, s61, 2\n\t"
    "s_and_b32 s61, s61, 3\n\t"
    "s_cmp_lt_u32 s61, 2\n\t"
    "s_mov_b32 s97, 0x64753210\n\t"
    "s_cmp_eq_u32 s61, 1\n\t"
    "s_cselect_b32 s97, 0x62751430, s97\n\t"
    "s_cmp_eq_u32 s61, 2\n\t"
    "s_cselect_b32 s97, 0x42731650, s97\n\t"
    "s_cmp_eq_u32 s61, 3\n\t"
    "s_cselect_b32 s97, 0x64253170, s97\n\t"
    "s_mov_b32 s83, s97\n\t"
    "s_lshl_b32 s62, s82, 19\n\t"
    "s_or_b32 s68, s68, s62\n\t"
    "s_lshl_b32 s79, s82, 7\n\t"
    "s_add_u32 s96, s67, s79\n\t"
    "s_waitcnt lgkmcnt(0)\n\t"
    "s_mov_b64 s[40:41], s[10:11]\n\t"
    "s_lshl_b32 s61, s60, 9\n\t"
    "s_lshl_b32 s62, s57, 8\n\t"
    "s_add_u32 s61, s61, s62\n\t"
    "s_add_u32 s40, s40, s61\n\t"
    "s_addc_u32 s41, s41, 0\n\t"
    "s_sub_u32 s42, 0x400000, s61\n\t"
    "s_mov_b32 s43, 0x20000\n\t"
    "s_mov_b64 s[36:37], s[4:5]\n\t"
    "s_mul_i32 s61, s59, 0x30000\n\t"
    "s_add_u32 s36, s36, s61\n\t"
    "s_addc_u32 s37, s37, 0\n\t"
    "s_mov_b32 s38, 0x30000\n\t"
    "s_mov_b32 s39, 0x20000\n\t"
    "s_add_u32 m0, s65, 0x8000\n\t"
    "s_add_u32 s78, s68, 0\n\t"
    "buffer_load_dwordx4 v15, s[40:43], s78 offen lds\n\t"
    "s_add_u32 m0, s65, 0x8400\n\t"
    "s_add_u32 s78, s68, 0x8000\n\t"
    "buffer_load_dwordx4 v15, s[40:43], s78 offen lds\n\t"
    "s_add_u32 m0, s65, 0x8800\n\t"
    "s_add_u32 s78, s68, 0x10000\n\t"
    "buffer_load_dwordx4 v15, s[40:43], s78 offen lds\n\t"
    "s_add_u32 m0, s65, 0x8c00\n\t"
    "s_add_u32 s78, s68, 0x18000\n\t"
    "buffer_load_dwordx4 v15, s[40:43], s78 offen lds\n\t"
    "s_add_u32 s68, s68, 0x40000\n\t"
    "s_and_b32 s68, s68, 0x3fffff\n\t"
    "s_add_u32 m0, s64, 0x2000\n\t"
    "s_add_u32 s78, s96, 0\n\t"
    "buffer_load_dwordx4 v13, s[36:39], s78 offen lds\n\t"
    "s_add_u32 m0, s64, 0x2400\n\t"
    "s_add_u32 s78, s96, 0x2000\n\t"
    "buffer_load_dwordx4 v14, s[36:39], s78 offen lds\n\t"
    "s_add_u32 m0, s64, 0x2800\n\t"
    "s_add_u32 s78, s96, 0x4000\n\t"
    "buffer_load_dwordx4 v13, s[36:39], s78 offen lds\n\t"
    "s_add_u32 m0, s64, 0x2c00\n\t"
    "s_add_u32 s78, s96, 0x6000\n\t"
    "buffer_load_dwordx4 v14, s[36:39], s78 offen lds\n\t"
    "s_add_u32 m0, s64, 0x3000\n\t"
    "s_add_u32 s78, s96, 0x8000\n\t"
    "buffer_load_dwordx4 v13, s[36:39], s78 offen lds\n\t"
    "s_add_u32 m0, s64, 0x3400\n\t"
    "s_add_u32 s78, s96, 0xa000\n\t"
    "buffer_load_dwordx4 v14, s[36:39], s78 offen lds\n\t"
    "s_lshl_b32 s61, s59, 5\n\t"
    "s_lshl_b32 s62, s60, 1\n\t"
    "s_add_u32 s61, s61, s62\n\t"
    "s_add_u32 s61, s61, s57\n\t"
    "s_lshl_b32 s62, s61, 17\n\t"
    "s_mov_b64 s[44:45], s[6:7]\n\t"
    "s_add_u32 s44, s44, s62\n\t"
    "s_addc_u32 s45, s45, 0\n\t"
    "s_mov_b32 s46, 0x20000\n\t"
    "s_mov_b32 s47, 0x20000\n\t"
    "s_mov_b64 s[48:49], s[8:9]\n\t"
    "s_add_u32 s48, s48, s62\n\t"
    "s_addc_u32 s49, s49, 0\n\t"
    "s_mov_b32 s50, 0x20000\n\t"
    "s_mov_b32 s51, 0x20000\n\t"
    "s_mul_i32 s62, s61, 0xc000\n\t"
    "s_mul_i32 s63, s58, 0x1800\n\t"
    "s_add_u32 s62, s62, s63\n\t"
    "s_mov_b64 s[52:53], s[12:13]\n\t"
    "s_add_u32 s52, s52, s62\n\t"
    "s_addc_u32 s53, s53, 0\n\t"
    "s_mov_b32 s54, 0xc000\n\t"
    "s_mov_b32 s55, 0x20000\n\t"
    "s_mul_i32 s80, s57, 0xc00\n\t"
    "s_sub_u32 s81, 0xc00, s80\n\t"
    "s_lshl_b32 s61, s57, 16\n\t"
    "s_add_u32 s61, s61, 0\n\t"
    "s_lshl_b32 s62, s58, 12\n\t"
    "s_add_u32 s66, s61, s62\n\t"
    "s_lshl_b32 s63, s60, 1\n\t"
    "s_add_u32 s63, s63, s57\n\t"
    "s_mul_i32 s69, s59, 3\n\t"
    "s_add_u32 s63, s63, s69\n\t"
    "s_and_b32 s63, s63, 15\n\t"
    "s_lshl_b32 s63, s63, 13\n\t"
    "s_add_u32 s62, s62, s63\n\t"
    "s_mov_b32 s69, s62\n\t"
    "s_mov_b32 s70, s62\n\t"
    "v_xor_b32 v64, v71, v70\n\t"
    "v_lshlrev_b32 v65, 8, v70\n\t"
    "v_lshl_add_u32 v7, v64, 4, v65\n\t"
    "v_xor_b32 v8, 64, v7\n\t"
    "v_xor_b32 v9, 0x80, v7\n\t"
    "v_xor_b32 v10, 0xc0, v7\n\t"
    "v_lshlrev_b32 v64, 8, v72\n\t"
    "v_lshl_add_u32 v64, v70, 6, v64\n\t"
    "v_lshl_add_u32 v11, v73, 4, v64\n\t"
    "v_xor_b32 v12, 32, v11\n\t"
    "v_xor_b32 v64, v71, v70\n\t"
    "v_lshlrev_b32 v65, 8, v71\n\t"
    "v_lshl_add_u32 v64, v64, 4, v65\n\t"
    "v_xor_b32 v65, 0, v64\n\t"
    "v_add_u32 v1, s61, v65\n\t"
    "v_xor_b32 v65, 64, v64\n\t"
    "v_add_u32 v2, s61, v65\n\t"
    "v_xor_b32 v65, 0x80, v64\n\t"
    "v_add_u32 v3, s61, v65\n\t"
    "v_xor_b32 v65, 0xc0, v64\n\t"
    "v_add_u32 v4, s61, v65\n\t"
    "v_lshlrev_b32 v64, 10, v70\n\t"
    "v_lshl_add_u32 v64, v72, 6, v64\n\t"
    "v_lshl_add_u32 v64, v73, 3, v64\n\t"
    "v_and_b32 v65, 1, v70\n\t"
    "v_lshl_add_u32 v64, v65, 5, v64\n\t"
    "v_add_u32 v64, 0x2000, v64\n\t"
    "v_xor_b32 v65, 32, v64\n\t"
    "v_add_u32 v5, s61, v64\n\t"
    "v_add_u32 v6, s61, v65\n\t"
    "s_lshl_b32 s62, s57, 13\n\t"
    "s_add_u32 s62, s62, 0\n\t"
    "s_add_u32 s62, s62, 0x8000\n\t"
    "v_lshlrev_b32 v64, 11, v70\n\t"
    "v_lshl_add_u32 v64, v72, 6, v64\n\t"
    "v_lshl_add_u32 v64, v73, 4, v64\n\t"
    "v_add_u32 v18, s62, v64\n\t"
    "v_add_u32 v19, 0x16000, v18\n\t"
    "s_mul_i32 s62, s58, 0x3000\n\t"
    "s_add_u32 s62, s62, 0\n\t"
    "s_add_u32 s62, s62, 0x2000\n\t"
    "v_lshrrev_b32 v64, 1, v71\n\t"
    "v_xor_b32 v64, v64, v70\n\t"
    "v_lshlrev_b32 v65, 7, v71\n\t"
    "v_lshl_add_u32 v64, v64, 4, v65\n\t"
    "v_xor_b32 v65, 64, v64\n\t"
    "s_lshl_b32 s63, s80, 1\n\t"
    "s_add_u32 s63, s63, s62\n\t"
    "v_add_u32 v16, s63, v64\n\t"
    "v_add_u32 v17, s63, v65\n\t"
    "v_add_u32 v21, 0x10000, v16\n\t"
    "v_add_u32 v61, 0x10000, v17\n\t"
    "s_lshl_b32 s63, s81, 1\n\t"
    "s_add_u32 s63, s63, s62\n\t"
    "v_add_u32 v56, s63, v64\n\t"
    "v_add_u32 v57, s63, v65\n\t"
    "v_add_u32 v22, 0x10000, v56\n\t"
    "v_add_u32 v23, 0x10000, v57\n\t"
    "v_lshlrev_b32 v20, 4, v0\n\t"
    "s_mov_b32 s72, 0x3e0293ee\n\t"
    "s_mov_b32 s73, 0x3e0293ee\n\t"
    "s_mov_b32 s71, 0x47800000\n\t"
    "s_mov_b32 s75, 0x37800000\n\t"
    "s_mov_b32 s74, 0xff61b1e6\n\t"
    "v_mov_b32 v24, 0\n\t"
    "v_mov_b32 v25, 0\n\t"
    "v_mov_b32 v26, 0\n\t"
    "v_mov_b32 v27, 0\n\t"
    "v_mov_b32 v28, 0\n\t"
    "v_mov_b32 v29, 0\n\t"
    "v_mov_b32 v30, 0\n\t"
    "v_mov_b32 v31, 0\n\t"
    "v_mov_b32 v32, 0\n\t"
    "v_mov_b32 v33, 0\n\t"
    "v_mov_b32 v34, 0\n\t"
    "v_mov_b32 v35, 0\n\t"
    "v_mov_b32 v36, 0\n\t"
    "v_mov_b32 v37, 0\n\t"
    "v_mov_b32 v38, 0\n\t"
    "v_mov_b32 v39, 0\n\t"
    "v_mov_b32 v40, 0\n\t"
    "v_mov_b32 v41, 0\n\t"
    "v_mov_b32 v42, 0\n\t"
    "v_mov_b32 v43, 0\n\t"
    "v_mov_b32 v44, 0\n\t"
    "v_mov_b32 v45, 0\n\t"
    "v_mov_b32 v46, 0\n\t"
    "v_mov_b32 v47, 0\n\t"
    "v_mov_b32 v48, 0\n\t"
    "v_mov_b32 v49, 0\n\t"
    "v_mov_b32 v50, 0\n\t"
    "v_mov_b32 v51, 0\n\t"
    "v_mov_b32 v52, 0\n\t"
    "v_mov_b32 v53, 0\n\t"
    "v_add_u32 v62, s66, v20\n\t"
    "s_lshr_b32 s83, s83, 4\n\t"
    "s_add_u32 s79, s83, s82\n\t"
    "s_and_b32 s79, s79, 7\n\t"
    "s_lshl_b32 s79, s79, 7\n\t"
    "s_add_u32 s96, s67, s79\n\t"
    "s_add_u32 m0, s65, 0xc000\n\t"
    "s_add_u32 s78, s68, 0\n\t"
    "buffer_load_dwordx4 v15, s[40:43], s78 offen lds\n\t"
    "s_add_u32 m0, s65, 0xc400\n\t"
    "s_add_u32 s78, s68, 0x8000\n\t"
    "buffer_load_dwordx4 v15, s[40:43], s78 offen lds\n\t"
    "s_add_u32 m0, s65, 0xc800\n\t"
    "s_add_u32 s78, s68, 0x10000\n\t"
    "buffer_load_dwordx4 v15, s[40:43], s78 offen lds\n\t"
    "s_add_u32 m0, s65, 0xcc00\n\t"
    "s_add_u32 s78, s68, 0x18000\n\t"
    "buffer_load_dwordx4 v15, s[40:43], s78 offen lds\n\t"
    "s_lshr_b32 s97, s97, 4\n\t"
    "s_add_u32 s63, s97, s82\n\t"
    "s_and_b32 s63, s63, 7\n\t"
    "s_lshl_b32 s63, s63, 19\n\t"
    "s_and_b32 s68, s68, 0x3ffff\n\t"
    "s_or_b32 s68, s68, s63\n\t"
    "s_add_u32 m0, s64, 0x12000\n\t"
    "s_add_u32 s78, s96, 0\n\t"
    "buffer_load_dwordx4 v13, s[36:39], s78 offen lds\n\t"
    "s_add_u32 m0, s64, 0x12400\n\t"
    "s_add_u32 s78, s96, 0x2000\n\t"
    "buffer_load_dwordx4 v14, s[36:39], s78 offen lds\n\t"
    "s_add_u32 m0, s64, 0x12800\n\t"
    "s_add_u32 s78, s96, 0x4000\n\t"
    "buffer_load_dwordx4 v13, s[36:39], s78 offen lds\n\t"
    "s_mov_b64 s[84:85], s[12:13]\n\t"
    "s_add_u32 s84, s84, 0x3800000\n\t"
    "s_addc_u32 s85, s85, 0\n\t"
    "s_mov_b32 s86, 0x60000\n\t"
    "s_mov_b32 s87, 0x20000\n\t"
    "v_mov_b32 v60, 0\n\t"
    "buffer_load_dwordx2 v[58:59], v60, s[84:87], 0 offen sc1\n\t"
    "v_lshlrev_b32 v76, 7, v0\n\t"
    "s_getpc_b64 s[92:93]\n\t"
    "Lpc_%=:\n\t"
    "s_mov_b32 s94, Lend_%=-Lpc_%=\n\t"
    "s_mov_b32 s95, 0x20000\n\t"
    "s_lshr_b32 s62, %1, 3\n\t"
    "s_lshl_b32 s62, s62, 2\n\t"
    "s_add_u32 s62, s62, s56\n\t"
    "s_lshl_b32 s62, s62, 10\n\t"
    "v_cmp_gt_u32 vcc, 8, v0\n\t"
    "v_mov_b32 v66, 0x40000000\n\t"
    "s_nop 0\n\t"
    "v_cndmask_b32 v76, v66, v76, vcc\n\t"
    "buffer_load_dword a255, v76, s[92:95], s62 offen\n\t"
    "s_waitcnt vmcnt(9)\n\t"
    "s_barrier\n\t"
    "ds_read_b128 a[64:67], v16 offset:0\n\t"
    "ds_read_b128 a[68:71], v16 offset:2048\n\t"
    "ds_read_b128 a[72:75], v16 offset:4096\n\t"
    "ds_read_b128 a[76:79], v56 offset:0\n\t"
    "ds_read_b128 a[80:83], v56 offset:2048\n\t"
    "ds_read_b128 a[84:87], v56 offset:4096\n\t"
    "ds_read_b64_tr_b16 a[0:1], v18 offset:0\n\t"
    "ds_read_b64_tr_b16 a[2:3], v18 offset:1024\n\t"
    "ds_read_b64_tr_b16 a[4:5], v18 offset:8\n\t"
    "ds_read_b64_tr_b16 a[6:7], v18 offset:1032\n\t"
    "ds_read_b64_tr_b16 a[8:9], v18 offset:256\n\t"
    "ds_read_b64_tr_b16 a[10:11], v18 offset:1280\n\t"
    "ds_read_b64_tr_b16 a[12:13], v18 offset:264\n\t"
    "ds_read_b64_tr_b16 a[14:15], v18 offset:1288\n\t"
    "ds_read_b64_tr_b16 a[16:17], v18 offset:512\n\t"
    "ds_read_b64_tr_b16 a[18:19], v18 offset:1536\n\t"
    "ds_read_b64_tr_b16 a[20:21], v18 offset:520\n\t"
    "ds_read_b64_tr_b16 a[22:23], v18 offset:1544\n\t"
    "ds_read_b64_tr_b16 a[24:25], v18 offset:768\n\t"
    "ds_read_b64_tr_b16 a[26:27], v18 offset:1792\n\t"
    "ds_read_b64_tr_b16 a[28:29], v18 offset:776\n\t"
    "ds_read_b64_tr_b16 a[30:31], v18 offset:1800\n\t"
    "s_waitcnt lgkmcnt(14)\n\t"
    "v_mfma_f32_16x16x32_bf16 v[64:67], a[0:3], a[64:67], 0\n\t"
    "s_waitcnt lgkmcnt(12)\n\t"
    "v_mfma_f32_16x16x32_bf16 v[68:71], a[4:7], a[64:67], 0\n\t"
    "s_waitcnt lgkmcnt(10)\n\t"
    "v_mfma_f32_16x16x32_bf16 v[72:75], a[8:11], a[64:67], 0\n\t"
    "s_waitcnt lgkmcnt(8)\n\t"
    "v_mfma_f32_16x16x32_bf16 v[76:79], a[12:15], a[64:67], 0\n\t"
    "s_waitcnt lgkmcnt(6)\n\t"
    "v_mfma_f32_16x16x32_bf16 v[80:83], a[16:19], a[64:67], 0\n\t"
    "s_waitcnt lgkmcnt(0)\n\t"
    "v_mfma_f32_16x16x32_bf16 v[84:87], a[20:23], a[64:67], 0\n\t"
    "v_mfma_f32_16x16x32_bf16 v[88:91], a[24:27], a[64:67], 0\n\t"
    "v_mfma_f32_16x16x32_bf16 v[92:95], a[28:31], a[64:67], 0\n\t"
    "v_mfma_f32_16x16x32_bf16 v[96:99], a[0:3], a[68:71], 0\n\t"
    "s_waitcnt vmcnt(5) lgkmcnt(0)\n\t"
    "s_barrier\n\t"
    "v_mfma_f32_16x16x32_bf16 v[100:103], a[4:7], a[68:71], 0\n\t"
    "s_add_u32 m0, s64, 0x12c00\n\t"
    "ds_read_b128 a[88:91], v17 offset:0\n\t"
    "v_mfma_f32_16x16x32_bf16 v[104:107], a[8:11], a[68:71], 0\n\t"
    "s_add_u32 s78, s96, 0x6000\n\t"
    "buffer_load_dwordx4 v14, s[36:39], s78 offen lds\n\t"
    "v_mfma_f32_16x16x32_bf16 v[108:111], a[12:15], a[68:71], 0\n\t"
    "ds_read_b64_tr_b16 a[32:33], v18 offset:16384\n\t"
    "v_mfma_f32_16x16x32_bf16 v[112:115], a[16:19], a[68:71], 0\n\t"
    "ds_read_b64_tr_b16 a[34:35], v18 offset:17408\n\t"
    "v_mfma_f32_16x16x32_bf16 v[116:119], a[20:23], a[68:71], 0\n\t"
    "s_add_u32 m0, s64, 0x13000\n\t"
    "ds_read_b64_tr_b16 a[36:37], v18 offset:16392\n\t"
    "v_mfma_f32_16x16x32_bf16 v[120:123], a[24:27], a[68:71], 0\n\t"
    "s_add_u32 s78, s96, 0x8000\n\t"
    "buffer_load_dwordx4 v13, s[36:39], s78 offen lds\n\t"
    "v_mfma_f32_16x16x32_bf16 v[124:127], a[28:31], a[68:71], 0\n\t"
    "ds_read_b64_tr_b16 a[38:39], v18 offset:17416\n\t"
    "v_mfma_f32_16x16x32_bf16 v[128:131], a[0:3], a[72:75], 0\n\t"
    "ds_read_b64_tr_b16 a[40:41], v18 offset:16640\n\t"
    "v_mfma_f32_16x16x32_bf16 v[132:135], a[4:7], a[72:75], 0\n\t"
    "s_add_u32 m0, s64, 0x13400\n\t"
    "ds_read_b64_tr_b16 a[42:43], v18 offset:17664\n\t"
    "v_mfma_f32_16x16x32_bf16 v[136:139], a[8:11], a[72:75], 0\n\t"
    "s_add_u32 s78, s96, 0xa000\n\t"
    "buffer_load_dwordx4 v14, s[36:39], s78 offen lds\n\t"
    "v_mfma_f32_16x16x32_bf16 v[140:143], a[12:15], a[72:75], 0\n\t"
    "ds_read_b64_tr_b16 a[44:45], v18 offset:16648\n\t"
    "v_mfma_f32_16x16x32_bf16 v[144:147], a[16:19], a[72:75], 0\n\t"
    "ds_read_b64_tr_b16 a[46:47], v18 offset:17672\n\t"
    "v_mfma_f32_16x16x32_bf16 v[148:151], a[20:23], a[72:75], 0\n\t"
    "s_lshr_b32 s83, s83, 4\n\t"
    "s_add_u32 s79, s83, s82\n\t"
    "s_and_b32 s79, s79, 7\n\t"
    "s_lshl_b32 s79, s79, 7\n\t"
    "s_add_u32 s96, s67, s79\n\t"
    "s_add_u32 m0, s65, 0x1e000\n\t"
    "ds_read_b64_tr_b16 a[48:49], v18 offset:16896\n\t"
    "v_mfma_f32_16x16x32_bf16 v[152:155], a[24:27], a[72:75], 0\n\t"
    "s_add_u32 s78, s68, 0\n\t"
    "buffer_load_dwordx4 v15, s[40:43], s78 offen lds\n\t"
    "v_mfma_f32_16x16x32_bf16 v[156:159], a[28:31], a[72:75], 0\n\t"
    "ds_read_b64_tr_b16 a[50:51], v18 offset:17920\n\t"
    "v_mfma_f32_16x16x32_bf16 v[160:163], a[0:3], a[76:79], 0\n\t"
    "ds_read_b64_tr_b16 a[52:53], v18 offset:16904\n\t"
    "v_mfma_f32_16x16x32_bf16 v[164:167], a[4:7], a[76:79], 0\n\t"
    "s_add_u32 m0, s65, 0x1e400\n\t"
    "ds_read_b64_tr_b16 a[54:55], v18 offset:17928\n\t"
    "v_mfma_f32_16x16x32_bf16 v[168:171], a[8:11], a[76:79], 0\n\t"
    "s_add_u32 s78, s68, 0x8000\n\t"
    "buffer_load_dwordx4 v15, s[40:43], s78 offen lds\n\t"
    "v_mfma_f32_16x16x32_bf16 v[172:175], a[12:15], a[76:79], 0\n\t"
    "ds_read_b64_tr_b16 a[56:57], v18 offset:17152\n\t"
    "v_mfma_f32_16x16x32_bf16 v[176:179], a[16:19], a[76:79], 0\n\t"
    "ds_read_b64_tr_b16 a[58:59], v18 offset:18176\n\t"
    "v_mfma_f32_16x16x32_bf16 v[180:183], a[20:23], a[76:79], 0\n\t"
    "s_add_u32 m0, s65, 0x1e800\n\t"
    "ds_read_b64_tr_b16 a[60:61], v18 offset:17160\n\t"
    "v_mfma_f32_16x16x32_bf16 v[184:187], a[24:27], a[76:79], 0\n\t"
    "s_add_u32 s78, s68, 0x10000\n\t"
    "buffer_load_dwordx4 v15, s[40:43], s78 offen lds\n\t"
    "v_mfma_f32_16x16x32_bf16 v[188:191], a[28:31], a[76:79], 0\n\t"
    "ds_read_b64_tr_b16 a[62:63], v18 offset:18184\n\t"
    "v_mfma_f32_16x16x32_bf16 v[192:195], a[0:3], a[80:83], 0\n\t"
    "ds_read_b128 a[92:95], v17 offset:2048\n\t"
    "v_mfma_f32_16x16x32_bf16 v[196:199], a[4:7], a[80:83], 0\n\t"
    "s_add_u32 m0, s65, 0x1ec00\n\t"
    "ds_read_b128 a[96:99], v17 offset:4096\n\t"
    "v_mfma_f32_16x16x32_bf16 v[200:203], a[8:11], a[80:83], 0\n\t"
    "s_add_u32 s78, s68, 0x18000\n\t"
    "buffer_load_dwordx4 v15, s[40:43], s78 offen lds\n\t"
    "v_mfma_f32_16x16x32_bf16 v[204:207], a[12:15], a[80:83], 0\n\t"
    "ds_read_b128 a[100:103], v57 offset:0\n\t"
    "v_mfma_f32_16x16x32_bf16 v[208:211], a[16:19], a[80:83], 0\n\t"
    "ds_read_b128 a[104:107], v57 offset:2048\n\t"
    "v_mfma_f32_16x16x32_bf16 v[212:215], a[20:23], a[80:83], 0\n\t"
    "s_add_u32 s68, s68, 0x40000\n\t"
    "s_and_b32 s68, s68, 0x3fffff\n\t"
    "s_add_u32 m0, s65, 0x22000\n\t"
    "ds_read_b128 a[108:111], v57 offset:4096\n\t"
    "v_mfma_f32_16x16x32_bf16 v[216:219], a[24:27], a[80:83], 0\n\t"
    "s_add_u32 s78, s68, 0\n\t"
    "buffer_load_dwordx4 v15, s[40:43], s78 offen lds\n\t"
    "v_mfma_f32_16x16x32_bf16 v[220:223], a[28:31], a[80:83], 0\n\t"
    "v_mfma_f32_16x16x32_bf16 v[224:227], a[0:3], a[84:87], 0\n\t"
    "v_mfma_f32_16x16x32_bf16 v[228:231], a[4:7], a[84:87], 0\n\t"
    "s_add_u32 m0, s65, 0x22400\n\t"
    "v_mfma_f32_16x16x32_bf16 v[232:235], a[8:11], a[84:87], 0\n\t"
    "s_add_u32 s78, s68, 0x8000\n\t"
    "buffer_load_dwordx4 v15, s[40:43], s78 offen lds\n\t"
    "v_mfma_f32_16x16x32_bf16 v[236:239], a[12:15], a[84:87], 0\n\t"
    "v_mfma_f32_16x16x32_bf16 v[240:243], a[16:19], a[84:87], 0\n\t"
    "v_mfma_f32_16x16x32_bf16 v[244:247], a[20:23], a[84:87], 0\n\t"
    "v_mfma_f32_16x16x32_bf16 v[248:251], a[24:27], a[84:87], 0\n\t"
    "v_mfma_f32_16x16x32_bf16 v[252:255], a[28:31], a[84:87], 0\n\t"
    "s_waitcnt lgkmcnt(0)\n\t"
    "v_mfma_f32_16x16x32_bf16 v[64:67], a[32:35], a[88:91], v[64:67]\n\t"
    "v_mfma_f32_16x16x32_bf16 v[68:71], a[36:39], a[88:91], v[68:71]\n\t"
    "v_mfma_f32_16x16x32_bf16 v[72:75], a[40:43], a[88:91], v[72:75]\n\t"
    "v_mfma_f32_16x16x32_bf16 v[76:79], a[44:47], a[88:91], v[76:79]\n\t"
    "v_mfma_f32_16x16x32_bf16 v[80:83], a[48:51], a[88:91], v[80:83]\n\t"
    "v_mfma_f32_16x16x32_bf16 v[84:87], a[52:55], a[88:91], v[84:87]\n\t"
    "v_mfma_f32_16x16x32_bf16 v[88:91], a[56:59], a[88:91], v[88:91]\n\t"
    "v_mfma_f32_16x16x32_bf16 v[92:95], a[60:63], a[88:91], v[92:95]\n\t"
    "v_mfma_f32_16x16x32_bf16 v[96:99], a[32:35], a[92:95], v[96:99]\n\t"
    "s_waitcnt vmcnt(2) lgkmcnt(0)\n\t"
    "s_barrier\n\t"
    "v_mfma_f32_16x16x32_bf16 v[100:103], a[36:39], a[92:95], v[100:103]\n\t"
    "s_add_u32 m0, s65, 0x22800\n\t"
    "ds_read_b128 a[64:67], v21 offset:0\n\t"
    "v_mfma_f32_16x16x32_bf16 v[104:107], a[40:43], a[92:95], v[104:107]\n\t"
    "s_add_u32 s78, s68, 0x10000\n\t"
    "buffer_load_dwordx4 v15, s[40:43], s78 offen lds\n\t"
    "v_mfma_f32_16x16x32_bf16 v[108:111], a[44:47], a[92:95], v[108:111]\n\t"
    "ds_read_b64_tr_b16 a[0:1], v19 offset:0\n\t"
    "v_mfma_f32_16x16x32_bf16 v[112:115], a[48:51], a[92:95], v[112:115]\n\t"
    "ds_read_b64_tr_b16 a[2:3], v19 offset:1024\n\t"
    "v_mfma_f32_16x16x32_bf16 v[116:119], a[52:55], a[92:95], v[116:119]\n\t"
    "s_add_u32 m0, s65, 0x22c00\n\t"
    "ds_read_b64_tr_b16 a[4:5], v19 offset:8\n\t"
    "v_mfma_f32_16x16x32_bf16 v[120:123], a[56:59], a[92:95], v[120:123]\n\t"
    "s_add_u32 s78, s68, 0x18000\n\t"
    "buffer_load_dwordx4 v15, s[40:43], s78 offen lds\n\t"
    "v_mfma_f32_16x16x32_bf16 v[124:127], a[60:63], a[92:95], v[124:127]\n\t"
    "ds_read_b64_tr_b16 a[6:7], v19 offset:1032\n\t"
    "v_mfma_f32_16x16x32_bf16 v[128:131], a[32:35], a[96:99], v[128:131]\n\t"
    "ds_read_b64_tr_b16 a[8:9], v19 offset:256\n\t"
    "v_mfma_f32_16x16x32_bf16 v[132:135], a[36:39], a[96:99], v[132:135]\n\t"
    "s_lshr_b32 s97, s97, 4\n\t"
    "s_add_u32 s63, s97, s82\n\t"
    "s_and_b32 s63, s63, 7\n\t"
    "s_lshl_b32 s63, s63, 19\n\t"
    "s_and_b32 s68, s68, 0x3ffff\n\t"
    "s_or_b32 s68, s68, s63\n\t"
    "s_add_u32 m0, s64, 0x18000\n\t"
    "ds_read_b64_tr_b16 a[10:11], v19 offset:1280\n\t"
    "v_mfma_f32_16x16x32_bf16 v[136:139], a[40:43], a[96:99], v[136:139]\n\t"
    "s_add_u32 s78, s96, 0\n\t"
    "buffer_load_dwordx4 v13, s[36:39], s78 offen lds\n\t"
    "v_mfma_f32_16x16x32_bf16 v[140:143], a[44:47], a[96:99], v[140:143]\n\t"
    "ds_read_b64_tr_b16 a[12:13], v19 offset:264\n\t"
    "v_mfma_f32_16x16x32_bf16 v[144:147], a[48:51], a[96:99], v[144:147]\n\t"
    "ds_read_b64_tr_b16 a[14:15], v19 offset:1288\n\t"
    "v_mfma_f32_16x16x32_bf16 v[148:151], a[52:55], a[96:99], v[148:151]\n\t"
    "s_add_u32 m0, s64, 0x18400\n\t"
    "ds_read_b64_tr_b16 a[16:17], v19 offset:512\n\t"
    "v_mfma_f32_16x16x32_bf16 v[152:155], a[56:59], a[96:99], v[152:155]\n\t"
    "s_add_u32 s78, s96, 0x2000\n\t"
    "buffer_load_dwordx4 v14, s[36:39], s78 offen lds\n\t"
    "v_mfma_f32_16x16x32_bf16 v[156:159], a[60:63], a[96:99], v[156:159]\n\t"
    "ds_read_b64_tr_b16 a[18:19], v19 offset:1536\n\t"
    "v_mfma_f32_16x16x32_bf16 v[160:163], a[32:35], a[100:103], v[160:163]\n\t"
    "ds_read_b64_tr_b16 a[20:21], v19 offset:520\n\t"
    "v_mfma_f32_16x16x32_bf16 v[164:167], a[36:39], a[100:103], v[164:167]\n\t"
    "s_add_u32 m0, s64, 0x18800\n\t"
    "ds_read_b64_tr_b16 a[22:23], v19 offset:1544\n\t"
    "v_mfma_f32_16x16x32_bf16 v[168:171], a[40:43], a[100:103], v[168:171]\n\t"
    "s_add_u32 s78, s96, 0x4000\n\t"
    "buffer_load_dwordx4 v13, s[36:39], s78 offen lds\n\t"
    "v_mfma_f32_16x16x32_bf16 v[172:175], a[44:47], a[100:103], v[172:175]\n\t"
    "ds_read_b64_tr_b16 a[24:25], v19 offset:768\n\t"
    "v_mfma_f32_16x16x32_bf16 v[176:179], a[48:51], a[100:103], v[176:179]\n\t"
    "ds_read_b64_tr_b16 a[26:27], v19 offset:1792\n\t"
    "v_mfma_f32_16x16x32_bf16 v[180:183], a[52:55], a[100:103], v[180:183]\n\t"
    "s_add_u32 m0, s64, 0x18c00\n\t"
    "ds_read_b64_tr_b16 a[28:29], v19 offset:776\n\t"
    "v_mfma_f32_16x16x32_bf16 v[184:187], a[56:59], a[100:103], v[184:187]\n\t"
    "s_add_u32 s78, s96, 0x6000\n\t"
    "buffer_load_dwordx4 v14, s[36:39], s78 offen lds\n\t"
    "v_mfma_f32_16x16x32_bf16 v[188:191], a[60:63], a[100:103], v[188:191]\n\t"
    "ds_read_b64_tr_b16 a[30:31], v19 offset:1800\n\t"
    "v_mfma_f32_16x16x32_bf16 v[192:195], a[32:35], a[104:107], v[192:195]\n\t"
    "ds_read_b128 a[68:71], v21 offset:2048\n\t"
    "v_mfma_f32_16x16x32_bf16 v[196:199], a[36:39], a[104:107], v[196:199]\n\t"
    "s_add_u32 m0, s64, 0x19000\n\t"
    "ds_read_b128 a[72:75], v21 offset:4096\n\t"
    "v_mfma_f32_16x16x32_bf16 v[200:203], a[40:43], a[104:107], v[200:203]\n\t"
    "s_add_u32 s78, s96, 0x8000\n\t"
    "buffer_load_dwordx4 v13, s[36:39], s78 offen lds\n\t"
    "v_mfma_f32_16x16x32_bf16 v[204:207], a[44:47], a[104:107], v[204:207]\n\t"
    "ds_read_b128 a[76:79], v22 offset:0\n\t"
    "v_mfma_f32_16x16x32_bf16 v[208:211], a[48:51], a[104:107], v[208:211]\n\t"
    "ds_read_b128 a[80:83], v22 offset:2048\n\t"
    "v_mfma_f32_16x16x32_bf16 v[212:215], a[52:55], a[104:107], v[212:215]\n\t"
    "s_add_u32 m0, s64, 0x19400\n\t"
    "ds_read_b128 a[84:87], v22 offset:4096\n\t"
    "v_mfma_f32_16x16x32_bf16 v[216:219], a[56:59], a[104:107], v[216:219]\n\t"
    "s_add_u32 s78, s96, 0xa000\n\t"
    "buffer_load_dwordx4 v14, s[36:39], s78 offen lds\n\t"
    "v_mfma_f32_16x16x32_bf16 v[220:223], a[60:63], a[104:107], v[220:223]\n\t"
    "v_mfma_f32_16x16x32_bf16 v[224:227], a[32:35], a[108:111], v[224:227]\n\t"
    "v_mfma_f32_16x16x32_bf16 v[228:231], a[36:39], a[108:111], v[228:231]\n\t"
    "s_lshr_b32 s83, s83, 4\n\t"
    "s_add_u32 s79, s83, s82\n\t"
    "s_and_b32 s79, s79, 7\n\t"
    "s_lshl_b32 s79, s79, 7\n\t"
    "s_add_u32 s96, s67, s79\n\t"
    "s_add_u32 m0, s65, 0x8000\n\t"
    "v_mfma_f32_16x16x32_bf16 v[232:235], a[40:43], a[108:111], v[232:235]\n\t"
    "s_add_u32 s78, s68, 0\n\t"
    "buffer_load_dwordx4 v15, s[40:43], s78 offen lds\n\t"
    "v_mfma_f32_16x16x32_bf16 v[236:239], a[44:47], a[108:111], v[236:239]\n\t"
    "v_mfma_f32_16x16x32_bf16 v[240:243], a[48:51], a[108:111], v[240:243]\n\t"
    "v_mfma_f32_16x16x32_bf16 v[244:247], a[52:55], a[108:111], v[244:247]\n\t"
    "v_mfma_f32_16x16x32_bf16 v[248:251], a[56:59], a[108:111], v[248:251]\n\t"
    "v_mfma_f32_16x16x32_bf16 v[252:255], a[60:63], a[108:111], v[252:255]\n\t"
    "s_waitcnt lgkmcnt(0)\n\t"
    "v_mfma_f32_16x16x32_bf16 v[64:67], a[0:3], a[64:67], v[64:67]\n\t"
    "v_mfma_f32_16x16x32_bf16 v[68:71], a[4:7], a[64:67], v[68:71]\n\t"
    "v_mfma_f32_16x16x32_bf16 v[72:75], a[8:11], a[64:67], v[72:75]\n\t"
    "v_mfma_f32_16x16x32_bf16 v[76:79], a[12:15], a[64:67], v[76:79]\n\t"
    "v_mfma_f32_16x16x32_bf16 v[80:83], a[16:19], a[64:67], v[80:83]\n\t"
    "v_mfma_f32_16x16x32_bf16 v[84:87], a[20:23], a[64:67], v[84:87]\n\t"
    "v_mfma_f32_16x16x32_bf16 v[88:91], a[24:27], a[64:67], v[88:91]\n\t"
    "v_mfma_f32_16x16x32_bf16 v[92:95], a[28:31], a[64:67], v[92:95]\n\t"
    "v_mfma_f32_16x16x32_bf16 v[96:99], a[0:3], a[68:71], v[96:99]\n\t"
    "s_waitcnt vmcnt(7) lgkmcnt(0)\n\t"
    "s_barrier\n\t"
    "v_mfma_f32_16x16x32_bf16 v[100:103], a[4:7], a[68:71], v[100:103]\n\t"
    "s_add_u32 m0, s65, 0x8400\n\t"
    "ds_read_b128 a[88:91], v61 offset:0\n\t"
    "v_mfma_f32_16x16x32_bf16 v[104:107], a[8:11], a[68:71], v[104:107]\n\t"
    "s_add_u32 s78, s68, 0x8000\n\t"
    "buffer_load_dwordx4 v15, s[40:43], s78 offen lds\n\t"
    "v_mfma_f32_16x16x32_bf16 v[108:111], a[12:15], a[68:71], v[108:111]\n\t"
    "ds_read_b64_tr_b16 a[32:33], v19 offset:16384\n\t"
    "v_mfma_f32_16x16x32_bf16 v[112:115], a[16:19], a[68:71], v[112:115]\n\t"
    "ds_read_b64_tr_b16 a[34:35], v19 offset:17408\n\t"
    "v_mfma_f32_16x16x32_bf16 v[116:119], a[20:23], a[68:71], v[116:119]\n\t"
    "s_add_u32 m0, s65, 0x8800\n\t"
    "ds_read_b64_tr_b16 a[36:37], v19 offset:16392\n\t"
    "v_mfma_f32_16x16x32_bf16 v[120:123], a[24:27], a[68:71], v[120:123]\n\t"
    "s_add_u32 s78, s68, 0x10000\n\t"
    "buffer_load_dwordx4 v15, s[40:43], s78 offen lds\n\t"
    "v_mfma_f32_16x16x32_bf16 v[124:127], a[28:31], a[68:71], v[124:127]\n\t"
    "ds_read_b64_tr_b16 a[38:39], v19 offset:17416\n\t"
    "v_mfma_f32_16x16x32_bf16 v[128:131], a[0:3], a[72:75], v[128:131]\n\t"
    "ds_read_b64_tr_b16 a[40:41], v19 offset:16640\n\t"
    "v_mfma_f32_16x16x32_bf16 v[132:135], a[4:7], a[72:75], v[132:135]\n\t"
    "s_add_u32 m0, s65, 0x8c00\n\t"
    "ds_read_b64_tr_b16 a[42:43], v19 offset:17664\n\t"
    "v_mfma_f32_16x16x32_bf16 v[136:139], a[8:11], a[72:75], v[136:139]\n\t"
    "s_add_u32 s78, s68, 0x18000\n\t"
    "buffer_load_dwordx4 v15, s[40:43], s78 offen lds\n\t"
    "v_mfma_f32_16x16x32_bf16 v[140:143], a[12:15], a[72:75], v[140:143]\n\t"
    "ds_read_b64_tr_b16 a[44:45], v19 offset:16648\n\t"
    "v_mfma_f32_16x16x32_bf16 v[144:147], a[16:19], a[72:75], v[144:147]\n\t"
    "ds_read_b64_tr_b16 a[46:47], v19 offset:17672\n\t"
    "v_mfma_f32_16x16x32_bf16 v[148:151], a[20:23], a[72:75], v[148:151]\n\t"
    "s_add_u32 s68, s68, 0x40000\n\t"
    "s_and_b32 s68, s68, 0x3fffff\n\t"
    "s_add_u32 m0, s65, 0xc000\n\t"
    "ds_read_b64_tr_b16 a[48:49], v19 offset:16896\n\t"
    "v_mfma_f32_16x16x32_bf16 v[152:155], a[24:27], a[72:75], v[152:155]\n\t"
    "s_add_u32 s78, s68, 0\n\t"
    "buffer_load_dwordx4 v15, s[40:43], s78 offen lds\n\t"
    "v_mfma_f32_16x16x32_bf16 v[156:159], a[28:31], a[72:75], v[156:159]\n\t"
    "ds_read_b64_tr_b16 a[50:51], v19 offset:17920\n\t"
    "v_mfma_f32_16x16x32_bf16 v[160:163], a[0:3], a[76:79], v[160:163]\n\t"
    "ds_read_b64_tr_b16 a[52:53], v19 offset:16904\n\t"
    "v_mfma_f32_16x16x32_bf16 v[164:167], a[4:7], a[76:79], v[164:167]\n\t"
    "s_add_u32 m0, s65, 0xc400\n\t"
    "ds_read_b64_tr_b16 a[54:55], v19 offset:17928\n\t"
    "v_mfma_f32_16x16x32_bf16 v[168:171], a[8:11], a[76:79], v[168:171]\n\t"
    "s_add_u32 s78, s68, 0x8000\n\t"
    "buffer_load_dwordx4 v15, s[40:43], s78 offen lds\n\t"
    "v_mfma_f32_16x16x32_bf16 v[172:175], a[12:15], a[76:79], v[172:175]\n\t"
    "ds_read_b64_tr_b16 a[56:57], v19 offset:17152\n\t"
    "v_mfma_f32_16x16x32_bf16 v[176:179], a[16:19], a[76:79], v[176:179]\n\t"
    "ds_read_b64_tr_b16 a[58:59], v19 offset:18176\n\t"
    "v_mfma_f32_16x16x32_bf16 v[180:183], a[20:23], a[76:79], v[180:183]\n\t"
    "s_add_u32 m0, s65, 0xc800\n\t"
    "ds_read_b64_tr_b16 a[60:61], v19 offset:17160\n\t"
    "v_mfma_f32_16x16x32_bf16 v[184:187], a[24:27], a[76:79], v[184:187]\n\t"
    "s_add_u32 s78, s68, 0x10000\n\t"
    "buffer_load_dwordx4 v15, s[40:43], s78 offen lds\n\t"
    "v_mfma_f32_16x16x32_bf16 v[188:191], a[28:31], a[76:79], v[188:191]\n\t"
    "ds_read_b64_tr_b16 a[62:63], v19 offset:18184\n\t"
    "v_mfma_f32_16x16x32_bf16 v[192:195], a[0:3], a[80:83], v[192:195]\n\t"
    "ds_read_b128 a[92:95], v61 offset:2048\n\t"
    "v_mfma_f32_16x16x32_bf16 v[196:199], a[4:7], a[80:83], v[196:199]\n\t"
    "s_add_u32 m0, s65, 0xcc00\n\t"
    "ds_read_b128 a[96:99], v61 offset:4096\n\t"
    "v_mfma_f32_16x16x32_bf16 v[200:203], a[8:11], a[80:83], v[200:203]\n\t"
    "s_add_u32 s78, s68, 0x18000\n\t"
    "buffer_load_dwordx4 v15, s[40:43], s78 offen lds\n\t"
    "v_mfma_f32_16x16x32_bf16 v[204:207], a[12:15], a[80:83], v[204:207]\n\t"
    "ds_read_b128 a[100:103], v23 offset:0\n\t"
    "v_mfma_f32_16x16x32_bf16 v[208:211], a[16:19], a[80:83], v[208:211]\n\t"
    "ds_read_b128 a[104:107], v23 offset:2048\n\t"
    "v_mfma_f32_16x16x32_bf16 v[212:215], a[20:23], a[80:83], v[212:215]\n\t"
    "s_lshr_b32 s97, s97, 4\n\t"
    "s_add_u32 s63, s97, s82\n\t"
    "s_and_b32 s63, s63, 7\n\t"
    "s_lshl_b32 s63, s63, 19\n\t"
    "s_and_b32 s68, s68, 0x3ffff\n\t"
    "s_or_b32 s68, s68, s63\n\t"
    "s_add_u32 m0, s64, 0x2000\n\t"
    "ds_read_b128 a[108:111], v23 offset:4096\n\t"
    "v_mfma_f32_16x16x32_bf16 v[216:219], a[24:27], a[80:83], v[216:219]\n\t"
    "s_add_u32 s78, s96, 0\n\t"
    "buffer_load_dwordx4 v13, s[36:39], s78 offen lds\n\t"
    "v_mfma_f32_16x16x32_bf16 v[220:223], a[28:31], a[80:83], v[220:223]\n\t"
    "v_mfma_f32_16x16x32_bf16 v[224:227], a[0:3], a[84:87], v[224:227]\n\t"
    "v_mfma_f32_16x16x32_bf16 v[228:231], a[4:7], a[84:87], v[228:231]\n\t"
    "s_add_u32 m0, s64, 0x2400\n\t"
    "v_mfma_f32_16x16x32_bf16 v[232:235], a[8:11], a[84:87], v[232:235]\n\t"
    "s_add_u32 s78, s96, 0x2000\n\t"
    "buffer_load_dwordx4 v14, s[36:39], s78 offen lds\n\t"
    "v_mfma_f32_16x16x32_bf16 v[236:239], a[12:15], a[84:87], v[236:239]\n\t"
    "v_mfma_f32_16x16x32_bf16 v[240:243], a[16:19], a[84:87], v[240:243]\n\t"
    "v_mfma_f32_16x16x32_bf16 v[244:247], a[20:23], a[84:87], v[244:247]\n\t"
    "v_mfma_f32_16x16x32_bf16 v[248:251], a[24:27], a[84:87], v[248:251]\n\t"
    "v_mfma_f32_16x16x32_bf16 v[252:255], a[28:31], a[84:87], v[252:255]\n\t"
    "s_waitcnt lgkmcnt(0)\n\t"
    "v_mfma_f32_16x16x32_bf16 v[64:67], a[32:35], a[88:91], v[64:67]\n\t"
    "v_mfma_f32_16x16x32_bf16 v[68:71], a[36:39], a[88:91], v[68:71]\n\t"
    "v_mfma_f32_16x16x32_bf16 v[72:75], a[40:43], a[88:91], v[72:75]\n\t"
    "v_mfma_f32_16x16x32_bf16 v[76:79], a[44:47], a[88:91], v[76:79]\n\t"
    "v_mfma_f32_16x16x32_bf16 v[80:83], a[48:51], a[88:91], v[80:83]\n\t"
    "v_mfma_f32_16x16x32_bf16 v[84:87], a[52:55], a[88:91], v[84:87]\n\t"
    "v_mfma_f32_16x16x32_bf16 v[88:91], a[56:59], a[88:91], v[88:91]\n\t"
    "v_mfma_f32_16x16x32_bf16 v[92:95], a[60:63], a[88:91], v[92:95]\n\t"
    "v_mfma_f32_16x16x32_bf16 v[96:99], a[32:35], a[92:95], v[96:99]\n\t"
    "s_waitcnt vmcnt(6) lgkmcnt(0)\n\t"
    "s_barrier\n\t"
    "v_mfma_f32_16x16x32_bf16 v[100:103], a[36:39], a[92:95], v[100:103]\n\t"
    "s_add_u32 m0, s64, 0x2800\n\t"
    "ds_read_b128 a[64:67], v21 offset:24576\n\t"
    "v_mfma_f32_16x16x32_bf16 v[104:107], a[40:43], a[92:95], v[104:107]\n\t"
    "s_add_u32 s78, s96, 0x4000\n\t"
    "buffer_load_dwordx4 v13, s[36:39], s78 offen lds\n\t"
    "v_mfma_f32_16x16x32_bf16 v[108:111], a[44:47], a[92:95], v[108:111]\n\t"
    "ds_read_b64_tr_b16 a[0:1], v18 offset:0\n\t"
    "v_mfma_f32_16x16x32_bf16 v[112:115], a[48:51], a[92:95], v[112:115]\n\t"
    "s_add_u32 m0, s65, 0x1e000\n\t"
    "ds_read_b64_tr_b16 a[2:3], v18 offset:1024\n\t"
    "v_mfma_f32_16x16x32_bf16 v[116:119], a[52:55], a[92:95], v[116:119]\n\t"
    "s_add_u32 s78, s68, 0\n\t"
    "buffer_load_dwordx4 v15, s[40:43], s78 offen lds\n\t"
    "v_mfma_f32_16x16x32_bf16 v[120:123], a[56:59], a[92:95], v[120:123]\n\t"
    "ds_read_b64_tr_b16 a[4:5], v18 offset:8\n\t"
    "v_mfma_f32_16x16x32_bf16 v[124:127], a[60:63], a[92:95], v[124:127]\n\t"
    "s_add_u32 m0, s65, 0x1e400\n\t"
    "ds_read_b64_tr_b16 a[6:7], v18 offset:1032\n\t"
    "v_mfma_f32_16x16x32_bf16 v[128:131], a[32:35], a[96:99], v[128:131]\n\t"
    "s_add_u32 s78, s68, 0x8000\n\t"
    "buffer_load_dwordx4 v15, s[40:43], s78 offen lds\n\t"
    "v_mfma_f32_16x16x32_bf16 v[132:135], a[36:39], a[96:99], v[132:135]\n\t"
    "ds_read_b64_tr_b16 a[8:9], v18 offset:256\n\t"
    "v_mfma_f32_16x16x32_bf16 v[136:139], a[40:43], a[96:99], v[136:139]\n\t"
    "s_add_u32 m0, s65, 0x1e800\n\t"
    "ds_read_b64_tr_b16 a[10:11], v18 offset:1280\n\t"
    "v_mfma_f32_16x16x32_bf16 v[140:143], a[44:47], a[96:99], v[140:143]\n\t"
    "s_add_u32 s78, s68, 0x10000\n\t"
    "buffer_load_dwordx4 v15, s[40:43], s78 offen lds\n\t"
    "v_mfma_f32_16x16x32_bf16 v[144:147], a[48:51], a[96:99], v[144:147]\n\t"
    "ds_read_b64_tr_b16 a[12:13], v18 offset:264\n\t"
    "v_mfma_f32_16x16x32_bf16 v[148:151], a[52:55], a[96:99], v[148:151]\n\t"
    "s_add_u32 m0, s65, 0x1ec00\n\t"
    "ds_read_b64_tr_b16 a[14:15], v18 offset:1288\n\t"
    "v_mfma_f32_16x16x32_bf16 v[152:155], a[56:59], a[96:99], v[152:155]\n\t"
    "s_add_u32 s78, s68, 0x18000\n\t"
    "buffer_load_dwordx4 v15, s[40:43], s78 offen lds\n\t"
    "v_mfma_f32_16x16x32_bf16 v[156:159], a[60:63], a[96:99], v[156:159]\n\t"
    "ds_read_b64_tr_b16 a[16:17], v18 offset:512\n\t"
    "v_mfma_f32_16x16x32_bf16 v[160:163], a[32:35], a[100:103], v[160:163]\n\t"
    "s_add_u32 s68, s68, 0x40000\n\t"
    "s_and_b32 s68, s68, 0x3fffff\n\t"
    "s_add_u32 m0, s64, 0x2c00\n\t"
    "ds_read_b64_tr_b16 a[18:19], v18 offset:1536\n\t"
    "v_mfma_f32_16x16x32_bf16 v[164:167], a[36:39], a[100:103], v[164:167]\n\t"
    "s_add_u32 s78, s96, 0x6000\n\t"
    "buffer_load_dwordx4 v14, s[36:39], s78 offen lds\n\t"
    "v_mfma_f32_16x16x32_bf16 v[168:171], a[40:43], a[100:103], v[168:171]\n\t"
    "ds_read_b64_tr_b16 a[20:21], v18 offset:520\n\t"
    "v_mfma_f32_16x16x32_bf16 v[172:175], a[44:47], a[100:103], v[172:175]\n\t"
    "s_add_u32 m0, s64, 0x3000\n\t"
    "ds_read_b64_tr_b16 a[22:23], v18 offset:1544\n\t"
    "v_mfma_f32_16x16x32_bf16 v[176:179], a[48:51], a[100:103], v[176:179]\n\t"
    "s_add_u32 s78, s96, 0x8000\n\t"
    "buffer_load_dwordx4 v13, s[36:39], s78 offen lds\n\t"
    "v_mfma_f32_16x16x32_bf16 v[180:183], a[52:55], a[100:103], v[180:183]\n\t"
    "ds_read_b64_tr_b16 a[24:25], v18 offset:768\n\t"
    "v_mfma_f32_16x16x32_bf16 v[184:187], a[56:59], a[100:103], v[184:187]\n\t"
    "s_add_u32 m0, s64, 0x3400\n\t"
    "ds_read_b64_tr_b16 a[26:27], v18 offset:1792\n\t"
    "v_mfma_f32_16x16x32_bf16 v[188:191], a[60:63], a[100:103], v[188:191]\n\t"
    "s_add_u32 s78, s96, 0xa000\n\t"
    "buffer_load_dwordx4 v14, s[36:39], s78 offen lds\n\t"
    "v_mfma_f32_16x16x32_bf16 v[192:195], a[32:35], a[104:107], v[192:195]\n\t"
    "ds_read_b64_tr_b16 a[28:29], v18 offset:776\n\t"
    "v_mfma_f32_16x16x32_bf16 v[196:199], a[36:39], a[104:107], v[196:199]\n\t"
    "s_lshr_b32 s83, s83, 4\n\t"
    "s_add_u32 s79, s83, s82\n\t"
    "s_and_b32 s79, s79, 7\n\t"
    "s_lshl_b32 s79, s79, 7\n\t"
    "s_add_u32 s96, s67, s79\n\t"
    "s_add_u32 m0, s65, 0x22000\n\t"
    "ds_read_b64_tr_b16 a[30:31], v18 offset:1800\n\t"
    "v_mfma_f32_16x16x32_bf16 v[200:203], a[40:43], a[104:107], v[200:203]\n\t"
    "s_add_u32 s78, s68, 0\n\t"
    "buffer_load_dwordx4 v15, s[40:43], s78 offen lds\n\t"
    "v_mfma_f32_16x16x32_bf16 v[204:207], a[44:47], a[104:107], v[204:207]\n\t"
    "ds_read_b128 a[68:71], v21 offset:26624\n\t"
    "v_mfma_f32_16x16x32_bf16 v[208:211], a[48:51], a[104:107], v[208:211]\n\t"
    "s_add_u32 m0, s66, 0\n\t"
    "ds_read_b128 a[72:75], v21 offset:28672\n\t"
    "v_mfma_f32_16x16x32_bf16 v[212:215], a[52:55], a[104:107], v[212:215]\n\t"
    "s_add_u32 s78, s69, 0\n\t"
    "s_and_b32 s78, s78, 0x1ffff\n\t"
    "buffer_load_dwordx4 v7, s[44:47], s78 offen nt lds\n\t"
    "v_mfma_f32_16x16x32_bf16 v[216:219], a[56:59], a[104:107], v[216:219]\n\t"
    "ds_read_b128 a[76:79], v22 offset:24576\n\t"
    "v_mfma_f32_16x16x32_bf16 v[220:223], a[60:63], a[104:107], v[220:223]\n\t"
    "s_add_u32 m0, s66, 0\n\t"
    "ds_read_b128 a[80:83], v22 offset:26624\n\t"
    "v_mfma_f32_16x16x32_bf16 v[224:227], a[32:35], a[108:111], v[224:227]\n\t"
    "s_add_u32 s78, s69, 0\n\t"
    "s_and_b32 s78, s78, 0x1ffff\n\t"
    "buffer_load_dwordx4 v8, s[44:47], s78 offen offset:1024 nt lds\n\t"
    "v_mfma_f32_16x16x32_bf16 v[228:231], a[36:39], a[108:111], v[228:231]\n\t"
    "ds_read_b128 a[84:87], v22 offset:28672\n\t"
    "v_mfma_f32_16x16x32_bf16 v[232:235], a[40:43], a[108:111], v[232:235]\n\t"
    "v_mfma_f32_16x16x32_bf16 v[236:239], a[44:47], a[108:111], v[236:239]\n\t"
    "v_mfma_f32_16x16x32_bf16 v[240:243], a[48:51], a[108:111], v[240:243]\n\t"
    "v_mfma_f32_16x16x32_bf16 v[244:247], a[52:55], a[108:111], v[244:247]\n\t"
    "v_mfma_f32_16x16x32_bf16 v[248:251], a[56:59], a[108:111], v[248:251]\n\t"
    "v_mfma_f32_16x16x32_bf16 v[252:255], a[60:63], a[108:111], v[252:255]\n\t"
    "s_waitcnt lgkmcnt(0)\n\t"
    "v_mfma_f32_16x16x32_bf16 v[64:67], a[0:3], a[64:67], v[64:67]\n\t"
    "v_mfma_f32_16x16x32_bf16 v[68:71], a[4:7], a[64:67], v[68:71]\n\t"
    "s_add_u32 m0, s66, 0\n\t"
    "v_mfma_f32_16x16x32_bf16 v[72:75], a[8:11], a[64:67], v[72:75]\n\t"
    "s_add_u32 s78, s69, 0\n\t"
    "s_and_b32 s78, s78, 0x1ffff\n\t"
    "buffer_load_dwordx4 v9, s[44:47], s78 offen offset:2048 nt lds\n\t"
    "v_mfma_f32_16x16x32_bf16 v[76:79], a[12:15], a[64:67], v[76:79]\n\t"
    "v_mfma_f32_16x16x32_bf16 v[80:83], a[16:19], a[64:67], v[80:83]\n\t"
    "v_mfma_f32_16x16x32_bf16 v[84:87], a[20:23], a[64:67], v[84:87]\n\t"
    "s_add_u32 m0, s66, 0\n\t"
    "v_mfma_f32_16x16x32_bf16 v[88:91], a[24:27], a[64:67], v[88:91]\n\t"
    "s_add_u32 s78, s69, 0\n\t"
    "s_and_b32 s78, s78, 0x1ffff\n\t"
    "buffer_load_dwordx4 v10, s[44:47], s78 offen offset:3072 nt lds\n\t"
    "v_mfma_f32_16x16x32_bf16 v[92:95], a[28:31], a[64:67], v[92:95]\n\t"
    "v_mfma_f32_16x16x32_bf16 v[96:99], a[0:3], a[68:71], v[96:99]\n\t"
    "s_waitcnt vmcnt(15) lgkmcnt(0)\n\t"
    "s_barrier\n\t"
    "v_mfma_f32_16x16x32_bf16 v[100:103], a[4:7], a[68:71], v[100:103]\n\t"
    "s_add_u32 m0, s65, 0x22400\n\t"
    "ds_read_b128 a[88:91], v61 offset:24576\n\t"
    "v_mfma_f32_16x16x32_bf16 v[104:107], a[8:11], a[68:71], v[104:107]\n\t"
    "s_add_u32 s78, s68, 0x8000\n\t"
    "buffer_load_dwordx4 v15, s[40:43], s78 offen lds\n\t"
    "v_mfma_f32_16x16x32_bf16 v[108:111], a[12:15], a[68:71], v[108:111]\n\t"
    "ds_read_b64_tr_b16 a[32:33], v18 offset:16384\n\t"
    "v_mfma_f32_16x16x32_bf16 v[112:115], a[16:19], a[68:71], v[112:115]\n\t"
    "ds_read_b64_tr_b16 a[34:35], v18 offset:17408\n\t"
    "v_mfma_f32_16x16x32_bf16 v[116:119], a[20:23], a[68:71], v[116:119]\n\t"
    "s_add_u32 m0, s65, 0x22800\n\t"
    "ds_read_b64_tr_b16 a[36:37], v18 offset:16392\n\t"
    "v_mfma_f32_16x16x32_bf16 v[120:123], a[24:27], a[68:71], v[120:123]\n\t"
    "s_add_u32 s78, s68, 0x10000\n\t"
    "buffer_load_dwordx4 v15, s[40:43], s78 offen lds\n\t"
    "v_mfma_f32_16x16x32_bf16 v[124:127], a[28:31], a[68:71], v[124:127]\n\t"
    "ds_read_b64_tr_b16 a[38:39], v18 offset:17416\n\t"
    "v_mfma_f32_16x16x32_bf16 v[128:131], a[0:3], a[72:75], v[128:131]\n\t"
    "ds_read_b64_tr_b16 a[40:41], v18 offset:16640\n\t"
    "v_mfma_f32_16x16x32_bf16 v[132:135], a[4:7], a[72:75], v[132:135]\n\t"
    "s_add_u32 m0, s65, 0x22c00\n\t"
    "ds_read_b64_tr_b16 a[42:43], v18 offset:17664\n\t"
    "v_mfma_f32_16x16x32_bf16 v[136:139], a[8:11], a[72:75], v[136:139]\n\t"
    "s_add_u32 s78, s68, 0x18000\n\t"
    "buffer_load_dwordx4 v15, s[40:43], s78 offen lds\n\t"
    "v_mfma_f32_16x16x32_bf16 v[140:143], a[12:15], a[72:75], v[140:143]\n\t"
    "ds_read_b64_tr_b16 a[44:45], v18 offset:16648\n\t"
    "v_mfma_f32_16x16x32_bf16 v[144:147], a[16:19], a[72:75], v[144:147]\n\t"
    "ds_read_b64_tr_b16 a[46:47], v18 offset:17672\n\t"
    "v_mfma_f32_16x16x32_bf16 v[148:151], a[20:23], a[72:75], v[148:151]\n\t"
    "s_lshr_b32 s97, s97, 4\n\t"
    "s_add_u32 s63, s97, s82\n\t"
    "s_and_b32 s63, s63, 7\n\t"
    "s_lshl_b32 s63, s63, 19\n\t"
    "s_and_b32 s68, s68, 0x3ffff\n\t"
    "s_or_b32 s68, s68, s63\n\t"
    "s_add_u32 m0, s64, 0x12000\n\t"
    "ds_read_b64_tr_b16 a[48:49], v18 offset:16896\n\t"
    "v_mfma_f32_16x16x32_bf16 v[152:155], a[24:27], a[72:75], v[152:155]\n\t"
    "s_add_u32 s78, s96, 0\n\t"
    "buffer_load_dwordx4 v13, s[36:39], s78 offen lds\n\t"
    "v_mfma_f32_16x16x32_bf16 v[156:159], a[28:31], a[72:75], v[156:159]\n\t"
    "ds_read_b64_tr_b16 a[50:51], v18 offset:17920\n\t"
    "v_mfma_f32_16x16x32_bf16 v[160:163], a[0:3], a[76:79], v[160:163]\n\t"
    "ds_read_b64_tr_b16 a[52:53], v18 offset:16904\n\t"
    "v_mfma_f32_16x16x32_bf16 v[164:167], a[4:7], a[76:79], v[164:167]\n\t"
    "s_add_u32 m0, s64, 0x12400\n\t"
    "ds_read_b64_tr_b16 a[54:55], v18 offset:17928\n\t"
    "v_mfma_f32_16x16x32_bf16 v[168:171], a[8:11], a[76:79], v[168:171]\n\t"
    "s_add_u32 s78, s96, 0x2000\n\t"
    "buffer_load_dwordx4 v14, s[36:39], s78 offen lds\n\t"
    "v_mfma_f32_16x16x32_bf16 v[172:175], a[12:15], a[76:79], v[172:175]\n\t"
    "ds_read_b64_tr_b16 a[56:57], v18 offset:17152\n\t"
    "v_mfma_f32_16x16x32_bf16 v[176:179], a[16:19], a[76:79], v[176:179]\n\t"
    "ds_read_b64_tr_b16 a[58:59], v18 offset:18176\n\t"
    "v_mfma_f32_16x16x32_bf16 v[180:183], a[20:23], a[76:79], v[180:183]\n\t"
    "s_add_u32 m0, s64, 0x12800\n\t"
    "ds_read_b64_tr_b16 a[60:61], v18 offset:17160\n\t"
    "v_mfma_f32_16x16x32_bf16 v[184:187], a[24:27], a[76:79], v[184:187]\n\t"
    "s_add_u32 s78, s96, 0x4000\n\t"
    "buffer_load_dwordx4 v13, s[36:39], s78 offen lds\n\t"
    "v_mfma_f32_16x16x32_bf16 v[188:191], a[28:31], a[76:79], v[188:191]\n\t"
    "ds_read_b64_tr_b16 a[62:63], v18 offset:18184\n\t"
    "v_mfma_f32_16x16x32_bf16 v[192:195], a[0:3], a[80:83], v[192:195]\n\t"
    "ds_read_b128 a[92:95], v61 offset:26624\n\t"
    "v_mfma_f32_16x16x32_bf16 v[196:199], a[4:7], a[80:83], v[196:199]\n\t"
    "s_add_u32 m0, s65, 0x8000\n\t"
    "ds_read_b128 a[96:99], v61 offset:28672\n\t"
    "v_mfma_f32_16x16x32_bf16 v[200:203], a[8:11], a[80:83], v[200:203]\n\t"
    "s_add_u32 s78, s68, 0\n\t"
    "buffer_load_dwordx4 v15, s[40:43], s78 offen lds\n\t"
    "v_mfma_f32_16x16x32_bf16 v[204:207], a[12:15], a[80:83], v[204:207]\n\t"
    "ds_read_b128 a[100:103], v23 offset:24576\n\t"
    "v_mfma_f32_16x16x32_bf16 v[208:211], a[16:19], a[80:83], v[208:211]\n\t"
    "ds_read_b128 a[104:107], v23 offset:26624\n\t"
    "v_mfma_f32_16x16x32_bf16 v[212:215], a[20:23], a[80:83], v[212:215]\n\t"
    "s_add_u32 m0, s65, 0x8400\n\t"
    "ds_read_b128 a[108:111], v23 offset:28672\n\t"
    "v_mfma_f32_16x16x32_bf16 v[216:219], a[24:27], a[80:83], v[216:219]\n\t"
    "s_add_u32 s78, s68, 0x8000\n\t"
    "buffer_load_dwordx4 v15, s[40:43], s78 offen lds\n\t"
    "v_mfma_f32_16x16x32_bf16 v[220:223], a[28:31], a[80:83], v[220:223]\n\t"
    "v_mfma_f32_16x16x32_bf16 v[224:227], a[0:3], a[84:87], v[224:227]\n\t"
    "v_mfma_f32_16x16x32_bf16 v[228:231], a[4:7], a[84:87], v[228:231]\n\t"
    "s_add_u32 m0, s65, 0x8800\n\t"
    "v_mfma_f32_16x16x32_bf16 v[232:235], a[8:11], a[84:87], v[232:235]\n\t"
    "s_add_u32 s78, s68, 0x10000\n\t"
    "buffer_load_dwordx4 v15, s[40:43], s78 offen lds\n\t"
    "v_mfma_f32_16x16x32_bf16 v[236:239], a[12:15], a[84:87], v[236:239]\n\t"
    "v_mfma_f32_16x16x32_bf16 v[240:243], a[16:19], a[84:87], v[240:243]\n\t"
    "v_mfma_f32_16x16x32_bf16 v[244:247], a[20:23], a[84:87], v[244:247]\n\t"
    "v_mfma_f32_16x16x32_bf16 v[248:251], a[24:27], a[84:87], v[248:251]\n\t"
    "v_mfma_f32_16x16x32_bf16 v[252:255], a[28:31], a[84:87], v[252:255]\n\t"
    "s_waitcnt lgkmcnt(0)\n\t"
    "v_mfma_f32_16x16x32_bf16 v[64:67], a[32:35], a[88:91], v[64:67]\n\t"
    "v_mfma_f32_16x16x32_bf16 v[68:71], a[36:39], a[88:91], v[68:71]\n\t"
    "v_mfma_f32_16x16x32_bf16 v[72:75], a[40:43], a[88:91], v[72:75]\n\t"
    "v_mfma_f32_16x16x32_bf16 v[76:79], a[44:47], a[88:91], v[76:79]\n\t"
    "v_mfma_f32_16x16x32_bf16 v[80:83], a[48:51], a[88:91], v[80:83]\n\t"
    "v_mfma_f32_16x16x32_bf16 v[84:87], a[52:55], a[88:91], v[84:87]\n\t"
    "v_mfma_f32_16x16x32_bf16 v[88:91], a[56:59], a[88:91], v[88:91]\n\t"
    "v_mfma_f32_16x16x32_bf16 v[92:95], a[60:63], a[88:91], v[92:95]\n\t"
    "v_mfma_f32_16x16x32_bf16 v[96:99], a[32:35], a[92:95], v[96:99]\n\t"
    "s_waitcnt vmcnt(14) lgkmcnt(0)\n\t"
    "s_barrier\n\t"
    "v_mfma_f32_16x16x32_bf16 v[100:103], a[36:39], a[92:95], v[100:103]\n\t"
    "s_add_u32 m0, s65, 0x8c00\n\t"
    "ds_read_b128 a[64:67], v16 offset:0\n\t"
    "v_mfma_f32_16x16x32_bf16 v[104:107], a[40:43], a[92:95], v[104:107]\n\t"
    "s_add_u32 s78, s68, 0x18000\n\t"
    "buffer_load_dwordx4 v15, s[40:43], s78 offen lds\n\t"
    "v_mfma_f32_16x16x32_bf16 v[108:111], a[44:47], a[92:95], v[108:111]\n\t"
    "ds_read_b64_tr_b16 a[0:1], v19 offset:0\n\t"
    "v_mfma_f32_16x16x32_bf16 v[112:115], a[48:51], a[92:95], v[112:115]\n\t"
    "s_add_u32 s68, s68, 0x40000\n\t"
    "s_and_b32 s68, s68, 0x3fffff\n\t"
    "s_add_u32 m0, s64, 0x12c00\n\t"
    "ds_read_b64_tr_b16 a[2:3], v19 offset:1024\n\t"
    "v_mfma_f32_16x16x32_bf16 v[116:119], a[52:55], a[92:95], v[116:119]\n\t"
    "s_add_u32 s78, s96, 0x6000\n\t"
    "buffer_load_dwordx4 v14, s[36:39], s78 offen lds\n\t"
    "v_mfma_f32_16x16x32_bf16 v[120:123], a[56:59], a[92:95], v[120:123]\n\t"
    "ds_read_b64_tr_b16 a[4:5], v19 offset:8\n\t"
    "v_mfma_f32_16x16x32_bf16 v[124:127], a[60:63], a[92:95], v[124:127]\n\t"
    "s_add_u32 m0, s64, 0x13000\n\t"
    "ds_read_b64_tr_b16 a[6:7], v19 offset:1032\n\t"
    "v_mfma_f32_16x16x32_bf16 v[128:131], a[32:35], a[96:99], v[128:131]\n\t"
    "s_add_u32 s78, s96, 0x8000\n\t"
    "buffer_load_dwordx4 v13, s[36:39], s78 offen lds\n\t"
    "v_mfma_f32_16x16x32_bf16 v[132:135], a[36:39], a[96:99], v[132:135]\n\t"
    "ds_read_b64_tr_b16 a[8:9], v19 offset:256\n\t"
    "v_mfma_f32_16x16x32_bf16 v[136:139], a[40:43], a[96:99], v[136:139]\n\t"
    "s_add_u32 m0, s64, 0x13400\n\t"
    "ds_read_b64_tr_b16 a[10:11], v19 offset:1280\n\t"
    "v_mfma_f32_16x16x32_bf16 v[140:143], a[44:47], a[96:99], v[140:143]\n\t"
    "s_add_u32 s78, s96, 0xa000\n\t"
    "buffer_load_dwordx4 v14, s[36:39], s78 offen lds\n\t"
    "v_mfma_f32_16x16x32_bf16 v[144:147], a[48:51], a[96:99], v[144:147]\n\t"
    "ds_read_b64_tr_b16 a[12:13], v19 offset:264\n\t"
    "v_mfma_f32_16x16x32_bf16 v[148:151], a[52:55], a[96:99], v[148:151]\n\t"
    "s_lshr_b32 s83, s83, 4\n\t"
    "s_add_u32 s79, s83, s82\n\t"
    "s_and_b32 s79, s79, 7\n\t"
    "s_lshl_b32 s79, s79, 7\n\t"
    "s_add_u32 s96, s67, s79\n\t"
    "s_add_u32 m0, s65, 0xc000\n\t"
    "ds_read_b64_tr_b16 a[14:15], v19 offset:1288\n\t"
    "v_mfma_f32_16x16x32_bf16 v[152:155], a[56:59], a[96:99], v[152:155]\n\t"
    "s_add_u32 s78, s68, 0\n\t"
    "buffer_load_dwordx4 v15, s[40:43], s78 offen lds\n\t"
    "v_mfma_f32_16x16x32_bf16 v[156:159], a[60:63], a[96:99], v[156:159]\n\t"
    "ds_read_b64_tr_b16 a[16:17], v19 offset:512\n\t"
    "v_mfma_f32_16x16x32_bf16 v[160:163], a[32:35], a[100:103], v[160:163]\n\t"
    "s_add_u32 m0, s65, 0xc400\n\t"
    "ds_read_b64_tr_b16 a[18:19], v19 offset:1536\n\t"
    "v_mfma_f32_16x16x32_bf16 v[164:167], a[36:39], a[100:103], v[164:167]\n\t"
    "s_add_u32 s78, s68, 0x8000\n\t"
    "buffer_load_dwordx4 v15, s[40:43], s78 offen lds\n\t"
    "v_mfma_f32_16x16x32_bf16 v[168:171], a[40:43], a[100:103], v[168:171]\n\t"
    "ds_read_b64_tr_b16 a[20:21], v19 offset:520\n\t"
    "v_mfma_f32_16x16x32_bf16 v[172:175], a[44:47], a[100:103], v[172:175]\n\t"
    "s_add_u32 m0, s65, 0xc800\n\t"
    "ds_read_b64_tr_b16 a[22:23], v19 offset:1544\n\t"
    "v_mfma_f32_16x16x32_bf16 v[176:179], a[48:51], a[100:103], v[176:179]\n\t"
    "s_add_u32 s78, s68, 0x10000\n\t"
    "buffer_load_dwordx4 v15, s[40:43], s78 offen lds\n\t"
    "v_mfma_f32_16x16x32_bf16 v[180:183], a[52:55], a[100:103], v[180:183]\n\t"
    "ds_read_b64_tr_b16 a[24:25], v19 offset:768\n\t"
    "v_mfma_f32_16x16x32_bf16 v[184:187], a[56:59], a[100:103], v[184:187]\n\t"
    "s_add_u32 m0, s65, 0xcc00\n\t"
    "ds_read_b64_tr_b16 a[26:27], v19 offset:1792\n\t"
    "v_mfma_f32_16x16x32_bf16 v[188:191], a[60:63], a[100:103], v[188:191]\n\t"
    "s_add_u32 s78, s68, 0x18000\n\t"
    "buffer_load_dwordx4 v15, s[40:43], s78 offen lds\n\t"
    "v_mfma_f32_16x16x32_bf16 v[192:195], a[32:35], a[104:107], v[192:195]\n\t"
    "ds_read_b64_tr_b16 a[28:29], v19 offset:776\n\t"
    "v_mfma_f32_16x16x32_bf16 v[196:199], a[36:39], a[104:107], v[196:199]\n\t"
    "s_lshr_b32 s97, s97, 4\n\t"
    "s_add_u32 s63, s97, s82\n\t"
    "s_and_b32 s63, s63, 7\n\t"
    "s_lshl_b32 s63, s63, 19\n\t"
    "s_and_b32 s68, s68, 0x3ffff\n\t"
    "s_or_b32 s68, s68, s63\n\t"
    "s_add_u32 m0, s64, 0x18000\n\t"
    "ds_read_b64_tr_b16 a[30:31], v19 offset:1800\n\t"
    "v_mfma_f32_16x16x32_bf16 v[200:203], a[40:43], a[104:107], v[200:203]\n\t"
    "s_add_u32 s78, s96, 0\n\t"
    "buffer_load_dwordx4 v13, s[36:39], s78 offen lds\n\t"
    "v_mfma_f32_16x16x32_bf16 v[204:207], a[44:47], a[104:107], v[204:207]\n\t"
    "ds_read_b128 a[68:71], v16 offset:2048\n\t"
    "v_mfma_f32_16x16x32_bf16 v[208:211], a[48:51], a[104:107], v[208:211]\n\t"
    "ds_read_b128 a[72:75], v16 offset:4096\n\t"
    "v_mfma_f32_16x16x32_bf16 v[212:215], a[52:55], a[104:107], v[212:215]\n\t"
    "s_add_u32 s78, s69, 0\n\t"
    "s_and_b32 s78, s78, 0x1ffff\n\t"
    "buffer_load_dwordx4 a[192:195], v11, s[48:51], s78 offen nt\n\t"
    "v_mfma_f32_16x16x32_bf16 v[216:219], a[56:59], a[104:107], v[216:219]\n\t"
    "ds_read_b128 a[76:79], v56 offset:0\n\t"
    "v_mfma_f32_16x16x32_bf16 v[220:223], a[60:63], a[104:107], v[220:223]\n\t"
    "ds_read_b128 a[80:83], v56 offset:2048\n\t"
    "v_mfma_f32_16x16x32_bf16 v[224:227], a[32:35], a[108:111], v[224:227]\n\t"
    "s_add_u32 s78, s69, 0\n\t"
    "s_and_b32 s78, s78, 0x1ffff\n\t"
    "buffer_load_dwordx4 a[196:199], v12, s[48:51], s78 offen offset:1024 nt\n\t"
    "v_mfma_f32_16x16x32_bf16 v[228:231], a[36:39], a[108:111], v[228:231]\n\t"
    "ds_read_b128 a[84:87], v56 offset:4096\n\t"
    "v_mfma_f32_16x16x32_bf16 v[232:235], a[40:43], a[108:111], v[232:235]\n\t"
    "v_mfma_f32_16x16x32_bf16 v[236:239], a[44:47], a[108:111], v[236:239]\n\t"
    "v_mfma_f32_16x16x32_bf16 v[240:243], a[48:51], a[108:111], v[240:243]\n\t"
    "v_mfma_f32_16x16x32_bf16 v[244:247], a[52:55], a[108:111], v[244:247]\n\t"
    "v_mfma_f32_16x16x32_bf16 v[248:251], a[56:59], a[108:111], v[248:251]\n\t"
    "v_mfma_f32_16x16x32_bf16 v[252:255], a[60:63], a[108:111], v[252:255]\n\t"
    "s_waitcnt lgkmcnt(0)\n\t"
    "v_mfma_f32_16x16x32_bf16 v[64:67], a[0:3], a[64:67], v[64:67]\n\t"
    "v_mfma_f32_16x16x32_bf16 v[68:71], a[4:7], a[64:67], v[68:71]\n\t"
    "v_mfma_f32_16x16x32_bf16 v[72:75], a[8:11], a[64:67], v[72:75]\n\t"
    "s_add_u32 s78, s69, 0\n\t"
    "s_and_b32 s78, s78, 0x1ffff\n\t"
    "buffer_load_dwordx4 a[200:203], v11, s[48:51], s78 offen offset:2048 nt\n\t"
    "v_mfma_f32_16x16x32_bf16 v[76:79], a[12:15], a[64:67], v[76:79]\n\t"
    "v_mfma_f32_16x16x32_bf16 v[80:83], a[16:19], a[64:67], v[80:83]\n\t"
    "v_mfma_f32_16x16x32_bf16 v[84:87], a[20:23], a[64:67], v[84:87]\n\t"
    "v_mfma_f32_16x16x32_bf16 v[88:91], a[24:27], a[64:67], v[88:91]\n\t"
    "s_add_u32 s78, s69, 0\n\t"
    "s_and_b32 s78, s78, 0x1ffff\n\t"
    "buffer_load_dwordx4 a[204:207], v12, s[48:51], s78 offen offset:3072 nt\n\t"
    "v_mfma_f32_16x16x32_bf16 v[92:95], a[28:31], a[64:67], v[92:95]\n\t"
    "v_mfma_f32_16x16x32_bf16 v[96:99], a[0:3], a[68:71], v[96:99]\n\t"
    "s_waitcnt vmcnt(19) lgkmcnt(0)\n\t"
    "s_barrier\n\t"
    "v_mfma_f32_16x16x32_bf16 v[100:103], a[4:7], a[68:71], v[100:103]\n\t"
    "s_add_u32 m0, s64, 0x18400\n\t"
    "ds_read_b128 a[88:91], v17 offset:0\n\t"
    "v_mfma_f32_16x16x32_bf16 v[104:107], a[8:11], a[68:71], v[104:107]\n\t"
    "s_add_u32 s78, s96, 0x2000\n\t"
    "buffer_load_dwordx4 v14, s[36:39], s78 offen lds\n\t"
    "v_mfma_f32_16x16x32_bf16 v[108:111], a[12:15], a[68:71], v[108:111]\n\t"
    "ds_read_b64_tr_b16 a[32:33], v19 offset:16384\n\t"
    "v_mfma_f32_16x16x32_bf16 v[112:115], a[16:19], a[68:71], v[112:115]\n\t"
    "ds_read_b64_tr_b16 a[34:35], v19 offset:17408\n\t"
    "v_mfma_f32_16x16x32_bf16 v[116:119], a[20:23], a[68:71], v[116:119]\n\t"
    "s_add_u32 m0, s64, 0x18800\n\t"
    "ds_read_b64_tr_b16 a[36:37], v19 offset:16392\n\t"
    "v_mfma_f32_16x16x32_bf16 v[120:123], a[24:27], a[68:71], v[120:123]\n\t"
    "s_add_u32 s78, s96, 0x4000\n\t"
    "buffer_load_dwordx4 v13, s[36:39], s78 offen lds\n\t"
    "v_mfma_f32_16x16x32_bf16 v[124:127], a[28:31], a[68:71], v[124:127]\n\t"
    "ds_read_b64_tr_b16 a[38:39], v19 offset:17416\n\t"
    "v_mfma_f32_16x16x32_bf16 v[128:131], a[0:3], a[72:75], v[128:131]\n\t"
    "ds_read_b64_tr_b16 a[40:41], v19 offset:16640\n\t"
    "v_mfma_f32_16x16x32_bf16 v[132:135], a[4:7], a[72:75], v[132:135]\n\t"
    "s_add_u32 m0, s65, 0x1e000\n\t"
    "ds_read_b64_tr_b16 a[42:43], v19 offset:17664\n\t"
    "v_mfma_f32_16x16x32_bf16 v[136:139], a[8:11], a[72:75], v[136:139]\n\t"
    "s_add_u32 s78, s68, 0\n\t"
    "buffer_load_dwordx4 v15, s[40:43], s78 offen lds\n\t"
    "v_mfma_f32_16x16x32_bf16 v[140:143], a[12:15], a[72:75], v[140:143]\n\t"
    "ds_read_b64_tr_b16 a[44:45], v19 offset:16648\n\t"
    "v_mfma_f32_16x16x32_bf16 v[144:147], a[16:19], a[72:75], v[144:147]\n\t"
    "ds_read_b64_tr_b16 a[46:47], v19 offset:17672\n\t"
    "v_mfma_f32_16x16x32_bf16 v[148:151], a[20:23], a[72:75], v[148:151]\n\t"
    "s_add_u32 m0, s65, 0x1e400\n\t"
    "ds_read_b64_tr_b16 a[48:49], v19 offset:16896\n\t"
    "v_mfma_f32_16x16x32_bf16 v[152:155], a[24:27], a[72:75], v[152:155]\n\t"
    "s_add_u32 s78, s68, 0x8000\n\t"
    "buffer_load_dwordx4 v15, s[40:43], s78 offen lds\n\t"
    "v_mfma_f32_16x16x32_bf16 v[156:159], a[28:31], a[72:75], v[156:159]\n\t"
    "ds_read_b64_tr_b16 a[50:51], v19 offset:17920\n\t"
    "v_mfma_f32_16x16x32_bf16 v[160:163], a[0:3], a[76:79], v[160:163]\n\t"
    "ds_read_b64_tr_b16 a[52:53], v19 offset:16904\n\t"
    "v_mfma_f32_16x16x32_bf16 v[164:167], a[4:7], a[76:79], v[164:167]\n\t"
    "s_add_u32 m0, s65, 0x1e800\n\t"
    "ds_read_b64_tr_b16 a[54:55], v19 offset:17928\n\t"
    "v_mfma_f32_16x16x32_bf16 v[168:171], a[8:11], a[76:79], v[168:171]\n\t"
    "s_add_u32 s78, s68, 0x10000\n\t"
    "buffer_load_dwordx4 v15, s[40:43], s78 offen lds\n\t"
    "v_mfma_f32_16x16x32_bf16 v[172:175], a[12:15], a[76:79], v[172:175]\n\t"
    "ds_read_b64_tr_b16 a[56:57], v19 offset:17152\n\t"
    "v_mfma_f32_16x16x32_bf16 v[176:179], a[16:19], a[76:79], v[176:179]\n\t"
    "ds_read_b64_tr_b16 a[58:59], v19 offset:18176\n\t"
    "v_mfma_f32_16x16x32_bf16 v[180:183], a[20:23], a[76:79], v[180:183]\n\t"
    "s_add_u32 m0, s65, 0x1ec00\n\t"
    "ds_read_b64_tr_b16 a[60:61], v19 offset:17160\n\t"
    "v_mfma_f32_16x16x32_bf16 v[184:187], a[24:27], a[76:79], v[184:187]\n\t"
    "s_add_u32 s78, s68, 0x18000\n\t"
    "buffer_load_dwordx4 v15, s[40:43], s78 offen lds\n\t"
    "v_mfma_f32_16x16x32_bf16 v[188:191], a[28:31], a[76:79], v[188:191]\n\t"
    "ds_read_b64_tr_b16 a[62:63], v19 offset:18184\n\t"
    "v_mfma_f32_16x16x32_bf16 v[192:195], a[0:3], a[80:83], v[192:195]\n\t"
    "ds_read_b128 a[92:95], v17 offset:2048\n\t"
    "v_mfma_f32_16x16x32_bf16 v[196:199], a[4:7], a[80:83], v[196:199]\n\t"
    "s_add_u32 s68, s68, 0x40000\n\t"
    "s_and_b32 s68, s68, 0x3fffff\n\t"
    "s_add_u32 m0, s64, 0x18c00\n\t"
    "ds_read_b128 a[96:99], v17 offset:4096\n\t"
    "v_mfma_f32_16x16x32_bf16 v[200:203], a[8:11], a[80:83], v[200:203]\n\t"
    "s_add_u32 s78, s96, 0x6000\n\t"
    "buffer_load_dwordx4 v14, s[36:39], s78 offen lds\n\t"
    "v_mfma_f32_16x16x32_bf16 v[204:207], a[12:15], a[80:83], v[204:207]\n\t"
    "ds_read_b128 a[100:103], v57 offset:0\n\t"
    "v_mfma_f32_16x16x32_bf16 v[208:211], a[16:19], a[80:83], v[208:211]\n\t"
    "ds_read_b128 a[104:107], v57 offset:2048\n\t"
    "v_mfma_f32_16x16x32_bf16 v[212:215], a[20:23], a[80:83], v[212:215]\n\t"
    "s_add_u32 m0, s64, 0x19000\n\t"
    "ds_read_b128 a[108:111], v57 offset:4096\n\t"
    "v_mfma_f32_16x16x32_bf16 v[216:219], a[24:27], a[80:83], v[216:219]\n\t"
    "s_add_u32 s78, s96, 0x8000\n\t"
    "buffer_load_dwordx4 v13, s[36:39], s78 offen lds\n\t"
    "v_mfma_f32_16x16x32_bf16 v[220:223], a[28:31], a[80:83], v[220:223]\n\t"
    "v_mfma_f32_16x16x32_bf16 v[224:227], a[0:3], a[84:87], v[224:227]\n\t"
    "v_mfma_f32_16x16x32_bf16 v[228:231], a[4:7], a[84:87], v[228:231]\n\t"
    "s_add_u32 m0, s64, 0x19400\n\t"
    "v_mfma_f32_16x16x32_bf16 v[232:235], a[8:11], a[84:87], v[232:235]\n\t"
    "s_add_u32 s78, s96, 0xa000\n\t"
    "buffer_load_dwordx4 v14, s[36:39], s78 offen lds\n\t"
    "v_mfma_f32_16x16x32_bf16 v[236:239], a[12:15], a[84:87], v[236:239]\n\t"
    "v_mfma_f32_16x16x32_bf16 v[240:243], a[16:19], a[84:87], v[240:243]\n\t"
    "v_mfma_f32_16x16x32_bf16 v[244:247], a[20:23], a[84:87], v[244:247]\n\t"
    "v_mfma_f32_16x16x32_bf16 v[248:251], a[24:27], a[84:87], v[248:251]\n\t"
    "v_mfma_f32_16x16x32_bf16 v[252:255], a[28:31], a[84:87], v[252:255]\n\t"
    "s_waitcnt lgkmcnt(0)\n\t"
    "v_mfma_f32_16x16x32_bf16 v[64:67], a[32:35], a[88:91], v[64:67]\n\t"
    "v_mfma_f32_16x16x32_bf16 v[68:71], a[36:39], a[88:91], v[68:71]\n\t"
    "v_mfma_f32_16x16x32_bf16 v[72:75], a[40:43], a[88:91], v[72:75]\n\t"
    "v_mfma_f32_16x16x32_bf16 v[76:79], a[44:47], a[88:91], v[76:79]\n\t"
    "v_mfma_f32_16x16x32_bf16 v[80:83], a[48:51], a[88:91], v[80:83]\n\t"
    "v_mfma_f32_16x16x32_bf16 v[84:87], a[52:55], a[88:91], v[84:87]\n\t"
    "v_mfma_f32_16x16x32_bf16 v[88:91], a[56:59], a[88:91], v[88:91]\n\t"
    "v_mfma_f32_16x16x32_bf16 v[92:95], a[60:63], a[88:91], v[92:95]\n\t"
    "v_mfma_f32_16x16x32_bf16 v[96:99], a[32:35], a[92:95], v[96:99]\n\t"
    "s_waitcnt vmcnt(18) lgkmcnt(0)\n\t"
    "s_barrier\n\t"
    "v_mfma_f32_16x16x32_bf16 v[100:103], a[36:39], a[92:95], v[100:103]\n\t"
    "s_lshr_b32 s83, s83, 4\n\t"
    "s_add_u32 s79, s83, s82\n\t"
    "s_and_b32 s79, s79, 7\n\t"
    "s_lshl_b32 s79, s79, 7\n\t"
    "s_add_u32 s96, s67, s79\n\t"
    "s_add_u32 m0, s65, 0x22000\n\t"
    "ds_read_b128 a[64:67], v21 offset:0\n\t"
    "v_mfma_f32_16x16x32_bf16 v[104:107], a[40:43], a[92:95], v[104:107]\n\t"
    "s_add_u32 s78, s68, 0\n\t"
    "buffer_load_dwordx4 v15, s[40:43], s78 offen lds\n\t"
    "v_mfma_f32_16x16x32_bf16 v[108:111], a[44:47], a[92:95], v[108:111]\n\t"
    "ds_read_b64_tr_b16 a[0:1], v18 offset:0\n\t"
    "v_mfma_f32_16x16x32_bf16 v[112:115], a[48:51], a[92:95], v[112:115]\n\t"
    "ds_read_b64_tr_b16 a[2:3], v18 offset:1024\n\t"
    "v_mfma_f32_16x16x32_bf16 v[116:119], a[52:55], a[92:95], v[116:119]\n\t"
    "s_add_u32 m0, s65, 0x22400\n\t"
    "ds_read_b64_tr_b16 a[4:5], v18 offset:8\n\t"
    "v_mfma_f32_16x16x32_bf16 v[120:123], a[56:59], a[92:95], v[120:123]\n\t"
    "s_add_u32 s78, s68, 0x8000\n\t"
    "buffer_load_dwordx4 v15, s[40:43], s78 offen lds\n\t"
    "v_mfma_f32_16x16x32_bf16 v[124:127], a[60:63], a[92:95], v[124:127]\n\t"
    "ds_read_b64_tr_b16 a[6:7], v18 offset:1032\n\t"
    "v_mfma_f32_16x16x32_bf16 v[128:131], a[32:35], a[96:99], v[128:131]\n\t"
    "ds_read_b64_tr_b16 a[8:9], v18 offset:256\n\t"
    "v_mfma_f32_16x16x32_bf16 v[132:135], a[36:39], a[96:99], v[132:135]\n\t"
    "s_add_u32 m0, s65, 0x22800\n\t"
    "ds_read_b64_tr_b16 a[10:11], v18 offset:1280\n\t"
    "v_mfma_f32_16x16x32_bf16 v[136:139], a[40:43], a[96:99], v[136:139]\n\t"
    "s_add_u32 s78, s68, 0x10000\n\t"
    "buffer_load_dwordx4 v15, s[40:43], s78 offen lds\n\t"
    "v_mfma_f32_16x16x32_bf16 v[140:143], a[44:47], a[96:99], v[140:143]\n\t"
    "ds_read_b64_tr_b16 a[12:13], v18 offset:264\n\t"
    "v_mfma_f32_16x16x32_bf16 v[144:147], a[48:51], a[96:99], v[144:147]\n\t"
    "ds_read_b64_tr_b16 a[14:15], v18 offset:1288\n\t"
    "v_mfma_f32_16x16x32_bf16 v[148:151], a[52:55], a[96:99], v[148:151]\n\t"
    "s_add_u32 m0, s65, 0x22c00\n\t"
    "ds_read_b64_tr_b16 a[16:17], v18 offset:512\n\t"
    "v_mfma_f32_16x16x32_bf16 v[152:155], a[56:59], a[96:99], v[152:155]\n\t"
    "s_add_u32 s78, s68, 0x18000\n\t"
    "buffer_load_dwordx4 v15, s[40:43], s78 offen lds\n\t"
    "v_mfma_f32_16x16x32_bf16 v[156:159], a[60:63], a[96:99], v[156:159]\n\t"
    "ds_read_b64_tr_b16 a[18:19], v18 offset:1536\n\t"
    "v_mfma_f32_16x16x32_bf16 v[160:163], a[32:35], a[100:103], v[160:163]\n\t"
    "ds_read_b64_tr_b16 a[20:21], v18 offset:520\n\t"
    "v_mfma_f32_16x16x32_bf16 v[164:167], a[36:39], a[100:103], v[164:167]\n\t"
    "s_lshr_b32 s97, s97, 4\n\t"
    "s_add_u32 s63, s97, s82\n\t"
    "s_and_b32 s63, s63, 7\n\t"
    "s_lshl_b32 s63, s63, 19\n\t"
    "s_and_b32 s68, s68, 0x3ffff\n\t"
    "s_or_b32 s68, s68, s63\n\t"
    "s_add_u32 m0, s64, 0x2000\n\t"
    "ds_read_b64_tr_b16 a[22:23], v18 offset:1544\n\t"
    "v_mfma_f32_16x16x32_bf16 v[168:171], a[40:43], a[100:103], v[168:171]\n\t"
    "s_add_u32 s78, s96, 0\n\t"
    "buffer_load_dwordx4 v13, s[36:39], s78 offen lds\n\t"
    "v_mfma_f32_16x16x32_bf16 v[172:175], a[44:47], a[100:103], v[172:175]\n\t"
    "ds_read_b64_tr_b16 a[24:25], v18 offset:768\n\t"
    "v_mfma_f32_16x16x32_bf16 v[176:179], a[48:51], a[100:103], v[176:179]\n\t"
    "ds_read_b64_tr_b16 a[26:27], v18 offset:1792\n\t"
    "v_mfma_f32_16x16x32_bf16 v[180:183], a[52:55], a[100:103], v[180:183]\n\t"
    "s_add_u32 m0, s64, 0x2400\n\t"
    "ds_read_b64_tr_b16 a[28:29], v18 offset:776\n\t"
    "v_mfma_f32_16x16x32_bf16 v[184:187], a[56:59], a[100:103], v[184:187]\n\t"
    "s_add_u32 s78, s96, 0x2000\n\t"
    "buffer_load_dwordx4 v14, s[36:39], s78 offen lds\n\t"
    "v_mfma_f32_16x16x32_bf16 v[188:191], a[60:63], a[100:103], v[188:191]\n\t"
    "ds_read_b64_tr_b16 a[30:31], v18 offset:1800\n\t"
    "v_mfma_f32_16x16x32_bf16 v[192:195], a[32:35], a[104:107], v[192:195]\n\t"
    "ds_read_b128 a[68:71], v21 offset:2048\n\t"
    "v_mfma_f32_16x16x32_bf16 v[196:199], a[36:39], a[104:107], v[196:199]\n\t"
    "s_add_u32 m0, s64, 0x2800\n\t"
    "ds_read_b128 a[72:75], v21 offset:4096\n\t"
    "v_mfma_f32_16x16x32_bf16 v[200:203], a[40:43], a[104:107], v[200:203]\n\t"
    "s_add_u32 s78, s96, 0x4000\n\t"
    "buffer_load_dwordx4 v13, s[36:39], s78 offen lds\n\t"
    "v_mfma_f32_16x16x32_bf16 v[204:207], a[44:47], a[104:107], v[204:207]\n\t"
    "ds_read_b128 a[76:79], v22 offset:0\n\t"
    "v_mfma_f32_16x16x32_bf16 v[208:211], a[48:51], a[104:107], v[208:211]\n\t"
    "ds_read_b128 a[80:83], v22 offset:2048\n\t"
    "v_mfma_f32_16x16x32_bf16 v[212:215], a[52:55], a[104:107], v[212:215]\n\t"
    "ds_read_b128 a[84:87], v22 offset:4096\n\t"
    "v_mfma_f32_16x16x32_bf16 v[216:219], a[56:59], a[104:107], v[216:219]\n\t"
    "s_add_u32 s78, s69, 0x2000\n\t"
    "s_and_b32 s78, s78, 0x1ffff\n\t"
    "buffer_load_dwordx4 a[208:211], v7, s[44:47], s78 offen nt\n\t"
    "v_mfma_f32_16x16x32_bf16 v[220:223], a[60:63], a[104:107], v[220:223]\n\t"
    "v_mfma_f32_16x16x32_bf16 v[224:227], a[32:35], a[108:111], v[224:227]\n\t"
    "v_mfma_f32_16x16x32_bf16 v[228:231], a[36:39], a[108:111], v[228:231]\n\t"
    "v_mfma_f32_16x16x32_bf16 v[232:235], a[40:43], a[108:111], v[232:235]\n\t"
    "s_add_u32 s78, s69, 0x2000\n\t"
    "s_and_b32 s78, s78, 0x1ffff\n\t"
    "buffer_load_dwordx4 a[212:215], v8, s[44:47], s78 offen offset:1024 nt\n\t"
    "v_mfma_f32_16x16x32_bf16 v[236:239], a[44:47], a[108:111], v[236:239]\n\t"
    "v_mfma_f32_16x16x32_bf16 v[240:243], a[48:51], a[108:111], v[240:243]\n\t"
    "v_mfma_f32_16x16x32_bf16 v[244:247], a[52:55], a[108:111], v[244:247]\n\t"
    "v_mfma_f32_16x16x32_bf16 v[248:251], a[56:59], a[108:111], v[248:251]\n\t"
    "v_mfma_f32_16x16x32_bf16 v[252:255], a[60:63], a[108:111], v[252:255]\n\t"
    "s_waitcnt lgkmcnt(0)\n\t"
    "v_mfma_f32_16x16x32_bf16 v[64:67], a[0:3], a[64:67], v[64:67]\n\t"
    "v_mfma_f32_16x16x32_bf16 v[68:71], a[4:7], a[64:67], v[68:71]\n\t"
    "v_mfma_f32_16x16x32_bf16 v[72:75], a[8:11], a[64:67], v[72:75]\n\t"
    "s_add_u32 s78, s69, 0x2000\n\t"
    "s_and_b32 s78, s78, 0x1ffff\n\t"
    "buffer_load_dwordx4 a[216:219], v9, s[44:47], s78 offen offset:2048 nt\n\t"
    "v_mfma_f32_16x16x32_bf16 v[76:79], a[12:15], a[64:67], v[76:79]\n\t"
    "v_mfma_f32_16x16x32_bf16 v[80:83], a[16:19], a[64:67], v[80:83]\n\t"
    "v_mfma_f32_16x16x32_bf16 v[84:87], a[20:23], a[64:67], v[84:87]\n\t"
    "v_mfma_f32_16x16x32_bf16 v[88:91], a[24:27], a[64:67], v[88:91]\n\t"
    "s_add_u32 s78, s69, 0x2000\n\t"
    "s_and_b32 s78, s78, 0x1ffff\n\t"
    "buffer_load_dwordx4 a[220:223], v10, s[44:47], s78 offen offset:3072 nt\n\t"
    "v_mfma_f32_16x16x32_bf16 v[92:95], a[28:31], a[64:67], v[92:95]\n\t"
    "v_mfma_f32_16x16x32_bf16 v[96:99], a[0:3], a[68:71], v[96:99]\n\t"
    "s_waitcnt vmcnt(25) lgkmcnt(0)\n\t"
    "s_barrier\n\t"
    "v_mfma_f32_16x16x32_bf16 v[100:103], a[4:7], a[68:71], v[100:103]\n\t"
    "s_add_u32 m0, s65, 0x8000\n\t"
    "ds_read_b128 a[88:91], v61 offset:0\n\t"
    "v_mfma_f32_16x16x32_bf16 v[104:107], a[8:11], a[68:71], v[104:107]\n\t"
    "s_add_u32 s78, s68, 0\n\t"
    "buffer_load_dwordx4 v15, s[40:43], s78 offen lds\n\t"
    "v_mfma_f32_16x16x32_bf16 v[108:111], a[12:15], a[68:71], v[108:111]\n\t"
    "ds_read_b64_tr_b16 a[32:33], v18 offset:16384\n\t"
    "v_mfma_f32_16x16x32_bf16 v[112:115], a[16:19], a[68:71], v[112:115]\n\t"
    "ds_read_b64_tr_b16 a[34:35], v18 offset:17408\n\t"
    "v_mfma_f32_16x16x32_bf16 v[116:119], a[20:23], a[68:71], v[116:119]\n\t"
    "s_add_u32 m0, s65, 0x8400\n\t"
    "ds_read_b64_tr_b16 a[36:37], v18 offset:16392\n\t"
    "v_mfma_f32_16x16x32_bf16 v[120:123], a[24:27], a[68:71], v[120:123]\n\t"
    "s_add_u32 s78, s68, 0x8000\n\t"
    "buffer_load_dwordx4 v15, s[40:43], s78 offen lds\n\t"
    "v_mfma_f32_16x16x32_bf16 v[124:127], a[28:31], a[68:71], v[124:127]\n\t"
    "ds_read_b64_tr_b16 a[38:39], v18 offset:17416\n\t"
    "v_mfma_f32_16x16x32_bf16 v[128:131], a[0:3], a[72:75], v[128:131]\n\t"
    "ds_read_b64_tr_b16 a[40:41], v18 offset:16640\n\t"
    "v_mfma_f32_16x16x32_bf16 v[132:135], a[4:7], a[72:75], v[132:135]\n\t"
    "s_add_u32 m0, s65, 0x8800\n\t"
    "ds_read_b64_tr_b16 a[42:43], v18 offset:17664\n\t"
    "v_mfma_f32_16x16x32_bf16 v[136:139], a[8:11], a[72:75], v[136:139]\n\t"
    "s_add_u32 s78, s68, 0x10000\n\t"
    "buffer_load_dwordx4 v15, s[40:43], s78 offen lds\n\t"
    "v_mfma_f32_16x16x32_bf16 v[140:143], a[12:15], a[72:75], v[140:143]\n\t"
    "ds_read_b64_tr_b16 a[44:45], v18 offset:16648\n\t"
    "v_mfma_f32_16x16x32_bf16 v[144:147], a[16:19], a[72:75], v[144:147]\n\t"
    "ds_read_b64_tr_b16 a[46:47], v18 offset:17672\n\t"
    "v_mfma_f32_16x16x32_bf16 v[148:151], a[20:23], a[72:75], v[148:151]\n\t"
    "s_add_u32 m0, s65, 0x8c00\n\t"
    "ds_read_b64_tr_b16 a[48:49], v18 offset:16896\n\t"
    "v_mfma_f32_16x16x32_bf16 v[152:155], a[24:27], a[72:75], v[152:155]\n\t"
    "s_add_u32 s78, s68, 0x18000\n\t"
    "buffer_load_dwordx4 v15, s[40:43], s78 offen lds\n\t"
    "v_mfma_f32_16x16x32_bf16 v[156:159], a[28:31], a[72:75], v[156:159]\n\t"
    "ds_read_b64_tr_b16 a[50:51], v18 offset:17920\n\t"
    "v_mfma_f32_16x16x32_bf16 v[160:163], a[0:3], a[76:79], v[160:163]\n\t"
    "ds_read_b64_tr_b16 a[52:53], v18 offset:16904\n\t"
    "v_mfma_f32_16x16x32_bf16 v[164:167], a[4:7], a[76:79], v[164:167]\n\t"
    "s_add_u32 s68, s68, 0x40000\n\t"
    "s_and_b32 s68, s68, 0x3fffff\n\t"
    "s_add_u32 m0, s64, 0x2c00\n\t"
    "ds_read_b64_tr_b16 a[54:55], v18 offset:17928\n\t"
    "v_mfma_f32_16x16x32_bf16 v[168:171], a[8:11], a[76:79], v[168:171]\n\t"
    "s_add_u32 s78, s96, 0x6000\n\t"
    "buffer_load_dwordx4 v14, s[36:39], s78 offen lds\n\t"
    "v_mfma_f32_16x16x32_bf16 v[172:175], a[12:15], a[76:79], v[172:175]\n\t"
    "ds_read_b64_tr_b16 a[56:57], v18 offset:17152\n\t"
    "v_mfma_f32_16x16x32_bf16 v[176:179], a[16:19], a[76:79], v[176:179]\n\t"
    "ds_read_b64_tr_b16 a[58:59], v18 offset:18176\n\t"
    "v_mfma_f32_16x16x32_bf16 v[180:183], a[20:23], a[76:79], v[180:183]\n\t"
    "s_add_u32 m0, s64, 0x3000\n\t"
    "ds_read_b64_tr_b16 a[60:61], v18 offset:17160\n\t"
    "v_mfma_f32_16x16x32_bf16 v[184:187], a[24:27], a[76:79], v[184:187]\n\t"
    "s_add_u32 s78, s96, 0x8000\n\t"
    "buffer_load_dwordx4 v13, s[36:39], s78 offen lds\n\t"
    "v_mfma_f32_16x16x32_bf16 v[188:191], a[28:31], a[76:79], v[188:191]\n\t"
    "ds_read_b64_tr_b16 a[62:63], v18 offset:18184\n\t"
    "v_mfma_f32_16x16x32_bf16 v[192:195], a[0:3], a[80:83], v[192:195]\n\t"
    "ds_read_b128 a[92:95], v61 offset:2048\n\t"
    "v_mfma_f32_16x16x32_bf16 v[196:199], a[4:7], a[80:83], v[196:199]\n\t"
    "s_add_u32 m0, s64, 0x3400\n\t"
    "ds_read_b128 a[96:99], v61 offset:4096\n\t"
    "v_mfma_f32_16x16x32_bf16 v[200:203], a[8:11], a[80:83], v[200:203]\n\t"
    "s_add_u32 s78, s96, 0xa000\n\t"
    "buffer_load_dwordx4 v14, s[36:39], s78 offen lds\n\t"
    "v_mfma_f32_16x16x32_bf16 v[204:207], a[12:15], a[80:83], v[204:207]\n\t"
    "ds_read_b128 a[100:103], v23 offset:0\n\t"
    "v_mfma_f32_16x16x32_bf16 v[208:211], a[16:19], a[80:83], v[208:211]\n\t"
    "ds_read_b128 a[104:107], v23 offset:2048\n\t"
    "v_mfma_f32_16x16x32_bf16 v[212:215], a[20:23], a[80:83], v[212:215]\n\t"
    "ds_read_b128 a[108:111], v23 offset:4096\n\t"
    "v_mfma_f32_16x16x32_bf16 v[216:219], a[24:27], a[80:83], v[216:219]\n\t"
    "v_mfma_f32_16x16x32_bf16 v[220:223], a[28:31], a[80:83], v[220:223]\n\t"
    "v_mfma_f32_16x16x32_bf16 v[224:227], a[0:3], a[84:87], v[224:227]\n\t"
    "v_mfma_f32_16x16x32_bf16 v[228:231], a[4:7], a[84:87], v[228:231]\n\t"
    "v_mfma_f32_16x16x32_bf16 v[232:235], a[8:11], a[84:87], v[232:235]\n\t"
    "v_mfma_f32_16x16x32_bf16 v[236:239], a[12:15], a[84:87], v[236:239]\n\t"
    "v_mfma_f32_16x16x32_bf16 v[240:243], a[16:19], a[84:87], v[240:243]\n\t"
    "v_mfma_f32_16x16x32_bf16 v[244:247], a[20:23], a[84:87], v[244:247]\n\t"
    "v_mfma_f32_16x16x32_bf16 v[248:251], a[24:27], a[84:87], v[248:251]\n\t"
    "v_mfma_f32_16x16x32_bf16 v[252:255], a[28:31], a[84:87], v[252:255]\n\t"
    "s_waitcnt lgkmcnt(0)\n\t"
    "v_mfma_f32_16x16x32_bf16 v[64:67], a[32:35], a[88:91], v[64:67]\n\t"
    "v_mfma_f32_16x16x32_bf16 v[68:71], a[36:39], a[88:91], v[68:71]\n\t"
    "v_mfma_f32_16x16x32_bf16 v[72:75], a[40:43], a[88:91], v[72:75]\n\t"
    "v_mfma_f32_16x16x32_bf16 v[76:79], a[44:47], a[88:91], v[76:79]\n\t"
    "v_mfma_f32_16x16x32_bf16 v[80:83], a[48:51], a[88:91], v[80:83]\n\t"
    "v_mfma_f32_16x16x32_bf16 v[84:87], a[52:55], a[88:91], v[84:87]\n\t"
    "v_mfma_f32_16x16x32_bf16 v[88:91], a[56:59], a[88:91], v[88:91]\n\t"
    "v_mfma_f32_16x16x32_bf16 v[92:95], a[60:63], a[88:91], v[92:95]\n\t"
    "v_mfma_f32_16x16x32_bf16 v[96:99], a[32:35], a[92:95], v[96:99]\n\t"
    "s_waitcnt vmcnt(18) lgkmcnt(0)\n\t"
    "s_barrier\n\t"
    "v_mfma_f32_16x16x32_bf16 v[100:103], a[36:39], a[92:95], v[100:103]\n\t"
    "s_lshr_b32 s83, s83, 4\n\t"
    "s_add_u32 s79, s83, s82\n\t"
    "s_and_b32 s79, s79, 7\n\t"
    "s_lshl_b32 s79, s79, 7\n\t"
    "s_add_u32 s96, s67, s79\n\t"
    "s_add_u32 m0, s65, 0xc000\n\t"
    "ds_read_b128 a[64:67], v21 offset:24576\n\t"
    "v_mfma_f32_16x16x32_bf16 v[104:107], a[40:43], a[92:95], v[104:107]\n\t"
    "s_add_u32 s78, s68, 0\n\t"
    "buffer_load_dwordx4 v15, s[40:43], s78 offen lds\n\t"
    "v_mfma_f32_16x16x32_bf16 v[108:111], a[44:47], a[92:95], v[108:111]\n\t"
    "ds_read_b64_tr_b16 a[0:1], v19 offset:0\n\t"
    "v_mfma_f32_16x16x32_bf16 v[112:115], a[48:51], a[92:95], v[112:115]\n\t"
    "ds_read_b64_tr_b16 a[2:3], v19 offset:1024\n\t"
    "v_mfma_f32_16x16x32_bf16 v[116:119], a[52:55], a[92:95], v[116:119]\n\t"
    "s_add_u32 m0, s65, 0xc400\n\t"
    "ds_read_b64_tr_b16 a[4:5], v19 offset:8\n\t"
    "v_mfma_f32_16x16x32_bf16 v[120:123], a[56:59], a[92:95], v[120:123]\n\t"
    "s_add_u32 s78, s68, 0x8000\n\t"
    "buffer_load_dwordx4 v15, s[40:43], s78 offen lds\n\t"
    "v_mfma_f32_16x16x32_bf16 v[124:127], a[60:63], a[92:95], v[124:127]\n\t"
    "ds_read_b64_tr_b16 a[6:7], v19 offset:1032\n\t"
    "v_mfma_f32_16x16x32_bf16 v[128:131], a[32:35], a[96:99], v[128:131]\n\t"
    "ds_read_b64_tr_b16 a[8:9], v19 offset:256\n\t"
    "v_mfma_f32_16x16x32_bf16 v[132:135], a[36:39], a[96:99], v[132:135]\n\t"
    "s_add_u32 m0, s65, 0xc800\n\t"
    "ds_read_b64_tr_b16 a[10:11], v19 offset:1280\n\t"
    "v_mfma_f32_16x16x32_bf16 v[136:139], a[40:43], a[96:99], v[136:139]\n\t"
    "s_add_u32 s78, s68, 0x10000\n\t"
    "buffer_load_dwordx4 v15, s[40:43], s78 offen lds\n\t"
    "v_mfma_f32_16x16x32_bf16 v[140:143], a[44:47], a[96:99], v[140:143]\n\t"
    "ds_read_b64_tr_b16 a[12:13], v19 offset:264\n\t"
    "v_mfma_f32_16x16x32_bf16 v[144:147], a[48:51], a[96:99], v[144:147]\n\t"
    "ds_read_b64_tr_b16 a[14:15], v19 offset:1288\n\t"
    "v_mfma_f32_16x16x32_bf16 v[148:151], a[52:55], a[96:99], v[148:151]\n\t"
    "s_add_u32 m0, s65, 0xcc00\n\t"
    "ds_read_b64_tr_b16 a[16:17], v19 offset:512\n\t"
    "v_mfma_f32_16x16x32_bf16 v[152:155], a[56:59], a[96:99], v[152:155]\n\t"
    "s_add_u32 s78, s68, 0x18000\n\t"
    "buffer_load_dwordx4 v15, s[40:43], s78 offen lds\n\t"
    "v_mfma_f32_16x16x32_bf16 v[156:159], a[60:63], a[96:99], v[156:159]\n\t"
    "ds_read_b64_tr_b16 a[18:19], v19 offset:1536\n\t"
    "v_mfma_f32_16x16x32_bf16 v[160:163], a[32:35], a[100:103], v[160:163]\n\t"
    "ds_read_b64_tr_b16 a[20:21], v19 offset:520\n\t"
    "v_mfma_f32_16x16x32_bf16 v[164:167], a[36:39], a[100:103], v[164:167]\n\t"
    "s_lshr_b32 s97, s97, 4\n\t"
    "s_add_u32 s63, s97, s82\n\t"
    "s_and_b32 s63, s63, 7\n\t"
    "s_lshl_b32 s63, s63, 19\n\t"
    "s_and_b32 s68, s68, 0x3ffff\n\t"
    "s_or_b32 s68, s68, s63\n\t"
    "s_add_u32 m0, s64, 0x12000\n\t"
    "ds_read_b64_tr_b16 a[22:23], v19 offset:1544\n\t"
    "v_mfma_f32_16x16x32_bf16 v[168:171], a[40:43], a[100:103], v[168:171]\n\t"
    "s_add_u32 s78, s96, 0\n\t"
    "buffer_load_dwordx4 v13, s[36:39], s78 offen lds\n\t"
    "v_mfma_f32_16x16x32_bf16 v[172:175], a[44:47], a[100:103], v[172:175]\n\t"
    "ds_read_b64_tr_b16 a[24:25], v19 offset:768\n\t"
    "v_mfma_f32_16x16x32_bf16 v[176:179], a[48:51], a[100:103], v[176:179]\n\t"
    "ds_read_b64_tr_b16 a[26:27], v19 offset:1792\n\t"
    "v_mfma_f32_16x16x32_bf16 v[180:183], a[52:55], a[100:103], v[180:183]\n\t"
    "s_add_u32 m0, s64, 0x12400\n\t"
    "ds_read_b64_tr_b16 a[28:29], v19 offset:776\n\t"
    "v_mfma_f32_16x16x32_bf16 v[184:187], a[56:59], a[100:103], v[184:187]\n\t"
    "s_add_u32 s78, s96, 0x2000\n\t"
    "buffer_load_dwordx4 v14, s[36:39], s78 offen lds\n\t"
    "v_mfma_f32_16x16x32_bf16 v[188:191], a[60:63], a[100:103], v[188:191]\n\t"
    "ds_read_b64_tr_b16 a[30:31], v19 offset:1800\n\t"
    "v_mfma_f32_16x16x32_bf16 v[192:195], a[32:35], a[104:107], v[192:195]\n\t"
    "ds_read_b128 a[68:71], v21 offset:26624\n\t"
    "v_mfma_f32_16x16x32_bf16 v[196:199], a[36:39], a[104:107], v[196:199]\n\t"
    "s_add_u32 m0, s64, 0x12800\n\t"
    "ds_read_b128 a[72:75], v21 offset:28672\n\t"
    "v_mfma_f32_16x16x32_bf16 v[200:203], a[40:43], a[104:107], v[200:203]\n\t"
    "s_add_u32 s78, s96, 0x4000\n\t"
    "buffer_load_dwordx4 v13, s[36:39], s78 offen lds\n\t"
    "v_mfma_f32_16x16x32_bf16 v[204:207], a[44:47], a[104:107], v[204:207]\n\t"
    "ds_read_b128 a[76:79], v22 offset:24576\n\t"
    "v_mfma_f32_16x16x32_bf16 v[208:211], a[48:51], a[104:107], v[208:211]\n\t"
    "ds_read_b128 a[80:83], v22 offset:26624\n\t"
    "v_mfma_f32_16x16x32_bf16 v[212:215], a[52:55], a[104:107], v[212:215]\n\t"
    "ds_read_b128 a[84:87], v22 offset:28672\n\t"
    "v_mfma_f32_16x16x32_bf16 v[216:219], a[56:59], a[104:107], v[216:219]\n\t"
    "s_add_u32 s78, s69, 0x2000\n\t"
    "s_and_b32 s78, s78, 0x1ffff\n\t"
    "buffer_load_dwordx4 a[224:227], v11, s[48:51], s78 offen nt\n\t"
    "v_mfma_f32_16x16x32_bf16 v[220:223], a[60:63], a[104:107], v[220:223]\n\t"
    "v_mfma_f32_16x16x32_bf16 v[224:227], a[32:35], a[108:111], v[224:227]\n\t"
    "v_mfma_f32_16x16x32_bf16 v[228:231], a[36:39], a[108:111], v[228:231]\n\t"
    "v_mfma_f32_16x16x32_bf16 v[232:235], a[40:43], a[108:111], v[232:235]\n\t"
    "s_add_u32 s78, s69, 0x2000\n\t"
    "s_and_b32 s78, s78, 0x1ffff\n\t"
    "buffer_load_dwordx4 a[228:231], v12, s[48:51], s78 offen offset:1024 nt\n\t"
    "v_mfma_f32_16x16x32_bf16 v[236:239], a[44:47], a[108:111], v[236:239]\n\t"
    "v_mfma_f32_16x16x32_bf16 v[240:243], a[48:51], a[108:111], v[240:243]\n\t"
    "v_mfma_f32_16x16x32_bf16 v[244:247], a[52:55], a[108:111], v[244:247]\n\t"
    "v_mfma_f32_16x16x32_bf16 v[248:251], a[56:59], a[108:111], v[248:251]\n\t"
    "v_mfma_f32_16x16x32_bf16 v[252:255], a[60:63], a[108:111], v[252:255]\n\t"
    "s_waitcnt lgkmcnt(0)\n\t"
    "v_mfma_f32_16x16x32_bf16 v[64:67], a[0:3], a[64:67], v[64:67]\n\t"
    "v_mfma_f32_16x16x32_bf16 v[68:71], a[4:7], a[64:67], v[68:71]\n\t"
    "v_mfma_f32_16x16x32_bf16 v[72:75], a[8:11], a[64:67], v[72:75]\n\t"
    "s_add_u32 s78, s69, 0x2000\n\t"
    "s_and_b32 s78, s78, 0x1ffff\n\t"
    "buffer_load_dwordx4 a[232:235], v11, s[48:51], s78 offen offset:2048 nt\n\t"
    "v_mfma_f32_16x16x32_bf16 v[76:79], a[12:15], a[64:67], v[76:79]\n\t"
    "v_mfma_f32_16x16x32_bf16 v[80:83], a[16:19], a[64:67], v[80:83]\n\t"
    "v_mfma_f32_16x16x32_bf16 v[84:87], a[20:23], a[64:67], v[84:87]\n\t"
    "v_mfma_f32_16x16x32_bf16 v[88:91], a[24:27], a[64:67], v[88:91]\n\t"
    "s_add_u32 s78, s69, 0x2000\n\t"
    "s_and_b32 s78, s78, 0x1ffff\n\t"
    "buffer_load_dwordx4 a[236:239], v12, s[48:51], s78 offen offset:3072 nt\n\t"
    "v_mfma_f32_16x16x32_bf16 v[92:95], a[28:31], a[64:67], v[92:95]\n\t"
    "v_mfma_f32_16x16x32_bf16 v[96:99], a[0:3], a[68:71], v[96:99]\n\t"
    "s_waitcnt vmcnt(25) lgkmcnt(0)\n\t"
    "s_barrier\n\t"
    "v_mfma_f32_16x16x32_bf16 v[100:103], a[4:7], a[68:71], v[100:103]\n\t"
    "s_add_u32 m0, s65, 0x1e000\n\t"
    "ds_read_b128 a[88:91], v61 offset:24576\n\t"
    "v_mfma_f32_16x16x32_bf16 v[104:107], a[8:11], a[68:71], v[104:107]\n\t"
    "s_add_u32 s78, s68, 0\n\t"
    "buffer_load_dwordx4 v15, s[40:43], s78 offen lds\n\t"
    "v_mfma_f32_16x16x32_bf16 v[108:111], a[12:15], a[68:71], v[108:111]\n\t"
    "ds_read_b64_tr_b16 a[32:33], v19 offset:16384\n\t"
    "v_mfma_f32_16x16x32_bf16 v[112:115], a[16:19], a[68:71], v[112:115]\n\t"
    "ds_read_b64_tr_b16 a[34:35], v19 offset:17408\n\t"
    "v_mfma_f32_16x16x32_bf16 v[116:119], a[20:23], a[68:71], v[116:119]\n\t"
    "s_add_u32 m0, s65, 0x1e400\n\t"
    "ds_read_b64_tr_b16 a[36:37], v19 offset:16392\n\t"
    "v_mfma_f32_16x16x32_bf16 v[120:123], a[24:27], a[68:71], v[120:123]\n\t"
    "s_add_u32 s78, s68, 0x8000\n\t"
    "buffer_load_dwordx4 v15, s[40:43], s78 offen lds\n\t"
    "v_mfma_f32_16x16x32_bf16 v[124:127], a[28:31], a[68:71], v[124:127]\n\t"
    "ds_read_b64_tr_b16 a[38:39], v19 offset:17416\n\t"
    "v_mfma_f32_16x16x32_bf16 v[128:131], a[0:3], a[72:75], v[128:131]\n\t"
    "ds_read_b64_tr_b16 a[40:41], v19 offset:16640\n\t"
    "v_mfma_f32_16x16x32_bf16 v[132:135], a[4:7], a[72:75], v[132:135]\n\t"
    "s_add_u32 m0, s65, 0x1e800\n\t"
    "ds_read_b64_tr_b16 a[42:43], v19 offset:17664\n\t"
    "v_mfma_f32_16x16x32_bf16 v[136:139], a[8:11], a[72:75], v[136:139]\n\t"
    "s_add_u32 s78, s68, 0x10000\n\t"
    "buffer_load_dwordx4 v15, s[40:43], s78 offen lds\n\t"
    "v_mfma_f32_16x16x32_bf16 v[140:143], a[12:15], a[72:75], v[140:143]\n\t"
    "ds_read_b64_tr_b16 a[44:45], v19 offset:16648\n\t"
    "v_mfma_f32_16x16x32_bf16 v[144:147], a[16:19], a[72:75], v[144:147]\n\t"
    "ds_read_b64_tr_b16 a[46:47], v19 offset:17672\n\t"
    "v_mfma_f32_16x16x32_bf16 v[148:151], a[20:23], a[72:75], v[148:151]\n\t"
    "s_add_u32 m0, s65, 0x1ec00\n\t"
    "ds_read_b64_tr_b16 a[48:49], v19 offset:16896\n\t"
    "v_mfma_f32_16x16x32_bf16 v[152:155], a[24:27], a[72:75], v[152:155]\n\t"
    "s_add_u32 s78, s68, 0x18000\n\t"
    "buffer_load_dwordx4 v15, s[40:43], s78 offen lds\n\t"
    "v_mfma_f32_16x16x32_bf16 v[156:159], a[28:31], a[72:75], v[156:159]\n\t"
    "ds_read_b64_tr_b16 a[50:51], v19 offset:17920\n\t"
    "v_mfma_f32_16x16x32_bf16 v[160:163], a[0:3], a[76:79], v[160:163]\n\t"
    "ds_read_b64_tr_b16 a[52:53], v19 offset:16904\n\t"
    "v_mfma_f32_16x16x32_bf16 v[164:167], a[4:7], a[76:79], v[164:167]\n\t"
    "s_add_u32 s68, s68, 0x40000\n\t"
    "s_and_b32 s68, s68, 0x3fffff\n\t"
    "s_add_u32 m0, s64, 0x12c00\n\t"
    "ds_read_b64_tr_b16 a[54:55], v19 offset:17928\n\t"
    "v_mfma_f32_16x16x32_bf16 v[168:171], a[8:11], a[76:79], v[168:171]\n\t"
    "s_add_u32 s78, s96, 0x6000\n\t"
    "buffer_load_dwordx4 v14, s[36:39], s78 offen lds\n\t"
    "v_mfma_f32_16x16x32_bf16 v[172:175], a[12:15], a[76:79], v[172:175]\n\t"
    "ds_read_b64_tr_b16 a[56:57], v19 offset:17152\n\t"
    "v_mfma_f32_16x16x32_bf16 v[176:179], a[16:19], a[76:79], v[176:179]\n\t"
    "ds_read_b64_tr_b16 a[58:59], v19 offset:18176\n\t"
    "v_mfma_f32_16x16x32_bf16 v[180:183], a[20:23], a[76:79], v[180:183]\n\t"
    "s_add_u32 m0, s64, 0x13000\n\t"
    "ds_read_b64_tr_b16 a[60:61], v19 offset:17160\n\t"
    "v_mfma_f32_16x16x32_bf16 v[184:187], a[24:27], a[76:79], v[184:187]\n\t"
    "s_add_u32 s78, s96, 0x8000\n\t"
    "buffer_load_dwordx4 v13, s[36:39], s78 offen lds\n\t"
    "v_mfma_f32_16x16x32_bf16 v[188:191], a[28:31], a[76:79], v[188:191]\n\t"
    "ds_read_b64_tr_b16 a[62:63], v19 offset:18184\n\t"
    "v_mfma_f32_16x16x32_bf16 v[192:195], a[0:3], a[80:83], v[192:195]\n\t"
    "ds_read_b128 a[92:95], v61 offset:26624\n\t"
    "v_mfma_f32_16x16x32_bf16 v[196:199], a[4:7], a[80:83], v[196:199]\n\t"
    "s_add_u32 m0, s64, 0x13400\n\t"
    "ds_read_b128 a[96:99], v61 offset:28672\n\t"
    "v_mfma_f32_16x16x32_bf16 v[200:203], a[8:11], a[80:83], v[200:203]\n\t"
    "s_add_u32 s78, s96, 0xa000\n\t"
    "buffer_load_dwordx4 v14, s[36:39], s78 offen lds\n\t"
    "v_mfma_f32_16x16x32_bf16 v[204:207], a[12:15], a[80:83], v[204:207]\n\t"
    "ds_read_b128 a[100:103], v23 offset:24576\n\t"
    "v_mfma_f32_16x16x32_bf16 v[208:211], a[16:19], a[80:83], v[208:211]\n\t"
    "ds_read_b128 a[104:107], v23 offset:26624\n\t"
    "v_mfma_f32_16x16x32_bf16 v[212:215], a[20:23], a[80:83], v[212:215]\n\t"
    "ds_read_b128 a[108:111], v23 offset:28672\n\t"
    "v_mfma_f32_16x16x32_bf16 v[216:219], a[24:27], a[80:83], v[216:219]\n\t"
    "v_mfma_f32_16x16x32_bf16 v[220:223], a[28:31], a[80:83], v[220:223]\n\t"
    "v_mfma_f32_16x16x32_bf16 v[224:227], a[0:3], a[84:87], v[224:227]\n\t"
    "v_mfma_f32_16x16x32_bf16 v[228:231], a[4:7], a[84:87], v[228:231]\n\t"
    "v_mfma_f32_16x16x32_bf16 v[232:235], a[8:11], a[84:87], v[232:235]\n\t"
    "v_mfma_f32_16x16x32_bf16 v[236:239], a[12:15], a[84:87], v[236:239]\n\t"
    "v_mfma_f32_16x16x32_bf16 v[240:243], a[16:19], a[84:87], v[240:243]\n\t"
    "v_mfma_f32_16x16x32_bf16 v[244:247], a[20:23], a[84:87], v[244:247]\n\t"
    "v_mfma_f32_16x16x32_bf16 v[248:251], a[24:27], a[84:87], v[248:251]\n\t"
    "v_mfma_f32_16x16x32_bf16 v[252:255], a[28:31], a[84:87], v[252:255]\n\t"
    "s_waitcnt lgkmcnt(0)\n\t"
    "v_mfma_f32_16x16x32_bf16 v[64:67], a[32:35], a[88:91], v[64:67]\n\t"
    "v_mfma_f32_16x16x32_bf16 v[68:71], a[36:39], a[88:91], v[68:71]\n\t"
    "v_mfma_f32_16x16x32_bf16 v[72:75], a[40:43], a[88:91], v[72:75]\n\t"
    "v_mfma_f32_16x16x32_bf16 v[76:79], a[44:47], a[88:91], v[76:79]\n\t"
    "v_mfma_f32_16x16x32_bf16 v[80:83], a[48:51], a[88:91], v[80:83]\n\t"
    "v_mfma_f32_16x16x32_bf16 v[84:87], a[52:55], a[88:91], v[84:87]\n\t"
    "v_mfma_f32_16x16x32_bf16 v[88:91], a[56:59], a[88:91], v[88:91]\n\t"
    "v_mfma_f32_16x16x32_bf16 v[92:95], a[60:63], a[88:91], v[92:95]\n\t"
    "v_mfma_f32_16x16x32_bf16 v[96:99], a[32:35], a[92:95], v[96:99]\n\t"
    "s_waitcnt vmcnt(18) lgkmcnt(0)\n\t"
    "s_barrier\n\t"
    "v_mfma_f32_16x16x32_bf16 v[100:103], a[36:39], a[92:95], v[100:103]\n\t"
    "s_lshr_b32 s83, s83, 4\n\t"
    "s_add_u32 s79, s83, s82\n\t"
    "s_and_b32 s79, s79, 7\n\t"
    "s_lshl_b32 s79, s79, 7\n\t"
    "s_add_u32 s96, s67, s79\n\t"
    "s_add_u32 m0, s65, 0x22000\n\t"
    "ds_read_b128 a[64:67], v16 offset:0\n\t"
    "v_mfma_f32_16x16x32_bf16 v[104:107], a[40:43], a[92:95], v[104:107]\n\t"
    "s_add_u32 s78, s68, 0\n\t"
    "buffer_load_dwordx4 v15, s[40:43], s78 offen lds\n\t"
    "v_mfma_f32_16x16x32_bf16 v[108:111], a[44:47], a[92:95], v[108:111]\n\t"
    "ds_read_b64_tr_b16 a[0:1], v18 offset:0\n\t"
    "v_mfma_f32_16x16x32_bf16 v[112:115], a[48:51], a[92:95], v[112:115]\n\t"
    "ds_read_b64_tr_b16 a[2:3], v18 offset:1024\n\t"
    "v_mfma_f32_16x16x32_bf16 v[116:119], a[52:55], a[92:95], v[116:119]\n\t"
    "s_add_u32 m0, s65, 0x22400\n\t"
    "ds_read_b64_tr_b16 a[4:5], v18 offset:8\n\t"
    "v_mfma_f32_16x16x32_bf16 v[120:123], a[56:59], a[92:95], v[120:123]\n\t"
    "s_add_u32 s78, s68, 0x8000\n\t"
    "buffer_load_dwordx4 v15, s[40:43], s78 offen lds\n\t"
    "v_mfma_f32_16x16x32_bf16 v[124:127], a[60:63], a[92:95], v[124:127]\n\t"
    "ds_read_b64_tr_b16 a[6:7], v18 offset:1032\n\t"
    "v_mfma_f32_16x16x32_bf16 v[128:131], a[32:35], a[96:99], v[128:131]\n\t"
    "ds_read_b64_tr_b16 a[8:9], v18 offset:256\n\t"
    "v_mfma_f32_16x16x32_bf16 v[132:135], a[36:39], a[96:99], v[132:135]\n\t"
    "s_add_u32 m0, s65, 0x22800\n\t"
    "ds_read_b64_tr_b16 a[10:11], v18 offset:1280\n\t"
    "v_mfma_f32_16x16x32_bf16 v[136:139], a[40:43], a[96:99], v[136:139]\n\t"
    "s_add_u32 s78, s68, 0x10000\n\t"
    "buffer_load_dwordx4 v15, s[40:43], s78 offen lds\n\t"
    "v_mfma_f32_16x16x32_bf16 v[140:143], a[44:47], a[96:99], v[140:143]\n\t"
    "ds_read_b64_tr_b16 a[12:13], v18 offset:264\n\t"
    "v_mfma_f32_16x16x32_bf16 v[144:147], a[48:51], a[96:99], v[144:147]\n\t"
    "ds_read_b64_tr_b16 a[14:15], v18 offset:1288\n\t"
    "v_mfma_f32_16x16x32_bf16 v[148:151], a[52:55], a[96:99], v[148:151]\n\t"
    "s_add_u32 m0, s65, 0x22c00\n\t"
    "ds_read_b64_tr_b16 a[16:17], v18 offset:512\n\t"
    "v_mfma_f32_16x16x32_bf16 v[152:155], a[56:59], a[96:99], v[152:155]\n\t"
    "s_add_u32 s78, s68, 0x18000\n\t"
    "buffer_load_dwordx4 v15, s[40:43], s78 offen lds\n\t"
    "v_mfma_f32_16x16x32_bf16 v[156:159], a[60:63], a[96:99], v[156:159]\n\t"
    "ds_read_b64_tr_b16 a[18:19], v18 offset:1536\n\t"
    "v_mfma_f32_16x16x32_bf16 v[160:163], a[32:35], a[100:103], v[160:163]\n\t"
    "ds_read_b64_tr_b16 a[20:21], v18 offset:520\n\t"
    "v_mfma_f32_16x16x32_bf16 v[164:167], a[36:39], a[100:103], v[164:167]\n\t"
    "ds_read_b64_tr_b16 a[22:23], v18 offset:1544\n\t"
    "v_mfma_f32_16x16x32_bf16 v[168:171], a[40:43], a[100:103], v[168:171]\n\t"
    "s_add_u32 s78, s69, 0x4000\n\t"
    "s_and_b32 s78, s78, 0x1ffff\n\t"
    "buffer_load_dwordx4 a[240:243], v7, s[44:47], s78 offen nt\n\t"
    "v_mfma_f32_16x16x32_bf16 v[172:175], a[44:47], a[100:103], v[172:175]\n\t"
    "ds_read_b64_tr_b16 a[24:25], v18 offset:768\n\t"
    "v_mfma_f32_16x16x32_bf16 v[176:179], a[48:51], a[100:103], v[176:179]\n\t"
    "ds_read_b64_tr_b16 a[26:27], v18 offset:1792\n\t"
    "v_mfma_f32_16x16x32_bf16 v[180:183], a[52:55], a[100:103], v[180:183]\n\t"
    "ds_read_b64_tr_b16 a[28:29], v18 offset:776\n\t"
    "v_mfma_f32_16x16x32_bf16 v[184:187], a[56:59], a[100:103], v[184:187]\n\t"
    "s_add_u32 s78, s69, 0x4000\n\t"
    "s_and_b32 s78, s78, 0x1ffff\n\t"
    "buffer_load_dwordx4 a[244:247], v8, s[44:47], s78 offen offset:1024 nt\n\t"
    "v_mfma_f32_16x16x32_bf16 v[188:191], a[60:63], a[100:103], v[188:191]\n\t"
    "ds_read_b64_tr_b16 a[30:31], v18 offset:1800\n\t"
    "v_mfma_f32_16x16x32_bf16 v[192:195], a[32:35], a[104:107], v[192:195]\n\t"
    "ds_read_b128 a[68:71], v16 offset:2048\n\t"
    "v_mfma_f32_16x16x32_bf16 v[196:199], a[36:39], a[104:107], v[196:199]\n\t"
    "ds_read_b128 a[72:75], v16 offset:4096\n\t"
    "v_mfma_f32_16x16x32_bf16 v[200:203], a[40:43], a[104:107], v[200:203]\n\t"
    "s_add_u32 s78, s69, 0x4000\n\t"
    "s_and_b32 s78, s78, 0x1ffff\n\t"
    "buffer_load_dwordx4 a[248:251], v9, s[44:47], s78 offen offset:2048 nt\n\t"
    "v_mfma_f32_16x16x32_bf16 v[204:207], a[44:47], a[104:107], v[204:207]\n\t"
    "ds_read_b128 a[76:79], v56 offset:0\n\t"
    "v_mfma_f32_16x16x32_bf16 v[208:211], a[48:51], a[104:107], v[208:211]\n\t"
    "ds_read_b128 a[80:83], v56 offset:2048\n\t"
    "v_mfma_f32_16x16x32_bf16 v[212:215], a[52:55], a[104:107], v[212:215]\n\t"
    "ds_read_b128 a[84:87], v56 offset:4096\n\t"
    "v_mfma_f32_16x16x32_bf16 v[216:219], a[56:59], a[104:107], v[216:219]\n\t"
    "s_add_u32 s78, s69, 0x4000\n\t"
    "s_and_b32 s78, s78, 0x1ffff\n\t"
    "buffer_load_dwordx4 a[252:255], v10, s[44:47], s78 offen offset:3072 nt\n\t"
    "v_mfma_f32_16x16x32_bf16 v[220:223], a[60:63], a[104:107], v[220:223]\n\t"
    "v_mfma_f32_16x16x32_bf16 v[224:227], a[32:35], a[108:111], v[224:227]\n\t"
    "v_mfma_f32_16x16x32_bf16 v[228:231], a[36:39], a[108:111], v[228:231]\n\t"
    "v_mfma_f32_16x16x32_bf16 v[232:235], a[40:43], a[108:111], v[232:235]\n\t"
    "v_mfma_f32_16x16x32_bf16 v[236:239], a[44:47], a[108:111], v[236:239]\n\t"
    "v_mfma_f32_16x16x32_bf16 v[240:243], a[48:51], a[108:111], v[240:243]\n\t"
    "v_mfma_f32_16x16x32_bf16 v[244:247], a[52:55], a[108:111], v[244:247]\n\t"
    "v_mfma_f32_16x16x32_bf16 v[248:251], a[56:59], a[108:111], v[248:251]\n\t"
    "v_mfma_f32_16x16x32_bf16 v[252:255], a[60:63], a[108:111], v[252:255]\n\t"
    "s_waitcnt lgkmcnt(0)\n\t"
    "v_mfma_f32_16x16x32_bf16 v[64:67], a[0:3], a[64:67], v[64:67]\n\t"
    "v_mfma_f32_16x16x32_bf16 v[68:71], a[4:7], a[64:67], v[68:71]\n\t"
    "v_mfma_f32_16x16x32_bf16 v[72:75], a[8:11], a[64:67], v[72:75]\n\t"
    "v_mfma_f32_16x16x32_bf16 v[76:79], a[12:15], a[64:67], v[76:79]\n\t"
    "v_mfma_f32_16x16x32_bf16 v[80:83], a[16:19], a[64:67], v[80:83]\n\t"
    "v_mfma_f32_16x16x32_bf16 v[84:87], a[20:23], a[64:67], v[84:87]\n\t"
    "v_mfma_f32_16x16x32_bf16 v[88:91], a[24:27], a[64:67], v[88:91]\n\t"
    "v_mfma_f32_16x16x32_bf16 v[92:95], a[28:31], a[64:67], v[92:95]\n\t"
    "v_mfma_f32_16x16x32_bf16 v[96:99], a[0:3], a[68:71], v[96:99]\n\t"
    "s_waitcnt vmcnt(22) lgkmcnt(0)\n\t"
    "s_barrier\n\t"
    "v_mfma_f32_16x16x32_bf16 v[100:103], a[4:7], a[68:71], v[100:103]\n\t"
    "ds_read_b128 a[88:91], v17 offset:0\n\t"
    "v_mfma_f32_16x16x32_bf16 v[104:107], a[8:11], a[68:71], v[104:107]\n\t"
    "s_add_u32 s78, s69, 0x4000\n\t"
    "s_and_b32 s78, s78, 0x1ffff\n\t"
    "buffer_load_dwordx4 a[112:115], v11, s[48:51], s78 offen nt\n\t"
    "v_mfma_f32_16x16x32_bf16 v[108:111], a[12:15], a[68:71], v[108:111]\n\t"
    "ds_read_b64_tr_b16 a[32:33], v18 offset:16384\n\t"
    "v_mfma_f32_16x16x32_bf16 v[112:115], a[16:19], a[68:71], v[112:115]\n\t"
    "ds_read_b64_tr_b16 a[34:35], v18 offset:17408\n\t"
    "v_mfma_f32_16x16x32_bf16 v[116:119], a[20:23], a[68:71], v[116:119]\n\t"
    "ds_read_b64_tr_b16 a[36:37], v18 offset:16392\n\t"
    "v_mfma_f32_16x16x32_bf16 v[120:123], a[24:27], a[68:71], v[120:123]\n\t"
    "s_add_u32 s78, s69, 0x4000\n\t"
    "s_and_b32 s78, s78, 0x1ffff\n\t"
    "buffer_load_dwordx4 a[116:119], v12, s[48:51], s78 offen offset:1024 nt\n\t"
    "v_mfma_f32_16x16x32_bf16 v[124:127], a[28:31], a[68:71], v[124:127]\n\t"
    "ds_read_b64_tr_b16 a[38:39], v18 offset:17416\n\t"
    "v_mfma_f32_16x16x32_bf16 v[128:131], a[0:3], a[72:75], v[128:131]\n\t"
    "ds_read_b64_tr_b16 a[40:41], v18 offset:16640\n\t"
    "v_mfma_f32_16x16x32_bf16 v[132:135], a[4:7], a[72:75], v[132:135]\n\t"
    "ds_read_b64_tr_b16 a[42:43], v18 offset:17664\n\t"
    "v_mfma_f32_16x16x32_bf16 v[136:139], a[8:11], a[72:75], v[136:139]\n\t"
    "s_add_u32 s78, s69, 0x4000\n\t"
    "s_and_b32 s78, s78, 0x1ffff\n\t"
    "buffer_load_dwordx4 a[120:123], v11, s[48:51], s78 offen offset:2048 nt\n\t"
    "v_mfma_f32_16x16x32_bf16 v[140:143], a[12:15], a[72:75], v[140:143]\n\t"
    "ds_read_b64_tr_b16 a[44:45], v18 offset:16648\n\t"
    "v_mfma_f32_16x16x32_bf16 v[144:147], a[16:19], a[72:75], v[144:147]\n\t"
    "ds_read_b64_tr_b16 a[46:47], v18 offset:17672\n\t"
    "v_mfma_f32_16x16x32_bf16 v[148:151], a[20:23], a[72:75], v[148:151]\n\t"
    "ds_read_b64_tr_b16 a[48:49], v18 offset:16896\n\t"
    "v_mfma_f32_16x16x32_bf16 v[152:155], a[24:27], a[72:75], v[152:155]\n\t"
    "s_add_u32 s78, s69, 0x4000\n\t"
    "s_and_b32 s78, s78, 0x1ffff\n\t"
    "buffer_load_dwordx4 a[124:127], v12, s[48:51], s78 offen offset:3072 nt\n\t"
    "v_mfma_f32_16x16x32_bf16 v[156:159], a[28:31], a[72:75], v[156:159]\n\t"
    "ds_read_b64_tr_b16 a[50:51], v18 offset:17920\n\t"
    "v_mfma_f32_16x16x32_bf16 v[160:163], a[0:3], a[76:79], v[160:163]\n\t"
    "ds_read_b64_tr_b16 a[52:53], v18 offset:16904\n\t"
    "v_mfma_f32_16x16x32_bf16 v[164:167], a[4:7], a[76:79], v[164:167]\n\t"
    "ds_read_b64_tr_b16 a[54:55], v18 offset:17928\n\t"
    "v_mfma_f32_16x16x32_bf16 v[168:171], a[8:11], a[76:79], v[168:171]\n\t"
    "ds_read_b64_tr_b16 a[56:57], v18 offset:17152\n\t"
    "v_mfma_f32_16x16x32_bf16 v[172:175], a[12:15], a[76:79], v[172:175]\n\t"
    "ds_read_b64_tr_b16 a[58:59], v18 offset:18176\n\t"
    "v_mfma_f32_16x16x32_bf16 v[176:179], a[16:19], a[76:79], v[176:179]\n\t"
    "ds_read_b64_tr_b16 a[60:61], v18 offset:17160\n\t"
    "v_mfma_f32_16x16x32_bf16 v[180:183], a[20:23], a[76:79], v[180:183]\n\t"
    "ds_read_b64_tr_b16 a[62:63], v18 offset:18184\n\t"
    "v_mfma_f32_16x16x32_bf16 v[184:187], a[24:27], a[76:79], v[184:187]\n\t"
    "ds_read_b128 a[92:95], v17 offset:2048\n\t"
    "v_mfma_f32_16x16x32_bf16 v[188:191], a[28:31], a[76:79], v[188:191]\n\t"
    "ds_read_b128 a[96:99], v17 offset:4096\n\t"
    "v_mfma_f32_16x16x32_bf16 v[192:195], a[0:3], a[80:83], v[192:195]\n\t"
    "ds_read_b128 a[100:103], v57 offset:0\n\t"
    "v_mfma_f32_16x16x32_bf16 v[196:199], a[4:7], a[80:83], v[196:199]\n\t"
    "ds_read_b128 a[104:107], v57 offset:2048\n\t"
    "v_mfma_f32_16x16x32_bf16 v[200:203], a[8:11], a[80:83], v[200:203]\n\t"
    "ds_read_b128 a[108:111], v57 offset:4096\n\t"
    "v_mfma_f32_16x16x32_bf16 v[204:207], a[12:15], a[80:83], v[204:207]\n\t"
    "v_mfma_f32_16x16x32_bf16 v[208:211], a[16:19], a[80:83], v[208:211]\n\t"
    "v_mfma_f32_16x16x32_bf16 v[212:215], a[20:23], a[80:83], v[212:215]\n\t"
    "v_mfma_f32_16x16x32_bf16 v[216:219], a[24:27], a[80:83], v[216:219]\n\t"
    "v_mfma_f32_16x16x32_bf16 v[220:223], a[28:31], a[80:83], v[220:223]\n\t"
    "v_mfma_f32_16x16x32_bf16 v[224:227], a[0:3], a[84:87], v[224:227]\n\t"
    "v_mfma_f32_16x16x32_bf16 v[228:231], a[4:7], a[84:87], v[228:231]\n\t"
    "v_mfma_f32_16x16x32_bf16 v[232:235], a[8:11], a[84:87], v[232:235]\n\t"
    "v_mfma_f32_16x16x32_bf16 v[236:239], a[12:15], a[84:87], v[236:239]\n\t"
    "v_mfma_f32_16x16x32_bf16 v[240:243], a[16:19], a[84:87], v[240:243]\n\t"
    "v_mfma_f32_16x16x32_bf16 v[244:247], a[20:23], a[84:87], v[244:247]\n\t"
    "v_mfma_f32_16x16x32_bf16 v[248:251], a[24:27], a[84:87], v[248:251]\n\t"
    "v_mfma_f32_16x16x32_bf16 v[252:255], a[28:31], a[84:87], v[252:255]\n\t"
    "s_waitcnt lgkmcnt(0)\n\t"
    "v_mfma_f32_16x16x32_bf16 v[64:67], a[32:35], a[88:91], v[64:67]\n\t"
    "v_mfma_f32_16x16x32_bf16 v[68:71], a[36:39], a[88:91], v[68:71]\n\t"
    "v_mfma_f32_16x16x32_bf16 v[72:75], a[40:43], a[88:91], v[72:75]\n\t"
    "v_mfma_f32_16x16x32_bf16 v[76:79], a[44:47], a[88:91], v[76:79]\n\t"
    "v_mfma_f32_16x16x32_bf16 v[80:83], a[48:51], a[88:91], v[80:83]\n\t"
    "v_mfma_f32_16x16x32_bf16 v[84:87], a[52:55], a[88:91], v[84:87]\n\t"
    "v_mfma_f32_16x16x32_bf16 v[88:91], a[56:59], a[88:91], v[88:91]\n\t"
    "v_mfma_f32_16x16x32_bf16 v[92:95], a[60:63], a[88:91], v[92:95]\n\t"
    "v_mfma_f32_16x16x32_bf16 v[96:99], a[32:35], a[92:95], v[96:99]\n\t"
    "s_waitcnt vmcnt(12) lgkmcnt(0)\n\t"
    "s_barrier\n\t"
    "v_mfma_f32_16x16x32_bf16 v[100:103], a[36:39], a[92:95], v[100:103]\n\t"
    "ds_read_b128 a[64:67], v21 offset:0\n\t"
    "v_mfma_f32_16x16x32_bf16 v[104:107], a[40:43], a[92:95], v[104:107]\n\t"
    "s_add_u32 s78, s69, 0x6000\n\t"
    "s_and_b32 s78, s78, 0x1ffff\n\t"
    "buffer_load_dwordx4 a[128:131], v7, s[44:47], s78 offen nt\n\t"
    "v_mfma_f32_16x16x32_bf16 v[108:111], a[44:47], a[92:95], v[108:111]\n\t"
    "ds_read_b64_tr_b16 a[0:1], v19 offset:0\n\t"
    "v_mfma_f32_16x16x32_bf16 v[112:115], a[48:51], a[92:95], v[112:115]\n\t"
    "ds_read_b64_tr_b16 a[2:3], v19 offset:1024\n\t"
    "v_mfma_f32_16x16x32_bf16 v[116:119], a[52:55], a[92:95], v[116:119]\n\t"
    "ds_read_b64_tr_b16 a[4:5], v19 offset:8\n\t"
    "v_mfma_f32_16x16x32_bf16 v[120:123], a[56:59], a[92:95], v[120:123]\n\t"
    "s_add_u32 s78, s69, 0x6000\n\t"
    "s_and_b32 s78, s78, 0x1ffff\n\t"
    "buffer_load_dwordx4 a[132:135], v8, s[44:47], s78 offen offset:1024 nt\n\t"
    "v_mfma_f32_16x16x32_bf16 v[124:127], a[60:63], a[92:95], v[124:127]\n\t"
    "ds_read_b64_tr_b16 a[6:7], v19 offset:1032\n\t"
    "v_mfma_f32_16x16x32_bf16 v[128:131], a[32:35], a[96:99], v[128:131]\n\t"
    "ds_read_b64_tr_b16 a[8:9], v19 offset:256\n\t"
    "v_mfma_f32_16x16x32_bf16 v[132:135], a[36:39], a[96:99], v[132:135]\n\t"
    "ds_read_b64_tr_b16 a[10:11], v19 offset:1280\n\t"
    "v_mfma_f32_16x16x32_bf16 v[136:139], a[40:43], a[96:99], v[136:139]\n\t"
    "s_add_u32 s78, s69, 0x6000\n\t"
    "s_and_b32 s78, s78, 0x1ffff\n\t"
    "buffer_load_dwordx4 a[136:139], v9, s[44:47], s78 offen offset:2048 nt\n\t"
    "v_mfma_f32_16x16x32_bf16 v[140:143], a[44:47], a[96:99], v[140:143]\n\t"
    "ds_read_b64_tr_b16 a[12:13], v19 offset:264\n\t"
    "v_mfma_f32_16x16x32_bf16 v[144:147], a[48:51], a[96:99], v[144:147]\n\t"
    "ds_read_b64_tr_b16 a[14:15], v19 offset:1288\n\t"
    "v_mfma_f32_16x16x32_bf16 v[148:151], a[52:55], a[96:99], v[148:151]\n\t"
    "ds_read_b64_tr_b16 a[16:17], v19 offset:512\n\t"
    "v_mfma_f32_16x16x32_bf16 v[152:155], a[56:59], a[96:99], v[152:155]\n\t"
    "s_add_u32 s78, s69, 0x6000\n\t"
    "s_and_b32 s78, s78, 0x1ffff\n\t"
    "buffer_load_dwordx4 a[140:143], v10, s[44:47], s78 offen offset:3072 nt\n\t"
    "v_mfma_f32_16x16x32_bf16 v[156:159], a[60:63], a[96:99], v[156:159]\n\t"
    "ds_read_b64_tr_b16 a[18:19], v19 offset:1536\n\t"
    "v_mfma_f32_16x16x32_bf16 v[160:163], a[32:35], a[100:103], v[160:163]\n\t"
    "ds_read_b64_tr_b16 a[20:21], v19 offset:520\n\t"
    "v_mfma_f32_16x16x32_bf16 v[164:167], a[36:39], a[100:103], v[164:167]\n\t"
    "ds_read_b64_tr_b16 a[22:23], v19 offset:1544\n\t"
    "v_mfma_f32_16x16x32_bf16 v[168:171], a[40:43], a[100:103], v[168:171]\n\t"
    "ds_read_b64_tr_b16 a[24:25], v19 offset:768\n\t"
    "v_mfma_f32_16x16x32_bf16 v[172:175], a[44:47], a[100:103], v[172:175]\n\t"
    "ds_read_b64_tr_b16 a[26:27], v19 offset:1792\n\t"
    "v_mfma_f32_16x16x32_bf16 v[176:179], a[48:51], a[100:103], v[176:179]\n\t"
    "ds_read_b64_tr_b16 a[28:29], v19 offset:776\n\t"
    "v_mfma_f32_16x16x32_bf16 v[180:183], a[52:55], a[100:103], v[180:183]\n\t"
    "ds_read_b64_tr_b16 a[30:31], v19 offset:1800\n\t"
    "v_mfma_f32_16x16x32_bf16 v[184:187], a[56:59], a[100:103], v[184:187]\n\t"
    "ds_read_b128 a[68:71], v21 offset:2048\n\t"
    "v_mfma_f32_16x16x32_bf16 v[188:191], a[60:63], a[100:103], v[188:191]\n\t"
    "ds_read_b128 a[72:75], v21 offset:4096\n\t"
    "v_mfma_f32_16x16x32_bf16 v[192:195], a[32:35], a[104:107], v[192:195]\n\t"
    "ds_read_b128 a[76:79], v22 offset:0\n\t"
    "v_mfma_f32_16x16x32_bf16 v[196:199], a[36:39], a[104:107], v[196:199]\n\t"
    "ds_read_b128 a[80:83], v22 offset:2048\n\t"
    "v_mfma_f32_16x16x32_bf16 v[200:203], a[40:43], a[104:107], v[200:203]\n\t"
    "ds_read_b128 a[84:87], v22 offset:4096\n\t"
    "v_mfma_f32_16x16x32_bf16 v[204:207], a[44:47], a[104:107], v[204:207]\n\t"
    "v_mfma_f32_16x16x32_bf16 v[208:211], a[48:51], a[104:107], v[208:211]\n\t"
    "v_mfma_f32_16x16x32_bf16 v[212:215], a[52:55], a[104:107], v[212:215]\n\t"
    "v_mfma_f32_16x16x32_bf16 v[216:219], a[56:59], a[104:107], v[216:219]\n\t"
    "v_mfma_f32_16x16x32_bf16 v[220:223], a[60:63], a[104:107], v[220:223]\n\t"
    "v_mfma_f32_16x16x32_bf16 v[224:227], a[32:35], a[108:111], v[224:227]\n\t"
    "v_mfma_f32_16x16x32_bf16 v[228:231], a[36:39], a[108:111], v[228:231]\n\t"
    "v_mfma_f32_16x16x32_bf16 v[232:235], a[40:43], a[108:111], v[232:235]\n\t"
    "v_mfma_f32_16x16x32_bf16 v[236:239], a[44:47], a[108:111], v[236:239]\n\t"
    "v_mfma_f32_16x16x32_bf16 v[240:243], a[48:51], a[108:111], v[240:243]\n\t"
    "v_mfma_f32_16x16x32_bf16 v[244:247], a[52:55], a[108:111], v[244:247]\n\t"
    "v_mfma_f32_16x16x32_bf16 v[248:251], a[56:59], a[108:111], v[248:251]\n\t"
    "v_mfma_f32_16x16x32_bf16 v[252:255], a[60:63], a[108:111], v[252:255]\n\t"
    "s_waitcnt lgkmcnt(0)\n\t"
    "v_mfma_f32_16x16x32_bf16 v[64:67], a[0:3], a[64:67], v[64:67]\n\t"
    "v_mfma_f32_16x16x32_bf16 v[68:71], a[4:7], a[64:67], v[68:71]\n\t"
    "v_mfma_f32_16x16x32_bf16 v[72:75], a[8:11], a[64:67], v[72:75]\n\t"
    "v_mfma_f32_16x16x32_bf16 v[76:79], a[12:15], a[64:67], v[76:79]\n\t"
    "v_mfma_f32_16x16x32_bf16 v[80:83], a[16:19], a[64:67], v[80:83]\n\t"
    "v_mfma_f32_16x16x32_bf16 v[84:87], a[20:23], a[64:67], v[84:87]\n\t"
    "v_mfma_f32_16x16x32_bf16 v[88:91], a[24:27], a[64:67], v[88:91]\n\t"
    "v_mfma_f32_16x16x32_bf16 v[92:95], a[28:31], a[64:67], v[92:95]\n\t"
    "v_mfma_f32_16x16x32_bf16 v[96:99], a[0:3], a[68:71], v[96:99]\n\t"
    "s_waitcnt vmcnt(12) lgkmcnt(0)\n\t"
    "s_barrier\n\t"
    "v_mfma_f32_16x16x32_bf16 v[100:103], a[4:7], a[68:71], v[100:103]\n\t"
    "ds_read_b128 a[88:91], v61 offset:0\n\t"
    "v_mfma_f32_16x16x32_bf16 v[104:107], a[8:11], a[68:71], v[104:107]\n\t"
    "s_add_u32 s78, s69, 0x6000\n\t"
    "s_and_b32 s78, s78, 0x1ffff\n\t"
    "buffer_load_dwordx4 a[144:147], v11, s[48:51], s78 offen nt\n\t"
    "v_mfma_f32_16x16x32_bf16 v[108:111], a[12:15], a[68:71], v[108:111]\n\t"
    "ds_read_b64_tr_b16 a[32:33], v19 offset:16384\n\t"
    "v_mfma_f32_16x16x32_bf16 v[112:115], a[16:19], a[68:71], v[112:115]\n\t"
    "ds_read_b64_tr_b16 a[34:35], v19 offset:17408\n\t"
    "v_mfma_f32_16x16x32_bf16 v[116:119], a[20:23], a[68:71], v[116:119]\n\t"
    "ds_read_b64_tr_b16 a[36:37], v19 offset:16392\n\t"
    "v_mfma_f32_16x16x32_bf16 v[120:123], a[24:27], a[68:71], v[120:123]\n\t"
    "s_add_u32 s78, s69, 0x6000\n\t"
    "s_and_b32 s78, s78, 0x1ffff\n\t"
    "buffer_load_dwordx4 a[148:151], v12, s[48:51], s78 offen offset:1024 nt\n\t"
    "v_mfma_f32_16x16x32_bf16 v[124:127], a[28:31], a[68:71], v[124:127]\n\t"
    "ds_read_b64_tr_b16 a[38:39], v19 offset:17416\n\t"
    "v_mfma_f32_16x16x32_bf16 v[128:131], a[0:3], a[72:75], v[128:131]\n\t"
    "ds_read_b64_tr_b16 a[40:41], v19 offset:16640\n\t"
    "v_mfma_f32_16x16x32_bf16 v[132:135], a[4:7], a[72:75], v[132:135]\n\t"
    "ds_read_b64_tr_b16 a[42:43], v19 offset:17664\n\t"
    "v_mfma_f32_16x16x32_bf16 v[136:139], a[8:11], a[72:75], v[136:139]\n\t"
    "s_add_u32 s78, s69, 0x6000\n\t"
    "s_and_b32 s78, s78, 0x1ffff\n\t"
    "buffer_load_dwordx4 a[152:155], v11, s[48:51], s78 offen offset:2048 nt\n\t"
    "v_mfma_f32_16x16x32_bf16 v[140:143], a[12:15], a[72:75], v[140:143]\n\t"
    "ds_read_b64_tr_b16 a[44:45], v19 offset:16648\n\t"
    "v_mfma_f32_16x16x32_bf16 v[144:147], a[16:19], a[72:75], v[144:147]\n\t"
    "ds_read_b64_tr_b16 a[46:47], v19 offset:17672\n\t"
    "v_mfma_f32_16x16x32_bf16 v[148:151], a[20:23], a[72:75], v[148:151]\n\t"
    "ds_read_b64_tr_b16 a[48:49], v19 offset:16896\n\t"
    "v_mfma_f32_16x16x32_bf16 v[152:155], a[24:27], a[72:75], v[152:155]\n\t"
    "s_add_u32 s78, s69, 0x6000\n\t"
    "s_and_b32 s78, s78, 0x1ffff\n\t"
    "buffer_load_dwordx4 a[156:159], v12, s[48:51], s78 offen offset:3072 nt\n\t"
    "v_mfma_f32_16x16x32_bf16 v[156:159], a[28:31], a[72:75], v[156:159]\n\t"
    "ds_read_b64_tr_b16 a[50:51], v19 offset:17920\n\t"
    "v_mfma_f32_16x16x32_bf16 v[160:163], a[0:3], a[76:79], v[160:163]\n\t"
    "ds_read_b64_tr_b16 a[52:53], v19 offset:16904\n\t"
    "v_mfma_f32_16x16x32_bf16 v[164:167], a[4:7], a[76:79], v[164:167]\n\t"
    "ds_read_b64_tr_b16 a[54:55], v19 offset:17928\n\t"
    "v_mfma_f32_16x16x32_bf16 v[168:171], a[8:11], a[76:79], v[168:171]\n\t"
    "s_add_u32 s78, s69, 0x8000\n\t"
    "s_and_b32 s78, s78, 0x1ffff\n\t"
    "buffer_load_dwordx4 a[160:163], v7, s[44:47], s78 offen nt\n\t"
    "v_mfma_f32_16x16x32_bf16 v[172:175], a[12:15], a[76:79], v[172:175]\n\t"
    "ds_read_b64_tr_b16 a[56:57], v19 offset:17152\n\t"
    "v_mfma_f32_16x16x32_bf16 v[176:179], a[16:19], a[76:79], v[176:179]\n\t"
    "ds_read_b64_tr_b16 a[58:59], v19 offset:18176\n\t"
    "v_mfma_f32_16x16x32_bf16 v[180:183], a[20:23], a[76:79], v[180:183]\n\t"
    "ds_read_b64_tr_b16 a[60:61], v19 offset:17160\n\t"
    "v_mfma_f32_16x16x32_bf16 v[184:187], a[24:27], a[76:79], v[184:187]\n\t"
    "s_add_u32 s78, s69, 0x8000\n\t"
    "s_and_b32 s78, s78, 0x1ffff\n\t"
    "buffer_load_dwordx4 a[164:167], v8, s[44:47], s78 offen offset:1024 nt\n\t"
    "v_mfma_f32_16x16x32_bf16 v[188:191], a[28:31], a[76:79], v[188:191]\n\t"
    "ds_read_b64_tr_b16 a[62:63], v19 offset:18184\n\t"
    "v_mfma_f32_16x16x32_bf16 v[192:195], a[0:3], a[80:83], v[192:195]\n\t"
    "ds_read_b128 a[92:95], v61 offset:2048\n\t"
    "v_mfma_f32_16x16x32_bf16 v[196:199], a[4:7], a[80:83], v[196:199]\n\t"
    "ds_read_b128 a[96:99], v61 offset:4096\n\t"
    "v_mfma_f32_16x16x32_bf16 v[200:203], a[8:11], a[80:83], v[200:203]\n\t"
    "ds_read_b128 a[100:103], v23 offset:0\n\t"
    "v_mfma_f32_16x16x32_bf16 v[204:207], a[12:15], a[80:83], v[204:207]\n\t"
    "ds_read_b128 a[104:107], v23 offset:2048\n\t"
    "v_mfma_f32_16x16x32_bf16 v[208:211], a[16:19], a[80:83], v[208:211]\n\t"
    "ds_read_b128 a[108:111], v23 offset:4096\n\t"
    "v_mfma_f32_16x16x32_bf16 v[212:215], a[20:23], a[80:83], v[212:215]\n\t"
    "v_mfma_f32_16x16x32_bf16 v[216:219], a[24:27], a[80:83], v[216:219]\n\t"
    "v_mfma_f32_16x16x32_bf16 v[220:223], a[28:31], a[80:83], v[220:223]\n\t"
    "v_mfma_f32_16x16x32_bf16 v[224:227], a[0:3], a[84:87], v[224:227]\n\t"
    "v_mfma_f32_16x16x32_bf16 v[228:231], a[4:7], a[84:87], v[228:231]\n\t"
    "v_mfma_f32_16x16x32_bf16 v[232:235], a[8:11], a[84:87], v[232:235]\n\t"
    "v_mfma_f32_16x16x32_bf16 v[236:239], a[12:15], a[84:87], v[236:239]\n\t"
    "v_mfma_f32_16x16x32_bf16 v[240:243], a[16:19], a[84:87], v[240:243]\n\t"
    "v_mfma_f32_16x16x32_bf16 v[244:247], a[20:23], a[84:87], v[244:247]\n\t"
    "v_mfma_f32_16x16x32_bf16 v[248:251], a[24:27], a[84:87], v[248:251]\n\t"
    "v_mfma_f32_16x16x32_bf16 v[252:255], a[28:31], a[84:87], v[252:255]\n\t"
    "s_waitcnt lgkmcnt(0)\n\t"
    "v_mfma_f32_16x16x32_bf16 v[64:67], a[32:35], a[88:91], v[64:67]\n\t"
    "s_waitcnt vmcnt(14) lgkmcnt(0)\n\t"
    "s_barrier\n\t"
    "v_mfma_f32_16x16x32_bf16 v[68:71], a[36:39], a[88:91], v[68:71]\n\t"
    "ds_write_b128 v62, a[192:195] offset:8192\n\t"
    "v_mfma_f32_16x16x32_bf16 v[72:75], a[40:43], a[88:91], v[72:75]\n\t"
    "s_add_u32 s78, s69, 0x8000\n\t"
    "s_and_b32 s78, s78, 0x1ffff\n\t"
    "buffer_load_dwordx4 a[168:171], v9, s[44:47], s78 offen offset:2048 nt\n\t"
    "v_mfma_f32_16x16x32_bf16 v[76:79], a[44:47], a[88:91], v[76:79]\n\t"
    "ds_write_b128 v62, a[196:199] offset:9216\n\t"
    "v_mfma_f32_16x16x32_bf16 v[80:83], a[48:51], a[88:91], v[80:83]\n\t"
    "ds_write_b128 v62, a[200:203] offset:10240\n\t"
    "v_mfma_f32_16x16x32_bf16 v[84:87], a[52:55], a[88:91], v[84:87]\n\t"
    "ds_write_b128 v62, a[204:207] offset:11264\n\t"
    "v_mfma_f32_16x16x32_bf16 v[88:91], a[56:59], a[88:91], v[88:91]\n\t"
    "s_add_u32 s78, s69, 0x8000\n\t"
    "s_and_b32 s78, s78, 0x1ffff\n\t"
    "buffer_load_dwordx4 a[172:175], v10, s[44:47], s78 offen offset:3072 nt\n\t"
    "v_mfma_f32_16x16x32_bf16 v[92:95], a[60:63], a[88:91], v[92:95]\n\t"
    "ds_write_b128 v62, a[208:211] offset:16384\n\t"
    "v_mfma_f32_16x16x32_bf16 v[96:99], a[32:35], a[92:95], v[96:99]\n\t"
    "ds_write_b128 v62, a[212:215] offset:17408\n\t"
    "v_mfma_f32_16x16x32_bf16 v[100:103], a[36:39], a[92:95], v[100:103]\n\t"
    "ds_write_b128 v62, a[216:219] offset:18432\n\t"
    "v_mfma_f32_16x16x32_bf16 v[104:107], a[40:43], a[92:95], v[104:107]\n\t"
    "s_add_u32 s78, s69, 0x8000\n\t"
    "s_and_b32 s78, s78, 0x1ffff\n\t"
    "buffer_load_dwordx4 a[176:179], v11, s[48:51], s78 offen nt\n\t"
    "v_mfma_f32_16x16x32_bf16 v[108:111], a[44:47], a[92:95], v[108:111]\n\t"
    "ds_write_b128 v62, a[220:223] offset:19456\n\t"
    "v_mfma_f32_16x16x32_bf16 v[112:115], a[48:51], a[92:95], v[112:115]\n\t"
    "ds_read_b128 a[192:195], v1 offset:0\n\t"
    "v_mfma_f32_16x16x32_bf16 v[116:119], a[52:55], a[92:95], v[116:119]\n\t"
    "ds_read_b128 a[196:199], v2 offset:0\n\t"
    "v_mfma_f32_16x16x32_bf16 v[120:123], a[56:59], a[92:95], v[120:123]\n\t"
    "s_add_u32 s78, s69, 0x8000\n\t"
    "s_and_b32 s78, s78, 0x1ffff\n\t"
    "buffer_load_dwordx4 a[180:183], v12, s[48:51], s78 offen offset:1024 nt\n\t"
    "v_mfma_f32_16x16x32_bf16 v[124:127], a[60:63], a[92:95], v[124:127]\n\t"
    "ds_read_b128 a[200:203], v3 offset:0\n\t"
    "v_mfma_f32_16x16x32_bf16 v[128:131], a[32:35], a[96:99], v[128:131]\n\t"
    "ds_read_b128 a[204:207], v4 offset:0\n\t"
    "v_mfma_f32_16x16x32_bf16 v[132:135], a[36:39], a[96:99], v[132:135]\n\t"
    "ds_read_b128 a[208:211], v1 offset:4096\n\t"
    "v_mfma_f32_16x16x32_bf16 v[136:139], a[40:43], a[96:99], v[136:139]\n\t"
    "s_add_u32 s78, s69, 0x8000\n\t"
    "s_and_b32 s78, s78, 0x1ffff\n\t"
    "buffer_load_dwordx4 a[184:187], v11, s[48:51], s78 offen offset:2048 nt\n\t"
    "v_mfma_f32_16x16x32_bf16 v[140:143], a[44:47], a[96:99], v[140:143]\n\t"
    "ds_read_b128 a[212:215], v2 offset:4096\n\t"
    "v_mfma_f32_16x16x32_bf16 v[144:147], a[48:51], a[96:99], v[144:147]\n\t"
    "ds_read_b128 a[216:219], v3 offset:4096\n\t"
    "v_mfma_f32_16x16x32_bf16 v[148:151], a[52:55], a[96:99], v[148:151]\n\t"
    "ds_read_b128 a[220:223], v4 offset:4096\n\t"
    "v_mfma_f32_16x16x32_bf16 v[152:155], a[56:59], a[96:99], v[152:155]\n\t"
    "s_add_u32 s78, s69, 0x8000\n\t"
    "s_and_b32 s78, s78, 0x1ffff\n\t"
    "buffer_load_dwordx4 a[188:191], v12, s[48:51], s78 offen offset:3072 nt\n\t"
    "v_mfma_f32_16x16x32_bf16 v[156:159], a[60:63], a[96:99], v[156:159]\n\t"
    "ds_write_b128 v62, a[224:227] offset:24576\n\t"
    "v_mfma_f32_16x16x32_bf16 v[160:163], a[32:35], a[100:103], v[160:163]\n\t"
    "ds_write_b128 v62, a[228:231] offset:25600\n\t"
    "v_mfma_f32_16x16x32_bf16 v[164:167], a[36:39], a[100:103], v[164:167]\n\t"
    "ds_write_b128 v62, a[232:235] offset:26624\n\t"
    "v_mfma_f32_16x16x32_bf16 v[168:171], a[40:43], a[100:103], v[168:171]\n\t"
    "ds_write_b128 v62, a[236:239] offset:27648\n\t"
    "v_mfma_f32_16x16x32_bf16 v[172:175], a[44:47], a[100:103], v[172:175]\n\t"
    "ds_write_b128 v62, a[240:243] offset:32768\n\t"
    "v_mfma_f32_16x16x32_bf16 v[176:179], a[48:51], a[100:103], v[176:179]\n\t"
    "ds_write_b128 v62, a[244:247] offset:33792\n\t"
    "v_mfma_f32_16x16x32_bf16 v[180:183], a[52:55], a[100:103], v[180:183]\n\t"
    "ds_write_b128 v62, a[248:251] offset:34816\n\t"
    "v_mfma_f32_16x16x32_bf16 v[184:187], a[56:59], a[100:103], v[184:187]\n\t"
    "ds_write_b128 v62, a[252:255] offset:35840\n\t"
    "v_mul_f32 v64, s72, v64\n\t"
    "v_mul_f32 v65, s72, v65\n\t"
    "v_mfma_f32_16x16x32_bf16 v[188:191], a[60:63], a[100:103], v[188:191]\n\t"
    "v_mul_f32 v66, s72, v66\n\t"
    "v_mul_f32 v67, s72, v67\n\t"
    "v_mfma_f32_16x16x32_bf16 v[192:195], a[32:35], a[104:107], v[192:195]\n\t"
    "v_mul_f32 v68, s72, v68\n\t"
    "v_mul_f32 v69, s72, v69\n\t"
    "v_mfma_f32_16x16x32_bf16 v[196:199], a[36:39], a[104:107], v[196:199]\n\t"
    "v_mul_f32 v70, s72, v70\n\t"
    "v_mul_f32 v71, s72, v71\n\t"
    "v_mfma_f32_16x16x32_bf16 v[200:203], a[40:43], a[104:107], v[200:203]\n\t"
    "v_cvt_pk_bf16_f32 v64, v64, v65\n\t"
    "v_cvt_pk_bf16_f32 v65, v66, v67\n\t"
    "v_mfma_f32_16x16x32_bf16 v[204:207], a[44:47], a[104:107], v[204:207]\n\t"
    "v_cvt_pk_bf16_f32 v66, v68, v69\n\t"
    "v_cvt_pk_bf16_f32 v67, v70, v71\n\t"
    "v_mfma_f32_16x16x32_bf16 v[208:211], a[48:51], a[104:107], v[208:211]\n\t"
    "v_mul_f32 v72, s72, v72\n\t"
    "v_mul_f32 v73, s72, v73\n\t"
    "v_mfma_f32_16x16x32_bf16 v[212:215], a[52:55], a[104:107], v[212:215]\n\t"
    "v_mul_f32 v74, s72, v74\n\t"
    "v_mul_f32 v75, s72, v75\n\t"
    "v_mfma_f32_16x16x32_bf16 v[216:219], a[56:59], a[104:107], v[216:219]\n\t"
    "v_mul_f32 v76, s72, v76\n\t"
    "v_mul_f32 v77, s72, v77\n\t"
    "v_mfma_f32_16x16x32_bf16 v[220:223], a[60:63], a[104:107], v[220:223]\n\t"
    "v_mul_f32 v78, s72, v78\n\t"
    "v_mul_f32 v79, s72, v79\n\t"
    "v_mfma_f32_16x16x32_bf16 v[224:227], a[32:35], a[108:111], v[224:227]\n\t"
    "v_cvt_pk_bf16_f32 v68, v72, v73\n\t"
    "v_cvt_pk_bf16_f32 v69, v74, v75\n\t"
    "v_mfma_f32_16x16x32_bf16 v[228:231], a[36:39], a[108:111], v[228:231]\n\t"
    "v_cvt_pk_bf16_f32 v70, v76, v77\n\t"
    "v_cvt_pk_bf16_f32 v71, v78, v79\n\t"
    "v_mfma_f32_16x16x32_bf16 v[232:235], a[40:43], a[108:111], v[232:235]\n\t"
    "v_mul_f32 v80, s72, v80\n\t"
    "v_mul_f32 v81, s72, v81\n\t"
    "v_mfma_f32_16x16x32_bf16 v[236:239], a[44:47], a[108:111], v[236:239]\n\t"
    "v_mul_f32 v82, s72, v82\n\t"
    "v_mul_f32 v83, s72, v83\n\t"
    "v_mfma_f32_16x16x32_bf16 v[240:243], a[48:51], a[108:111], v[240:243]\n\t"
    "v_mul_f32 v84, s72, v84\n\t"
    "v_mul_f32 v85, s72, v85\n\t"
    "v_mfma_f32_16x16x32_bf16 v[244:247], a[52:55], a[108:111], v[244:247]\n\t"
    "v_mul_f32 v86, s72, v86\n\t"
    "v_mul_f32 v87, s72, v87\n\t"
    "v_mfma_f32_16x16x32_bf16 v[248:251], a[56:59], a[108:111], v[248:251]\n\t"
    "v_cvt_pk_bf16_f32 v72, v80, v81\n\t"
    "v_cvt_pk_bf16_f32 v73, v82, v83\n\t"
    "v_mfma_f32_16x16x32_bf16 v[252:255], a[60:63], a[108:111], v[252:255]\n\t"
    "s_waitcnt lgkmcnt(0)\n\t"
    "v_cvt_pk_bf16_f32 v74, v84, v85\n\t"
    "v_cvt_pk_bf16_f32 v75, v86, v87\n\t"
    "s_nop 7\n\t"
    "v_pk_mul_f32 v[88:89], v[88:89], s[72:73]\n\t"
    "v_pk_mul_f32 v[90:91], v[90:91], s[72:73]\n\t"
    "v_pk_mul_f32 v[92:93], v[92:93], s[72:73]\n\t"
    "v_pk_mul_f32 v[94:95], v[94:95], s[72:73]\n\t"
    "v_cvt_pk_bf16_f32 v76, v88, v89\n\t"
    "v_cvt_pk_bf16_f32 v77, v90, v91\n\t"
    "v_cvt_pk_bf16_f32 v78, v92, v93\n\t"
    "v_cvt_pk_bf16_f32 v79, v94, v95\n\t"
    "v_pk_mul_f32 v[96:97], v[96:97], s[72:73]\n\t"
    "v_pk_mul_f32 v[98:99], v[98:99], s[72:73]\n\t"
    "v_pk_mul_f32 v[100:101], v[100:101], s[72:73]\n\t"
    "v_pk_mul_f32 v[102:103], v[102:103], s[72:73]\n\t"
    "v_cvt_pk_bf16_f32 v80, v96, v97\n\t"
    "v_cvt_pk_bf16_f32 v81, v98, v99\n\t"
    "v_cvt_pk_bf16_f32 v82, v100, v101\n\t"
    "v_cvt_pk_bf16_f32 v83, v102, v103\n\t"
    "v_pk_mul_f32 v[104:105], v[104:105], s[72:73]\n\t"
    "v_pk_mul_f32 v[106:107], v[106:107], s[72:73]\n\t"
    "v_pk_mul_f32 v[108:109], v[108:109], s[72:73]\n\t"
    "v_pk_mul_f32 v[110:111], v[110:111], s[72:73]\n\t"
    "v_cvt_pk_bf16_f32 v84, v104, v105\n\t"
    "v_cvt_pk_bf16_f32 v85, v106, v107\n\t"
    "v_cvt_pk_bf16_f32 v86, v108, v109\n\t"
    "v_cvt_pk_bf16_f32 v87, v110, v111\n\t"
    "v_pk_mul_f32 v[112:113], v[112:113], s[72:73]\n\t"
    "v_pk_mul_f32 v[114:115], v[114:115], s[72:73]\n\t"
    "v_pk_mul_f32 v[116:117], v[116:117], s[72:73]\n\t"
    "v_pk_mul_f32 v[118:119], v[118:119], s[72:73]\n\t"
    "v_cvt_pk_bf16_f32 v88, v112, v113\n\t"
    "v_cvt_pk_bf16_f32 v89, v114, v115\n\t"
    "v_cvt_pk_bf16_f32 v90, v116, v117\n\t"
    "v_cvt_pk_bf16_f32 v91, v118, v119\n\t"
    "v_pk_mul_f32 v[120:121], v[120:121], s[72:73]\n\t"
    "v_pk_mul_f32 v[122:123], v[122:123], s[72:73]\n\t"
    "v_pk_mul_f32 v[124:125], v[124:125], s[72:73]\n\t"
    "v_pk_mul_f32 v[126:127], v[126:127], s[72:73]\n\t"
    "v_cvt_pk_bf16_f32 v92, v120, v121\n\t"
    "v_cvt_pk_bf16_f32 v93, v122, v123\n\t"
    "v_cvt_pk_bf16_f32 v94, v124, v125\n\t"
    "v_cvt_pk_bf16_f32 v95, v126, v127\n\t"
    "v_pk_mul_f32 v[128:129], v[128:129], s[72:73]\n\t"
    "v_pk_mul_f32 v[130:131], v[130:131], s[72:73]\n\t"
    "v_pk_mul_f32 v[132:133], v[132:133], s[72:73]\n\t"
    "v_pk_mul_f32 v[134:135], v[134:135], s[72:73]\n\t"
    "v_cvt_pk_bf16_f32 v96, v128, v129\n\t"
    "v_cvt_pk_bf16_f32 v97, v130, v131\n\t"
    "v_cvt_pk_bf16_f32 v98, v132, v133\n\t"
    "v_cvt_pk_bf16_f32 v99, v134, v135\n\t"
    "v_pk_mul_f32 v[136:137], v[136:137], s[72:73]\n\t"
    "v_pk_mul_f32 v[138:139], v[138:139], s[72:73]\n\t"
    "v_pk_mul_f32 v[140:141], v[140:141], s[72:73]\n\t"
    "v_pk_mul_f32 v[142:143], v[142:143], s[72:73]\n\t"
    "v_cvt_pk_bf16_f32 v100, v136, v137\n\t"
    "v_cvt_pk_bf16_f32 v101, v138, v139\n\t"
    "v_cvt_pk_bf16_f32 v102, v140, v141\n\t"
    "v_cvt_pk_bf16_f32 v103, v142, v143\n\t"
    "v_pk_mul_f32 v[144:145], v[144:145], s[72:73]\n\t"
    "v_pk_mul_f32 v[146:147], v[146:147], s[72:73]\n\t"
    "v_pk_mul_f32 v[148:149], v[148:149], s[72:73]\n\t"
    "v_pk_mul_f32 v[150:151], v[150:151], s[72:73]\n\t"
    "v_cvt_pk_bf16_f32 v104, v144, v145\n\t"
    "v_cvt_pk_bf16_f32 v105, v146, v147\n\t"
    "v_cvt_pk_bf16_f32 v106, v148, v149\n\t"
    "v_cvt_pk_bf16_f32 v107, v150, v151\n\t"
    "v_pk_mul_f32 v[152:153], v[152:153], s[72:73]\n\t"
    "v_pk_mul_f32 v[154:155], v[154:155], s[72:73]\n\t"
    "v_pk_mul_f32 v[156:157], v[156:157], s[72:73]\n\t"
    "v_pk_mul_f32 v[158:159], v[158:159], s[72:73]\n\t"
    "v_cvt_pk_bf16_f32 v108, v152, v153\n\t"
    "v_cvt_pk_bf16_f32 v109, v154, v155\n\t"
    "v_cvt_pk_bf16_f32 v110, v156, v157\n\t"
    "v_cvt_pk_bf16_f32 v111, v158, v159\n\t"
    "v_pk_mul_f32 v[160:161], v[160:161], s[72:73]\n\t"
    "v_pk_mul_f32 v[162:163], v[162:163], s[72:73]\n\t"
    "v_pk_mul_f32 v[164:165], v[164:165], s[72:73]\n\t"
    "v_pk_mul_f32 v[166:167], v[166:167], s[72:73]\n\t"
    "v_cvt_pk_bf16_f32 v112, v160, v161\n\t"
    "v_cvt_pk_bf16_f32 v113, v162, v163\n\t"
    "v_cvt_pk_bf16_f32 v114, v164, v165\n\t"
    "v_cvt_pk_bf16_f32 v115, v166, v167\n\t"
    "v_pk_mul_f32 v[168:169], v[168:169], s[72:73]\n\t"
    "v_pk_mul_f32 v[170:171], v[170:171], s[72:73]\n\t"
    "v_pk_mul_f32 v[172:173], v[172:173], s[72:73]\n\t"
    "v_pk_mul_f32 v[174:175], v[174:175], s[72:73]\n\t"
    "v_cvt_pk_bf16_f32 v116, v168, v169\n\t"
    "v_cvt_pk_bf16_f32 v117, v170, v171\n\t"
    "v_cvt_pk_bf16_f32 v118, v172, v173\n\t"
    "v_cvt_pk_bf16_f32 v119, v174, v175\n\t"
    "v_pk_mul_f32 v[176:177], v[176:177], s[72:73]\n\t"
    "v_pk_mul_f32 v[178:179], v[178:179], s[72:73]\n\t"
    "v_pk_mul_f32 v[180:181], v[180:181], s[72:73]\n\t"
    "v_pk_mul_f32 v[182:183], v[182:183], s[72:73]\n\t"
    "v_cvt_pk_bf16_f32 v120, v176, v177\n\t"
    "v_cvt_pk_bf16_f32 v121, v178, v179\n\t"
    "v_cvt_pk_bf16_f32 v122, v180, v181\n\t"
    "v_cvt_pk_bf16_f32 v123, v182, v183\n\t"
    "v_pk_mul_f32 v[184:185], v[184:185], s[72:73]\n\t"
    "v_pk_mul_f32 v[186:187], v[186:187], s[72:73]\n\t"
    "v_pk_mul_f32 v[188:189], v[188:189], s[72:73]\n\t"
    "v_pk_mul_f32 v[190:191], v[190:191], s[72:73]\n\t"
    "v_cvt_pk_bf16_f32 v124, v184, v185\n\t"
    "v_cvt_pk_bf16_f32 v125, v186, v187\n\t"
    "v_cvt_pk_bf16_f32 v126, v188, v189\n\t"
    "v_cvt_pk_bf16_f32 v127, v190, v191\n\t"
    "v_pk_mul_f32 v[192:193], v[192:193], s[72:73]\n\t"
    "v_pk_mul_f32 v[194:195], v[194:195], s[72:73]\n\t"
    "v_pk_mul_f32 v[196:197], v[196:197], s[72:73]\n\t"
    "v_pk_mul_f32 v[198:199], v[198:199], s[72:73]\n\t"
    "v_cvt_pk_bf16_f32 v128, v192, v193\n\t"
    "v_cvt_pk_bf16_f32 v129, v194, v195\n\t"
    "v_cvt_pk_bf16_f32 v130, v196, v197\n\t"
    "v_cvt_pk_bf16_f32 v131, v198, v199\n\t"
    "v_pk_mul_f32 v[200:201], v[200:201], s[72:73]\n\t"
    "v_pk_mul_f32 v[202:203], v[202:203], s[72:73]\n\t"
    "v_pk_mul_f32 v[204:205], v[204:205], s[72:73]\n\t"
    "v_pk_mul_f32 v[206:207], v[206:207], s[72:73]\n\t"
    "v_cvt_pk_bf16_f32 v132, v200, v201\n\t"
    "v_cvt_pk_bf16_f32 v133, v202, v203\n\t"
    "v_cvt_pk_bf16_f32 v134, v204, v205\n\t"
    "v_cvt_pk_bf16_f32 v135, v206, v207\n\t"
    "v_pk_mul_f32 v[208:209], v[208:209], s[72:73]\n\t"
    "v_pk_mul_f32 v[210:211], v[210:211], s[72:73]\n\t"
    "v_pk_mul_f32 v[212:213], v[212:213], s[72:73]\n\t"
    "v_pk_mul_f32 v[214:215], v[214:215], s[72:73]\n\t"
    "v_cvt_pk_bf16_f32 v136, v208, v209\n\t"
    "v_cvt_pk_bf16_f32 v137, v210, v211\n\t"
    "v_cvt_pk_bf16_f32 v138, v212, v213\n\t"
    "v_cvt_pk_bf16_f32 v139, v214, v215\n\t"
    "v_pk_mul_f32 v[216:217], v[216:217], s[72:73]\n\t"
    "v_pk_mul_f32 v[218:219], v[218:219], s[72:73]\n\t"
    "v_pk_mul_f32 v[220:221], v[220:221], s[72:73]\n\t"
    "v_pk_mul_f32 v[222:223], v[222:223], s[72:73]\n\t"
    "v_cvt_pk_bf16_f32 v140, v216, v217\n\t"
    "v_cvt_pk_bf16_f32 v141, v218, v219\n\t"
    "v_cvt_pk_bf16_f32 v142, v220, v221\n\t"
    "v_cvt_pk_bf16_f32 v143, v222, v223\n\t"
    "v_pk_mul_f32 v[224:225], v[224:225], s[72:73]\n\t"
    "v_pk_mul_f32 v[226:227], v[226:227], s[72:73]\n\t"
    "v_pk_mul_f32 v[228:229], v[228:229], s[72:73]\n\t"
    "v_pk_mul_f32 v[230:231], v[230:231], s[72:73]\n\t"
    "v_cvt_pk_bf16_f32 v144, v224, v225\n\t"
    "v_cvt_pk_bf16_f32 v145, v226, v227\n\t"
    "v_cvt_pk_bf16_f32 v146, v228, v229\n\t"
    "v_cvt_pk_bf16_f32 v147, v230, v231\n\t"
    "v_pk_mul_f32 v[232:233], v[232:233], s[72:73]\n\t"
    "v_pk_mul_f32 v[234:235], v[234:235], s[72:73]\n\t"
    "v_pk_mul_f32 v[236:237], v[236:237], s[72:73]\n\t"
    "v_pk_mul_f32 v[238:239], v[238:239], s[72:73]\n\t"
    "v_cvt_pk_bf16_f32 v148, v232, v233\n\t"
    "v_cvt_pk_bf16_f32 v149, v234, v235\n\t"
    "v_cvt_pk_bf16_f32 v150, v236, v237\n\t"
    "v_cvt_pk_bf16_f32 v151, v238, v239\n\t"
    "v_pk_mul_f32 v[240:241], v[240:241], s[72:73]\n\t"
    "v_pk_mul_f32 v[242:243], v[242:243], s[72:73]\n\t"
    "v_pk_mul_f32 v[244:245], v[244:245], s[72:73]\n\t"
    "v_pk_mul_f32 v[246:247], v[246:247], s[72:73]\n\t"
    "v_cvt_pk_bf16_f32 v152, v240, v241\n\t"
    "v_cvt_pk_bf16_f32 v153, v242, v243\n\t"
    "v_cvt_pk_bf16_f32 v154, v244, v245\n\t"
    "v_cvt_pk_bf16_f32 v155, v246, v247\n\t"
    "v_pk_mul_f32 v[248:249], v[248:249], s[72:73]\n\t"
    "v_pk_mul_f32 v[250:251], v[250:251], s[72:73]\n\t"
    "v_pk_mul_f32 v[252:253], v[252:253], s[72:73]\n\t"
    "v_pk_mul_f32 v[254:255], v[254:255], s[72:73]\n\t"
    "v_cvt_pk_bf16_f32 v156, v248, v249\n\t"
    "v_cvt_pk_bf16_f32 v157, v250, v251\n\t"
    "v_cvt_pk_bf16_f32 v158, v252, v253\n\t"
    "v_cvt_pk_bf16_f32 v159, v254, v255\n\t"
    "v_mfma_f32_16x16x32_bf16 v[160:163], a[192:195], v[64:67], v[24:27]\n\t"
    "s_add_u32 s70, s69, 0xa000\n\t"
    "s_add_u32 s69, s69, 0xa000\n\t"
    "v_mfma_f32_16x16x32_bf16 v[160:163], a[196:199], v[68:71], v[160:163]\n\t"
    "s_and_b32 s70, s70, 0x1ffff\n\t"
    "s_and_b32 s69, s69, 0x1ffff\n\t"
    "v_mfma_f32_16x16x32_bf16 v[160:163], a[200:203], v[72:75], v[160:163]\n\t"
    "s_mul_i32 s78, s57, 0xffff4000\n\t"
    "s_add_u32 s78, s78, 0x20000\n\t"
    "v_add_u32 v200, s78, v1\n\t"
    "v_add_u32 v201, s78, v2\n\t"
    "v_mfma_f32_16x16x32_bf16 v[160:163], a[204:207], v[76:79], v[160:163]\n\t"
    "v_add_u32 v202, s78, v3\n\t"
    "v_add_u32 v203, s78, v4\n\t"
    "v_mfma_f32_16x16x32_bf16 v[164:167], a[208:211], v[64:67], v[24:27]\n\t"
    "v_add_u32 v204, s78, v5\n\t"
    "v_add_u32 v205, s78, v6\n\t"
    "v_mfma_f32_16x16x32_bf16 v[164:167], a[212:215], v[68:71], v[164:167]\n\t"
    "v_add_u32 v207, s78, v62\n\t"
    "v_mfma_f32_16x16x32_bf16 v[164:167], a[216:219], v[72:75], v[164:167]\n\t"
    "s_add_u32 s97, s66, s78\n\t"
    "s_lshr_b32 s61, s60, 2\n\t"
    "v_mfma_f32_16x16x32_bf16 v[164:167], a[220:223], v[76:79], v[164:167]\n\t"
    "s_and_b32 s62, s60, 3\n\t"
    "s_mov_b64 s[36:37], s[14:15]\n\t"
    "s_lshl_b32 s78, s61, 20\n\t"
    "s_lshl_b32 s63, s62, 8\n\t"
    "s_memrealtime s[98:99]\n\t"
    "s_add_u32 s78, s78, s63\n\t"
    "s_lshl_b32 s63, s56, 13\n\t"
    "s_add_u32 s78, s78, s63\n\t"
    "s_add_u32 s36, s36, s78\n\t"
    "s_addc_u32 s37, s37, 0\n\t"
    "v_exp_f32 v208, v160\n\t"
    "s_sub_u32 s38, 0x400000, s78\n\t"
    "v_lshrrev_b32 v252, 1, v0\n\t"
    "v_lshlrev_b32 v252, 10, v252\n\t"
    "v_and_b32 v253, 1, v0\n\t"
    "v_lshl_add_u32 v252, v253, 7, v252\n\t"
    "s_lshr_b32 s4, %1, 5\n\t"
    "s_lshl_b32 s4, s4, 17\n\t"
    "v_exp_f32 v209, v161\n\t"
    "s_mul_i32 s5, s56, 0x6000\n\t"
    "s_add_u32 s4, s4, s5\n\t"
    "v_mov_b32 v54, 0x3f803f80\n\t"
    "v_mov_b32 v55, 0x3f803f80\n\t"
    "v_mov_b32 v56, 0x3f803f80\n\t"
    "v_mov_b32 v57, 0x3f803f80\n\t"
    "v_cvt_pk_bf16_f32 v184, v208, v209\n\t"
    "buffer_load_dword v253, v252, s[36:39], s4 offen\n\t"
    "v_exp_f32 v210, v162\n\t"
    "v_exp_f32 v211, v163\n\t"
    "v_mfma_f32_16x16x32_bf16 v[168:171], a[192:195], v[80:83], v[28:31]\n\t"
    "s_waitcnt vmcnt(17)\n\t"
    "ds_write_b128 v62, a[112:115] offset:40960\n\t"
    "v_cvt_pk_bf16_f32 v185, v210, v211\n\t"
    "v_mfma_f32_16x16x32_bf16 v[168:171], a[196:199], v[84:87], v[168:171]\n\t"
    "s_waitcnt lgkmcnt(0)\n\t"
    "v_exp_f32 v212, v164\n\t"
    "v_mfma_f32_16x16x32_bf16 v[168:171], a[200:203], v[88:91], v[168:171]\n\t"
    "ds_write_b128 v62, a[116:119] offset:41984\n\t"
    "v_mfma_f32_16x16x32_bf16 v[168:171], a[204:207], v[92:95], v[168:171]\n\t"
    "s_waitcnt lgkmcnt(0)\n\t"
    "v_exp_f32 v213, v165\n\t"
    "v_mfma_f32_16x16x32_bf16 v[172:175], a[208:211], v[80:83], v[28:31]\n\t"
    "ds_write_b128 v62, a[120:123] offset:43008\n\t"
    "v_cvt_pk_bf16_f32 v186, v212, v213\n\t"
    "v_mfma_f32_16x16x32_bf16 v[172:175], a[212:215], v[84:87], v[172:175]\n\t"
    "s_barrier\n\t"
    "v_exp_f32 v214, v166\n\t"
    "v_mfma_f32_16x16x32_bf16 v[172:175], a[216:219], v[88:91], v[172:175]\n\t"
    "ds_write_b128 v62, a[124:127] offset:44032\n\t"
    "v_mfma_f32_16x16x32_bf16 v[172:175], a[220:223], v[92:95], v[172:175]\n\t"
    "s_add_u32 s18, s98, 0x72\n\t"
    "v_exp_f32 v215, v167\n\t"
    "s_nop 0\n\t"
    "v_cvt_pk_bf16_f32 v187, v214, v215\n\t"
    "ds_read_b64_tr_b16 a[224:225], v5 offset:0\n\t"
    "ds_read_b64_tr_b16 a[226:227], v5 offset:4096\n\t"
    "v_exp_f32 v208, v168\n\t"
    "v_mfma_f32_16x16x32_bf16 v[196:199], v[54:57], v[184:187], 0\n\t"
    "ds_read_b64_tr_b16 a[228:229], v6 offset:0\n\t"
    "ds_read_b64_tr_b16 a[230:231], v6 offset:4096\n\t"
    "v_exp_f32 v209, v169\n\t"
    "ds_read_b64_tr_b16 a[232:233], v5 offset:256\n\t"
    "v_cvt_pk_bf16_f32 v188, v208, v209\n\t"
    "v_exp_f32 v210, v170\n\t"
    "ds_read_b64_tr_b16 a[234:235], v5 offset:4352\n\t"
    "ds_read_b64_tr_b16 a[236:237], v6 offset:256\n\t"
    "v_exp_f32 v211, v171\n\t"
    "v_mfma_f32_16x16x32_bf16 v[176:179], a[192:195], v[96:99], v[32:35]\n\t"
    "ds_read_b64_tr_b16 a[238:239], v6 offset:4352\n\t"
    "v_cmp_gt_f32 s[76:77], s75, v196\n\t"
    "v_cmp_lt_f32 vcc, s71, v196\n\t"
    "v_cvt_pk_bf16_f32 v189, v210, v211\n\t"
    "v_mfma_f32_16x16x32_bf16 v[176:179], a[196:199], v[100:103], v[176:179]\n\t"
    "v_exp_f32 v212, v172\n\t"
    "v_mfma_f32_16x16x32_bf16 v[176:179], a[200:203], v[104:107], v[176:179]\n\t"
    "s_or_b64 vcc, vcc, s[76:77]\n\t"
    "s_cbranch_vccnz Lstub_c0_0_%=\n\t"
    "Lret_c0_0_%=:\n\t"
    "ds_read_b64_tr_b16 a[240:241], v5 offset:512\n\t"
    "v_add_f32 v48, v48, v196\n\t"
    "v_mfma_f32_16x16x32_bf16 v[176:179], a[204:207], v[108:111], v[176:179]\n\t"
    "v_exp_f32 v213, v173\n\t"
    "v_mfma_f32_16x16x32_bf16 v[180:183], a[208:211], v[96:99], v[32:35]\n\t"
    "ds_read_b64_tr_b16 a[242:243], v5 offset:4608\n\t"
    "v_cvt_pk_bf16_f32 v190, v212, v213\n\t"
    "v_mfma_f32_16x16x32_bf16 v[180:183], a[212:215], v[100:103], v[180:183]\n\t"
    "v_exp_f32 v214, v174\n\t"
    "v_mfma_f32_16x16x32_bf16 v[180:183], a[216:219], v[104:107], v[180:183]\n\t"
    "ds_read_b64_tr_b16 a[244:245], v6 offset:512\n\t"
    "ds_read_b64_tr_b16 a[246:247], v6 offset:4608\n\t"
    "v_mfma_f32_16x16x32_bf16 v[180:183], a[220:223], v[108:111], v[180:183]\n\t"
    "s_waitcnt lgkmcnt(3)\n\t"
    "v_exp_f32 v215, v175\n\t"
    "v_mfma_f32_16x16x32_bf16 a[0:3], a[224:227], v[184:187], 0\n\t"
    "ds_read_b64_tr_b16 a[248:249], v5 offset:768\n\t"
    "v_cvt_pk_bf16_f32 v191, v214, v215\n\t"
    "v_mfma_f32_16x16x32_bf16 a[4:7], a[228:231], v[184:187], 0\n\t"
    "ds_read_b64_tr_b16 a[250:251], v5 offset:4864\n\t"
    "ds_read_b64_tr_b16 a[252:253], v6 offset:768\n\t"
    "v_mfma_f32_16x16x32_bf16 a[8:11], a[232:235], v[184:187], 0\n\t"
    "v_exp_f32 v208, v176\n\t"
    "v_mfma_f32_16x16x32_bf16 v[196:199], v[54:57], v[188:191], 0\n\t"
    "ds_read_b64_tr_b16 a[254:255], v6 offset:4864\n\t"
    "v_mfma_f32_16x16x32_bf16 a[12:15], a[236:239], v[184:187], 0\n\t"
    "s_waitcnt lgkmcnt(4)\n\t"
    "v_exp_f32 v209, v177\n\t"
    "v_mfma_f32_16x16x32_bf16 a[16:19], a[240:243], v[184:187], 0\n\t"
    "v_cvt_pk_bf16_f32 v192, v208, v209\n\t"
    "v_mfma_f32_16x16x32_bf16 a[20:23], a[244:247], v[184:187], 0\n\t"
    "s_waitcnt lgkmcnt(1)\n\t"
    "v_exp_f32 v210, v178\n\t"
    "v_mfma_f32_16x16x32_bf16 a[24:27], a[248:251], v[184:187], 0\n\t"
    "s_waitcnt lgkmcnt(0)\n\t"
    "v_mfma_f32_16x16x32_bf16 a[28:31], a[252:255], v[184:187], 0\n\t"
    "v_exp_f32 v211, v179\n\t"
    "v_mfma_f32_16x16x32_bf16 v[160:163], a[192:195], v[112:115], v[36:39]\n\t"
    "v_cmp_gt_f32 s[76:77], s75, v196\n\t"
    "v_cmp_lt_f32 vcc, s71, v196\n\t"
    "v_cvt_pk_bf16_f32 v193, v210, v211\n\t"
    "v_mfma_f32_16x16x32_bf16 v[160:163], a[196:199], v[116:119], v[160:163]\n\t"
    "v_exp_f32 v212, v180\n\t"
    "v_mfma_f32_16x16x32_bf16 v[160:163], a[200:203], v[120:123], v[160:163]\n\t"
    "s_or_b64 vcc, vcc, s[76:77]\n\t"
    "s_cbranch_vccnz Lstub_c0_1_%=\n\t"
    "Lret_c0_1_%=:\n\t"
    "v_add_f32 v49, v49, v196\n\t"
    "v_mfma_f32_16x16x32_bf16 v[160:163], a[204:207], v[124:127], v[160:163]\n\t"
    "v_exp_f32 v213, v181\n\t"
    "v_mfma_f32_16x16x32_bf16 v[164:167], a[208:211], v[112:115], v[36:39]\n\t"
    "v_cvt_pk_bf16_f32 v194, v212, v213\n\t"
    "v_mfma_f32_16x16x32_bf16 v[164:167], a[212:215], v[116:119], v[164:167]\n\t"
    "v_exp_f32 v214, v182\n\t"
    "v_mfma_f32_16x16x32_bf16 v[164:167], a[216:219], v[120:123], v[164:167]\n\t"
    "s_waitcnt vmcnt(13)\n\t"
    "ds_write_b128 v62, a[128:131] offset:49152\n\t"
    "v_mfma_f32_16x16x32_bf16 v[164:167], a[220:223], v[124:127], v[164:167]\n\t"
    "v_exp_f32 v215, v183\n\t"
    "v_mfma_f32_16x16x32_bf16 a[32:35], a[224:227], v[188:191], 0\n\t"
    "ds_write_b128 v62, a[132:135] offset:50176\n\t"
    "v_cvt_pk_bf16_f32 v195, v214, v215\n\t"
    "v_mfma_f32_16x16x32_bf16 a[36:39], a[228:231], v[188:191], 0\n\t"
    "ds_write_b128 v62, a[136:139] offset:51200\n\t"
    "s_getpc_b64 s[4:5]\n\t"
    "Lrt0_%=:\n\t"
    "s_mov_b32 s6, Lend_%=-Lrt0_%=\n\t"
    "s_mov_b32 s7, 0x20000\n\t"
    "s_lshr_b32 s8, %1, 3\n\t"
    "s_lshl_b32 s8, s8, 2\n\t"
    "s_add_u32 s8, s8, s56\n\t"
    "s_lshl_b32 s8, s8, 9\n\t"
    "v_cmp_gt_u32 s[76:77], 4, v0\n\t"
    "v_lshlrev_b32 v229, 7, v0\n\t"
    "v_mov_b32 v230, 0x40000000\n\t"
    "v_cndmask_b32 v229, v230, v229, s[76:77]\n\t"
    "buffer_load_dword v23, v229, s[4:7], s8 offen\n\t"
    "v_mfma_f32_16x16x32_bf16 a[40:43], a[232:235], v[188:191], 0\n\t"
    "v_exp_f32 v208, v160\n\t"
    "v_mfma_f32_16x16x32_bf16 v[196:199], v[54:57], v[192:195], 0\n\t"
    "ds_write_b128 v62, a[140:143] offset:52224\n\t"
    "v_mfma_f32_16x16x32_bf16 a[44:47], a[236:239], v[188:191], 0\n\t"
    "v_exp_f32 v209, v161\n\t"
    "v_mfma_f32_16x16x32_bf16 a[48:51], a[240:243], v[188:191], 0\n\t"
    "v_cvt_pk_bf16_f32 v184, v208, v209\n\t"
    "v_mfma_f32_16x16x32_bf16 a[52:55], a[244:247], v[188:191], 0\n\t"
    "v_exp_f32 v210, v162\n\t"
    "v_mfma_f32_16x16x32_bf16 a[56:59], a[248:251], v[188:191], 0\n\t"
    "v_mfma_f32_16x16x32_bf16 a[60:63], a[252:255], v[188:191], 0\n\t"
    "v_exp_f32 v211, v163\n\t"
    "v_mfma_f32_16x16x32_bf16 v[168:171], a[192:195], v[128:131], v[40:43]\n\t"
    "v_cmp_gt_f32 s[76:77], s75, v196\n\t"
    "v_cmp_lt_f32 vcc, s71, v196\n\t"
    "v_cvt_pk_bf16_f32 v185, v210, v211\n\t"
    "v_mfma_f32_16x16x32_bf16 v[168:171], a[196:199], v[132:135], v[168:171]\n\t"
    "v_exp_f32 v212, v164\n\t"
    "v_mfma_f32_16x16x32_bf16 v[168:171], a[200:203], v[136:139], v[168:171]\n\t"
    "s_or_b64 vcc, vcc, s[76:77]\n\t"
    "s_cbranch_vccnz Lstub_c0_2_%=\n\t"
    "Lret_c0_2_%=:\n\t"
    "v_add_f32 v50, v50, v196\n\t"
    "v_mfma_f32_16x16x32_bf16 v[168:171], a[204:207], v[140:143], v[168:171]\n\t"
    "v_exp_f32 v213, v165\n\t"
    "v_mfma_f32_16x16x32_bf16 v[172:175], a[208:211], v[128:131], v[40:43]\n\t"
    "v_cvt_pk_bf16_f32 v186, v212, v213\n\t"
    "v_mfma_f32_16x16x32_bf16 v[172:175], a[212:215], v[132:135], v[172:175]\n\t"
    "v_exp_f32 v214, v166\n\t"
    "v_mfma_f32_16x16x32_bf16 v[172:175], a[216:219], v[136:139], v[172:175]\n\t"
    "v_mfma_f32_16x16x32_bf16 v[172:175], a[220:223], v[140:143], v[172:175]\n\t"
    "v_exp_f32 v215, v167\n\t"
    "v_mfma_f32_16x16x32_bf16 a[64:67], a[224:227], v[192:195], 0\n\t"
    "v_cvt_pk_bf16_f32 v187, v214, v215\n\t"
    "v_mfma_f32_16x16x32_bf16 a[68:71], a[228:231], v[192:195], 0\n\t"
    "s_waitcnt vmcnt(10)\n\t"
    "ds_write_b128 v62, a[144:147] offset:57344\n\t"
    "v_mfma_f32_16x16x32_bf16 a[72:75], a[232:235], v[192:195], 0\n\t"
    "v_exp_f32 v208, v168\n\t"
    "v_mfma_f32_16x16x32_bf16 v[196:199], v[54:57], v[184:187], 0\n\t"
    "ds_write_b128 v62, a[148:151] offset:58368\n\t"
    "v_mfma_f32_16x16x32_bf16 a[76:79], a[236:239], v[192:195], 0\n\t"
    "v_exp_f32 v209, v169\n\t"
    "v_mfma_f32_16x16x32_bf16 a[80:83], a[240:243], v[192:195], 0\n\t"
    "ds_write_b128 v62, a[152:155] offset:59392\n\t"
    "v_cvt_pk_bf16_f32 v188, v208, v209\n\t"
    "v_mfma_f32_16x16x32_bf16 a[84:87], a[244:247], v[192:195], 0\n\t"
    "v_exp_f32 v210, v170\n\t"
    "v_mfma_f32_16x16x32_bf16 a[88:91], a[248:251], v[192:195], 0\n\t"
    "ds_write_b128 v62, a[156:159] offset:60416\n\t"
    "v_mfma_f32_16x16x32_bf16 a[92:95], a[252:255], v[192:195], 0\n\t"
    "v_exp_f32 v211, v171\n\t"
    "v_mfma_f32_16x16x32_bf16 v[176:179], a[192:195], v[144:147], v[44:47]\n\t"
    "v_cmp_gt_f32 s[76:77], s75, v196\n\t"
    "v_cmp_lt_f32 vcc, s71, v196\n\t"
    "v_cvt_pk_bf16_f32 v189, v210, v211\n\t"
    "v_mfma_f32_16x16x32_bf16 v[176:179], a[196:199], v[148:151], v[176:179]\n\t"
    "v_exp_f32 v212, v172\n\t"
    "v_mfma_f32_16x16x32_bf16 v[176:179], a[200:203], v[152:155], v[176:179]\n\t"
    "s_or_b64 vcc, vcc, s[76:77]\n\t"
    "s_cbranch_vccnz Lstub_c0_3_%=\n\t"
    "Lret_c0_3_%=:\n\t"
    "ds_read_b128 a[192:195], v1 offset:16384\n\t"
    "v_add_f32 v51, v51, v196\n\t"
    "v_mfma_f32_16x16x32_bf16 v[176:179], a[204:207], v[156:159], v[176:179]\n\t"
    "v_exp_f32 v213, v173\n\t"
    "v_mfma_f32_16x16x32_bf16 v[180:183], a[208:211], v[144:147], v[44:47]\n\t"
    "ds_read_b128 a[196:199], v2 offset:16384\n\t"
    "v_cvt_pk_bf16_f32 v190, v212, v213\n\t"
    "v_mfma_f32_16x16x32_bf16 v[180:183], a[212:215], v[148:151], v[180:183]\n\t"
    "v_exp_f32 v214, v174\n\t"
    "v_mfma_f32_16x16x32_bf16 v[180:183], a[216:219], v[152:155], v[180:183]\n\t"
    "ds_read_b128 a[200:203], v3 offset:16384\n\t"
    "ds_read_b128 a[204:207], v4 offset:16384\n\t"
    "v_mfma_f32_16x16x32_bf16 v[180:183], a[220:223], v[156:159], v[180:183]\n\t"
    "v_exp_f32 v215, v175\n\t"
    "v_mfma_f32_16x16x32_bf16 a[96:99], a[224:227], v[184:187], 0\n\t"
    "ds_read_b128 a[208:211], v1 offset:20480\n\t"
    "v_cvt_pk_bf16_f32 v191, v214, v215\n\t"
    "v_mfma_f32_16x16x32_bf16 a[100:103], a[228:231], v[184:187], 0\n\t"
    "ds_read_b128 a[212:215], v2 offset:20480\n\t"
    "ds_read_b128 a[216:219], v3 offset:20480\n\t"
    "v_mfma_f32_16x16x32_bf16 a[104:107], a[232:235], v[184:187], 0\n\t"
    "v_exp_f32 v208, v176\n\t"
    "v_mfma_f32_16x16x32_bf16 v[196:199], v[54:57], v[188:191], 0\n\t"
    "s_waitcnt vmcnt(6)\n\t"
    "ds_read_b128 a[220:223], v4 offset:20480\n\t"
    "ds_write_b128 v207, a[160:163] offset:0\n\t"
    "v_mfma_f32_16x16x32_bf16 a[108:111], a[236:239], v[184:187], 0\n\t"
    "v_exp_f32 v209, v177\n\t"
    "v_mfma_f32_16x16x32_bf16 a[112:115], a[240:243], v[184:187], 0\n\t"
    "ds_write_b128 v207, a[164:167] offset:1024\n\t"
    "v_cvt_pk_bf16_f32 v192, v208, v209\n\t"
    "v_mfma_f32_16x16x32_bf16 a[116:119], a[244:247], v[184:187], 0\n\t"
    "v_exp_f32 v210, v178\n\t"
    "v_mfma_f32_16x16x32_bf16 a[120:123], a[248:251], v[184:187], 0\n\t"
    "ds_write_b128 v207, a[168:171] offset:2048\n\t"
    "v_mfma_f32_16x16x32_bf16 a[124:127], a[252:255], v[184:187], 0\n\t"
    "s_waitcnt lgkmcnt(2)\n\t"
    "v_exp_f32 v211, v179\n\t"
    "v_mfma_f32_16x16x32_bf16 v[160:163], a[192:195], v[64:67], v[24:27]\n\t"
    "v_cmp_gt_f32 s[76:77], s75, v196\n\t"
    "v_cmp_lt_f32 vcc, s71, v196\n\t"
    "v_cvt_pk_bf16_f32 v193, v210, v211\n\t"
    "v_mfma_f32_16x16x32_bf16 v[160:163], a[196:199], v[68:71], v[160:163]\n\t"
    "v_exp_f32 v212, v180\n\t"
    "v_mfma_f32_16x16x32_bf16 v[160:163], a[200:203], v[72:75], v[160:163]\n\t"
    "s_or_b64 vcc, vcc, s[76:77]\n\t"
    "s_cbranch_vccnz Lstub_c0_4_%=\n\t"
    "Lret_c0_4_%=:\n\t"
    "ds_write_b128 v207, a[172:175] offset:3072\n\t"
    "v_add_f32 v52, v52, v196\n\t"
    "v_mfma_f32_16x16x32_bf16 v[160:163], a[204:207], v[76:79], v[160:163]\n\t"
    "v_exp_f32 v213, v181\n\t"
    "v_mfma_f32_16x16x32_bf16 v[164:167], a[208:211], v[64:67], v[24:27]\n\t"
    "s_waitcnt vmcnt(2)\n\t"
    "ds_write_b128 v207, a[176:179] offset:8192\n\t"
    "v_cvt_pk_bf16_f32 v194, v212, v213\n\t"
    "v_mfma_f32_16x16x32_bf16 v[164:167], a[212:215], v[68:71], v[164:167]\n\t"
    "v_exp_f32 v214, v182\n\t"
    "v_mfma_f32_16x16x32_bf16 v[164:167], a[216:219], v[72:75], v[164:167]\n\t"
    "ds_write_b128 v207, a[180:183] offset:9216\n\t"
    "v_mfma_f32_16x16x32_bf16 v[164:167], a[220:223], v[76:79], v[164:167]\n\t"
    "v_exp_f32 v215, v183\n\t"
    "v_mfma_f32_16x16x32_bf16 a[128:131], a[224:227], v[188:191], 0\n\t"
    "s_memrealtime s[98:99]\n\t"
    "ds_write_b128 v207, a[184:187] offset:10240\n\t"
    "v_cvt_pk_bf16_f32 v195, v214, v215\n\t"
    "v_mfma_f32_16x16x32_bf16 a[132:135], a[228:231], v[188:191], 0\n\t"
    "ds_write_b128 v207, a[188:191] offset:11264\n\t"
    "v_mfma_f32_16x16x32_bf16 a[136:139], a[232:235], v[188:191], 0\n\t"
    "s_add_u32 m0, s66, 0\n\t"
    "v_exp_f32 v208, v160\n\t"
    "v_mfma_f32_16x16x32_bf16 v[196:199], v[54:57], v[192:195], 0\n\t"
    "buffer_load_dwordx4 v7, s[44:47], s69 offen nt lds\n\t"
    "v_mfma_f32_16x16x32_bf16 a[140:143], a[236:239], v[188:191], 0\n\t"
    "v_exp_f32 v209, v161\n\t"
    "v_mfma_f32_16x16x32_bf16 a[144:147], a[240:243], v[188:191], 0\n\t"
    "v_cvt_pk_bf16_f32 v184, v208, v209\n\t"
    "v_mfma_f32_16x16x32_bf16 a[148:151], a[244:247], v[188:191], 0\n\t"
    "v_exp_f32 v210, v162\n\t"
    "v_mfma_f32_16x16x32_bf16 a[152:155], a[248:251], v[188:191], 0\n\t"
    "buffer_load_dwordx4 v8, s[44:47], s69 offen offset:1024 nt lds\n\t"
    "v_mfma_f32_16x16x32_bf16 a[156:159], a[252:255], v[188:191], 0\n\t"
    "v_exp_f32 v211, v163\n\t"
    "v_mfma_f32_16x16x32_bf16 v[168:171], a[192:195], v[80:83], v[28:31]\n\t"
    "v_cmp_gt_f32 s[76:77], s75, v196\n\t"
    "v_cmp_lt_f32 vcc, s71, v196\n\t"
    "v_cvt_pk_bf16_f32 v185, v210, v211\n\t"
    "v_mfma_f32_16x16x32_bf16 v[168:171], a[196:199], v[84:87], v[168:171]\n\t"
    "s_waitcnt lgkmcnt(0)\n\t"
    "v_exp_f32 v212, v164\n\t"
    "v_mfma_f32_16x16x32_bf16 v[168:171], a[200:203], v[88:91], v[168:171]\n\t"
    "s_or_b64 vcc, vcc, s[76:77]\n\t"
    "s_cbranch_vccnz Lstub_c0_5_%=\n\t"
    "Lret_c0_5_%=:\n\t"
    "v_add_f32 v53, v53, v196\n\t"
    "v_mfma_f32_16x16x32_bf16 v[168:171], a[204:207], v[92:95], v[168:171]\n\t"
    "s_waitcnt lgkmcnt(0)\n\t"
    "v_exp_f32 v213, v165\n\t"
    "v_mfma_f32_16x16x32_bf16 v[172:175], a[208:211], v[80:83], v[28:31]\n\t"
    "s_mov_b32 s74, 0\n\t"
    "s_sub_u32 s19, s18, s98\n\t"
    "v_cvt_pk_bf16_f32 v186, v212, v213\n\t"
    "v_mfma_f32_16x16x32_bf16 v[172:175], a[212:215], v[84:87], v[172:175]\n\t"
    "s_barrier\n\t"
    "v_exp_f32 v214, v166\n\t"
    "v_mfma_f32_16x16x32_bf16 v[172:175], a[216:219], v[88:91], v[172:175]\n\t"
    "s_cmp_gt_i32 s19, 0\n\t"
    "s_cbranch_scc1 Lpace1_%=\n\t"
    "Lpaced1_%=:\n\t"
    "v_mfma_f32_16x16x32_bf16 v[172:175], a[220:223], v[92:95], v[172:175]\n\t"
    "s_add_u32 s18, s18, 0x72\n\t"
    "v_exp_f32 v215, v167\n\t"
    "v_mfma_f32_16x16x32_bf16 a[160:163], a[224:227], v[192:195], 0\n\t"
    "v_cvt_pk_bf16_f32 v187, v214, v215\n\t"
    "v_mfma_f32_16x16x32_bf16 a[164:167], a[228:231], v[192:195], 0\n\t"
    "ds_read_b64_tr_b16 a[224:225], v5 offset:16384\n\t"
    "ds_read_b64_tr_b16 a[226:227], v5 offset:20480\n\t"
    "v_mfma_f32_16x16x32_bf16 a[168:171], a[232:235], v[192:195], 0\n\t"
    "v_exp_f32 v208, v168\n\t"
    "v_mfma_f32_16x16x32_bf16 v[196:199], v[54:57], v[184:187], 0\n\t"
    "ds_read_b64_tr_b16 a[228:229], v6 offset:16384\n\t"
    "ds_read_b64_tr_b16 a[230:231], v6 offset:20480\n\t"
    "v_mfma_f32_16x16x32_bf16 a[172:175], a[236:239], v[192:195], 0\n\t"
    "v_exp_f32 v209, v169\n\t"
    "v_mfma_f32_16x16x32_bf16 a[176:179], a[240:243], v[192:195], 0\n\t"
    "ds_read_b64_tr_b16 a[232:233], v5 offset:16640\n\t"
    "v_cvt_pk_bf16_f32 v188, v208, v209\n\t"
    "v_mfma_f32_16x16x32_bf16 a[180:183], a[244:247], v[192:195], 0\n\t"
    "v_exp_f32 v210, v170\n\t"
    "v_mfma_f32_16x16x32_bf16 a[184:187], a[248:251], v[192:195], 0\n\t"
    "ds_read_b64_tr_b16 a[234:235], v5 offset:20736\n\t"
    "ds_read_b64_tr_b16 a[236:237], v6 offset:16640\n\t"
    "v_mfma_f32_16x16x32_bf16 a[188:191], a[252:255], v[192:195], 0\n\t"
    "v_exp_f32 v211, v171\n\t"
    "v_mfma_f32_16x16x32_bf16 v[176:179], a[192:195], v[96:99], v[32:35]\n\t"
    "ds_read_b64_tr_b16 a[238:239], v6 offset:20736\n\t"
    "v_cmp_lt_f32 vcc, s71, v196\n\t"
    "v_cvt_pk_bf16_f32 v189, v210, v211\n\t"
    "v_mfma_f32_16x16x32_bf16 v[176:179], a[196:199], v[100:103], v[176:179]\n\t"
    "v_exp_f32 v212, v172\n\t"
    "v_mfma_f32_16x16x32_bf16 v[176:179], a[200:203], v[104:107], v[176:179]\n\t"
    "s_cbranch_vccnz Lstub_c1_0_%=\n\t"
    "Lret_c1_0_%=:\n\t"
    "ds_read_b64_tr_b16 a[240:241], v5 offset:16896\n\t"
    "v_add_f32 v48, v48, v196\n\t"
    "v_mfma_f32_16x16x32_bf16 v[176:179], a[204:207], v[108:111], v[176:179]\n\t"
    "v_exp_f32 v213, v173\n\t"
    "v_mfma_f32_16x16x32_bf16 v[180:183], a[208:211], v[96:99], v[32:35]\n\t"
    "ds_read_b64_tr_b16 a[242:243], v5 offset:20992\n\t"
    "v_cvt_pk_bf16_f32 v190, v212, v213\n\t"
    "v_mfma_f32_16x16x32_bf16 v[180:183], a[212:215], v[100:103], v[180:183]\n\t"
    "v_exp_f32 v214, v174\n\t"
    "v_mfma_f32_16x16x32_bf16 v[180:183], a[216:219], v[104:107], v[180:183]\n\t"
    "ds_read_b64_tr_b16 a[244:245], v6 offset:16896\n\t"
    "ds_read_b64_tr_b16 a[246:247], v6 offset:20992\n\t"
    "v_mfma_f32_16x16x32_bf16 v[180:183], a[220:223], v[108:111], v[180:183]\n\t"
    "s_waitcnt lgkmcnt(3)\n\t"
    "v_exp_f32 v215, v175\n\t"
    "v_mfma_f32_16x16x32_bf16 a[0:3], a[224:227], v[184:187], a[0:3]\n\t"
    "ds_read_b64_tr_b16 a[248:249], v5 offset:17152\n\t"
    "v_cvt_pk_bf16_f32 v191, v214, v215\n\t"
    "v_mfma_f32_16x16x32_bf16 a[4:7], a[228:231], v[184:187], a[4:7]\n\t"
    "ds_read_b64_tr_b16 a[250:251], v5 offset:21248\n\t"
    "ds_read_b64_tr_b16 a[252:253], v6 offset:17152\n\t"
    "v_mfma_f32_16x16x32_bf16 a[8:11], a[232:235], v[184:187], a[8:11]\n\t"
    "s_add_u32 s98, s98, 0x68\n\t"
    "v_exp_f32 v208, v176\n\t"
    "v_mfma_f32_16x16x32_bf16 v[196:199], v[54:57], v[188:191], 0\n\t"
    "ds_read_b64_tr_b16 a[254:255], v6 offset:21248\n\t"
    "v_readfirstlane_b32 s82, v58\n\t"
    "v_mfma_f32_16x16x32_bf16 a[12:15], a[236:239], v[184:187], a[12:15]\n\t"
    "s_waitcnt lgkmcnt(4)\n\t"
    "s_max_u32 s18, s18, s98\n\t"
    "v_exp_f32 v209, v177\n\t"
    "v_mfma_f32_16x16x32_bf16 a[16:19], a[240:243], v[184:187], a[16:19]\n\t"
    "v_cvt_pk_bf16_f32 v192, v208, v209\n\t"
    "v_readfirstlane_b32 s83, v59\n\t"
    "v_mfma_f32_16x16x32_bf16 a[20:23], a[244:247], v[184:187], a[20:23]\n\t"
    "s_waitcnt lgkmcnt(1)\n\t"
    "s_add_u32 s82, s82, 0x7f4a7c15\n\t"
    "s_addc_u32 s83, s83, 0x9e3779b9\n\t"
    "v_exp_f32 v210, v178\n\t"
    "v_mfma_f32_16x16x32_bf16 a[24:27], a[248:251], v[184:187], a[24:27]\n\t"
    "s_waitcnt lgkmcnt(0)\n\t"
    "s_mov_b64 s[40:41], s[12:13]\n\t"
    "buffer_load_dwordx4 v9, s[44:47], s69 offen offset:2048 nt lds\n\t"
    "v_mfma_f32_16x16x32_bf16 a[28:31], a[252:255], v[184:187], a[28:31]\n\t"
    "s_lshl_b32 s78, s59, 2\n\t"
    "v_exp_f32 v211, v179\n\t"
    "v_mfma_f32_16x16x32_bf16 v[160:163], a[192:195], v[112:115], v[36:39]\n\t"
    "s_add_u32 s78, s78, s61\n\t"
    "v_cmp_lt_f32 vcc, s71, v196\n\t"
    "v_cvt_pk_bf16_f32 v193, v210, v211\n\t"
    "v_mfma_f32_16x16x32_bf16 v[160:163], a[196:199], v[116:119], v[160:163]\n\t"
    "s_mul_i32 s78, s78, 0x60000\n\t"
    "v_exp_f32 v212, v180\n\t"
    "v_mfma_f32_16x16x32_bf16 v[160:163], a[200:203], v[120:123], v[160:163]\n\t"
    "s_cbranch_vccnz Lstub_c1_1_%=\n\t"
    "Lret_c1_1_%=:\n\t"
    "v_add_f32 v49, v49, v196\n\t"
    "v_mfma_f32_16x16x32_bf16 v[160:163], a[204:207], v[124:127], v[160:163]\n\t"
    "s_mul_i32 s63, s58, 0x1800\n\t"
    "v_exp_f32 v213, v181\n\t"
    "v_mfma_f32_16x16x32_bf16 v[164:167], a[208:211], v[112:115], v[36:39]\n\t"
    "s_add_u32 s78, s78, s63\n\t"
    "v_cvt_pk_bf16_f32 v194, v212, v213\n\t"
    "v_mfma_f32_16x16x32_bf16 v[164:167], a[212:215], v[116:119], v[164:167]\n\t"
    "s_add_u32 s78, s78, s80\n\t"
    "v_exp_f32 v214, v182\n\t"
    "v_mfma_f32_16x16x32_bf16 v[164:167], a[216:219], v[120:123], v[164:167]\n\t"
    "s_add_u32 s40, s40, s78\n\t"
    "s_addc_u32 s41, s41, 0\n\t"
    "buffer_load_dwordx4 v10, s[44:47], s69 offen offset:3072 nt lds\n\t"
    "v_mfma_f32_16x16x32_bf16 v[164:167], a[220:223], v[124:127], v[164:167]\n\t"
    "s_add_u32 s69, s69, 0x2000\n\t"
    "v_exp_f32 v215, v183\n\t"
    "v_mfma_f32_16x16x32_bf16 a[32:35], a[224:227], v[188:191], a[32:35]\n\t"
    "s_and_b32 s69, s69, 0x1ffff\n\t"
    "s_add_u32 m0, s66, 0x2000\n\t"
    "v_cvt_pk_bf16_f32 v195, v214, v215\n\t"
    "v_mfma_f32_16x16x32_bf16 a[36:39], a[228:231], v[188:191], a[36:39]\n\t"
    "s_mov_b32 s42, 0x60000\n\t"
    "buffer_load_dwordx4 v11, s[48:51], s70 offen nt lds\n\t"
    "v_mfma_f32_16x16x32_bf16 a[40:43], a[232:235], v[188:191], a[40:43]\n\t"
    "s_mov_b64 s[88:89], s[12:13]\n\t"
    "v_exp_f32 v208, v160\n\t"
    "v_mfma_f32_16x16x32_bf16 v[196:199], v[54:57], v[192:195], 0\n\t"
    "s_mul_i32 s78, s56, 0xc00\n\t"
    "v_mfma_f32_16x16x32_bf16 a[44:47], a[236:239], v[188:191], a[44:47]\n\t"
    "s_add_u32 s78, s78, 0x2000000\n\t"
    "v_exp_f32 v209, v161\n\t"
    "v_mfma_f32_16x16x32_bf16 a[48:51], a[240:243], v[188:191], a[48:51]\n\t"
    "s_add_u32 s88, s88, s78\n\t"
    "s_addc_u32 s89, s89, 0\n\t"
    "v_cvt_pk_bf16_f32 v184, v208, v209\n\t"
    "v_mfma_f32_16x16x32_bf16 a[52:55], a[244:247], v[188:191], a[52:55]\n\t"
    "s_mov_b32 s90, 0x1200000\n\t"
    "v_exp_f32 v210, v162\n\t"
    "v_mfma_f32_16x16x32_bf16 a[56:59], a[248:251], v[188:191], a[56:59]\n\t"
    "s_mov_b32 s91, 0x20000\n\t"
    "v_mfma_f32_16x16x32_bf16 a[60:63], a[252:255], v[188:191], a[60:63]\n\t"
    "s_mov_b64 s[92:93], s[16:17]\n\t"
    "v_exp_f32 v211, v163\n\t"
    "v_mfma_f32_16x16x32_bf16 v[168:171], a[192:195], v[128:131], v[40:43]\n\t"
    "s_mul_i32 s78, s59, 0x30000\n\t"
    "v_cmp_lt_f32 vcc, s71, v196\n\t"
    "v_cvt_pk_bf16_f32 v185, v210, v211\n\t"
    "v_mfma_f32_16x16x32_bf16 v[168:171], a[196:199], v[132:135], v[168:171]\n\t"
    "s_mul_i32 s63, s58, 0x18000\n\t"
    "v_exp_f32 v212, v164\n\t"
    "v_mfma_f32_16x16x32_bf16 v[168:171], a[200:203], v[136:139], v[168:171]\n\t"
    "s_cbranch_vccnz Lstub_c1_2_%=\n\t"
    "Lret_c1_2_%=:\n\t"
    "v_add_f32 v50, v50, v196\n\t"
    "v_mfma_f32_16x16x32_bf16 v[168:171], a[204:207], v[140:143], v[168:171]\n\t"
    "s_add_u32 s78, s78, s63\n\t"
    "v_exp_f32 v213, v165\n\t"
    "v_mfma_f32_16x16x32_bf16 v[172:175], a[208:211], v[128:131], v[40:43]\n\t"
    "s_mul_i32 s63, s57, 0xc000\n\t"
    "v_cvt_pk_bf16_f32 v186, v212, v213\n\t"
    "v_mfma_f32_16x16x32_bf16 v[172:175], a[212:215], v[132:135], v[172:175]\n\t"
    "s_add_u32 s78, s78, s63\n\t"
    "v_exp_f32 v214, v166\n\t"
    "v_mfma_f32_16x16x32_bf16 v[172:175], a[216:219], v[136:139], v[172:175]\n\t"
    "s_lshl_b32 s63, s62, 8\n\t"
    "v_mfma_f32_16x16x32_bf16 v[172:175], a[220:223], v[140:143], v[172:175]\n\t"
    "s_add_u32 s78, s78, s63\n\t"
    "v_exp_f32 v215, v167\n\t"
    "v_mfma_f32_16x16x32_bf16 a[64:67], a[224:227], v[192:195], a[64:67]\n\t"
    "s_lshl_b32 s63, s61, 6\n\t"
    "v_cvt_pk_bf16_f32 v187, v214, v215\n\t"
    "v_mfma_f32_16x16x32_bf16 a[68:71], a[228:231], v[192:195], a[68:71]\n\t"
    "s_add_u32 s78, s78, s63\n\t"
    "buffer_load_dwordx4 v12, s[48:51], s70 offen offset:1024 nt lds\n\t"
    "v_mfma_f32_16x16x32_bf16 a[72:75], a[232:235], v[192:195], a[72:75]\n\t"
    "s_add_u32 s92, s92, s78\n\t"
    "s_addc_u32 s93, s93, 0\n\t"
    "v_exp_f32 v208, v168\n\t"
    "v_mfma_f32_16x16x32_bf16 v[196:199], v[54:57], v[184:187], 0\n\t"
    "s_mov_b32 s94, 0xc000\n\t"
    "v_mfma_f32_16x16x32_bf16 a[76:79], a[236:239], v[192:195], a[76:79]\n\t"
    "s_mov_b32 s95, 0x20000\n\t"
    "v_exp_f32 v209, v169\n\t"
    "v_mfma_f32_16x16x32_bf16 a[80:83], a[240:243], v[192:195], a[80:83]\n\t"
    "s_add_u32 s78, s62, 0\n\t"
    "v_cvt_pk_bf16_f32 v188, v208, v209\n\t"
    "v_mfma_f32_16x16x32_bf16 a[84:87], a[244:247], v[192:195], a[84:87]\n\t"
    "s_and_b32 s78, s78, 3\n\t"
    "v_exp_f32 v210, v170\n\t"
    "v_mfma_f32_16x16x32_bf16 a[88:91], a[248:251], v[192:195], a[88:91]\n\t"
    "s_lshl_b32 s64, s78, 18\n\t"
    "v_mfma_f32_16x16x32_bf16 a[92:95], a[252:255], v[192:195], a[92:95]\n\t"
    "s_add_u32 s78, s62, 1\n\t"
    "v_exp_f32 v211, v171\n\t"
    "v_mfma_f32_16x16x32_bf16 v[176:179], a[192:195], v[144:147], v[44:47]\n\t"
    "s_and_b32 s78, s78, 3\n\t"
    "v_cmp_lt_f32 vcc, s71, v196\n\t"
    "v_cvt_pk_bf16_f32 v189, v210, v211\n\t"
    "v_mfma_f32_16x16x32_bf16 v[176:179], a[196:199], v[148:151], v[176:179]\n\t"
    "s_lshl_b32 s65, s78, 18\n\t"
    "v_exp_f32 v212, v172\n\t"
    "v_mfma_f32_16x16x32_bf16 v[176:179], a[200:203], v[152:155], v[176:179]\n\t"
    "s_cbranch_vccnz Lstub_c1_3_%=\n\t"
    "Lret_c1_3_%=:\n\t"
    "ds_read_b128 a[192:195], v1 offset:32768\n\t"
    "v_add_f32 v51, v51, v196\n\t"
    "v_mfma_f32_16x16x32_bf16 v[176:179], a[204:207], v[156:159], v[176:179]\n\t"
    "s_add_u32 s78, s62, 2\n\t"
    "v_exp_f32 v213, v173\n\t"
    "v_mfma_f32_16x16x32_bf16 v[180:183], a[208:211], v[144:147], v[44:47]\n\t"
    "s_and_b32 s78, s78, 3\n\t"
    "ds_read_b128 a[196:199], v2 offset:32768\n\t"
    "v_cvt_pk_bf16_f32 v190, v212, v213\n\t"
    "v_mfma_f32_16x16x32_bf16 v[180:183], a[212:215], v[148:151], v[180:183]\n\t"
    "s_lshl_b32 s67, s78, 18\n\t"
    "v_exp_f32 v214, v174\n\t"
    "v_mfma_f32_16x16x32_bf16 v[180:183], a[216:219], v[152:155], v[180:183]\n\t"
    "s_add_u32 s78, s62, 3\n\t"
    "ds_read_b128 a[200:203], v3 offset:32768\n\t"
    "ds_read_b128 a[204:207], v4 offset:32768\n\t"
    "v_mfma_f32_16x16x32_bf16 v[180:183], a[220:223], v[156:159], v[180:183]\n\t"
    "s_and_b32 s78, s78, 3\n\t"
    "v_exp_f32 v215, v175\n\t"
    "v_mfma_f32_16x16x32_bf16 a[96:99], a[224:227], v[184:187], a[96:99]\n\t"
    "s_lshl_b32 s68, s78, 18\n\t"
    "ds_read_b128 a[208:211], v1 offset:36864\n\t"
    "v_cvt_pk_bf16_f32 v191, v214, v215\n\t"
    "v_mfma_f32_16x16x32_bf16 a[100:103], a[228:231], v[184:187], a[100:103]\n\t"
    "s_lshl_b32 s63, s59, 4\n\t"
    "ds_read_b128 a[212:215], v2 offset:36864\n\t"
    "ds_read_b128 a[216:219], v3 offset:36864\n\t"
    "v_mfma_f32_16x16x32_bf16 a[104:107], a[232:235], v[184:187], a[104:107]\n\t"
    "s_add_u32 s63, s63, s60\n\t"
    "v_exp_f32 v208, v176\n\t"
    "v_mfma_f32_16x16x32_bf16 v[196:199], v[54:57], v[188:191], 0\n\t"
    "s_lshl_b32 s59, s56, 11\n\t"
    "ds_read_b128 a[220:223], v4 offset:36864\n\t"
    "v_mfma_f32_16x16x32_bf16 a[108:111], a[236:239], v[184:187], a[108:111]\n\t"
    "s_add_u32 s59, s59, 0\n\t"
    "v_exp_f32 v209, v177\n\t"
    "v_mfma_f32_16x16x32_bf16 a[112:115], a[240:243], v[184:187], a[112:115]\n\t"
    "v_cvt_pk_bf16_f32 v192, v208, v209\n\t"
    "v_lshrrev_b32 v227, 4, v0\n\t"
    "v_mfma_f32_16x16x32_bf16 a[116:119], a[244:247], v[184:187], a[116:119]\n\t"
    "s_add_u32 s78, s61, 1\n\t"
    "v_exp_f32 v210, v178\n\t"
    "v_mfma_f32_16x16x32_bf16 a[120:123], a[248:251], v[184:187], a[120:123]\n\t"
    "buffer_load_dwordx4 v11, s[48:51], s70 offen offset:2048 nt lds\n\t"
    "v_mfma_f32_16x16x32_bf16 a[124:127], a[252:255], v[184:187], a[124:127]\n\t"
    "s_waitcnt lgkmcnt(0)\n\t"
    "v_exp_f32 v211, v179\n\t"
    "v_mfma_f32_16x16x32_bf16 v[160:163], a[192:195], v[64:67], v[24:27]\n\t"
    "v_cmp_lt_f32 vcc, s71, v196\n\t"
    "v_cvt_pk_bf16_f32 v193, v210, v211\n\t"
    "v_mfma_f32_16x16x32_bf16 v[160:163], a[196:199], v[68:71], v[160:163]\n\t"
    "v_exp_f32 v212, v180\n\t"
    "v_mfma_f32_16x16x32_bf16 v[160:163], a[200:203], v[72:75], v[160:163]\n\t"
    "s_cbranch_vccnz Lstub_c1_4_%=\n\t"
    "Lret_c1_4_%=:\n\t"
    "v_add_f32 v52, v52, v196\n\t"
    "v_add_u32 v227, s78, v227\n\t"
    "v_mfma_f32_16x16x32_bf16 v[160:163], a[204:207], v[76:79], v[160:163]\n\t"
    "v_exp_f32 v213, v181\n\t"
    "v_mfma_f32_16x16x32_bf16 v[164:167], a[208:211], v[64:67], v[24:27]\n\t"
    "v_cvt_pk_bf16_f32 v194, v212, v213\n\t"
    "v_and_b32 v227, 3, v227\n\t"
    "v_mfma_f32_16x16x32_bf16 v[164:167], a[212:215], v[68:71], v[164:167]\n\t"
    "v_exp_f32 v214, v182\n\t"
    "v_mfma_f32_16x16x32_bf16 v[164:167], a[216:219], v[72:75], v[164:167]\n\t"
    "buffer_load_dwordx4 v12, s[48:51], s70 offen offset:3072 nt lds\n\t"
    "v_mfma_f32_16x16x32_bf16 v[164:167], a[220:223], v[76:79], v[164:167]\n\t"
    "s_add_u32 s70, s70, 0x2000\n\t"
    "v_exp_f32 v215, v183\n\t"
    "v_mfma_f32_16x16x32_bf16 a[128:131], a[224:227], v[188:191], a[128:131]\n\t"
    "s_memrealtime s[98:99]\n\t"
    "s_and_b32 s70, s70, 0x1ffff\n\t"
    "v_cvt_pk_bf16_f32 v195, v214, v215\n\t"
    "v_lshlrev_b32 v227, 6, v227\n\t"
    "v_mfma_f32_16x16x32_bf16 a[132:135], a[228:231], v[188:191], a[132:135]\n\t"
    "v_bfe_u32 v228, v0, 2, 2\n\t"
    "v_mfma_f32_16x16x32_bf16 a[136:139], a[232:235], v[188:191], a[136:139]\n\t"
    "v_exp_f32 v208, v160\n\t"
    "v_mfma_f32_16x16x32_bf16 v[196:199], v[54:57], v[192:195], 0\n\t"
    "v_lshl_add_u32 v227, v228, 10, v227\n\t"
    "v_mfma_f32_16x16x32_bf16 a[140:143], a[236:239], v[188:191], a[140:143]\n\t"
    "v_exp_f32 v209, v161\n\t"
    "v_mfma_f32_16x16x32_bf16 a[144:147], a[240:243], v[188:191], a[144:147]\n\t"
    "v_cvt_pk_bf16_f32 v184, v208, v209\n\t"
    "v_and_b32 v228, 3, v0\n\t"
    "v_mfma_f32_16x16x32_bf16 a[148:151], a[244:247], v[188:191], a[148:151]\n\t"
    "v_exp_f32 v210, v162\n\t"
    "v_mfma_f32_16x16x32_bf16 a[152:155], a[248:251], v[188:191], a[152:155]\n\t"
    "v_lshl_add_u32 v62, v228, 4, v227\n\t"
    "v_mfma_f32_16x16x32_bf16 a[156:159], a[252:255], v[188:191], a[156:159]\n\t"
    "v_exp_f32 v211, v163\n\t"
    "v_mfma_f32_16x16x32_bf16 v[168:171], a[192:195], v[80:83], v[28:31]\n\t"
    "v_cmp_lt_f32 vcc, s71, v196\n\t"
    "v_cvt_pk_bf16_f32 v185, v210, v211\n\t"
    "v_mfma_f32_16x16x32_bf16 v[168:171], a[196:199], v[84:87], v[168:171]\n\t"
    "s_waitcnt lgkmcnt(0)\n\t"
    "v_exp_f32 v212, v164\n\t"
    "v_mfma_f32_16x16x32_bf16 v[168:171], a[200:203], v[88:91], v[168:171]\n\t"
    "s_cbranch_vccnz Lstub_c1_5_%=\n\t"
    "Lret_c1_5_%=:\n\t"
    "v_add_f32 v53, v53, v196\n\t"
    "v_xor_b32 v63, 32, v62\n\t"
    "v_mfma_f32_16x16x32_bf16 v[168:171], a[204:207], v[92:95], v[168:171]\n\t"
    "s_waitcnt lgkmcnt(0)\n\t"
    "v_exp_f32 v213, v165\n\t"
    "v_mfma_f32_16x16x32_bf16 v[172:175], a[208:211], v[80:83], v[28:31]\n\t"
    "s_sub_u32 s19, s18, s98\n\t"
    "v_cvt_pk_bf16_f32 v186, v212, v213\n\t"
    "v_mfma_f32_16x16x32_bf16 v[172:175], a[212:215], v[84:87], v[172:175]\n\t"
    "s_barrier\n\t"
    "v_exp_f32 v214, v166\n\t"
    "v_mfma_f32_16x16x32_bf16 v[172:175], a[216:219], v[88:91], v[172:175]\n\t"
    "s_cmp_gt_i32 s19, 0\n\t"
    "s_cbranch_scc1 Lpace2_%=\n\t"
    "Lpaced2_%=:\n\t"
    "v_mfma_f32_16x16x32_bf16 v[172:175], a[220:223], v[92:95], v[172:175]\n\t"
    "s_add_u32 s18, s18, 0x72\n\t"
    "v_exp_f32 v215, v167\n\t"
    "v_mfma_f32_16x16x32_bf16 a[160:163], a[224:227], v[192:195], a[160:163]\n\t"
    "v_cvt_pk_bf16_f32 v187, v214, v215\n\t"
    "v_mfma_f32_16x16x32_bf16 a[164:167], a[228:231], v[192:195], a[164:167]\n\t"
    "ds_read_b64_tr_b16 a[224:225], v5 offset:32768\n\t"
    "ds_read_b64_tr_b16 a[226:227], v5 offset:36864\n\t"
    "v_mfma_f32_16x16x32_bf16 a[168:171], a[232:235], v[192:195], a[168:171]\n\t"
    "v_exp_f32 v208, v168\n\t"
    "v_mfma_f32_16x16x32_bf16 v[196:199], v[54:57], v[184:187], 0\n\t"
    "ds_read_b64_tr_b16 a[228:229], v6 offset:32768\n\t"
    "ds_read_b64_tr_b16 a[230:231], v6 offset:36864\n\t"
    "v_mfma_f32_16x16x32_bf16 a[172:175], a[236:239], v[192:195], a[172:175]\n\t"
    "v_exp_f32 v209, v169\n\t"
    "v_mfma_f32_16x16x32_bf16 a[176:179], a[240:243], v[192:195], a[176:179]\n\t"
    "ds_read_b64_tr_b16 a[232:233], v5 offset:33024\n\t"
    "v_cvt_pk_bf16_f32 v188, v208, v209\n\t"
    "v_mfma_f32_16x16x32_bf16 a[180:183], a[244:247], v[192:195], a[180:183]\n\t"
    "v_exp_f32 v210, v170\n\t"
    "v_mfma_f32_16x16x32_bf16 a[184:187], a[248:251], v[192:195], a[184:187]\n\t"
    "ds_read_b64_tr_b16 a[234:235], v5 offset:37120\n\t"
    "ds_read_b64_tr_b16 a[236:237], v6 offset:33024\n\t"
    "v_mfma_f32_16x16x32_bf16 a[188:191], a[252:255], v[192:195], a[188:191]\n\t"
    "v_exp_f32 v211, v171\n\t"
    "v_mfma_f32_16x16x32_bf16 v[176:179], a[192:195], v[96:99], v[32:35]\n\t"
    "ds_read_b64_tr_b16 a[238:239], v6 offset:37120\n\t"
    "v_cmp_lt_f32 vcc, s71, v196\n\t"
    "v_cvt_pk_bf16_f32 v189, v210, v211\n\t"
    "v_mfma_f32_16x16x32_bf16 v[176:179], a[196:199], v[100:103], v[176:179]\n\t"
    "v_exp_f32 v212, v172\n\t"
    "v_mfma_f32_16x16x32_bf16 v[176:179], a[200:203], v[104:107], v[176:179]\n\t"
    "s_cbranch_vccnz Lstub_c2_0_%=\n\t"
    "Lret_c2_0_%=:\n\t"
    "ds_read_b64_tr_b16 a[240:241], v5 offset:33280\n\t"
    "v_add_f32 v48, v48, v196\n\t"
    "v_mfma_f32_16x16x32_bf16 v[176:179], a[204:207], v[108:111], v[176:179]\n\t"
    "v_exp_f32 v213, v173\n\t"
    "v_mfma_f32_16x16x32_bf16 v[180:183], a[208:211], v[96:99], v[32:35]\n\t"
    "ds_read_b64_tr_b16 a[242:243], v5 offset:37376\n\t"
    "v_cvt_pk_bf16_f32 v190, v212, v213\n\t"
    "v_mfma_f32_16x16x32_bf16 v[180:183], a[212:215], v[100:103], v[180:183]\n\t"
    "v_exp_f32 v214, v174\n\t"
    "v_mfma_f32_16x16x32_bf16 v[180:183], a[216:219], v[104:107], v[180:183]\n\t"
    "ds_read_b64_tr_b16 a[244:245], v6 offset:33280\n\t"
    "ds_read_b64_tr_b16 a[246:247], v6 offset:37376\n\t"
    "v_mfma_f32_16x16x32_bf16 v[180:183], a[220:223], v[108:111], v[180:183]\n\t"
    "s_waitcnt lgkmcnt(3)\n\t"
    "v_exp_f32 v215, v175\n\t"
    "v_mfma_f32_16x16x32_bf16 a[0:3], a[224:227], v[184:187], a[0:3]\n\t"
    "ds_read_b64_tr_b16 a[248:249], v5 offset:33536\n\t"
    "v_cvt_pk_bf16_f32 v191, v214, v215\n\t"
    "v_mfma_f32_16x16x32_bf16 a[4:7], a[228:231], v[184:187], a[4:7]\n\t"
    "ds_read_b64_tr_b16 a[250:251], v5 offset:37632\n\t"
    "ds_read_b64_tr_b16 a[252:253], v6 offset:33536\n\t"
    "v_mfma_f32_16x16x32_bf16 a[8:11], a[232:235], v[184:187], a[8:11]\n\t"
    "s_add_u32 s98, s98, 0x68\n\t"
    "v_exp_f32 v208, v176\n\t"
    "v_mfma_f32_16x16x32_bf16 v[196:199], v[54:57], v[188:191], 0\n\t"
    "ds_read_b64_tr_b16 a[254:255], v6 offset:37632\n\t"
    "v_mfma_f32_16x16x32_bf16 a[12:15], a[236:239], v[184:187], a[12:15]\n\t"
    "s_waitcnt lgkmcnt(4)\n\t"
    "s_max_u32 s18, s18, s98\n\t"
    "v_exp_f32 v209, v177\n\t"
    "v_mfma_f32_16x16x32_bf16 a[16:19], a[240:243], v[184:187], a[16:19]\n\t"
    "v_cvt_pk_bf16_f32 v192, v208, v209\n\t"
    "v_mfma_f32_16x16x32_bf16 a[20:23], a[244:247], v[184:187], a[20:23]\n\t"
    "s_waitcnt lgkmcnt(1)\n\t"
    "s_add_u32 m0, s66, 0x4000\n\t"
    "v_exp_f32 v210, v178\n\t"
    "v_mfma_f32_16x16x32_bf16 a[24:27], a[248:251], v[184:187], a[24:27]\n\t"
    "s_waitcnt lgkmcnt(0)\n\t"
    "buffer_load_dwordx4 v7, s[44:47], s69 offen nt lds\n\t"
    "v_mfma_f32_16x16x32_bf16 a[28:31], a[252:255], v[184:187], a[28:31]\n\t"
    "v_exp_f32 v211, v179\n\t"
    "v_mfma_f32_16x16x32_bf16 v[160:163], a[192:195], v[112:115], v[36:39]\n\t"
    "v_cmp_lt_f32 vcc, s71, v196\n\t"
    "v_cvt_pk_bf16_f32 v193, v210, v211\n\t"
    "v_mfma_f32_16x16x32_bf16 v[160:163], a[196:199], v[116:119], v[160:163]\n\t"
    "v_exp_f32 v212, v180\n\t"
    "v_mfma_f32_16x16x32_bf16 v[160:163], a[200:203], v[120:123], v[160:163]\n\t"
    "s_cbranch_vccnz Lstub_c2_1_%=\n\t"
    "Lret_c2_1_%=:\n\t"
    "v_add_f32 v49, v49, v196\n\t"
    "v_mfma_f32_16x16x32_bf16 v[160:163], a[204:207], v[124:127], v[160:163]\n\t"
    "v_exp_f32 v213, v181\n\t"
    "v_mfma_f32_16x16x32_bf16 v[164:167], a[208:211], v[112:115], v[36:39]\n\t"
    "v_cvt_pk_bf16_f32 v194, v212, v213\n\t"
    "v_mfma_f32_16x16x32_bf16 v[164:167], a[212:215], v[116:119], v[164:167]\n\t"
    "v_exp_f32 v214, v182\n\t"
    "v_mfma_f32_16x16x32_bf16 v[164:167], a[216:219], v[120:123], v[164:167]\n\t"
    "buffer_load_dwordx4 v8, s[44:47], s69 offen offset:1024 nt lds\n\t"
    "v_mfma_f32_16x16x32_bf16 v[164:167], a[220:223], v[124:127], v[164:167]\n\t"
    "v_exp_f32 v215, v183\n\t"
    "v_mfma_f32_16x16x32_bf16 a[32:35], a[224:227], v[188:191], a[32:35]\n\t"
    "v_cvt_pk_bf16_f32 v195, v214, v215\n\t"
    "v_mfma_f32_16x16x32_bf16 a[36:39], a[228:231], v[188:191], a[36:39]\n\t"
    "buffer_load_dwordx4 v9, s[44:47], s69 offen offset:2048 nt lds\n\t"
    "v_mfma_f32_16x16x32_bf16 a[40:43], a[232:235], v[188:191], a[40:43]\n\t"
    "v_exp_f32 v208, v160\n\t"
    "v_mfma_f32_16x16x32_bf16 v[196:199], v[54:57], v[192:195], 0\n\t"
    "buffer_load_dwordx4 v10, s[44:47], s69 offen offset:3072 nt lds\n\t"
    "v_mfma_f32_16x16x32_bf16 a[44:47], a[236:239], v[188:191], a[44:47]\n\t"
    "s_add_u32 s69, s69, 0x2000\n\t"
    "v_exp_f32 v209, v161\n\t"
    "v_mfma_f32_16x16x32_bf16 a[48:51], a[240:243], v[188:191], a[48:51]\n\t"
    "s_and_b32 s69, s69, 0x1ffff\n\t"
    "v_cvt_pk_bf16_f32 v184, v208, v209\n\t"
    "v_mfma_f32_16x16x32_bf16 a[52:55], a[244:247], v[188:191], a[52:55]\n\t"
    "v_exp_f32 v210, v162\n\t"
    "v_mfma_f32_16x16x32_bf16 a[56:59], a[248:251], v[188:191], a[56:59]\n\t"
    "v_mfma_f32_16x16x32_bf16 a[60:63], a[252:255], v[188:191], a[60:63]\n\t"
    "v_exp_f32 v211, v163\n\t"
    "v_mfma_f32_16x16x32_bf16 v[168:171], a[192:195], v[128:131], v[40:43]\n\t"
    "v_cmp_lt_f32 vcc, s71, v196\n\t"
    "v_cvt_pk_bf16_f32 v185, v210, v211\n\t"
    "v_mfma_f32_16x16x32_bf16 v[168:171], a[196:199], v[132:135], v[168:171]\n\t"
    "v_exp_f32 v212, v164\n\t"
    "v_mfma_f32_16x16x32_bf16 v[168:171], a[200:203], v[136:139], v[168:171]\n\t"
    "s_cbranch_vccnz Lstub_c2_2_%=\n\t"
    "Lret_c2_2_%=:\n\t"
    "v_add_f32 v50, v50, v196\n\t"
    "v_mfma_f32_16x16x32_bf16 v[168:171], a[204:207], v[140:143], v[168:171]\n\t"
    "v_exp_f32 v213, v165\n\t"
    "v_mfma_f32_16x16x32_bf16 v[172:175], a[208:211], v[128:131], v[40:43]\n\t"
    "v_cvt_pk_bf16_f32 v186, v212, v213\n\t"
    "v_mfma_f32_16x16x32_bf16 v[172:175], a[212:215], v[132:135], v[172:175]\n\t"
    "v_exp_f32 v214, v166\n\t"
    "v_mfma_f32_16x16x32_bf16 v[172:175], a[216:219], v[136:139], v[172:175]\n\t"
    "v_mfma_f32_16x16x32_bf16 v[172:175], a[220:223], v[140:143], v[172:175]\n\t"
    "v_exp_f32 v215, v167\n\t"
    "v_mfma_f32_16x16x32_bf16 a[64:67], a[224:227], v[192:195], a[64:67]\n\t"
    "s_add_u32 m0, s66, 0x6000\n\t"
    "v_cvt_pk_bf16_f32 v187, v214, v215\n\t"
    "v_mfma_f32_16x16x32_bf16 a[68:71], a[228:231], v[192:195], a[68:71]\n\t"
    "buffer_load_dwordx4 v11, s[48:51], s70 offen nt lds\n\t"
    "v_mfma_f32_16x16x32_bf16 a[72:75], a[232:235], v[192:195], a[72:75]\n\t"
    "v_exp_f32 v208, v168\n\t"
    "v_mfma_f32_16x16x32_bf16 v[196:199], v[54:57], v[184:187], 0\n\t"
    "v_mfma_f32_16x16x32_bf16 a[76:79], a[236:239], v[192:195], a[76:79]\n\t"
    "v_exp_f32 v209, v169\n\t"
    "v_mfma_f32_16x16x32_bf16 a[80:83], a[240:243], v[192:195], a[80:83]\n\t"
    "v_cvt_pk_bf16_f32 v188, v208, v209\n\t"
    "v_mfma_f32_16x16x32_bf16 a[84:87], a[244:247], v[192:195], a[84:87]\n\t"
    "v_exp_f32 v210, v170\n\t"
    "v_mfma_f32_16x16x32_bf16 a[88:91], a[248:251], v[192:195], a[88:91]\n\t"
    "v_mfma_f32_16x16x32_bf16 a[92:95], a[252:255], v[192:195], a[92:95]\n\t"
    "v_exp_f32 v211, v171\n\t"
    "v_mfma_f32_16x16x32_bf16 v[176:179], a[192:195], v[144:147], v[44:47]\n\t"
    "v_cmp_lt_f32 vcc, s71, v196\n\t"
    "v_cvt_pk_bf16_f32 v189, v210, v211\n\t"
    "v_mfma_f32_16x16x32_bf16 v[176:179], a[196:199], v[148:151], v[176:179]\n\t"
    "v_exp_f32 v212, v172\n\t"
    "v_mfma_f32_16x16x32_bf16 v[176:179], a[200:203], v[152:155], v[176:179]\n\t"
    "s_cbranch_vccnz Lstub_c2_3_%=\n\t"
    "Lret_c2_3_%=:\n\t"
    "ds_read_b128 a[192:195], v1 offset:49152\n\t"
    "v_add_f32 v51, v51, v196\n\t"
    "v_mfma_f32_16x16x32_bf16 v[176:179], a[204:207], v[156:159], v[176:179]\n\t"
    "v_exp_f32 v213, v173\n\t"
    "v_mfma_f32_16x16x32_bf16 v[180:183], a[208:211], v[144:147], v[44:47]\n\t"
    "ds_read_b128 a[196:199], v2 offset:49152\n\t"
    "v_cvt_pk_bf16_f32 v190, v212, v213\n\t"
    "v_mfma_f32_16x16x32_bf16 v[180:183], a[212:215], v[148:151], v[180:183]\n\t"
    "v_exp_f32 v214, v174\n\t"
    "v_mfma_f32_16x16x32_bf16 v[180:183], a[216:219], v[152:155], v[180:183]\n\t"
    "ds_read_b128 a[200:203], v3 offset:49152\n\t"
    "ds_read_b128 a[204:207], v4 offset:49152\n\t"
    "v_mfma_f32_16x16x32_bf16 v[180:183], a[220:223], v[156:159], v[180:183]\n\t"
    "v_exp_f32 v215, v175\n\t"
    "v_mfma_f32_16x16x32_bf16 a[96:99], a[224:227], v[184:187], a[96:99]\n\t"
    "ds_read_b128 a[208:211], v1 offset:53248\n\t"
    "v_cvt_pk_bf16_f32 v191, v214, v215\n\t"
    "v_mfma_f32_16x16x32_bf16 a[100:103], a[228:231], v[184:187], a[100:103]\n\t"
    "ds_read_b128 a[212:215], v2 offset:53248\n\t"
    "ds_read_b128 a[216:219], v3 offset:53248\n\t"
    "v_mfma_f32_16x16x32_bf16 a[104:107], a[232:235], v[184:187], a[104:107]\n\t"
    "v_exp_f32 v208, v176\n\t"
    "v_mfma_f32_16x16x32_bf16 v[196:199], v[54:57], v[188:191], 0\n\t"
    "ds_read_b128 a[220:223], v4 offset:53248\n\t"
    "v_mfma_f32_16x16x32_bf16 a[108:111], a[236:239], v[184:187], a[108:111]\n\t"
    "v_exp_f32 v209, v177\n\t"
    "v_mfma_f32_16x16x32_bf16 a[112:115], a[240:243], v[184:187], a[112:115]\n\t"
    "v_cvt_pk_bf16_f32 v192, v208, v209\n\t"
    "v_mfma_f32_16x16x32_bf16 a[116:119], a[244:247], v[184:187], a[116:119]\n\t"
    "v_exp_f32 v210, v178\n\t"
    "v_mfma_f32_16x16x32_bf16 a[120:123], a[248:251], v[184:187], a[120:123]\n\t"
    "buffer_load_dwordx4 v12, s[48:51], s70 offen offset:1024 nt lds\n\t"
    "v_mfma_f32_16x16x32_bf16 a[124:127], a[252:255], v[184:187], a[124:127]\n\t"
    "s_waitcnt lgkmcnt(0)\n\t"
    "v_exp_f32 v211, v179\n\t"
    "v_mfma_f32_16x16x32_bf16 v[160:163], a[192:195], v[64:67], v[24:27]\n\t"
    "v_cmp_lt_f32 vcc, s71, v196\n\t"
    "v_cvt_pk_bf16_f32 v193, v210, v211\n\t"
    "v_mfma_f32_16x16x32_bf16 v[160:163], a[196:199], v[68:71], v[160:163]\n\t"
    "v_exp_f32 v212, v180\n\t"
    "v_mfma_f32_16x16x32_bf16 v[160:163], a[200:203], v[72:75], v[160:163]\n\t"
    "s_cbranch_vccnz Lstub_c2_4_%=\n\t"
    "Lret_c2_4_%=:\n\t"
    "v_add_f32 v52, v52, v196\n\t"
    "v_mfma_f32_16x16x32_bf16 v[160:163], a[204:207], v[76:79], v[160:163]\n\t"
    "v_exp_f32 v213, v181\n\t"
    "v_mfma_f32_16x16x32_bf16 v[164:167], a[208:211], v[64:67], v[24:27]\n\t"
    "v_cvt_pk_bf16_f32 v194, v212, v213\n\t"
    "v_mfma_f32_16x16x32_bf16 v[164:167], a[212:215], v[68:71], v[164:167]\n\t"
    "v_exp_f32 v214, v182\n\t"
    "v_mfma_f32_16x16x32_bf16 v[164:167], a[216:219], v[72:75], v[164:167]\n\t"
    "buffer_load_dwordx4 v11, s[48:51], s70 offen offset:2048 nt lds\n\t"
    "v_mfma_f32_16x16x32_bf16 v[164:167], a[220:223], v[76:79], v[164:167]\n\t"
    "v_exp_f32 v215, v183\n\t"
    "v_mfma_f32_16x16x32_bf16 a[128:131], a[224:227], v[188:191], a[128:131]\n\t"
    "s_memrealtime s[98:99]\n\t"
    "v_cvt_pk_bf16_f32 v195, v214, v215\n\t"
    "v_mfma_f32_16x16x32_bf16 a[132:135], a[228:231], v[188:191], a[132:135]\n\t"
    "v_mfma_f32_16x16x32_bf16 a[136:139], a[232:235], v[188:191], a[136:139]\n\t"
    "v_exp_f32 v208, v160\n\t"
    "v_mfma_f32_16x16x32_bf16 v[196:199], v[54:57], v[192:195], 0\n\t"
    "v_mfma_f32_16x16x32_bf16 a[140:143], a[236:239], v[188:191], a[140:143]\n\t"
    "v_exp_f32 v209, v161\n\t"
    "v_mfma_f32_16x16x32_bf16 a[144:147], a[240:243], v[188:191], a[144:147]\n\t"
    "v_cvt_pk_bf16_f32 v184, v208, v209\n\t"
    "v_mfma_f32_16x16x32_bf16 a[148:151], a[244:247], v[188:191], a[148:151]\n\t"
    "v_exp_f32 v210, v162\n\t"
    "v_mfma_f32_16x16x32_bf16 a[152:155], a[248:251], v[188:191], a[152:155]\n\t"
    "v_mfma_f32_16x16x32_bf16 a[156:159], a[252:255], v[188:191], a[156:159]\n\t"
    "v_exp_f32 v211, v163\n\t"
    "v_mfma_f32_16x16x32_bf16 v[168:171], a[192:195], v[80:83], v[28:31]\n\t"
    "v_cmp_lt_f32 vcc, s71, v196\n\t"
    "v_cvt_pk_bf16_f32 v185, v210, v211\n\t"
    "v_mfma_f32_16x16x32_bf16 v[168:171], a[196:199], v[84:87], v[168:171]\n\t"
    "s_waitcnt lgkmcnt(0)\n\t"
    "v_exp_f32 v212, v164\n\t"
    "v_mfma_f32_16x16x32_bf16 v[168:171], a[200:203], v[88:91], v[168:171]\n\t"
    "s_cbranch_vccnz Lstub_c2_5_%=\n\t"
    "Lret_c2_5_%=:\n\t"
    "v_add_f32 v53, v53, v196\n\t"
    "v_mfma_f32_16x16x32_bf16 v[168:171], a[204:207], v[92:95], v[168:171]\n\t"
    "v_exp_f32 v213, v165\n\t"
    "v_mfma_f32_16x16x32_bf16 v[172:175], a[208:211], v[80:83], v[28:31]\n\t"
    "s_sub_u32 s19, s18, s98\n\t"
    "v_cvt_pk_bf16_f32 v186, v212, v213\n\t"
    "v_mfma_f32_16x16x32_bf16 v[172:175], a[212:215], v[84:87], v[172:175]\n\t"
    "s_barrier\n\t"
    "v_exp_f32 v214, v166\n\t"
    "v_mfma_f32_16x16x32_bf16 v[172:175], a[216:219], v[88:91], v[172:175]\n\t"
    "s_cmp_gt_i32 s19, 0\n\t"
    "s_cbranch_scc1 Lpace3_%=\n\t"
    "Lpaced3_%=:\n\t"
    "v_mfma_f32_16x16x32_bf16 v[172:175], a[220:223], v[92:95], v[172:175]\n\t"
    "s_add_u32 s18, s18, 0x72\n\t"
    "v_exp_f32 v215, v167\n\t"
    "v_mfma_f32_16x16x32_bf16 a[160:163], a[224:227], v[192:195], a[160:163]\n\t"
    "v_cvt_pk_bf16_f32 v187, v214, v215\n\t"
    "v_mfma_f32_16x16x32_bf16 a[164:167], a[228:231], v[192:195], a[164:167]\n\t"
    "ds_read_b64_tr_b16 a[224:225], v5 offset:49152\n\t"
    "ds_read_b64_tr_b16 a[226:227], v5 offset:53248\n\t"
    "v_mfma_f32_16x16x32_bf16 a[168:171], a[232:235], v[192:195], a[168:171]\n\t"
    "v_exp_f32 v208, v168\n\t"
    "v_mfma_f32_16x16x32_bf16 v[196:199], v[54:57], v[184:187], 0\n\t"
    "ds_read_b64_tr_b16 a[228:229], v6 offset:49152\n\t"
    "ds_read_b64_tr_b16 a[230:231], v6 offset:53248\n\t"
    "v_mfma_f32_16x16x32_bf16 a[172:175], a[236:239], v[192:195], a[172:175]\n\t"
    "v_exp_f32 v209, v169\n\t"
    "v_mfma_f32_16x16x32_bf16 a[176:179], a[240:243], v[192:195], a[176:179]\n\t"
    "ds_read_b64_tr_b16 a[232:233], v5 offset:49408\n\t"
    "v_cvt_pk_bf16_f32 v188, v208, v209\n\t"
    "v_mfma_f32_16x16x32_bf16 a[180:183], a[244:247], v[192:195], a[180:183]\n\t"
    "v_exp_f32 v210, v170\n\t"
    "v_mfma_f32_16x16x32_bf16 a[184:187], a[248:251], v[192:195], a[184:187]\n\t"
    "ds_read_b64_tr_b16 a[234:235], v5 offset:53504\n\t"
    "ds_read_b64_tr_b16 a[236:237], v6 offset:49408\n\t"
    "v_mfma_f32_16x16x32_bf16 a[188:191], a[252:255], v[192:195], a[188:191]\n\t"
    "v_exp_f32 v211, v171\n\t"
    "v_mfma_f32_16x16x32_bf16 v[176:179], a[192:195], v[96:99], v[32:35]\n\t"
    "ds_read_b64_tr_b16 a[238:239], v6 offset:53504\n\t"
    "v_cmp_lt_f32 vcc, s71, v196\n\t"
    "v_cvt_pk_bf16_f32 v189, v210, v211\n\t"
    "v_mfma_f32_16x16x32_bf16 v[176:179], a[196:199], v[100:103], v[176:179]\n\t"
    "v_exp_f32 v212, v172\n\t"
    "v_mfma_f32_16x16x32_bf16 v[176:179], a[200:203], v[104:107], v[176:179]\n\t"
    "s_cbranch_vccnz Lstub_c3_0_%=\n\t"
    "Lret_c3_0_%=:\n\t"
    "ds_read_b64_tr_b16 a[240:241], v5 offset:49664\n\t"
    "v_add_f32 v48, v48, v196\n\t"
    "v_mfma_f32_16x16x32_bf16 v[176:179], a[204:207], v[108:111], v[176:179]\n\t"
    "v_exp_f32 v213, v173\n\t"
    "v_mfma_f32_16x16x32_bf16 v[180:183], a[208:211], v[96:99], v[32:35]\n\t"
    "ds_read_b64_tr_b16 a[242:243], v5 offset:53760\n\t"
    "v_cvt_pk_bf16_f32 v190, v212, v213\n\t"
    "v_mfma_f32_16x16x32_bf16 v[180:183], a[212:215], v[100:103], v[180:183]\n\t"
    "v_exp_f32 v214, v174\n\t"
    "v_mfma_f32_16x16x32_bf16 v[180:183], a[216:219], v[104:107], v[180:183]\n\t"
    "ds_read_b64_tr_b16 a[244:245], v6 offset:49664\n\t"
    "ds_read_b64_tr_b16 a[246:247], v6 offset:53760\n\t"
    "v_mfma_f32_16x16x32_bf16 v[180:183], a[220:223], v[108:111], v[180:183]\n\t"
    "s_waitcnt lgkmcnt(3)\n\t"
    "v_exp_f32 v215, v175\n\t"
    "v_mfma_f32_16x16x32_bf16 a[0:3], a[224:227], v[184:187], a[0:3]\n\t"
    "ds_read_b64_tr_b16 a[248:249], v5 offset:49920\n\t"
    "v_cvt_pk_bf16_f32 v191, v214, v215\n\t"
    "v_mfma_f32_16x16x32_bf16 a[4:7], a[228:231], v[184:187], a[4:7]\n\t"
    "ds_read_b64_tr_b16 a[250:251], v5 offset:54016\n\t"
    "ds_read_b64_tr_b16 a[252:253], v6 offset:49920\n\t"
    "v_mfma_f32_16x16x32_bf16 a[8:11], a[232:235], v[184:187], a[8:11]\n\t"
    "s_add_u32 s98, s98, 0x68\n\t"
    "v_exp_f32 v208, v176\n\t"
    "v_mfma_f32_16x16x32_bf16 v[196:199], v[54:57], v[188:191], 0\n\t"
    "ds_read_b64_tr_b16 a[254:255], v6 offset:54016\n\t"
    "v_mfma_f32_16x16x32_bf16 a[12:15], a[236:239], v[184:187], a[12:15]\n\t"
    "s_waitcnt lgkmcnt(4)\n\t"
    "s_max_u32 s18, s18, s98\n\t"
    "v_exp_f32 v209, v177\n\t"
    "v_mfma_f32_16x16x32_bf16 a[16:19], a[240:243], v[184:187], a[16:19]\n\t"
    "v_cvt_pk_bf16_f32 v192, v208, v209\n\t"
    "v_mfma_f32_16x16x32_bf16 a[20:23], a[244:247], v[184:187], a[20:23]\n\t"
    "s_waitcnt lgkmcnt(1)\n\t"
    "v_exp_f32 v210, v178\n\t"
    "v_mfma_f32_16x16x32_bf16 a[24:27], a[248:251], v[184:187], a[24:27]\n\t"
    "s_waitcnt lgkmcnt(0)\n\t"
    "buffer_load_dwordx4 v12, s[48:51], s70 offen offset:3072 nt lds\n\t"
    "v_mfma_f32_16x16x32_bf16 a[28:31], a[252:255], v[184:187], a[28:31]\n\t"
    "s_add_u32 s70, s70, 0x2000\n\t"
    "v_exp_f32 v211, v179\n\t"
    "v_mfma_f32_16x16x32_bf16 v[160:163], a[192:195], v[112:115], v[36:39]\n\t"
    "s_and_b32 s70, s70, 0x1ffff\n\t"
    "v_cmp_lt_f32 vcc, s71, v196\n\t"
    "v_cvt_pk_bf16_f32 v193, v210, v211\n\t"
    "v_mfma_f32_16x16x32_bf16 v[160:163], a[196:199], v[116:119], v[160:163]\n\t"
    "v_exp_f32 v212, v180\n\t"
    "v_mfma_f32_16x16x32_bf16 v[160:163], a[200:203], v[120:123], v[160:163]\n\t"
    "s_cbranch_vccnz Lstub_c3_1_%=\n\t"
    "Lret_c3_1_%=:\n\t"
    "v_add_f32 v49, v49, v196\n\t"
    "v_mfma_f32_16x16x32_bf16 v[160:163], a[204:207], v[124:127], v[160:163]\n\t"
    "v_exp_f32 v213, v181\n\t"
    "v_mfma_f32_16x16x32_bf16 v[164:167], a[208:211], v[112:115], v[36:39]\n\t"
    "v_cvt_pk_bf16_f32 v194, v212, v213\n\t"
    "v_mfma_f32_16x16x32_bf16 v[164:167], a[212:215], v[116:119], v[164:167]\n\t"
    "s_add_u32 m0, s66, 0x8000\n\t"
    "v_exp_f32 v214, v182\n\t"
    "v_mfma_f32_16x16x32_bf16 v[164:167], a[216:219], v[120:123], v[164:167]\n\t"
    "buffer_load_dwordx4 v7, s[44:47], s69 offen nt lds\n\t"
    "v_mfma_f32_16x16x32_bf16 v[164:167], a[220:223], v[124:127], v[164:167]\n\t"
    "v_exp_f32 v215, v183\n\t"
    "v_mfma_f32_16x16x32_bf16 a[32:35], a[224:227], v[188:191], a[32:35]\n\t"
    "v_cvt_pk_bf16_f32 v195, v214, v215\n\t"
    "v_mfma_f32_16x16x32_bf16 a[36:39], a[228:231], v[188:191], a[36:39]\n\t"
    "buffer_load_dwordx4 v8, s[44:47], s69 offen offset:1024 nt lds\n\t"
    "v_mfma_f32_16x16x32_bf16 a[40:43], a[232:235], v[188:191], a[40:43]\n\t"
    "v_exp_f32 v208, v160\n\t"
    "v_mfma_f32_16x16x32_bf16 v[196:199], v[54:57], v[192:195], 0\n\t"
    "v_mfma_f32_16x16x32_bf16 a[44:47], a[236:239], v[188:191], a[44:47]\n\t"
    "v_exp_f32 v209, v161\n\t"
    "v_mfma_f32_16x16x32_bf16 a[48:51], a[240:243], v[188:191], a[48:51]\n\t"
    "v_cvt_pk_bf16_f32 v184, v208, v209\n\t"
    "v_mfma_f32_16x16x32_bf16 a[52:55], a[244:247], v[188:191], a[52:55]\n\t"
    "v_exp_f32 v210, v162\n\t"
    "v_mfma_f32_16x16x32_bf16 a[56:59], a[248:251], v[188:191], a[56:59]\n\t"
    "buffer_load_dwordx4 v9, s[44:47], s69 offen offset:2048 nt lds\n\t"
    "v_mfma_f32_16x16x32_bf16 a[60:63], a[252:255], v[188:191], a[60:63]\n\t"
    "v_exp_f32 v211, v163\n\t"
    "v_mfma_f32_16x16x32_bf16 v[168:171], a[192:195], v[128:131], v[40:43]\n\t"
    "v_cmp_lt_f32 vcc, s71, v196\n\t"
    "v_cvt_pk_bf16_f32 v185, v210, v211\n\t"
    "v_mfma_f32_16x16x32_bf16 v[168:171], a[196:199], v[132:135], v[168:171]\n\t"
    "v_exp_f32 v212, v164\n\t"
    "v_mfma_f32_16x16x32_bf16 v[168:171], a[200:203], v[136:139], v[168:171]\n\t"
    "s_cbranch_vccnz Lstub_c3_2_%=\n\t"
    "Lret_c3_2_%=:\n\t"
    "v_add_f32 v50, v50, v196\n\t"
    "v_mfma_f32_16x16x32_bf16 v[168:171], a[204:207], v[140:143], v[168:171]\n\t"
    "v_exp_f32 v213, v165\n\t"
    "v_mfma_f32_16x16x32_bf16 v[172:175], a[208:211], v[128:131], v[40:43]\n\t"
    "v_cvt_pk_bf16_f32 v186, v212, v213\n\t"
    "v_mfma_f32_16x16x32_bf16 v[172:175], a[212:215], v[132:135], v[172:175]\n\t"
    "v_exp_f32 v214, v166\n\t"
    "v_mfma_f32_16x16x32_bf16 v[172:175], a[216:219], v[136:139], v[172:175]\n\t"
    "v_mfma_f32_16x16x32_bf16 v[172:175], a[220:223], v[140:143], v[172:175]\n\t"
    "v_exp_f32 v215, v167\n\t"
    "v_mfma_f32_16x16x32_bf16 a[64:67], a[224:227], v[192:195], a[64:67]\n\t"
    "v_cvt_pk_bf16_f32 v187, v214, v215\n\t"
    "v_mfma_f32_16x16x32_bf16 a[68:71], a[228:231], v[192:195], a[68:71]\n\t"
    "v_mfma_f32_16x16x32_bf16 a[72:75], a[232:235], v[192:195], a[72:75]\n\t"
    "v_exp_f32 v208, v168\n\t"
    "v_mfma_f32_16x16x32_bf16 v[196:199], v[54:57], v[184:187], 0\n\t"
    "buffer_load_dwordx4 v10, s[44:47], s69 offen offset:3072 nt lds\n\t"
    "v_mfma_f32_16x16x32_bf16 a[76:79], a[236:239], v[192:195], a[76:79]\n\t"
    "s_add_u32 s69, s69, 0x2000\n\t"
    "v_exp_f32 v209, v169\n\t"
    "v_mfma_f32_16x16x32_bf16 a[80:83], a[240:243], v[192:195], a[80:83]\n\t"
    "s_and_b32 s69, s69, 0x1ffff\n\t"
    "v_cvt_pk_bf16_f32 v188, v208, v209\n\t"
    "v_mfma_f32_16x16x32_bf16 a[84:87], a[244:247], v[192:195], a[84:87]\n\t"
    "v_exp_f32 v210, v170\n\t"
    "v_mfma_f32_16x16x32_bf16 a[88:91], a[248:251], v[192:195], a[88:91]\n\t"
    "v_mfma_f32_16x16x32_bf16 a[92:95], a[252:255], v[192:195], a[92:95]\n\t"
    "v_exp_f32 v211, v171\n\t"
    "v_mfma_f32_16x16x32_bf16 v[176:179], a[192:195], v[144:147], v[44:47]\n\t"
    "v_cmp_lt_f32 vcc, s71, v196\n\t"
    "v_cvt_pk_bf16_f32 v189, v210, v211\n\t"
    "v_mfma_f32_16x16x32_bf16 v[176:179], a[196:199], v[148:151], v[176:179]\n\t"
    "v_exp_f32 v212, v172\n\t"
    "v_mfma_f32_16x16x32_bf16 v[176:179], a[200:203], v[152:155], v[176:179]\n\t"
    "s_cbranch_vccnz Lstub_c3_3_%=\n\t"
    "Lret_c3_3_%=:\n\t"
    "ds_read_b128 a[192:195], v200 offset:0\n\t"
    "v_add_f32 v51, v51, v196\n\t"
    "v_mfma_f32_16x16x32_bf16 v[176:179], a[204:207], v[156:159], v[176:179]\n\t"
    "v_exp_f32 v213, v173\n\t"
    "v_mfma_f32_16x16x32_bf16 v[180:183], a[208:211], v[144:147], v[44:47]\n\t"
    "ds_read_b128 a[196:199], v201 offset:0\n\t"
    "v_cvt_pk_bf16_f32 v190, v212, v213\n\t"
    "v_mfma_f32_16x16x32_bf16 v[180:183], a[212:215], v[148:151], v[180:183]\n\t"
    "v_exp_f32 v214, v174\n\t"
    "v_mfma_f32_16x16x32_bf16 v[180:183], a[216:219], v[152:155], v[180:183]\n\t"
    "ds_read_b128 a[200:203], v202 offset:0\n\t"
    "ds_read_b128 a[204:207], v203 offset:0\n\t"
    "v_mfma_f32_16x16x32_bf16 v[180:183], a[220:223], v[156:159], v[180:183]\n\t"
    "v_exp_f32 v215, v175\n\t"
    "v_mfma_f32_16x16x32_bf16 a[96:99], a[224:227], v[184:187], a[96:99]\n\t"
    "ds_read_b128 a[208:211], v200 offset:4096\n\t"
    "v_cvt_pk_bf16_f32 v191, v214, v215\n\t"
    "v_mfma_f32_16x16x32_bf16 a[100:103], a[228:231], v[184:187], a[100:103]\n\t"
    "ds_read_b128 a[212:215], v201 offset:4096\n\t"
    "ds_read_b128 a[216:219], v202 offset:4096\n\t"
    "v_mfma_f32_16x16x32_bf16 a[104:107], a[232:235], v[184:187], a[104:107]\n\t"
    "v_exp_f32 v208, v176\n\t"
    "v_mfma_f32_16x16x32_bf16 v[196:199], v[54:57], v[188:191], 0\n\t"
    "ds_read_b128 a[220:223], v203 offset:4096\n\t"
    "v_mfma_f32_16x16x32_bf16 a[108:111], a[236:239], v[184:187], a[108:111]\n\t"
    "v_exp_f32 v209, v177\n\t"
    "v_mfma_f32_16x16x32_bf16 a[112:115], a[240:243], v[184:187], a[112:115]\n\t"
    "v_cvt_pk_bf16_f32 v192, v208, v209\n\t"
    "v_mfma_f32_16x16x32_bf16 a[116:119], a[244:247], v[184:187], a[116:119]\n\t"
    "s_add_u32 m0, s66, 0xa000\n\t"
    "v_exp_f32 v210, v178\n\t"
    "v_mfma_f32_16x16x32_bf16 a[120:123], a[248:251], v[184:187], a[120:123]\n\t"
    "buffer_load_dwordx4 v11, s[48:51], s70 offen nt lds\n\t"
    "v_mfma_f32_16x16x32_bf16 a[124:127], a[252:255], v[184:187], a[124:127]\n\t"
    "s_waitcnt lgkmcnt(0)\n\t"
    "v_exp_f32 v211, v179\n\t"
    "v_mfma_f32_16x16x32_bf16 v[160:163], a[192:195], v[64:67], v[24:27]\n\t"
    "v_cmp_lt_f32 vcc, s71, v196\n\t"
    "v_cvt_pk_bf16_f32 v193, v210, v211\n\t"
    "v_mfma_f32_16x16x32_bf16 v[160:163], a[196:199], v[68:71], v[160:163]\n\t"
    "v_exp_f32 v212, v180\n\t"
    "v_mfma_f32_16x16x32_bf16 v[160:163], a[200:203], v[72:75], v[160:163]\n\t"
    "s_cbranch_vccnz Lstub_c3_4_%=\n\t"
    "Lret_c3_4_%=:\n\t"
    "v_add_f32 v52, v52, v196\n\t"
    "v_mfma_f32_16x16x32_bf16 v[160:163], a[204:207], v[76:79], v[160:163]\n\t"
    "v_exp_f32 v213, v181\n\t"
    "v_mfma_f32_16x16x32_bf16 v[164:167], a[208:211], v[64:67], v[24:27]\n\t"
    "v_cvt_pk_bf16_f32 v194, v212, v213\n\t"
    "v_mfma_f32_16x16x32_bf16 v[164:167], a[212:215], v[68:71], v[164:167]\n\t"
    "v_exp_f32 v214, v182\n\t"
    "v_mfma_f32_16x16x32_bf16 v[164:167], a[216:219], v[72:75], v[164:167]\n\t"
    "v_mfma_f32_16x16x32_bf16 v[164:167], a[220:223], v[76:79], v[164:167]\n\t"
    "v_exp_f32 v215, v183\n\t"
    "v_mfma_f32_16x16x32_bf16 a[128:131], a[224:227], v[188:191], a[128:131]\n\t"
    "s_memrealtime s[98:99]\n\t"
    "v_cvt_pk_bf16_f32 v195, v214, v215\n\t"
    "v_mfma_f32_16x16x32_bf16 a[132:135], a[228:231], v[188:191], a[132:135]\n\t"
    "buffer_load_dwordx4 v12, s[48:51], s70 offen offset:1024 nt lds\n\t"
    "v_mfma_f32_16x16x32_bf16 a[136:139], a[232:235], v[188:191], a[136:139]\n\t"
    "v_exp_f32 v208, v160\n\t"
    "v_mfma_f32_16x16x32_bf16 v[196:199], v[54:57], v[192:195], 0\n\t"
    "v_mfma_f32_16x16x32_bf16 a[140:143], a[236:239], v[188:191], a[140:143]\n\t"
    "v_exp_f32 v209, v161\n\t"
    "v_mfma_f32_16x16x32_bf16 a[144:147], a[240:243], v[188:191], a[144:147]\n\t"
    "v_cvt_pk_bf16_f32 v184, v208, v209\n\t"
    "v_mfma_f32_16x16x32_bf16 a[148:151], a[244:247], v[188:191], a[148:151]\n\t"
    "v_exp_f32 v210, v162\n\t"
    "v_mfma_f32_16x16x32_bf16 a[152:155], a[248:251], v[188:191], a[152:155]\n\t"
    "v_mfma_f32_16x16x32_bf16 a[156:159], a[252:255], v[188:191], a[156:159]\n\t"
    "v_exp_f32 v211, v163\n\t"
    "v_mfma_f32_16x16x32_bf16 v[168:171], a[192:195], v[80:83], v[28:31]\n\t"
    "v_cmp_lt_f32 vcc, s71, v196\n\t"
    "v_cvt_pk_bf16_f32 v185, v210, v211\n\t"
    "v_mfma_f32_16x16x32_bf16 v[168:171], a[196:199], v[84:87], v[168:171]\n\t"
    "s_waitcnt lgkmcnt(0)\n\t"
    "v_exp_f32 v212, v164\n\t"
    "v_mfma_f32_16x16x32_bf16 v[168:171], a[200:203], v[88:91], v[168:171]\n\t"
    "s_cbranch_vccnz Lstub_c3_5_%=\n\t"
    "Lret_c3_5_%=:\n\t"
    "v_add_f32 v53, v53, v196\n\t"
    "v_mfma_f32_16x16x32_bf16 v[168:171], a[204:207], v[92:95], v[168:171]\n\t"
    "s_waitcnt vmcnt(18)\n\t"
    "v_exp_f32 v213, v165\n\t"
    "v_mfma_f32_16x16x32_bf16 v[172:175], a[208:211], v[80:83], v[28:31]\n\t"
    "s_sub_u32 s19, s18, s98\n\t"
    "v_cvt_pk_bf16_f32 v186, v212, v213\n\t"
    "v_mfma_f32_16x16x32_bf16 v[172:175], a[212:215], v[84:87], v[172:175]\n\t"
    "s_barrier\n\t"
    "v_exp_f32 v214, v166\n\t"
    "v_mfma_f32_16x16x32_bf16 v[172:175], a[216:219], v[88:91], v[172:175]\n\t"
    "s_cmp_gt_i32 s19, 0\n\t"
    "s_cbranch_scc1 Lpace4_%=\n\t"
    "Lpaced4_%=:\n\t"
    "v_mfma_f32_16x16x32_bf16 v[172:175], a[220:223], v[92:95], v[172:175]\n\t"
    "s_add_u32 s18, s18, 0x72\n\t"
    "v_exp_f32 v215, v167\n\t"
    "v_mfma_f32_16x16x32_bf16 a[160:163], a[224:227], v[192:195], a[160:163]\n\t"
    "v_cvt_pk_bf16_f32 v187, v214, v215\n\t"
    "v_mfma_f32_16x16x32_bf16 a[164:167], a[228:231], v[192:195], a[164:167]\n\t"
    "ds_read_b64_tr_b16 a[224:225], v204 offset:0\n\t"
    "ds_read_b64_tr_b16 a[226:227], v204 offset:4096\n\t"
    "v_mfma_f32_16x16x32_bf16 a[168:171], a[232:235], v[192:195], a[168:171]\n\t"
    "v_exp_f32 v208, v168\n\t"
    "v_mfma_f32_16x16x32_bf16 v[196:199], v[54:57], v[184:187], 0\n\t"
    "ds_read_b64_tr_b16 a[228:229], v205 offset:0\n\t"
    "ds_read_b64_tr_b16 a[230:231], v205 offset:4096\n\t"
    "v_mfma_f32_16x16x32_bf16 a[172:175], a[236:239], v[192:195], a[172:175]\n\t"
    "v_exp_f32 v209, v169\n\t"
    "v_mfma_f32_16x16x32_bf16 a[176:179], a[240:243], v[192:195], a[176:179]\n\t"
    "ds_read_b64_tr_b16 a[232:233], v204 offset:256\n\t"
    "v_cvt_pk_bf16_f32 v188, v208, v209\n\t"
    "v_mfma_f32_16x16x32_bf16 a[180:183], a[244:247], v[192:195], a[180:183]\n\t"
    "v_exp_f32 v210, v170\n\t"
    "v_mfma_f32_16x16x32_bf16 a[184:187], a[248:251], v[192:195], a[184:187]\n\t"
    "ds_read_b64_tr_b16 a[234:235], v204 offset:4352\n\t"
    "ds_read_b64_tr_b16 a[236:237], v205 offset:256\n\t"
    "v_mfma_f32_16x16x32_bf16 a[188:191], a[252:255], v[192:195], a[188:191]\n\t"
    "v_exp_f32 v211, v171\n\t"
    "v_mfma_f32_16x16x32_bf16 v[176:179], a[192:195], v[96:99], v[32:35]\n\t"
    "ds_read_b64_tr_b16 a[238:239], v205 offset:4352\n\t"
    "v_cmp_lt_f32 vcc, s71, v196\n\t"
    "v_cvt_pk_bf16_f32 v189, v210, v211\n\t"
    "v_mfma_f32_16x16x32_bf16 v[176:179], a[196:199], v[100:103], v[176:179]\n\t"
    "v_exp_f32 v212, v172\n\t"
    "v_mfma_f32_16x16x32_bf16 v[176:179], a[200:203], v[104:107], v[176:179]\n\t"
    "s_cbranch_vccnz Lstub_c4_0_%=\n\t"
    "Lret_c4_0_%=:\n\t"
    "ds_read_b64_tr_b16 a[240:241], v204 offset:512\n\t"
    "v_add_f32 v48, v48, v196\n\t"
    "v_mfma_f32_16x16x32_bf16 v[176:179], a[204:207], v[108:111], v[176:179]\n\t"
    "v_exp_f32 v213, v173\n\t"
    "v_mfma_f32_16x16x32_bf16 v[180:183], a[208:211], v[96:99], v[32:35]\n\t"
    "ds_read_b64_tr_b16 a[242:243], v204 offset:4608\n\t"
    "v_cvt_pk_bf16_f32 v190, v212, v213\n\t"
    "v_mfma_f32_16x16x32_bf16 v[180:183], a[212:215], v[100:103], v[180:183]\n\t"
    "v_exp_f32 v214, v174\n\t"
    "v_mfma_f32_16x16x32_bf16 v[180:183], a[216:219], v[104:107], v[180:183]\n\t"
    "ds_read_b64_tr_b16 a[244:245], v205 offset:512\n\t"
    "ds_read_b64_tr_b16 a[246:247], v205 offset:4608\n\t"
    "v_mfma_f32_16x16x32_bf16 v[180:183], a[220:223], v[108:111], v[180:183]\n\t"
    "s_waitcnt lgkmcnt(3)\n\t"
    "v_exp_f32 v215, v175\n\t"
    "v_mfma_f32_16x16x32_bf16 a[0:3], a[224:227], v[184:187], a[0:3]\n\t"
    "ds_read_b64_tr_b16 a[248:249], v204 offset:768\n\t"
    "v_cvt_pk_bf16_f32 v191, v214, v215\n\t"
    "v_mfma_f32_16x16x32_bf16 a[4:7], a[228:231], v[184:187], a[4:7]\n\t"
    "ds_read_b64_tr_b16 a[250:251], v204 offset:4864\n\t"
    "ds_read_b64_tr_b16 a[252:253], v205 offset:768\n\t"
    "v_mfma_f32_16x16x32_bf16 a[8:11], a[232:235], v[184:187], a[8:11]\n\t"
    "s_add_u32 s98, s98, 0x68\n\t"
    "v_exp_f32 v208, v176\n\t"
    "v_mfma_f32_16x16x32_bf16 v[196:199], v[54:57], v[188:191], 0\n\t"
    "ds_read_b64_tr_b16 a[254:255], v205 offset:4864\n\t"
    "v_mfma_f32_16x16x32_bf16 a[12:15], a[236:239], v[184:187], a[12:15]\n\t"
    "s_waitcnt lgkmcnt(4)\n\t"
    "s_max_u32 s18, s18, s98\n\t"
    "v_exp_f32 v209, v177\n\t"
    "v_mfma_f32_16x16x32_bf16 a[16:19], a[240:243], v[184:187], a[16:19]\n\t"
    "v_cvt_pk_bf16_f32 v192, v208, v209\n\t"
    "v_mfma_f32_16x16x32_bf16 a[20:23], a[244:247], v[184:187], a[20:23]\n\t"
    "s_waitcnt lgkmcnt(1)\n\t"
    "v_exp_f32 v210, v178\n\t"
    "v_mfma_f32_16x16x32_bf16 a[24:27], a[248:251], v[184:187], a[24:27]\n\t"
    "s_waitcnt lgkmcnt(0)\n\t"
    "buffer_load_dwordx4 v11, s[48:51], s70 offen offset:2048 nt lds\n\t"
    "v_mfma_f32_16x16x32_bf16 a[28:31], a[252:255], v[184:187], a[28:31]\n\t"
    "v_exp_f32 v211, v179\n\t"
    "v_mfma_f32_16x16x32_bf16 v[160:163], a[192:195], v[112:115], v[36:39]\n\t"
    "v_cmp_lt_f32 vcc, s71, v196\n\t"
    "v_cvt_pk_bf16_f32 v193, v210, v211\n\t"
    "v_mfma_f32_16x16x32_bf16 v[160:163], a[196:199], v[116:119], v[160:163]\n\t"
    "v_exp_f32 v212, v180\n\t"
    "v_mfma_f32_16x16x32_bf16 v[160:163], a[200:203], v[120:123], v[160:163]\n\t"
    "s_cbranch_vccnz Lstub_c4_1_%=\n\t"
    "Lret_c4_1_%=:\n\t"
    "v_add_f32 v49, v49, v196\n\t"
    "v_mfma_f32_16x16x32_bf16 v[160:163], a[204:207], v[124:127], v[160:163]\n\t"
    "v_exp_f32 v213, v181\n\t"
    "v_mfma_f32_16x16x32_bf16 v[164:167], a[208:211], v[112:115], v[36:39]\n\t"
    "v_cvt_pk_bf16_f32 v194, v212, v213\n\t"
    "v_mfma_f32_16x16x32_bf16 v[164:167], a[212:215], v[116:119], v[164:167]\n\t"
    "v_exp_f32 v214, v182\n\t"
    "v_mfma_f32_16x16x32_bf16 v[164:167], a[216:219], v[120:123], v[164:167]\n\t"
    "buffer_load_dwordx4 v12, s[48:51], s70 offen offset:3072 nt lds\n\t"
    "v_mfma_f32_16x16x32_bf16 v[164:167], a[220:223], v[124:127], v[164:167]\n\t"
    "s_add_u32 s70, s70, 0x2000\n\t"
    "v_exp_f32 v215, v183\n\t"
    "v_mfma_f32_16x16x32_bf16 a[32:35], a[224:227], v[188:191], a[32:35]\n\t"
    "s_and_b32 s70, s70, 0x1ffff\n\t"
    "s_add_u32 m0, s66, 0xc000\n\t"
    "v_cvt_pk_bf16_f32 v195, v214, v215\n\t"
    "v_mfma_f32_16x16x32_bf16 a[36:39], a[228:231], v[188:191], a[36:39]\n\t"
    "buffer_load_dwordx4 v7, s[44:47], s69 offen nt lds\n\t"
    "v_mfma_f32_16x16x32_bf16 a[40:43], a[232:235], v[188:191], a[40:43]\n\t"
    "v_exp_f32 v208, v160\n\t"
    "v_mfma_f32_16x16x32_bf16 v[196:199], v[54:57], v[192:195], 0\n\t"
    "v_mfma_f32_16x16x32_bf16 a[44:47], a[236:239], v[188:191], a[44:47]\n\t"
    "v_exp_f32 v209, v161\n\t"
    "v_mfma_f32_16x16x32_bf16 a[48:51], a[240:243], v[188:191], a[48:51]\n\t"
    "v_cvt_pk_bf16_f32 v184, v208, v209\n\t"
    "v_mfma_f32_16x16x32_bf16 a[52:55], a[244:247], v[188:191], a[52:55]\n\t"
    "v_exp_f32 v210, v162\n\t"
    "v_mfma_f32_16x16x32_bf16 a[56:59], a[248:251], v[188:191], a[56:59]\n\t"
    "v_mfma_f32_16x16x32_bf16 a[60:63], a[252:255], v[188:191], a[60:63]\n\t"
    "v_exp_f32 v211, v163\n\t"
    "v_mfma_f32_16x16x32_bf16 v[168:171], a[192:195], v[128:131], v[40:43]\n\t"
    "v_cmp_lt_f32 vcc, s71, v196\n\t"
    "v_cvt_pk_bf16_f32 v185, v210, v211\n\t"
    "v_mfma_f32_16x16x32_bf16 v[168:171], a[196:199], v[132:135], v[168:171]\n\t"
    "v_exp_f32 v212, v164\n\t"
    "v_mfma_f32_16x16x32_bf16 v[168:171], a[200:203], v[136:139], v[168:171]\n\t"
    "s_cbranch_vccnz Lstub_c4_2_%=\n\t"
    "Lret_c4_2_%=:\n\t"
    "v_add_f32 v50, v50, v196\n\t"
    "v_mfma_f32_16x16x32_bf16 v[168:171], a[204:207], v[140:143], v[168:171]\n\t"
    "v_exp_f32 v213, v165\n\t"
    "v_mfma_f32_16x16x32_bf16 v[172:175], a[208:211], v[128:131], v[40:43]\n\t"
    "v_cvt_pk_bf16_f32 v186, v212, v213\n\t"
    "v_mfma_f32_16x16x32_bf16 v[172:175], a[212:215], v[132:135], v[172:175]\n\t"
    "v_exp_f32 v214, v166\n\t"
    "v_mfma_f32_16x16x32_bf16 v[172:175], a[216:219], v[136:139], v[172:175]\n\t"
    "buffer_load_dwordx4 v8, s[44:47], s69 offen offset:1024 nt lds\n\t"
    "v_mfma_f32_16x16x32_bf16 v[172:175], a[220:223], v[140:143], v[172:175]\n\t"
    "v_exp_f32 v215, v167\n\t"
    "v_mfma_f32_16x16x32_bf16 a[64:67], a[224:227], v[192:195], a[64:67]\n\t"
    "v_cvt_pk_bf16_f32 v187, v214, v215\n\t"
    "v_mfma_f32_16x16x32_bf16 a[68:71], a[228:231], v[192:195], a[68:71]\n\t"
    "v_mfma_f32_16x16x32_bf16 a[72:75], a[232:235], v[192:195], a[72:75]\n\t"
    "v_exp_f32 v208, v168\n\t"
    "v_mfma_f32_16x16x32_bf16 v[196:199], v[54:57], v[184:187], 0\n\t"
    "v_mfma_f32_16x16x32_bf16 a[76:79], a[236:239], v[192:195], a[76:79]\n\t"
    "v_exp_f32 v209, v169\n\t"
    "v_mfma_f32_16x16x32_bf16 a[80:83], a[240:243], v[192:195], a[80:83]\n\t"
    "v_cvt_pk_bf16_f32 v188, v208, v209\n\t"
    "v_mfma_f32_16x16x32_bf16 a[84:87], a[244:247], v[192:195], a[84:87]\n\t"
    "v_exp_f32 v210, v170\n\t"
    "v_mfma_f32_16x16x32_bf16 a[88:91], a[248:251], v[192:195], a[88:91]\n\t"
    "buffer_load_dwordx4 v9, s[44:47], s69 offen offset:2048 nt lds\n\t"
    "v_mfma_f32_16x16x32_bf16 a[92:95], a[252:255], v[192:195], a[92:95]\n\t"
    "v_exp_f32 v211, v171\n\t"
    "v_mfma_f32_16x16x32_bf16 v[176:179], a[192:195], v[144:147], v[44:47]\n\t"
    "v_cmp_lt_f32 vcc, s71, v196\n\t"
    "v_cvt_pk_bf16_f32 v189, v210, v211\n\t"
    "v_mfma_f32_16x16x32_bf16 v[176:179], a[196:199], v[148:151], v[176:179]\n\t"
    "v_exp_f32 v212, v172\n\t"
    "v_mfma_f32_16x16x32_bf16 v[176:179], a[200:203], v[152:155], v[176:179]\n\t"
    "s_cbranch_vccnz Lstub_c4_3_%=\n\t"
    "Lret_c4_3_%=:\n\t"
    "ds_read_b128 a[192:195], v1 offset:0\n\t"
    "v_add_f32 v51, v51, v196\n\t"
    "v_mfma_f32_16x16x32_bf16 v[176:179], a[204:207], v[156:159], v[176:179]\n\t"
    "v_exp_f32 v213, v173\n\t"
    "v_mfma_f32_16x16x32_bf16 v[180:183], a[208:211], v[144:147], v[44:47]\n\t"
    "ds_read_b128 a[196:199], v2 offset:0\n\t"
    "v_cvt_pk_bf16_f32 v190, v212, v213\n\t"
    "v_mfma_f32_16x16x32_bf16 v[180:183], a[212:215], v[148:151], v[180:183]\n\t"
    "v_exp_f32 v214, v174\n\t"
    "v_mfma_f32_16x16x32_bf16 v[180:183], a[216:219], v[152:155], v[180:183]\n\t"
    "ds_read_b128 a[200:203], v3 offset:0\n\t"
    "ds_read_b128 a[204:207], v4 offset:0\n\t"
    "v_mfma_f32_16x16x32_bf16 v[180:183], a[220:223], v[156:159], v[180:183]\n\t"
    "v_exp_f32 v215, v175\n\t"
    "v_mfma_f32_16x16x32_bf16 a[96:99], a[224:227], v[184:187], a[96:99]\n\t"
    "ds_read_b128 a[208:211], v1 offset:4096\n\t"
    "v_cvt_pk_bf16_f32 v191, v214, v215\n\t"
    "v_mfma_f32_16x16x32_bf16 a[100:103], a[228:231], v[184:187], a[100:103]\n\t"
    "ds_read_b128 a[212:215], v2 offset:4096\n\t"
    "ds_read_b128 a[216:219], v3 offset:4096\n\t"
    "v_mfma_f32_16x16x32_bf16 a[104:107], a[232:235], v[184:187], a[104:107]\n\t"
    "v_exp_f32 v208, v176\n\t"
    "v_mfma_f32_16x16x32_bf16 v[196:199], v[54:57], v[188:191], 0\n\t"
    "ds_read_b128 a[220:223], v4 offset:4096\n\t"
    "v_mfma_f32_16x16x32_bf16 a[108:111], a[236:239], v[184:187], a[108:111]\n\t"
    "v_exp_f32 v209, v177\n\t"
    "v_mfma_f32_16x16x32_bf16 a[112:115], a[240:243], v[184:187], a[112:115]\n\t"
    "v_cvt_pk_bf16_f32 v192, v208, v209\n\t"
    "v_mfma_f32_16x16x32_bf16 a[116:119], a[244:247], v[184:187], a[116:119]\n\t"
    "v_exp_f32 v210, v178\n\t"
    "v_mfma_f32_16x16x32_bf16 a[120:123], a[248:251], v[184:187], a[120:123]\n\t"
    "buffer_load_dwordx4 v10, s[44:47], s69 offen offset:3072 nt lds\n\t"
    "v_mfma_f32_16x16x32_bf16 a[124:127], a[252:255], v[184:187], a[124:127]\n\t"
    "s_waitcnt lgkmcnt(0)\n\t"
    "s_add_u32 s69, s69, 0x2000\n\t"
    "v_exp_f32 v211, v179\n\t"
    "v_mfma_f32_16x16x32_bf16 v[160:163], a[192:195], v[64:67], v[24:27]\n\t"
    "s_and_b32 s69, s69, 0x1ffff\n\t"
    "v_cmp_lt_f32 vcc, s71, v196\n\t"
    "v_cvt_pk_bf16_f32 v193, v210, v211\n\t"
    "v_mfma_f32_16x16x32_bf16 v[160:163], a[196:199], v[68:71], v[160:163]\n\t"
    "v_exp_f32 v212, v180\n\t"
    "v_mfma_f32_16x16x32_bf16 v[160:163], a[200:203], v[72:75], v[160:163]\n\t"
    "s_cbranch_vccnz Lstub_c4_4_%=\n\t"
    "Lret_c4_4_%=:\n\t"
    "v_add_f32 v52, v52, v196\n\t"
    "v_mfma_f32_16x16x32_bf16 v[160:163], a[204:207], v[76:79], v[160:163]\n\t"
    "v_exp_f32 v213, v181\n\t"
    "v_mfma_f32_16x16x32_bf16 v[164:167], a[208:211], v[64:67], v[24:27]\n\t"
    "v_cvt_pk_bf16_f32 v194, v212, v213\n\t"
    "v_mfma_f32_16x16x32_bf16 v[164:167], a[212:215], v[68:71], v[164:167]\n\t"
    "v_exp_f32 v214, v182\n\t"
    "v_mfma_f32_16x16x32_bf16 v[164:167], a[216:219], v[72:75], v[164:167]\n\t"
    "v_mfma_f32_16x16x32_bf16 v[164:167], a[220:223], v[76:79], v[164:167]\n\t"
    "v_exp_f32 v215, v183\n\t"
    "v_mfma_f32_16x16x32_bf16 a[128:131], a[224:227], v[188:191], a[128:131]\n\t"
    "s_memrealtime s[98:99]\n\t"
    "s_add_u32 m0, s66, 0xe000\n\t"
    "v_cvt_pk_bf16_f32 v195, v214, v215\n\t"
    "v_mfma_f32_16x16x32_bf16 a[132:135], a[228:231], v[188:191], a[132:135]\n\t"
    "buffer_load_dwordx4 v11, s[48:51], s70 offen nt lds\n\t"
    "v_mfma_f32_16x16x32_bf16 a[136:139], a[232:235], v[188:191], a[136:139]\n\t"
    "v_exp_f32 v208, v160\n\t"
    "v_mfma_f32_16x16x32_bf16 v[196:199], v[54:57], v[192:195], 0\n\t"
    "v_mfma_f32_16x16x32_bf16 a[140:143], a[236:239], v[188:191], a[140:143]\n\t"
    "v_exp_f32 v209, v161\n\t"
    "v_mfma_f32_16x16x32_bf16 a[144:147], a[240:243], v[188:191], a[144:147]\n\t"
    "v_cvt_pk_bf16_f32 v184, v208, v209\n\t"
    "v_mfma_f32_16x16x32_bf16 a[148:151], a[244:247], v[188:191], a[148:151]\n\t"
    "v_exp_f32 v210, v162\n\t"
    "v_mfma_f32_16x16x32_bf16 a[152:155], a[248:251], v[188:191], a[152:155]\n\t"
    "v_mfma_f32_16x16x32_bf16 a[156:159], a[252:255], v[188:191], a[156:159]\n\t"
    "v_exp_f32 v211, v163\n\t"
    "v_mfma_f32_16x16x32_bf16 v[168:171], a[192:195], v[80:83], v[28:31]\n\t"
    "v_cmp_lt_f32 vcc, s71, v196\n\t"
    "v_cvt_pk_bf16_f32 v185, v210, v211\n\t"
    "v_mfma_f32_16x16x32_bf16 v[168:171], a[196:199], v[84:87], v[168:171]\n\t"
    "s_waitcnt lgkmcnt(0)\n\t"
    "v_exp_f32 v212, v164\n\t"
    "v_mfma_f32_16x16x32_bf16 v[168:171], a[200:203], v[88:91], v[168:171]\n\t"
    "s_cbranch_vccnz Lstub_c4_5_%=\n\t"
    "Lret_c4_5_%=:\n\t"
    "v_add_f32 v53, v53, v196\n\t"
    "v_mfma_f32_16x16x32_bf16 v[168:171], a[204:207], v[92:95], v[168:171]\n\t"
    "s_waitcnt vmcnt(17)\n\t"
    "v_exp_f32 v213, v165\n\t"
    "v_mfma_f32_16x16x32_bf16 v[172:175], a[208:211], v[80:83], v[28:31]\n\t"
    "s_sub_u32 s19, s18, s98\n\t"
    "v_cvt_pk_bf16_f32 v186, v212, v213\n\t"
    "v_mfma_f32_16x16x32_bf16 v[172:175], a[212:215], v[84:87], v[172:175]\n\t"
    "s_barrier\n\t"
    "v_exp_f32 v214, v166\n\t"
    "v_mfma_f32_16x16x32_bf16 v[172:175], a[216:219], v[88:91], v[172:175]\n\t"
    "s_cmp_gt_i32 s19, 0\n\t"
    "s_cbranch_scc1 Lpace5_%=\n\t"
    "Lpaced5_%=:\n\t"
    "v_mfma_f32_16x16x32_bf16 v[172:175], a[220:223], v[92:95], v[172:175]\n\t"
    "s_add_u32 s18, s18, 0x72\n\t"
    "v_exp_f32 v215, v167\n\t"
    "v_mfma_f32_16x16x32_bf16 a[160:163], a[224:227], v[192:195], a[160:163]\n\t"
    "v_cvt_pk_bf16_f32 v187, v214, v215\n\t"
    "v_mfma_f32_16x16x32_bf16 a[164:167], a[228:231], v[192:195], a[164:167]\n\t"
    "ds_read_b64_tr_b16 a[224:225], v5 offset:0\n\t"
    "ds_read_b64_tr_b16 a[226:227], v5 offset:4096\n\t"
    "v_mfma_f32_16x16x32_bf16 a[168:171], a[232:235], v[192:195], a[168:171]\n\t"
    "v_exp_f32 v208, v168\n\t"
    "v_mfma_f32_16x16x32_bf16 v[196:199], v[54:57], v[184:187], 0\n\t"
    "ds_read_b64_tr_b16 a[228:229], v6 offset:0\n\t"
    "ds_read_b64_tr_b16 a[230:231], v6 offset:4096\n\t"
    "v_mfma_f32_16x16x32_bf16 a[172:175], a[236:239], v[192:195], a[172:175]\n\t"
    "v_exp_f32 v209, v169\n\t"
    "v_mfma_f32_16x16x32_bf16 a[176:179], a[240:243], v[192:195], a[176:179]\n\t"
    "ds_read_b64_tr_b16 a[232:233], v5 offset:256\n\t"
    "v_cvt_pk_bf16_f32 v188, v208, v209\n\t"
    "v_mfma_f32_16x16x32_bf16 a[180:183], a[244:247], v[192:195], a[180:183]\n\t"
    "v_exp_f32 v210, v170\n\t"
    "v_mfma_f32_16x16x32_bf16 a[184:187], a[248:251], v[192:195], a[184:187]\n\t"
    "ds_read_b64_tr_b16 a[234:235], v5 offset:4352\n\t"
    "ds_read_b64_tr_b16 a[236:237], v6 offset:256\n\t"
    "v_mfma_f32_16x16x32_bf16 a[188:191], a[252:255], v[192:195], a[188:191]\n\t"
    "v_exp_f32 v211, v171\n\t"
    "v_mfma_f32_16x16x32_bf16 v[176:179], a[192:195], v[96:99], v[32:35]\n\t"
    "ds_read_b64_tr_b16 a[238:239], v6 offset:4352\n\t"
    "v_cmp_lt_f32 vcc, s71, v196\n\t"
    "v_cvt_pk_bf16_f32 v189, v210, v211\n\t"
    "v_mfma_f32_16x16x32_bf16 v[176:179], a[196:199], v[100:103], v[176:179]\n\t"
    "v_exp_f32 v212, v172\n\t"
    "v_mfma_f32_16x16x32_bf16 v[176:179], a[200:203], v[104:107], v[176:179]\n\t"
    "s_cbranch_vccnz Lstub_c5_0_%=\n\t"
    "Lret_c5_0_%=:\n\t"
    "ds_read_b64_tr_b16 a[240:241], v5 offset:512\n\t"
    "v_add_f32 v48, v48, v196\n\t"
    "v_mfma_f32_16x16x32_bf16 v[176:179], a[204:207], v[108:111], v[176:179]\n\t"
    "v_exp_f32 v213, v173\n\t"
    "v_mfma_f32_16x16x32_bf16 v[180:183], a[208:211], v[96:99], v[32:35]\n\t"
    "ds_read_b64_tr_b16 a[242:243], v5 offset:4608\n\t"
    "v_cvt_pk_bf16_f32 v190, v212, v213\n\t"
    "v_mfma_f32_16x16x32_bf16 v[180:183], a[212:215], v[100:103], v[180:183]\n\t"
    "v_exp_f32 v214, v174\n\t"
    "v_mfma_f32_16x16x32_bf16 v[180:183], a[216:219], v[104:107], v[180:183]\n\t"
    "ds_read_b64_tr_b16 a[244:245], v6 offset:512\n\t"
    "ds_read_b64_tr_b16 a[246:247], v6 offset:4608\n\t"
    "v_mfma_f32_16x16x32_bf16 v[180:183], a[220:223], v[108:111], v[180:183]\n\t"
    "s_waitcnt lgkmcnt(3)\n\t"
    "v_exp_f32 v215, v175\n\t"
    "v_mfma_f32_16x16x32_bf16 a[0:3], a[224:227], v[184:187], a[0:3]\n\t"
    "ds_read_b64_tr_b16 a[248:249], v5 offset:768\n\t"
    "v_cvt_pk_bf16_f32 v191, v214, v215\n\t"
    "v_mfma_f32_16x16x32_bf16 a[4:7], a[228:231], v[184:187], a[4:7]\n\t"
    "ds_read_b64_tr_b16 a[250:251], v5 offset:4864\n\t"
    "ds_read_b64_tr_b16 a[252:253], v6 offset:768\n\t"
    "v_mfma_f32_16x16x32_bf16 a[8:11], a[232:235], v[184:187], a[8:11]\n\t"
    "s_add_u32 s98, s98, 0x68\n\t"
    "v_exp_f32 v208, v176\n\t"
    "v_mfma_f32_16x16x32_bf16 v[196:199], v[54:57], v[188:191], 0\n\t"
    "ds_read_b64_tr_b16 a[254:255], v6 offset:4864\n\t"
    "v_mfma_f32_16x16x32_bf16 a[12:15], a[236:239], v[184:187], a[12:15]\n\t"
    "s_waitcnt lgkmcnt(4)\n\t"
    "s_max_u32 s18, s18, s98\n\t"
    "v_exp_f32 v209, v177\n\t"
    "v_mfma_f32_16x16x32_bf16 a[16:19], a[240:243], v[184:187], a[16:19]\n\t"
    "v_cvt_pk_bf16_f32 v192, v208, v209\n\t"
    "v_mfma_f32_16x16x32_bf16 a[20:23], a[244:247], v[184:187], a[20:23]\n\t"
    "s_waitcnt lgkmcnt(1)\n\t"
    "v_exp_f32 v210, v178\n\t"
    "v_mfma_f32_16x16x32_bf16 a[24:27], a[248:251], v[184:187], a[24:27]\n\t"
    "s_waitcnt lgkmcnt(0)\n\t"
    "buffer_load_dwordx4 v12, s[48:51], s70 offen offset:1024 nt lds\n\t"
    "v_mfma_f32_16x16x32_bf16 a[28:31], a[252:255], v[184:187], a[28:31]\n\t"
    "v_exp_f32 v211, v179\n\t"
    "v_mfma_f32_16x16x32_bf16 v[160:163], a[192:195], v[112:115], v[36:39]\n\t"
    "v_cmp_lt_f32 vcc, s71, v196\n\t"
    "v_cvt_pk_bf16_f32 v193, v210, v211\n\t"
    "v_mfma_f32_16x16x32_bf16 v[160:163], a[196:199], v[116:119], v[160:163]\n\t"
    "v_exp_f32 v212, v180\n\t"
    "v_mfma_f32_16x16x32_bf16 v[160:163], a[200:203], v[120:123], v[160:163]\n\t"
    "s_cbranch_vccnz Lstub_c5_1_%=\n\t"
    "Lret_c5_1_%=:\n\t"
    "v_add_f32 v49, v49, v196\n\t"
    "v_mfma_f32_16x16x32_bf16 v[160:163], a[204:207], v[124:127], v[160:163]\n\t"
    "v_exp_f32 v213, v181\n\t"
    "v_mfma_f32_16x16x32_bf16 v[164:167], a[208:211], v[112:115], v[36:39]\n\t"
    "v_cvt_pk_bf16_f32 v194, v212, v213\n\t"
    "v_mfma_f32_16x16x32_bf16 v[164:167], a[212:215], v[116:119], v[164:167]\n\t"
    "v_exp_f32 v214, v182\n\t"
    "v_mfma_f32_16x16x32_bf16 v[164:167], a[216:219], v[120:123], v[164:167]\n\t"
    "buffer_load_dwordx4 v11, s[48:51], s70 offen offset:2048 nt lds\n\t"
    "v_mfma_f32_16x16x32_bf16 v[164:167], a[220:223], v[124:127], v[164:167]\n\t"
    "v_exp_f32 v215, v183\n\t"
    "v_mfma_f32_16x16x32_bf16 a[32:35], a[224:227], v[188:191], a[32:35]\n\t"
    "v_cvt_pk_bf16_f32 v195, v214, v215\n\t"
    "v_mfma_f32_16x16x32_bf16 a[36:39], a[228:231], v[188:191], a[36:39]\n\t"
    "buffer_load_dwordx4 v12, s[48:51], s70 offen offset:3072 nt lds\n\t"
    "s_getpc_b64 s[4:5]\n\t"
    "Lrt5_%=:\n\t"
    "s_mov_b32 s6, Lend_%=-Lrt5_%=\n\t"
    "s_mov_b32 s7, 0x20000\n\t"
    "s_lshr_b32 s8, %1, 3\n\t"
    "s_lshl_b32 s8, s8, 2\n\t"
    "s_add_u32 s8, s8, s56\n\t"
    "s_lshl_b32 s8, s8, 9\n\t"
    "v_cmp_gt_u32 s[76:77], 4, v0\n\t"
    "v_lshlrev_b32 v229, 7, v0\n\t"
    "v_mov_b32 v230, 0x40000000\n\t"
    "v_cndmask_b32 v229, v230, v229, s[76:77]\n\t"
    "buffer_load_dword v23, v229, s[4:7], s8 offen\n\t"
    "v_mfma_f32_16x16x32_bf16 a[40:43], a[232:235], v[188:191], a[40:43]\n\t"
    "s_add_u32 s70, s70, 0x2000\n\t"
    "v_exp_f32 v208, v160\n\t"
    "v_mfma_f32_16x16x32_bf16 v[196:199], v[54:57], v[192:195], 0\n\t"
    "s_and_b32 s70, s70, 0x1ffff\n\t"
    "v_mfma_f32_16x16x32_bf16 a[44:47], a[236:239], v[188:191], a[44:47]\n\t"
    "v_exp_f32 v209, v161\n\t"
    "v_mfma_f32_16x16x32_bf16 a[48:51], a[240:243], v[188:191], a[48:51]\n\t"
    "v_cvt_pk_bf16_f32 v184, v208, v209\n\t"
    "v_mfma_f32_16x16x32_bf16 a[52:55], a[244:247], v[188:191], a[52:55]\n\t"
    "v_exp_f32 v210, v162\n\t"
    "v_mfma_f32_16x16x32_bf16 a[56:59], a[248:251], v[188:191], a[56:59]\n\t"
    "v_mfma_f32_16x16x32_bf16 a[60:63], a[252:255], v[188:191], a[60:63]\n\t"
    "v_exp_f32 v211, v163\n\t"
    "v_mfma_f32_16x16x32_bf16 v[168:171], a[192:195], v[128:131], v[40:43]\n\t"
    "v_cmp_lt_f32 vcc, s71, v196\n\t"
    "v_cvt_pk_bf16_f32 v185, v210, v211\n\t"
    "v_mfma_f32_16x16x32_bf16 v[168:171], a[196:199], v[132:135], v[168:171]\n\t"
    "v_exp_f32 v212, v164\n\t"
    "v_mfma_f32_16x16x32_bf16 v[168:171], a[200:203], v[136:139], v[168:171]\n\t"
    "s_cbranch_vccnz Lstub_c5_2_%=\n\t"
    "Lret_c5_2_%=:\n\t"
    "v_add_f32 v50, v50, v196\n\t"
    "v_mfma_f32_16x16x32_bf16 v[168:171], a[204:207], v[140:143], v[168:171]\n\t"
    "v_exp_f32 v213, v165\n\t"
    "v_mfma_f32_16x16x32_bf16 v[172:175], a[208:211], v[128:131], v[40:43]\n\t"
    "v_cvt_pk_bf16_f32 v186, v212, v213\n\t"
    "v_mfma_f32_16x16x32_bf16 v[172:175], a[212:215], v[132:135], v[172:175]\n\t"
    "s_add_u32 m0, s97, 0\n\t"
    "v_exp_f32 v214, v166\n\t"
    "v_mfma_f32_16x16x32_bf16 v[172:175], a[216:219], v[136:139], v[172:175]\n\t"
    "buffer_load_dwordx4 v7, s[44:47], s69 offen nt lds\n\t"
    "v_mfma_f32_16x16x32_bf16 v[172:175], a[220:223], v[140:143], v[172:175]\n\t"
    "v_exp_f32 v215, v167\n\t"
    "v_mfma_f32_16x16x32_bf16 a[64:67], a[224:227], v[192:195], a[64:67]\n\t"
    "v_cvt_pk_bf16_f32 v187, v214, v215\n\t"
    "v_mfma_f32_16x16x32_bf16 a[68:71], a[228:231], v[192:195], a[68:71]\n\t"
    "v_mfma_f32_16x16x32_bf16 a[72:75], a[232:235], v[192:195], a[72:75]\n\t"
    "v_exp_f32 v208, v168\n\t"
    "v_mfma_f32_16x16x32_bf16 v[196:199], v[54:57], v[184:187], 0\n\t"
    "v_mfma_f32_16x16x32_bf16 a[76:79], a[236:239], v[192:195], a[76:79]\n\t"
    "v_exp_f32 v209, v169\n\t"
    "v_mfma_f32_16x16x32_bf16 a[80:83], a[240:243], v[192:195], a[80:83]\n\t"
    "v_cvt_pk_bf16_f32 v188, v208, v209\n\t"
    "v_mfma_f32_16x16x32_bf16 a[84:87], a[244:247], v[192:195], a[84:87]\n\t"
    "v_exp_f32 v210, v170\n\t"
    "v_mfma_f32_16x16x32_bf16 a[88:91], a[248:251], v[192:195], a[88:91]\n\t"
    "v_mfma_f32_16x16x32_bf16 a[92:95], a[252:255], v[192:195], a[92:95]\n\t"
    "v_exp_f32 v211, v171\n\t"
    "v_mfma_f32_16x16x32_bf16 v[176:179], a[192:195], v[144:147], v[44:47]\n\t"
    "v_cmp_lt_f32 vcc, s71, v196\n\t"
    "v_cvt_pk_bf16_f32 v189, v210, v211\n\t"
    "v_mfma_f32_16x16x32_bf16 v[176:179], a[196:199], v[148:151], v[176:179]\n\t"
    "v_exp_f32 v212, v172\n\t"
    "v_mfma_f32_16x16x32_bf16 v[176:179], a[200:203], v[152:155], v[176:179]\n\t"
    "s_cbranch_vccnz Lstub_c5_3_%=\n\t"
    "Lret_c5_3_%=:\n\t"
    "ds_read_b128 a[192:195], v1 offset:16384\n\t"
    "v_add_f32 v51, v51, v196\n\t"
    "v_mfma_f32_16x16x32_bf16 v[176:179], a[204:207], v[156:159], v[176:179]\n\t"
    "v_exp_f32 v213, v173\n\t"
    "v_mfma_f32_16x16x32_bf16 v[180:183], a[208:211], v[144:147], v[44:47]\n\t"
    "ds_read_b128 a[196:199], v2 offset:16384\n\t"
    "v_cvt_pk_bf16_f32 v190, v212, v213\n\t"
    "v_mfma_f32_16x16x32_bf16 v[180:183], a[212:215], v[148:151], v[180:183]\n\t"
    "v_exp_f32 v214, v174\n\t"
    "v_mfma_f32_16x16x32_bf16 v[180:183], a[216:219], v[152:155], v[180:183]\n\t"
    "ds_read_b128 a[200:203], v3 offset:16384\n\t"
    "ds_read_b128 a[204:207], v4 offset:16384\n\t"
    "v_mfma_f32_16x16x32_bf16 v[180:183], a[220:223], v[156:159], v[180:183]\n\t"
    "v_exp_f32 v215, v175\n\t"
    "v_mfma_f32_16x16x32_bf16 a[96:99], a[224:227], v[184:187], a[96:99]\n\t"
    "ds_read_b128 a[208:211], v1 offset:20480\n\t"
    "v_cvt_pk_bf16_f32 v191, v214, v215\n\t"
    "v_mfma_f32_16x16x32_bf16 a[100:103], a[228:231], v[184:187], a[100:103]\n\t"
    "ds_read_b128 a[212:215], v2 offset:20480\n\t"
    "ds_read_b128 a[216:219], v3 offset:20480\n\t"
    "v_mfma_f32_16x16x32_bf16 a[104:107], a[232:235], v[184:187], a[104:107]\n\t"
    "v_exp_f32 v208, v176\n\t"
    "v_mfma_f32_16x16x32_bf16 v[196:199], v[54:57], v[188:191], 0\n\t"
    "ds_read_b128 a[220:223], v4 offset:20480\n\t"
    "v_mfma_f32_16x16x32_bf16 a[108:111], a[236:239], v[184:187], a[108:111]\n\t"
    "v_exp_f32 v209, v177\n\t"
    "v_mfma_f32_16x16x32_bf16 a[112:115], a[240:243], v[184:187], a[112:115]\n\t"
    "v_cvt_pk_bf16_f32 v192, v208, v209\n\t"
    "v_mfma_f32_16x16x32_bf16 a[116:119], a[244:247], v[184:187], a[116:119]\n\t"
    "v_exp_f32 v210, v178\n\t"
    "v_mfma_f32_16x16x32_bf16 a[120:123], a[248:251], v[184:187], a[120:123]\n\t"
    "buffer_load_dwordx4 v8, s[44:47], s69 offen offset:1024 nt lds\n\t"
    "v_mfma_f32_16x16x32_bf16 a[124:127], a[252:255], v[184:187], a[124:127]\n\t"
    "s_waitcnt lgkmcnt(0)\n\t"
    "v_exp_f32 v211, v179\n\t"
    "v_mfma_f32_16x16x32_bf16 v[160:163], a[192:195], v[64:67], v[24:27]\n\t"
    "v_cmp_lt_f32 vcc, s71, v196\n\t"
    "v_cvt_pk_bf16_f32 v193, v210, v211\n\t"
    "v_mfma_f32_16x16x32_bf16 v[160:163], a[196:199], v[68:71], v[160:163]\n\t"
    "v_exp_f32 v212, v180\n\t"
    "v_mfma_f32_16x16x32_bf16 v[160:163], a[200:203], v[72:75], v[160:163]\n\t"
    "s_cbranch_vccnz Lstub_c5_4_%=\n\t"
    "Lret_c5_4_%=:\n\t"
    "v_add_f32 v52, v52, v196\n\t"
    "v_mfma_f32_16x16x32_bf16 v[160:163], a[204:207], v[76:79], v[160:163]\n\t"
    "v_exp_f32 v213, v181\n\t"
    "v_mfma_f32_16x16x32_bf16 v[164:167], a[208:211], v[64:67], v[24:27]\n\t"
    "v_cvt_pk_bf16_f32 v194, v212, v213\n\t"
    "v_mfma_f32_16x16x32_bf16 v[164:167], a[212:215], v[68:71], v[164:167]\n\t"
    "v_exp_f32 v214, v182\n\t"
    "v_mfma_f32_16x16x32_bf16 v[164:167], a[216:219], v[72:75], v[164:167]\n\t"
    "buffer_load_dwordx4 v9, s[44:47], s69 offen offset:2048 nt lds\n\t"
    "v_mfma_f32_16x16x32_bf16 v[164:167], a[220:223], v[76:79], v[164:167]\n\t"
    "v_exp_f32 v215, v183\n\t"
    "v_mfma_f32_16x16x32_bf16 a[128:131], a[224:227], v[188:191], a[128:131]\n\t"
    "s_memrealtime s[98:99]\n\t"
    "v_cvt_pk_bf16_f32 v195, v214, v215\n\t"
    "v_mfma_f32_16x16x32_bf16 a[132:135], a[228:231], v[188:191], a[132:135]\n\t"
    "v_mfma_f32_16x16x32_bf16 a[136:139], a[232:235], v[188:191], a[136:139]\n\t"
    "v_exp_f32 v208, v160\n\t"
    "v_mfma_f32_16x16x32_bf16 v[196:199], v[54:57], v[192:195], 0\n\t"
    "v_mfma_f32_16x16x32_bf16 a[140:143], a[236:239], v[188:191], a[140:143]\n\t"
    "v_exp_f32 v209, v161\n\t"
    "v_mfma_f32_16x16x32_bf16 a[144:147], a[240:243], v[188:191], a[144:147]\n\t"
    "v_cvt_pk_bf16_f32 v184, v208, v209\n\t"
    "v_mfma_f32_16x16x32_bf16 a[148:151], a[244:247], v[188:191], a[148:151]\n\t"
    "v_exp_f32 v210, v162\n\t"
    "v_mfma_f32_16x16x32_bf16 a[152:155], a[248:251], v[188:191], a[152:155]\n\t"
    "buffer_load_dwordx4 v10, s[44:47], s69 offen offset:3072 nt lds\n\t"
    "v_mfma_f32_16x16x32_bf16 a[156:159], a[252:255], v[188:191], a[156:159]\n\t"
    "s_add_u32 s69, s69, 0x2000\n\t"
    "v_exp_f32 v211, v163\n\t"
    "v_mfma_f32_16x16x32_bf16 v[168:171], a[192:195], v[80:83], v[28:31]\n\t"
    "s_and_b32 s69, s69, 0x1ffff\n\t"
    "v_cmp_lt_f32 vcc, s71, v196\n\t"
    "v_cvt_pk_bf16_f32 v185, v210, v211\n\t"
    "v_mfma_f32_16x16x32_bf16 v[168:171], a[196:199], v[84:87], v[168:171]\n\t"
    "s_waitcnt lgkmcnt(0)\n\t"
    "v_exp_f32 v212, v164\n\t"
    "v_mfma_f32_16x16x32_bf16 v[168:171], a[200:203], v[88:91], v[168:171]\n\t"
    "s_cbranch_vccnz Lstub_c5_5_%=\n\t"
    "Lret_c5_5_%=:\n\t"
    "v_add_f32 v53, v53, v196\n\t"
    "v_mfma_f32_16x16x32_bf16 v[168:171], a[204:207], v[92:95], v[168:171]\n\t"
    "s_waitcnt vmcnt(17)\n\t"
    "v_exp_f32 v213, v165\n\t"
    "v_mfma_f32_16x16x32_bf16 v[172:175], a[208:211], v[80:83], v[28:31]\n\t"
    "s_sub_u32 s19, s18, s98\n\t"
    "v_cvt_pk_bf16_f32 v186, v212, v213\n\t"
    "v_mfma_f32_16x16x32_bf16 v[172:175], a[212:215], v[84:87], v[172:175]\n\t"
    "s_barrier\n\t"
    "v_exp_f32 v214, v166\n\t"
    "v_mfma_f32_16x16x32_bf16 v[172:175], a[216:219], v[88:91], v[172:175]\n\t"
    "s_cmp_gt_i32 s19, 0\n\t"
    "s_cbranch_scc1 Lpace6_%=\n\t"
    "Lpaced6_%=:\n\t"
    "v_mfma_f32_16x16x32_bf16 v[172:175], a[220:223], v[92:95], v[172:175]\n\t"
    "s_add_u32 s18, s18, 0x72\n\t"
    "v_exp_f32 v215, v167\n\t"
    "v_mfma_f32_16x16x32_bf16 a[160:163], a[224:227], v[192:195], a[160:163]\n\t"
    "v_cvt_pk_bf16_f32 v187, v214, v215\n\t"
    "v_mfma_f32_16x16x32_bf16 a[164:167], a[228:231], v[192:195], a[164:167]\n\t"
    "ds_read_b64_tr_b16 a[224:225], v5 offset:16384\n\t"
    "ds_read_b64_tr_b16 a[226:227], v5 offset:20480\n\t"
    "v_mfma_f32_16x16x32_bf16 a[168:171], a[232:235], v[192:195], a[168:171]\n\t"
    "v_exp_f32 v208, v168\n\t"
    "v_mfma_f32_16x16x32_bf16 v[196:199], v[54:57], v[184:187], 0\n\t"
    "ds_read_b64_tr_b16 a[228:229], v6 offset:16384\n\t"
    "ds_read_b64_tr_b16 a[230:231], v6 offset:20480\n\t"
    "v_mfma_f32_16x16x32_bf16 a[172:175], a[236:239], v[192:195], a[172:175]\n\t"
    "v_exp_f32 v209, v169\n\t"
    "v_mfma_f32_16x16x32_bf16 a[176:179], a[240:243], v[192:195], a[176:179]\n\t"
    "ds_read_b64_tr_b16 a[232:233], v5 offset:16640\n\t"
    "v_cvt_pk_bf16_f32 v188, v208, v209\n\t"
    "v_mfma_f32_16x16x32_bf16 a[180:183], a[244:247], v[192:195], a[180:183]\n\t"
    "v_exp_f32 v210, v170\n\t"
    "v_mfma_f32_16x16x32_bf16 a[184:187], a[248:251], v[192:195], a[184:187]\n\t"
    "ds_read_b64_tr_b16 a[234:235], v5 offset:20736\n\t"
    "ds_read_b64_tr_b16 a[236:237], v6 offset:16640\n\t"
    "v_mfma_f32_16x16x32_bf16 a[188:191], a[252:255], v[192:195], a[188:191]\n\t"
    "v_exp_f32 v211, v171\n\t"
    "v_mfma_f32_16x16x32_bf16 v[176:179], a[192:195], v[96:99], v[32:35]\n\t"
    "ds_read_b64_tr_b16 a[238:239], v6 offset:20736\n\t"
    "v_cmp_lt_f32 vcc, s71, v196\n\t"
    "v_cvt_pk_bf16_f32 v189, v210, v211\n\t"
    "v_mfma_f32_16x16x32_bf16 v[176:179], a[196:199], v[100:103], v[176:179]\n\t"
    "v_exp_f32 v212, v172\n\t"
    "v_mfma_f32_16x16x32_bf16 v[176:179], a[200:203], v[104:107], v[176:179]\n\t"
    "s_cbranch_vccnz Lstub_c6_0_%=\n\t"
    "Lret_c6_0_%=:\n\t"
    "ds_read_b64_tr_b16 a[240:241], v5 offset:16896\n\t"
    "v_add_f32 v48, v48, v196\n\t"
    "v_mfma_f32_16x16x32_bf16 v[176:179], a[204:207], v[108:111], v[176:179]\n\t"
    "v_exp_f32 v213, v173\n\t"
    "v_mfma_f32_16x16x32_bf16 v[180:183], a[208:211], v[96:99], v[32:35]\n\t"
    "ds_read_b64_tr_b16 a[242:243], v5 offset:20992\n\t"
    "v_cvt_pk_bf16_f32 v190, v212, v213\n\t"
    "v_mfma_f32_16x16x32_bf16 v[180:183], a[212:215], v[100:103], v[180:183]\n\t"
    "v_exp_f32 v214, v174\n\t"
    "v_mfma_f32_16x16x32_bf16 v[180:183], a[216:219], v[104:107], v[180:183]\n\t"
    "ds_read_b64_tr_b16 a[244:245], v6 offset:16896\n\t"
    "ds_read_b64_tr_b16 a[246:247], v6 offset:20992\n\t"
    "v_mfma_f32_16x16x32_bf16 v[180:183], a[220:223], v[108:111], v[180:183]\n\t"
    "s_waitcnt lgkmcnt(3)\n\t"
    "v_exp_f32 v215, v175\n\t"
    "v_mfma_f32_16x16x32_bf16 a[0:3], a[224:227], v[184:187], a[0:3]\n\t"
    "ds_read_b64_tr_b16 a[248:249], v5 offset:17152\n\t"
    "v_cvt_pk_bf16_f32 v191, v214, v215\n\t"
    "v_mfma_f32_16x16x32_bf16 a[4:7], a[228:231], v[184:187], a[4:7]\n\t"
    "ds_read_b64_tr_b16 a[250:251], v5 offset:21248\n\t"
    "ds_read_b64_tr_b16 a[252:253], v6 offset:17152\n\t"
    "v_mfma_f32_16x16x32_bf16 a[8:11], a[232:235], v[184:187], a[8:11]\n\t"
    "s_add_u32 s98, s98, 0x68\n\t"
    "v_exp_f32 v208, v176\n\t"
    "v_mfma_f32_16x16x32_bf16 v[196:199], v[54:57], v[188:191], 0\n\t"
    "ds_read_b64_tr_b16 a[254:255], v6 offset:21248\n\t"
    "v_mfma_f32_16x16x32_bf16 a[12:15], a[236:239], v[184:187], a[12:15]\n\t"
    "s_waitcnt lgkmcnt(4)\n\t"
    "s_max_u32 s18, s18, s98\n\t"
    "v_exp_f32 v209, v177\n\t"
    "v_mfma_f32_16x16x32_bf16 a[16:19], a[240:243], v[184:187], a[16:19]\n\t"
    "v_cvt_pk_bf16_f32 v192, v208, v209\n\t"
    "v_mfma_f32_16x16x32_bf16 a[20:23], a[244:247], v[184:187], a[20:23]\n\t"
    "s_waitcnt lgkmcnt(1)\n\t"
    "s_add_u32 m0, s97, 0x2000\n\t"
    "v_exp_f32 v210, v178\n\t"
    "v_mfma_f32_16x16x32_bf16 a[24:27], a[248:251], v[184:187], a[24:27]\n\t"
    "s_waitcnt lgkmcnt(0)\n\t"
    "buffer_load_dwordx4 v11, s[48:51], s70 offen nt lds\n\t"
    "v_mfma_f32_16x16x32_bf16 a[28:31], a[252:255], v[184:187], a[28:31]\n\t"
    "v_exp_f32 v211, v179\n\t"
    "v_mfma_f32_16x16x32_bf16 v[160:163], a[192:195], v[112:115], v[36:39]\n\t"
    "v_cmp_lt_f32 vcc, s71, v196\n\t"
    "v_cvt_pk_bf16_f32 v193, v210, v211\n\t"
    "v_mfma_f32_16x16x32_bf16 v[160:163], a[196:199], v[116:119], v[160:163]\n\t"
    "v_exp_f32 v212, v180\n\t"
    "v_mfma_f32_16x16x32_bf16 v[160:163], a[200:203], v[120:123], v[160:163]\n\t"
    "s_cbranch_vccnz Lstub_c6_1_%=\n\t"
    "Lret_c6_1_%=:\n\t"
    "v_add_f32 v49, v49, v196\n\t"
    "v_mfma_f32_16x16x32_bf16 v[160:163], a[204:207], v[124:127], v[160:163]\n\t"
    "v_exp_f32 v213, v181\n\t"
    "v_mfma_f32_16x16x32_bf16 v[164:167], a[208:211], v[112:115], v[36:39]\n\t"
    "v_cvt_pk_bf16_f32 v194, v212, v213\n\t"
    "v_mfma_f32_16x16x32_bf16 v[164:167], a[212:215], v[116:119], v[164:167]\n\t"
    "v_exp_f32 v214, v182\n\t"
    "v_mfma_f32_16x16x32_bf16 v[164:167], a[216:219], v[120:123], v[164:167]\n\t"
    "buffer_load_dwordx4 v12, s[48:51], s70 offen offset:1024 nt lds\n\t"
    "v_mfma_f32_16x16x32_bf16 v[164:167], a[220:223], v[124:127], v[164:167]\n\t"
    "v_exp_f32 v215, v183\n\t"
    "v_mfma_f32_16x16x32_bf16 a[32:35], a[224:227], v[188:191], a[32:35]\n\t"
    "v_cvt_pk_bf16_f32 v195, v214, v215\n\t"
    "v_mfma_f32_16x16x32_bf16 a[36:39], a[228:231], v[188:191], a[36:39]\n\t"
    "buffer_load_dwordx4 v11, s[48:51], s70 offen offset:2048 nt lds\n\t"
    "v_mfma_f32_16x16x32_bf16 a[40:43], a[232:235], v[188:191], a[40:43]\n\t"
    "v_exp_f32 v208, v160\n\t"
    "v_mfma_f32_16x16x32_bf16 v[196:199], v[54:57], v[192:195], 0\n\t"
    "v_mfma_f32_16x16x32_bf16 a[44:47], a[236:239], v[188:191], a[44:47]\n\t"
    "v_exp_f32 v209, v161\n\t"
    "v_mfma_f32_16x16x32_bf16 a[48:51], a[240:243], v[188:191], a[48:51]\n\t"
    "v_cvt_pk_bf16_f32 v184, v208, v209\n\t"
    "v_mfma_f32_16x16x32_bf16 a[52:55], a[244:247], v[188:191], a[52:55]\n\t"
    "v_exp_f32 v210, v162\n\t"
    "v_mfma_f32_16x16x32_bf16 a[56:59], a[248:251], v[188:191], a[56:59]\n\t"
    "v_mfma_f32_16x16x32_bf16 a[60:63], a[252:255], v[188:191], a[60:63]\n\t"
    "v_exp_f32 v211, v163\n\t"
    "v_mfma_f32_16x16x32_bf16 v[168:171], a[192:195], v[128:131], v[40:43]\n\t"
    "v_cmp_lt_f32 vcc, s71, v196\n\t"
    "v_cvt_pk_bf16_f32 v185, v210, v211\n\t"
    "v_mfma_f32_16x16x32_bf16 v[168:171], a[196:199], v[132:135], v[168:171]\n\t"
    "v_exp_f32 v212, v164\n\t"
    "v_mfma_f32_16x16x32_bf16 v[168:171], a[200:203], v[136:139], v[168:171]\n\t"
    "s_cbranch_vccnz Lstub_c6_2_%=\n\t"
    "Lret_c6_2_%=:\n\t"
    "v_add_f32 v50, v50, v196\n\t"
    "v_mfma_f32_16x16x32_bf16 v[168:171], a[204:207], v[140:143], v[168:171]\n\t"
    "v_exp_f32 v213, v165\n\t"
    "v_mfma_f32_16x16x32_bf16 v[172:175], a[208:211], v[128:131], v[40:43]\n\t"
    "v_cvt_pk_bf16_f32 v186, v212, v213\n\t"
    "v_mfma_f32_16x16x32_bf16 v[172:175], a[212:215], v[132:135], v[172:175]\n\t"
    "v_exp_f32 v214, v166\n\t"
    "v_mfma_f32_16x16x32_bf16 v[172:175], a[216:219], v[136:139], v[172:175]\n\t"
    "buffer_load_dwordx4 v12, s[48:51], s70 offen offset:3072 nt lds\n\t"
    "v_mfma_f32_16x16x32_bf16 v[172:175], a[220:223], v[140:143], v[172:175]\n\t"
    "s_add_u32 s70, s70, 0x2000\n\t"
    "v_exp_f32 v215, v167\n\t"
    "v_mfma_f32_16x16x32_bf16 a[64:67], a[224:227], v[192:195], a[64:67]\n\t"
    "s_and_b32 s70, s70, 0x1ffff\n\t"
    "v_cvt_pk_bf16_f32 v187, v214, v215\n\t"
    "v_mfma_f32_16x16x32_bf16 a[68:71], a[228:231], v[192:195], a[68:71]\n\t"
    "v_mfma_f32_16x16x32_bf16 a[72:75], a[232:235], v[192:195], a[72:75]\n\t"
    "v_exp_f32 v208, v168\n\t"
    "v_mfma_f32_16x16x32_bf16 v[196:199], v[54:57], v[184:187], 0\n\t"
    "v_mfma_f32_16x16x32_bf16 a[76:79], a[236:239], v[192:195], a[76:79]\n\t"
    "v_exp_f32 v209, v169\n\t"
    "v_mfma_f32_16x16x32_bf16 a[80:83], a[240:243], v[192:195], a[80:83]\n\t"
    "v_cvt_pk_bf16_f32 v188, v208, v209\n\t"
    "v_mfma_f32_16x16x32_bf16 a[84:87], a[244:247], v[192:195], a[84:87]\n\t"
    "v_exp_f32 v210, v170\n\t"
    "v_mfma_f32_16x16x32_bf16 a[88:91], a[248:251], v[192:195], a[88:91]\n\t"
    "v_mfma_f32_16x16x32_bf16 a[92:95], a[252:255], v[192:195], a[92:95]\n\t"
    "v_exp_f32 v211, v171\n\t"
    "v_mfma_f32_16x16x32_bf16 v[176:179], a[192:195], v[144:147], v[44:47]\n\t"
    "v_cmp_lt_f32 vcc, s71, v196\n\t"
    "v_cvt_pk_bf16_f32 v189, v210, v211\n\t"
    "v_mfma_f32_16x16x32_bf16 v[176:179], a[196:199], v[148:151], v[176:179]\n\t"
    "v_exp_f32 v212, v172\n\t"
    "v_mfma_f32_16x16x32_bf16 v[176:179], a[200:203], v[152:155], v[176:179]\n\t"
    "s_cbranch_vccnz Lstub_c6_3_%=\n\t"
    "Lret_c6_3_%=:\n\t"
    "ds_read_b128 a[192:195], v1 offset:32768\n\t"
    "v_add_f32 v51, v51, v196\n\t"
    "v_mfma_f32_16x16x32_bf16 v[176:179], a[204:207], v[156:159], v[176:179]\n\t"
    "v_exp_f32 v213, v173\n\t"
    "v_mfma_f32_16x16x32_bf16 v[180:183], a[208:211], v[144:147], v[44:47]\n\t"
    "ds_read_b128 a[196:199], v2 offset:32768\n\t"
    "v_cvt_pk_bf16_f32 v190, v212, v213\n\t"
    "v_mfma_f32_16x16x32_bf16 v[180:183], a[212:215], v[148:151], v[180:183]\n\t"
    "v_exp_f32 v214, v174\n\t"
    "v_mfma_f32_16x16x32_bf16 v[180:183], a[216:219], v[152:155], v[180:183]\n\t"
    "ds_read_b128 a[200:203], v3 offset:32768\n\t"
    "ds_read_b128 a[204:207], v4 offset:32768\n\t"
    "v_mfma_f32_16x16x32_bf16 v[180:183], a[220:223], v[156:159], v[180:183]\n\t"
    "v_exp_f32 v215, v175\n\t"
    "v_mfma_f32_16x16x32_bf16 a[96:99], a[224:227], v[184:187], a[96:99]\n\t"
    "ds_read_b128 a[208:211], v1 offset:36864\n\t"
    "v_cvt_pk_bf16_f32 v191, v214, v215\n\t"
    "v_mfma_f32_16x16x32_bf16 a[100:103], a[228:231], v[184:187], a[100:103]\n\t"
    "ds_read_b128 a[212:215], v2 offset:36864\n\t"
    "ds_read_b128 a[216:219], v3 offset:36864\n\t"
    "v_mfma_f32_16x16x32_bf16 a[104:107], a[232:235], v[184:187], a[104:107]\n\t"
    "v_exp_f32 v208, v176\n\t"
    "v_mfma_f32_16x16x32_bf16 v[196:199], v[54:57], v[188:191], 0\n\t"
    "ds_read_b128 a[220:223], v4 offset:36864\n\t"
    "v_mfma_f32_16x16x32_bf16 a[108:111], a[236:239], v[184:187], a[108:111]\n\t"
    "v_exp_f32 v209, v177\n\t"
    "v_mfma_f32_16x16x32_bf16 a[112:115], a[240:243], v[184:187], a[112:115]\n\t"
    "v_cvt_pk_bf16_f32 v192, v208, v209\n\t"
    "v_mfma_f32_16x16x32_bf16 a[116:119], a[244:247], v[184:187], a[116:119]\n\t"
    "s_add_u32 m0, s66, 0\n\t"
    "v_exp_f32 v210, v178\n\t"
    "v_mfma_f32_16x16x32_bf16 a[120:123], a[248:251], v[184:187], a[120:123]\n\t"
    "buffer_load_dwordx4 v7, s[44:47], s69 offen nt lds\n\t"
    "v_mfma_f32_16x16x32_bf16 a[124:127], a[252:255], v[184:187], a[124:127]\n\t"
    "s_waitcnt lgkmcnt(0)\n\t"
    "v_exp_f32 v211, v179\n\t"
    "v_mfma_f32_16x16x32_bf16 v[160:163], a[192:195], v[64:67], v[24:27]\n\t"
    "v_cmp_lt_f32 vcc, s71, v196\n\t"
    "v_cvt_pk_bf16_f32 v193, v210, v211\n\t"
    "v_mfma_f32_16x16x32_bf16 v[160:163], a[196:199], v[68:71], v[160:163]\n\t"
    "v_exp_f32 v212, v180\n\t"
    "v_mfma_f32_16x16x32_bf16 v[160:163], a[200:203], v[72:75], v[160:163]\n\t"
    "s_cbranch_vccnz Lstub_c6_4_%=\n\t"
    "Lret_c6_4_%=:\n\t"
    "v_add_f32 v52, v52, v196\n\t"
    "v_mfma_f32_16x16x32_bf16 v[160:163], a[204:207], v[76:79], v[160:163]\n\t"
    "v_exp_f32 v213, v181\n\t"
    "v_mfma_f32_16x16x32_bf16 v[164:167], a[208:211], v[64:67], v[24:27]\n\t"
    "v_cvt_pk_bf16_f32 v194, v212, v213\n\t"
    "v_mfma_f32_16x16x32_bf16 v[164:167], a[212:215], v[68:71], v[164:167]\n\t"
    "v_exp_f32 v214, v182\n\t"
    "v_mfma_f32_16x16x32_bf16 v[164:167], a[216:219], v[72:75], v[164:167]\n\t"
    "buffer_load_dwordx4 v8, s[44:47], s69 offen offset:1024 nt lds\n\t"
    "v_mfma_f32_16x16x32_bf16 v[164:167], a[220:223], v[76:79], v[164:167]\n\t"
    "v_exp_f32 v215, v183\n\t"
    "v_mfma_f32_16x16x32_bf16 a[128:131], a[224:227], v[188:191], a[128:131]\n\t"
    "s_memrealtime s[98:99]\n\t"
    "v_cvt_pk_bf16_f32 v195, v214, v215\n\t"
    "v_mfma_f32_16x16x32_bf16 a[132:135], a[228:231], v[188:191], a[132:135]\n\t"
    "v_mfma_f32_16x16x32_bf16 a[136:139], a[232:235], v[188:191], a[136:139]\n\t"
    "v_exp_f32 v208, v160\n\t"
    "v_mfma_f32_16x16x32_bf16 v[196:199], v[54:57], v[192:195], 0\n\t"
    "v_mfma_f32_16x16x32_bf16 a[140:143], a[236:239], v[188:191], a[140:143]\n\t"
    "v_exp_f32 v209, v161\n\t"
    "v_mfma_f32_16x16x32_bf16 a[144:147], a[240:243], v[188:191], a[144:147]\n\t"
    "v_cvt_pk_bf16_f32 v184, v208, v209\n\t"
    "v_mfma_f32_16x16x32_bf16 a[148:151], a[244:247], v[188:191], a[148:151]\n\t"
    "v_exp_f32 v210, v162\n\t"
    "v_mfma_f32_16x16x32_bf16 a[152:155], a[248:251], v[188:191], a[152:155]\n\t"
    "buffer_load_dwordx4 v9, s[44:47], s69 offen offset:2048 nt lds\n\t"
    "v_mfma_f32_16x16x32_bf16 a[156:159], a[252:255], v[188:191], a[156:159]\n\t"
    "v_exp_f32 v211, v163\n\t"
    "v_mfma_f32_16x16x32_bf16 v[168:171], a[192:195], v[80:83], v[28:31]\n\t"
    "v_cmp_lt_f32 vcc, s71, v196\n\t"
    "v_cvt_pk_bf16_f32 v185, v210, v211\n\t"
    "v_mfma_f32_16x16x32_bf16 v[168:171], a[196:199], v[84:87], v[168:171]\n\t"
    "s_waitcnt lgkmcnt(0)\n\t"
    "v_exp_f32 v212, v164\n\t"
    "v_mfma_f32_16x16x32_bf16 v[168:171], a[200:203], v[88:91], v[168:171]\n\t"
    "s_cbranch_vccnz Lstub_c6_5_%=\n\t"
    "Lret_c6_5_%=:\n\t"
    "v_add_f32 v53, v53, v196\n\t"
    "v_mfma_f32_16x16x32_bf16 v[168:171], a[204:207], v[92:95], v[168:171]\n\t"
    "s_waitcnt vmcnt(16)\n\t"
    "v_exp_f32 v213, v165\n\t"
    "v_mfma_f32_16x16x32_bf16 v[172:175], a[208:211], v[80:83], v[28:31]\n\t"
    "s_sub_u32 s19, s18, s98\n\t"
    "v_cvt_pk_bf16_f32 v186, v212, v213\n\t"
    "v_mfma_f32_16x16x32_bf16 v[172:175], a[212:215], v[84:87], v[172:175]\n\t"
    "s_barrier\n\t"
    "v_exp_f32 v214, v166\n\t"
    "v_mfma_f32_16x16x32_bf16 v[172:175], a[216:219], v[88:91], v[172:175]\n\t"
    "s_cmp_gt_i32 s19, 0\n\t"
    "s_cbranch_scc1 Lpace7_%=\n\t"
    "Lpaced7_%=:\n\t"
    "v_mfma_f32_16x16x32_bf16 v[172:175], a[220:223], v[92:95], v[172:175]\n\t"
    "s_add_u32 s18, s18, 0x72\n\t"
    "v_exp_f32 v215, v167\n\t"
    "v_mfma_f32_16x16x32_bf16 a[160:163], a[224:227], v[192:195], a[160:163]\n\t"
    "v_cvt_pk_bf16_f32 v187, v214, v215\n\t"
    "v_mfma_f32_16x16x32_bf16 a[164:167], a[228:231], v[192:195], a[164:167]\n\t"
    "ds_read_b64_tr_b16 a[224:225], v5 offset:32768\n\t"
    "ds_read_b64_tr_b16 a[226:227], v5 offset:36864\n\t"
    "v_mfma_f32_16x16x32_bf16 a[168:171], a[232:235], v[192:195], a[168:171]\n\t"
    "v_exp_f32 v208, v168\n\t"
    "v_mfma_f32_16x16x32_bf16 v[196:199], v[54:57], v[184:187], 0\n\t"
    "ds_read_b64_tr_b16 a[228:229], v6 offset:32768\n\t"
    "ds_read_b64_tr_b16 a[230:231], v6 offset:36864\n\t"
    "v_mfma_f32_16x16x32_bf16 a[172:175], a[236:239], v[192:195], a[172:175]\n\t"
    "v_exp_f32 v209, v169\n\t"
    "v_mfma_f32_16x16x32_bf16 a[176:179], a[240:243], v[192:195], a[176:179]\n\t"
    "ds_read_b64_tr_b16 a[232:233], v5 offset:33024\n\t"
    "v_cvt_pk_bf16_f32 v188, v208, v209\n\t"
    "v_mfma_f32_16x16x32_bf16 a[180:183], a[244:247], v[192:195], a[180:183]\n\t"
    "v_exp_f32 v210, v170\n\t"
    "v_mfma_f32_16x16x32_bf16 a[184:187], a[248:251], v[192:195], a[184:187]\n\t"
    "ds_read_b64_tr_b16 a[234:235], v5 offset:37120\n\t"
    "ds_read_b64_tr_b16 a[236:237], v6 offset:33024\n\t"
    "v_mfma_f32_16x16x32_bf16 a[188:191], a[252:255], v[192:195], a[188:191]\n\t"
    "v_exp_f32 v211, v171\n\t"
    "v_mfma_f32_16x16x32_bf16 v[176:179], a[192:195], v[96:99], v[32:35]\n\t"
    "ds_read_b64_tr_b16 a[238:239], v6 offset:37120\n\t"
    "v_cmp_lt_f32 vcc, s71, v196\n\t"
    "v_cvt_pk_bf16_f32 v189, v210, v211\n\t"
    "v_mfma_f32_16x16x32_bf16 v[176:179], a[196:199], v[100:103], v[176:179]\n\t"
    "v_exp_f32 v212, v172\n\t"
    "v_mfma_f32_16x16x32_bf16 v[176:179], a[200:203], v[104:107], v[176:179]\n\t"
    "s_cbranch_vccnz Lstub_c7_0_%=\n\t"
    "Lret_c7_0_%=:\n\t"
    "ds_read_b64_tr_b16 a[240:241], v5 offset:33280\n\t"
    "v_add_f32 v48, v48, v196\n\t"
    "v_mfma_f32_16x16x32_bf16 v[176:179], a[204:207], v[108:111], v[176:179]\n\t"
    "v_exp_f32 v213, v173\n\t"
    "v_mfma_f32_16x16x32_bf16 v[180:183], a[208:211], v[96:99], v[32:35]\n\t"
    "ds_read_b64_tr_b16 a[242:243], v5 offset:37376\n\t"
    "v_cvt_pk_bf16_f32 v190, v212, v213\n\t"
    "v_mfma_f32_16x16x32_bf16 v[180:183], a[212:215], v[100:103], v[180:183]\n\t"
    "v_exp_f32 v214, v174\n\t"
    "v_mfma_f32_16x16x32_bf16 v[180:183], a[216:219], v[104:107], v[180:183]\n\t"
    "ds_read_b64_tr_b16 a[244:245], v6 offset:33280\n\t"
    "ds_read_b64_tr_b16 a[246:247], v6 offset:37376\n\t"
    "v_mfma_f32_16x16x32_bf16 v[180:183], a[220:223], v[108:111], v[180:183]\n\t"
    "s_waitcnt lgkmcnt(3)\n\t"
    "v_exp_f32 v215, v175\n\t"
    "v_mfma_f32_16x16x32_bf16 a[0:3], a[224:227], v[184:187], a[0:3]\n\t"
    "ds_read_b64_tr_b16 a[248:249], v5 offset:33536\n\t"
    "v_cvt_pk_bf16_f32 v191, v214, v215\n\t"
    "v_mfma_f32_16x16x32_bf16 a[4:7], a[228:231], v[184:187], a[4:7]\n\t"
    "ds_read_b64_tr_b16 a[250:251], v5 offset:37632\n\t"
    "ds_read_b64_tr_b16 a[252:253], v6 offset:33536\n\t"
    "v_mfma_f32_16x16x32_bf16 a[8:11], a[232:235], v[184:187], a[8:11]\n\t"
    "s_add_u32 s98, s98, 0x68\n\t"
    "v_exp_f32 v208, v176\n\t"
    "v_mfma_f32_16x16x32_bf16 v[196:199], v[54:57], v[188:191], 0\n\t"
    "ds_read_b64_tr_b16 a[254:255], v6 offset:37632\n\t"
    "v_mfma_f32_16x16x32_bf16 a[12:15], a[236:239], v[184:187], a[12:15]\n\t"
    "s_waitcnt lgkmcnt(4)\n\t"
    "s_max_u32 s18, s18, s98\n\t"
    "v_exp_f32 v209, v177\n\t"
    "v_mfma_f32_16x16x32_bf16 a[16:19], a[240:243], v[184:187], a[16:19]\n\t"
    "v_cvt_pk_bf16_f32 v192, v208, v209\n\t"
    "v_mfma_f32_16x16x32_bf16 a[20:23], a[244:247], v[184:187], a[20:23]\n\t"
    "s_waitcnt lgkmcnt(1)\n\t"
    "v_exp_f32 v210, v178\n\t"
    "v_mfma_f32_16x16x32_bf16 a[24:27], a[248:251], v[184:187], a[24:27]\n\t"
    "s_waitcnt lgkmcnt(0)\n\t"
    "buffer_load_dwordx4 v10, s[44:47], s69 offen offset:3072 nt lds\n\t"
    "v_mfma_f32_16x16x32_bf16 a[28:31], a[252:255], v[184:187], a[28:31]\n\t"
    "s_add_u32 s69, s69, 0x2000\n\t"
    "v_exp_f32 v211, v179\n\t"
    "v_mfma_f32_16x16x32_bf16 v[160:163], a[192:195], v[112:115], v[36:39]\n\t"
    "s_and_b32 s69, s69, 0x1ffff\n\t"
    "v_cmp_lt_f32 vcc, s71, v196\n\t"
    "v_cvt_pk_bf16_f32 v193, v210, v211\n\t"
    "v_mfma_f32_16x16x32_bf16 v[160:163], a[196:199], v[116:119], v[160:163]\n\t"
    "v_exp_f32 v212, v180\n\t"
    "v_mfma_f32_16x16x32_bf16 v[160:163], a[200:203], v[120:123], v[160:163]\n\t"
    "s_cbranch_vccnz Lstub_c7_1_%=\n\t"
    "Lret_c7_1_%=:\n\t"
    "v_add_f32 v49, v49, v196\n\t"
    "v_mfma_f32_16x16x32_bf16 v[160:163], a[204:207], v[124:127], v[160:163]\n\t"
    "v_exp_f32 v213, v181\n\t"
    "v_mfma_f32_16x16x32_bf16 v[164:167], a[208:211], v[112:115], v[36:39]\n\t"
    "v_cvt_pk_bf16_f32 v194, v212, v213\n\t"
    "v_mfma_f32_16x16x32_bf16 v[164:167], a[212:215], v[116:119], v[164:167]\n\t"
    "s_add_u32 m0, s66, 0x2000\n\t"
    "v_exp_f32 v214, v182\n\t"
    "v_mfma_f32_16x16x32_bf16 v[164:167], a[216:219], v[120:123], v[164:167]\n\t"
    "buffer_load_dwordx4 v11, s[48:51], s70 offen nt lds\n\t"
    "v_mfma_f32_16x16x32_bf16 v[164:167], a[220:223], v[124:127], v[164:167]\n\t"
    "v_exp_f32 v215, v183\n\t"
    "v_mfma_f32_16x16x32_bf16 a[32:35], a[224:227], v[188:191], a[32:35]\n\t"
    "v_cvt_pk_bf16_f32 v195, v214, v215\n\t"
    "v_mfma_f32_16x16x32_bf16 a[36:39], a[228:231], v[188:191], a[36:39]\n\t"
    "buffer_load_dwordx4 v12, s[48:51], s70 offen offset:1024 nt lds\n\t"
    "v_mfma_f32_16x16x32_bf16 a[40:43], a[232:235], v[188:191], a[40:43]\n\t"
    "v_exp_f32 v208, v160\n\t"
    "v_mfma_f32_16x16x32_bf16 v[196:199], v[54:57], v[192:195], 0\n\t"
    "v_mfma_f32_16x16x32_bf16 a[44:47], a[236:239], v[188:191], a[44:47]\n\t"
    "v_exp_f32 v209, v161\n\t"
    "v_mfma_f32_16x16x32_bf16 a[48:51], a[240:243], v[188:191], a[48:51]\n\t"
    "v_cvt_pk_bf16_f32 v184, v208, v209\n\t"
    "v_mfma_f32_16x16x32_bf16 a[52:55], a[244:247], v[188:191], a[52:55]\n\t"
    "v_exp_f32 v210, v162\n\t"
    "v_mfma_f32_16x16x32_bf16 a[56:59], a[248:251], v[188:191], a[56:59]\n\t"
    "v_mfma_f32_16x16x32_bf16 a[60:63], a[252:255], v[188:191], a[60:63]\n\t"
    "v_exp_f32 v211, v163\n\t"
    "v_mfma_f32_16x16x32_bf16 v[168:171], a[192:195], v[128:131], v[40:43]\n\t"
    "v_cmp_lt_f32 vcc, s71, v196\n\t"
    "v_cvt_pk_bf16_f32 v185, v210, v211\n\t"
    "v_mfma_f32_16x16x32_bf16 v[168:171], a[196:199], v[132:135], v[168:171]\n\t"
    "v_exp_f32 v212, v164\n\t"
    "v_mfma_f32_16x16x32_bf16 v[168:171], a[200:203], v[136:139], v[168:171]\n\t"
    "s_cbranch_vccnz Lstub_c7_2_%=\n\t"
    "Lret_c7_2_%=:\n\t"
    "v_add_f32 v50, v50, v196\n\t"
    "v_mfma_f32_16x16x32_bf16 v[168:171], a[204:207], v[140:143], v[168:171]\n\t"
    "v_exp_f32 v213, v165\n\t"
    "v_mfma_f32_16x16x32_bf16 v[172:175], a[208:211], v[128:131], v[40:43]\n\t"
    "v_cvt_pk_bf16_f32 v186, v212, v213\n\t"
    "v_mfma_f32_16x16x32_bf16 v[172:175], a[212:215], v[132:135], v[172:175]\n\t"
    "v_exp_f32 v214, v166\n\t"
    "v_mfma_f32_16x16x32_bf16 v[172:175], a[216:219], v[136:139], v[172:175]\n\t"
    "buffer_load_dwordx4 v11, s[48:51], s70 offen offset:2048 nt lds\n\t"
    "v_mfma_f32_16x16x32_bf16 v[172:175], a[220:223], v[140:143], v[172:175]\n\t"
    "v_exp_f32 v215, v167\n\t"
    "v_mfma_f32_16x16x32_bf16 a[64:67], a[224:227], v[192:195], a[64:67]\n\t"
    "v_cvt_pk_bf16_f32 v187, v214, v215\n\t"
    "v_mfma_f32_16x16x32_bf16 a[68:71], a[228:231], v[192:195], a[68:71]\n\t"
    "v_mfma_f32_16x16x32_bf16 a[72:75], a[232:235], v[192:195], a[72:75]\n\t"
    "v_exp_f32 v208, v168\n\t"
    "v_mfma_f32_16x16x32_bf16 v[196:199], v[54:57], v[184:187], 0\n\t"
    "v_mfma_f32_16x16x32_bf16 a[76:79], a[236:239], v[192:195], a[76:79]\n\t"
    "v_exp_f32 v209, v169\n\t"
    "v_mfma_f32_16x16x32_bf16 a[80:83], a[240:243], v[192:195], a[80:83]\n\t"
    "v_cvt_pk_bf16_f32 v188, v208, v209\n\t"
    "v_mfma_f32_16x16x32_bf16 a[84:87], a[244:247], v[192:195], a[84:87]\n\t"
    "v_exp_f32 v210, v170\n\t"
    "v_mfma_f32_16x16x32_bf16 a[88:91], a[248:251], v[192:195], a[88:91]\n\t"
    "v_mfma_f32_16x16x32_bf16 a[92:95], a[252:255], v[192:195], a[92:95]\n\t"
    "v_exp_f32 v211, v171\n\t"
    "v_mfma_f32_16x16x32_bf16 v[176:179], a[192:195], v[144:147], v[44:47]\n\t"
    "v_cmp_lt_f32 vcc, s71, v196\n\t"
    "v_cvt_pk_bf16_f32 v189, v210, v211\n\t"
    "v_mfma_f32_16x16x32_bf16 v[176:179], a[196:199], v[148:151], v[176:179]\n\t"
    "v_exp_f32 v212, v172\n\t"
    "v_mfma_f32_16x16x32_bf16 v[176:179], a[200:203], v[152:155], v[176:179]\n\t"
    "s_cbranch_vccnz Lstub_c7_3_%=\n\t"
    "Lret_c7_3_%=:\n\t"
    "ds_read_b128 a[192:195], v1 offset:49152\n\t"
    "v_add_f32 v51, v51, v196\n\t"
    "v_mfma_f32_16x16x32_bf16 v[176:179], a[204:207], v[156:159], v[176:179]\n\t"
    "v_exp_f32 v213, v173\n\t"
    "v_mfma_f32_16x16x32_bf16 v[180:183], a[208:211], v[144:147], v[44:47]\n\t"
    "ds_read_b128 a[196:199], v2 offset:49152\n\t"
    "v_cvt_pk_bf16_f32 v190, v212, v213\n\t"
    "v_mfma_f32_16x16x32_bf16 v[180:183], a[212:215], v[148:151], v[180:183]\n\t"
    "v_exp_f32 v214, v174\n\t"
    "v_mfma_f32_16x16x32_bf16 v[180:183], a[216:219], v[152:155], v[180:183]\n\t"
    "ds_read_b128 a[200:203], v3 offset:49152\n\t"
    "ds_read_b128 a[204:207], v4 offset:49152\n\t"
    "v_mfma_f32_16x16x32_bf16 v[180:183], a[220:223], v[156:159], v[180:183]\n\t"
    "v_exp_f32 v215, v175\n\t"
    "v_mfma_f32_16x16x32_bf16 a[96:99], a[224:227], v[184:187], a[96:99]\n\t"
    "ds_read_b128 a[208:211], v1 offset:53248\n\t"
    "v_cvt_pk_bf16_f32 v191, v214, v215\n\t"
    "v_mfma_f32_16x16x32_bf16 a[100:103], a[228:231], v[184:187], a[100:103]\n\t"
    "ds_read_b128 a[212:215], v2 offset:53248\n\t"
    "ds_read_b128 a[216:219], v3 offset:53248\n\t"
    "v_mfma_f32_16x16x32_bf16 a[104:107], a[232:235], v[184:187], a[104:107]\n\t"
    "v_exp_f32 v208, v176\n\t"
    "v_mfma_f32_16x16x32_bf16 v[196:199], v[54:57], v[188:191], 0\n\t"
    "ds_read_b128 a[220:223], v4 offset:53248\n\t"
    "v_mfma_f32_16x16x32_bf16 a[108:111], a[236:239], v[184:187], a[108:111]\n\t"
    "v_exp_f32 v209, v177\n\t"
    "v_mfma_f32_16x16x32_bf16 a[112:115], a[240:243], v[184:187], a[112:115]\n\t"
    "v_cvt_pk_bf16_f32 v192, v208, v209\n\t"
    "v_mfma_f32_16x16x32_bf16 a[116:119], a[244:247], v[184:187], a[116:119]\n\t"
    "v_exp_f32 v210, v178\n\t"
    "v_mfma_f32_16x16x32_bf16 a[120:123], a[248:251], v[184:187], a[120:123]\n\t"
    "buffer_load_dwordx4 v12, s[48:51], s70 offen offset:3072 nt lds\n\t"
    "v_mfma_f32_16x16x32_bf16 a[124:127], a[252:255], v[184:187], a[124:127]\n\t"
    "s_waitcnt lgkmcnt(0)\n\t"
    "s_add_u32 s70, s70, 0x2000\n\t"
    "v_exp_f32 v211, v179\n\t"
    "v_mfma_f32_16x16x32_bf16 v[160:163], a[192:195], v[64:67], v[24:27]\n\t"
    "s_and_b32 s70, s70, 0x1ffff\n\t"
    "v_cmp_lt_f32 vcc, s71, v196\n\t"
    "v_cvt_pk_bf16_f32 v193, v210, v211\n\t"
    "v_mfma_f32_16x16x32_bf16 v[160:163], a[196:199], v[68:71], v[160:163]\n\t"
    "v_exp_f32 v212, v180\n\t"
    "v_mfma_f32_16x16x32_bf16 v[160:163], a[200:203], v[72:75], v[160:163]\n\t"
    "s_cbranch_vccnz Lstub_c7_4_%=\n\t"
    "Lret_c7_4_%=:\n\t"
    "v_add_f32 v52, v52, v196\n\t"
    "v_mfma_f32_16x16x32_bf16 v[160:163], a[204:207], v[76:79], v[160:163]\n\t"
    "v_exp_f32 v213, v181\n\t"
    "v_mfma_f32_16x16x32_bf16 v[164:167], a[208:211], v[64:67], v[24:27]\n\t"
    "v_cvt_pk_bf16_f32 v194, v212, v213\n\t"
    "v_mfma_f32_16x16x32_bf16 v[164:167], a[212:215], v[68:71], v[164:167]\n\t"
    "s_add_u32 m0, s66, 0x4000\n\t"
    "v_exp_f32 v214, v182\n\t"
    "v_mfma_f32_16x16x32_bf16 v[164:167], a[216:219], v[72:75], v[164:167]\n\t"
    "buffer_load_dwordx4 v7, s[44:47], s69 offen nt lds\n\t"
    "v_mfma_f32_16x16x32_bf16 v[164:167], a[220:223], v[76:79], v[164:167]\n\t"
    "v_exp_f32 v215, v183\n\t"
    "v_mfma_f32_16x16x32_bf16 a[128:131], a[224:227], v[188:191], a[128:131]\n\t"
    "s_memrealtime s[98:99]\n\t"
    "v_cvt_pk_bf16_f32 v195, v214, v215\n\t"
    "v_mfma_f32_16x16x32_bf16 a[132:135], a[228:231], v[188:191], a[132:135]\n\t"
    "v_mfma_f32_16x16x32_bf16 a[136:139], a[232:235], v[188:191], a[136:139]\n\t"
    "v_exp_f32 v208, v160\n\t"
    "v_mfma_f32_16x16x32_bf16 v[196:199], v[54:57], v[192:195], 0\n\t"
    "v_mfma_f32_16x16x32_bf16 a[140:143], a[236:239], v[188:191], a[140:143]\n\t"
    "v_exp_f32 v209, v161\n\t"
    "v_mfma_f32_16x16x32_bf16 a[144:147], a[240:243], v[188:191], a[144:147]\n\t"
    "v_cvt_pk_bf16_f32 v184, v208, v209\n\t"
    "v_mfma_f32_16x16x32_bf16 a[148:151], a[244:247], v[188:191], a[148:151]\n\t"
    "v_exp_f32 v210, v162\n\t"
    "v_mfma_f32_16x16x32_bf16 a[152:155], a[248:251], v[188:191], a[152:155]\n\t"
    "v_mfma_f32_16x16x32_bf16 a[156:159], a[252:255], v[188:191], a[156:159]\n\t"
    "v_exp_f32 v211, v163\n\t"
    "v_mfma_f32_16x16x32_bf16 v[168:171], a[192:195], v[80:83], v[28:31]\n\t"
    "v_cmp_lt_f32 vcc, s71, v196\n\t"
    "v_cvt_pk_bf16_f32 v185, v210, v211\n\t"
    "v_mfma_f32_16x16x32_bf16 v[168:171], a[196:199], v[84:87], v[168:171]\n\t"
    "s_waitcnt lgkmcnt(0)\n\t"
    "v_exp_f32 v212, v164\n\t"
    "v_mfma_f32_16x16x32_bf16 v[168:171], a[200:203], v[88:91], v[168:171]\n\t"
    "s_cbranch_vccnz Lstub_c7_5_%=\n\t"
    "Lret_c7_5_%=:\n\t"
    "v_add_f32 v53, v53, v196\n\t"
    "v_mfma_f32_16x16x32_bf16 v[168:171], a[204:207], v[92:95], v[168:171]\n\t"
    "s_waitcnt vmcnt(13)\n\t"
    "v_exp_f32 v213, v165\n\t"
    "v_mfma_f32_16x16x32_bf16 v[172:175], a[208:211], v[80:83], v[28:31]\n\t"
    "s_sub_u32 s19, s18, s98\n\t"
    "v_cvt_pk_bf16_f32 v186, v212, v213\n\t"
    "v_mfma_f32_16x16x32_bf16 v[172:175], a[212:215], v[84:87], v[172:175]\n\t"
    "s_barrier\n\t"
    "v_exp_f32 v214, v166\n\t"
    "v_mfma_f32_16x16x32_bf16 v[172:175], a[216:219], v[88:91], v[172:175]\n\t"
    "s_cmp_gt_i32 s19, 0\n\t"
    "s_cbranch_scc1 Lpace8_%=\n\t"
    "Lpaced8_%=:\n\t"
    "v_mfma_f32_16x16x32_bf16 v[172:175], a[220:223], v[92:95], v[172:175]\n\t"
    "s_add_u32 s18, s18, 0x72\n\t"
    "v_exp_f32 v215, v167\n\t"
    "v_mfma_f32_16x16x32_bf16 a[160:163], a[224:227], v[192:195], a[160:163]\n\t"
    "v_cvt_pk_bf16_f32 v187, v214, v215\n\t"
    "v_mfma_f32_16x16x32_bf16 a[164:167], a[228:231], v[192:195], a[164:167]\n\t"
    "ds_read_b64_tr_b16 a[224:225], v5 offset:49152\n\t"
    "ds_read_b64_tr_b16 a[226:227], v5 offset:53248\n\t"
    "v_mfma_f32_16x16x32_bf16 a[168:171], a[232:235], v[192:195], a[168:171]\n\t"
    "v_exp_f32 v208, v168\n\t"
    "v_mfma_f32_16x16x32_bf16 v[196:199], v[54:57], v[184:187], 0\n\t"
    "ds_read_b64_tr_b16 a[228:229], v6 offset:49152\n\t"
    "ds_read_b64_tr_b16 a[230:231], v6 offset:53248\n\t"
    "v_mfma_f32_16x16x32_bf16 a[172:175], a[236:239], v[192:195], a[172:175]\n\t"
    "v_exp_f32 v209, v169\n\t"
    "v_mfma_f32_16x16x32_bf16 a[176:179], a[240:243], v[192:195], a[176:179]\n\t"
    "ds_read_b64_tr_b16 a[232:233], v5 offset:49408\n\t"
    "v_cvt_pk_bf16_f32 v188, v208, v209\n\t"
    "v_mfma_f32_16x16x32_bf16 a[180:183], a[244:247], v[192:195], a[180:183]\n\t"
    "v_exp_f32 v210, v170\n\t"
    "v_mfma_f32_16x16x32_bf16 a[184:187], a[248:251], v[192:195], a[184:187]\n\t"
    "ds_read_b64_tr_b16 a[234:235], v5 offset:53504\n\t"
    "ds_read_b64_tr_b16 a[236:237], v6 offset:49408\n\t"
    "v_mfma_f32_16x16x32_bf16 a[188:191], a[252:255], v[192:195], a[188:191]\n\t"
    "v_exp_f32 v211, v171\n\t"
    "v_mfma_f32_16x16x32_bf16 v[176:179], a[192:195], v[96:99], v[32:35]\n\t"
    "ds_read_b64_tr_b16 a[238:239], v6 offset:53504\n\t"
    "v_cmp_lt_f32 vcc, s71, v196\n\t"
    "v_cvt_pk_bf16_f32 v189, v210, v211\n\t"
    "v_mfma_f32_16x16x32_bf16 v[176:179], a[196:199], v[100:103], v[176:179]\n\t"
    "v_exp_f32 v212, v172\n\t"
    "v_mfma_f32_16x16x32_bf16 v[176:179], a[200:203], v[104:107], v[176:179]\n\t"
    "s_cbranch_vccnz Lstub_c8_0_%=\n\t"
    "Lret_c8_0_%=:\n\t"
    "ds_read_b64_tr_b16 a[240:241], v5 offset:49664\n\t"
    "v_add_f32 v48, v48, v196\n\t"
    "v_mfma_f32_16x16x32_bf16 v[176:179], a[204:207], v[108:111], v[176:179]\n\t"
    "v_exp_f32 v213, v173\n\t"
    "v_mfma_f32_16x16x32_bf16 v[180:183], a[208:211], v[96:99], v[32:35]\n\t"
    "ds_read_b64_tr_b16 a[242:243], v5 offset:53760\n\t"
    "v_cvt_pk_bf16_f32 v190, v212, v213\n\t"
    "v_mfma_f32_16x16x32_bf16 v[180:183], a[212:215], v[100:103], v[180:183]\n\t"
    "v_exp_f32 v214, v174\n\t"
    "v_mfma_f32_16x16x32_bf16 v[180:183], a[216:219], v[104:107], v[180:183]\n\t"
    "ds_read_b64_tr_b16 a[244:245], v6 offset:49664\n\t"
    "ds_read_b64_tr_b16 a[246:247], v6 offset:53760\n\t"
    "v_mfma_f32_16x16x32_bf16 v[180:183], a[220:223], v[108:111], v[180:183]\n\t"
    "s_waitcnt lgkmcnt(3)\n\t"
    "v_exp_f32 v215, v175\n\t"
    "v_mfma_f32_16x16x32_bf16 a[0:3], a[224:227], v[184:187], a[0:3]\n\t"
    "ds_read_b64_tr_b16 a[248:249], v5 offset:49920\n\t"
    "v_cvt_pk_bf16_f32 v191, v214, v215\n\t"
    "v_mfma_f32_16x16x32_bf16 a[4:7], a[228:231], v[184:187], a[4:7]\n\t"
    "ds_read_b64_tr_b16 a[250:251], v5 offset:54016\n\t"
    "ds_read_b64_tr_b16 a[252:253], v6 offset:49920\n\t"
    "v_mfma_f32_16x16x32_bf16 a[8:11], a[232:235], v[184:187], a[8:11]\n\t"
    "s_add_u32 s98, s98, 0x68\n\t"
    "v_exp_f32 v208, v176\n\t"
    "v_mfma_f32_16x16x32_bf16 v[196:199], v[54:57], v[188:191], 0\n\t"
    "ds_read_b64_tr_b16 a[254:255], v6 offset:54016\n\t"
    "v_mfma_f32_16x16x32_bf16 a[12:15], a[236:239], v[184:187], a[12:15]\n\t"
    "s_waitcnt lgkmcnt(4)\n\t"
    "s_max_u32 s18, s18, s98\n\t"
    "v_exp_f32 v209, v177\n\t"
    "v_mfma_f32_16x16x32_bf16 a[16:19], a[240:243], v[184:187], a[16:19]\n\t"
    "v_cvt_pk_bf16_f32 v192, v208, v209\n\t"
    "v_mfma_f32_16x16x32_bf16 a[20:23], a[244:247], v[184:187], a[20:23]\n\t"
    "s_waitcnt lgkmcnt(1)\n\t"
    "v_exp_f32 v210, v178\n\t"
    "v_mfma_f32_16x16x32_bf16 a[24:27], a[248:251], v[184:187], a[24:27]\n\t"
    "s_waitcnt lgkmcnt(0)\n\t"
    "buffer_load_dwordx4 v8, s[44:47], s69 offen offset:1024 nt lds\n\t"
    "v_mfma_f32_16x16x32_bf16 a[28:31], a[252:255], v[184:187], a[28:31]\n\t"
    "v_exp_f32 v211, v179\n\t"
    "v_mfma_f32_16x16x32_bf16 v[160:163], a[192:195], v[112:115], v[36:39]\n\t"
    "v_cmp_lt_f32 vcc, s71, v196\n\t"
    "v_cvt_pk_bf16_f32 v193, v210, v211\n\t"
    "v_mfma_f32_16x16x32_bf16 v[160:163], a[196:199], v[116:119], v[160:163]\n\t"
    "v_exp_f32 v212, v180\n\t"
    "v_mfma_f32_16x16x32_bf16 v[160:163], a[200:203], v[120:123], v[160:163]\n\t"
    "s_cbranch_vccnz Lstub_c8_1_%=\n\t"
    "Lret_c8_1_%=:\n\t"
    "v_add_f32 v49, v49, v196\n\t"
    "v_mfma_f32_16x16x32_bf16 v[160:163], a[204:207], v[124:127], v[160:163]\n\t"
    "v_exp_f32 v213, v181\n\t"
    "v_mfma_f32_16x16x32_bf16 v[164:167], a[208:211], v[112:115], v[36:39]\n\t"
    "v_cvt_pk_bf16_f32 v194, v212, v213\n\t"
    "v_mfma_f32_16x16x32_bf16 v[164:167], a[212:215], v[116:119], v[164:167]\n\t"
    "v_exp_f32 v214, v182\n\t"
    "v_mfma_f32_16x16x32_bf16 v[164:167], a[216:219], v[120:123], v[164:167]\n\t"
    "buffer_load_dwordx4 v9, s[44:47], s69 offen offset:2048 nt lds\n\t"
    "v_mfma_f32_16x16x32_bf16 v[164:167], a[220:223], v[124:127], v[164:167]\n\t"
    "v_exp_f32 v215, v183\n\t"
    "v_mfma_f32_16x16x32_bf16 a[32:35], a[224:227], v[188:191], a[32:35]\n\t"
    "v_cvt_pk_bf16_f32 v195, v214, v215\n\t"
    "v_mfma_f32_16x16x32_bf16 a[36:39], a[228:231], v[188:191], a[36:39]\n\t"
    "buffer_load_dwordx4 v10, s[44:47], s69 offen offset:3072 nt lds\n\t"
    "v_mfma_f32_16x16x32_bf16 a[40:43], a[232:235], v[188:191], a[40:43]\n\t"
    "s_add_u32 s69, s69, 0x2000\n\t"
    "s_add_u32 m0, s66, 0x6000\n\t"
    "v_exp_f32 v208, v160\n\t"
    "v_mfma_f32_16x16x32_bf16 v[196:199], v[54:57], v[192:195], 0\n\t"
    "s_and_b32 s69, s69, 0x1ffff\n\t"
    "buffer_load_dwordx4 v11, s[48:51], s70 offen nt lds\n\t"
    "v_mfma_f32_16x16x32_bf16 a[44:47], a[236:239], v[188:191], a[44:47]\n\t"
    "v_exp_f32 v209, v161\n\t"
    "v_mfma_f32_16x16x32_bf16 a[48:51], a[240:243], v[188:191], a[48:51]\n\t"
    "v_cvt_pk_bf16_f32 v184, v208, v209\n\t"
    "v_mfma_f32_16x16x32_bf16 a[52:55], a[244:247], v[188:191], a[52:55]\n\t"
    "v_exp_f32 v210, v162\n\t"
    "v_mfma_f32_16x16x32_bf16 a[56:59], a[248:251], v[188:191], a[56:59]\n\t"
    "v_mfma_f32_16x16x32_bf16 a[60:63], a[252:255], v[188:191], a[60:63]\n\t"
    "v_exp_f32 v211, v163\n\t"
    "v_mfma_f32_16x16x32_bf16 v[168:171], a[192:195], v[128:131], v[40:43]\n\t"
    "v_cmp_lt_f32 vcc, s71, v196\n\t"
    "v_cvt_pk_bf16_f32 v185, v210, v211\n\t"
    "v_mfma_f32_16x16x32_bf16 v[168:171], a[196:199], v[132:135], v[168:171]\n\t"
    "v_exp_f32 v212, v164\n\t"
    "v_mfma_f32_16x16x32_bf16 v[168:171], a[200:203], v[136:139], v[168:171]\n\t"
    "s_cbranch_vccnz Lstub_c8_2_%=\n\t"
    "Lret_c8_2_%=:\n\t"
    "v_add_f32 v50, v50, v196\n\t"
    "v_mfma_f32_16x16x32_bf16 v[168:171], a[204:207], v[140:143], v[168:171]\n\t"
    "v_exp_f32 v213, v165\n\t"
    "v_mfma_f32_16x16x32_bf16 v[172:175], a[208:211], v[128:131], v[40:43]\n\t"
    "v_cvt_pk_bf16_f32 v186, v212, v213\n\t"
    "v_mfma_f32_16x16x32_bf16 v[172:175], a[212:215], v[132:135], v[172:175]\n\t"
    "v_exp_f32 v214, v166\n\t"
    "v_mfma_f32_16x16x32_bf16 v[172:175], a[216:219], v[136:139], v[172:175]\n\t"
    "v_mfma_f32_16x16x32_bf16 v[172:175], a[220:223], v[140:143], v[172:175]\n\t"
    "v_exp_f32 v215, v167\n\t"
    "v_mfma_f32_16x16x32_bf16 a[64:67], a[224:227], v[192:195], a[64:67]\n\t"
    "v_cvt_pk_bf16_f32 v187, v214, v215\n\t"
    "v_mfma_f32_16x16x32_bf16 a[68:71], a[228:231], v[192:195], a[68:71]\n\t"
    "buffer_load_dwordx4 v12, s[48:51], s70 offen offset:1024 nt lds\n\t"
    "v_mfma_f32_16x16x32_bf16 a[72:75], a[232:235], v[192:195], a[72:75]\n\t"
    "v_exp_f32 v208, v168\n\t"
    "v_mfma_f32_16x16x32_bf16 v[196:199], v[54:57], v[184:187], 0\n\t"
    "v_mfma_f32_16x16x32_bf16 a[76:79], a[236:239], v[192:195], a[76:79]\n\t"
    "v_exp_f32 v209, v169\n\t"
    "v_mfma_f32_16x16x32_bf16 a[80:83], a[240:243], v[192:195], a[80:83]\n\t"
    "v_cvt_pk_bf16_f32 v188, v208, v209\n\t"
    "v_mfma_f32_16x16x32_bf16 a[84:87], a[244:247], v[192:195], a[84:87]\n\t"
    "v_exp_f32 v210, v170\n\t"
    "v_mfma_f32_16x16x32_bf16 a[88:91], a[248:251], v[192:195], a[88:91]\n\t"
    "v_mfma_f32_16x16x32_bf16 a[92:95], a[252:255], v[192:195], a[92:95]\n\t"
    "v_exp_f32 v211, v171\n\t"
    "v_mfma_f32_16x16x32_bf16 v[176:179], a[192:195], v[144:147], v[44:47]\n\t"
    "v_cmp_lt_f32 vcc, s71, v196\n\t"
    "v_cvt_pk_bf16_f32 v189, v210, v211\n\t"
    "v_mfma_f32_16x16x32_bf16 v[176:179], a[196:199], v[148:151], v[176:179]\n\t"
    "v_exp_f32 v212, v172\n\t"
    "v_mfma_f32_16x16x32_bf16 v[176:179], a[200:203], v[152:155], v[176:179]\n\t"
    "s_cbranch_vccnz Lstub_c8_3_%=\n\t"
    "Lret_c8_3_%=:\n\t"
    "ds_read_b128 a[192:195], v200 offset:0\n\t"
    "v_add_f32 v51, v51, v196\n\t"
    "v_mfma_f32_16x16x32_bf16 v[176:179], a[204:207], v[156:159], v[176:179]\n\t"
    "v_exp_f32 v213, v173\n\t"
    "v_mfma_f32_16x16x32_bf16 v[180:183], a[208:211], v[144:147], v[44:47]\n\t"
    "ds_read_b128 a[196:199], v201 offset:0\n\t"
    "v_cvt_pk_bf16_f32 v190, v212, v213\n\t"
    "v_mfma_f32_16x16x32_bf16 v[180:183], a[212:215], v[148:151], v[180:183]\n\t"
    "v_exp_f32 v214, v174\n\t"
    "v_mfma_f32_16x16x32_bf16 v[180:183], a[216:219], v[152:155], v[180:183]\n\t"
    "ds_read_b128 a[200:203], v202 offset:0\n\t"
    "ds_read_b128 a[204:207], v203 offset:0\n\t"
    "v_mfma_f32_16x16x32_bf16 v[180:183], a[220:223], v[156:159], v[180:183]\n\t"
    "v_exp_f32 v215, v175\n\t"
    "v_mfma_f32_16x16x32_bf16 a[96:99], a[224:227], v[184:187], a[96:99]\n\t"
    "ds_read_b128 a[208:211], v200 offset:4096\n\t"
    "v_cvt_pk_bf16_f32 v191, v214, v215\n\t"
    "v_mfma_f32_16x16x32_bf16 a[100:103], a[228:231], v[184:187], a[100:103]\n\t"
    "ds_read_b128 a[212:215], v201 offset:4096\n\t"
    "ds_read_b128 a[216:219], v202 offset:4096\n\t"
    "v_mfma_f32_16x16x32_bf16 a[104:107], a[232:235], v[184:187], a[104:107]\n\t"
    "v_exp_f32 v208, v176\n\t"
    "v_mfma_f32_16x16x32_bf16 v[196:199], v[54:57], v[188:191], 0\n\t"
    "ds_read_b128 a[220:223], v203 offset:4096\n\t"
    "v_mfma_f32_16x16x32_bf16 a[108:111], a[236:239], v[184:187], a[108:111]\n\t"
    "v_exp_f32 v209, v177\n\t"
    "v_mfma_f32_16x16x32_bf16 a[112:115], a[240:243], v[184:187], a[112:115]\n\t"
    "v_cvt_pk_bf16_f32 v192, v208, v209\n\t"
    "v_mfma_f32_16x16x32_bf16 a[116:119], a[244:247], v[184:187], a[116:119]\n\t"
    "v_exp_f32 v210, v178\n\t"
    "v_mfma_f32_16x16x32_bf16 a[120:123], a[248:251], v[184:187], a[120:123]\n\t"
    "buffer_load_dwordx4 v11, s[48:51], s70 offen offset:2048 nt lds\n\t"
    "v_mfma_f32_16x16x32_bf16 a[124:127], a[252:255], v[184:187], a[124:127]\n\t"
    "s_waitcnt lgkmcnt(0)\n\t"
    "v_exp_f32 v211, v179\n\t"
    "v_mfma_f32_16x16x32_bf16 v[160:163], a[192:195], v[64:67], v[24:27]\n\t"
    "v_cmp_lt_f32 vcc, s71, v196\n\t"
    "v_cvt_pk_bf16_f32 v193, v210, v211\n\t"
    "v_mfma_f32_16x16x32_bf16 v[160:163], a[196:199], v[68:71], v[160:163]\n\t"
    "v_exp_f32 v212, v180\n\t"
    "v_mfma_f32_16x16x32_bf16 v[160:163], a[200:203], v[72:75], v[160:163]\n\t"
    "s_cbranch_vccnz Lstub_c8_4_%=\n\t"
    "Lret_c8_4_%=:\n\t"
    "v_add_f32 v52, v52, v196\n\t"
    "v_mfma_f32_16x16x32_bf16 v[160:163], a[204:207], v[76:79], v[160:163]\n\t"
    "v_exp_f32 v213, v181\n\t"
    "v_mfma_f32_16x16x32_bf16 v[164:167], a[208:211], v[64:67], v[24:27]\n\t"
    "v_cvt_pk_bf16_f32 v194, v212, v213\n\t"
    "v_mfma_f32_16x16x32_bf16 v[164:167], a[212:215], v[68:71], v[164:167]\n\t"
    "v_exp_f32 v214, v182\n\t"
    "v_mfma_f32_16x16x32_bf16 v[164:167], a[216:219], v[72:75], v[164:167]\n\t"
    "buffer_load_dwordx4 v12, s[48:51], s70 offen offset:3072 nt lds\n\t"
    "v_mfma_f32_16x16x32_bf16 v[164:167], a[220:223], v[76:79], v[164:167]\n\t"
    "s_add_u32 s70, s70, 0x2000\n\t"
    "v_exp_f32 v215, v183\n\t"
    "v_mfma_f32_16x16x32_bf16 a[128:131], a[224:227], v[188:191], a[128:131]\n\t"
    "s_memrealtime s[98:99]\n\t"
    "s_and_b32 s70, s70, 0x1ffff\n\t"
    "v_cvt_pk_bf16_f32 v195, v214, v215\n\t"
    "v_mfma_f32_16x16x32_bf16 a[132:135], a[228:231], v[188:191], a[132:135]\n\t"
    "v_mfma_f32_16x16x32_bf16 a[136:139], a[232:235], v[188:191], a[136:139]\n\t"
    "v_exp_f32 v208, v160\n\t"
    "v_mfma_f32_16x16x32_bf16 v[196:199], v[54:57], v[192:195], 0\n\t"
    "v_mfma_f32_16x16x32_bf16 a[140:143], a[236:239], v[188:191], a[140:143]\n\t"
    "v_exp_f32 v209, v161\n\t"
    "v_mfma_f32_16x16x32_bf16 a[144:147], a[240:243], v[188:191], a[144:147]\n\t"
    "v_cvt_pk_bf16_f32 v184, v208, v209\n\t"
    "v_mfma_f32_16x16x32_bf16 a[148:151], a[244:247], v[188:191], a[148:151]\n\t"
    "v_exp_f32 v210, v162\n\t"
    "v_mfma_f32_16x16x32_bf16 a[152:155], a[248:251], v[188:191], a[152:155]\n\t"
    "v_mfma_f32_16x16x32_bf16 a[156:159], a[252:255], v[188:191], a[156:159]\n\t"
    "v_exp_f32 v211, v163\n\t"
    "v_mfma_f32_16x16x32_bf16 v[168:171], a[192:195], v[80:83], v[28:31]\n\t"
    "v_cmp_lt_f32 vcc, s71, v196\n\t"
    "v_cvt_pk_bf16_f32 v185, v210, v211\n\t"
    "v_mfma_f32_16x16x32_bf16 v[168:171], a[196:199], v[84:87], v[168:171]\n\t"
    "s_waitcnt lgkmcnt(0)\n\t"
    "v_exp_f32 v212, v164\n\t"
    "v_mfma_f32_16x16x32_bf16 v[168:171], a[200:203], v[88:91], v[168:171]\n\t"
    "s_cbranch_vccnz Lstub_c8_5_%=\n\t"
    "Lret_c8_5_%=:\n\t"
    "v_add_f32 v53, v53, v196\n\t"
    "v_mfma_f32_16x16x32_bf16 v[168:171], a[204:207], v[92:95], v[168:171]\n\t"
    "s_waitcnt vmcnt(12)\n\t"
    "v_exp_f32 v213, v165\n\t"
    "v_mfma_f32_16x16x32_bf16 v[172:175], a[208:211], v[80:83], v[28:31]\n\t"
    "s_sub_u32 s19, s18, s98\n\t"
    "v_cvt_pk_bf16_f32 v186, v212, v213\n\t"
    "v_mfma_f32_16x16x32_bf16 v[172:175], a[212:215], v[84:87], v[172:175]\n\t"
    "s_barrier\n\t"
    "v_exp_f32 v214, v166\n\t"
    "v_mfma_f32_16x16x32_bf16 v[172:175], a[216:219], v[88:91], v[172:175]\n\t"
    "s_cmp_gt_i32 s19, 0\n\t"
    "s_cbranch_scc1 Lpace9_%=\n\t"
    "Lpaced9_%=:\n\t"
    "v_mfma_f32_16x16x32_bf16 v[172:175], a[220:223], v[92:95], v[172:175]\n\t"
    "s_add_u32 s18, s18, 0x72\n\t"
    "v_exp_f32 v215, v167\n\t"
    "v_mfma_f32_16x16x32_bf16 a[160:163], a[224:227], v[192:195], a[160:163]\n\t"
    "v_cvt_pk_bf16_f32 v187, v214, v215\n\t"
    "v_mfma_f32_16x16x32_bf16 a[164:167], a[228:231], v[192:195], a[164:167]\n\t"
    "ds_read_b64_tr_b16 a[224:225], v204 offset:0\n\t"
    "ds_read_b64_tr_b16 a[226:227], v204 offset:4096\n\t"
    "v_mfma_f32_16x16x32_bf16 a[168:171], a[232:235], v[192:195], a[168:171]\n\t"
    "v_exp_f32 v208, v168\n\t"
    "v_mfma_f32_16x16x32_bf16 v[196:199], v[54:57], v[184:187], 0\n\t"
    "ds_read_b64_tr_b16 a[228:229], v205 offset:0\n\t"
    "ds_read_b64_tr_b16 a[230:231], v205 offset:4096\n\t"
    "v_mfma_f32_16x16x32_bf16 a[172:175], a[236:239], v[192:195], a[172:175]\n\t"
    "v_exp_f32 v209, v169\n\t"
    "v_mfma_f32_16x16x32_bf16 a[176:179], a[240:243], v[192:195], a[176:179]\n\t"
    "ds_read_b64_tr_b16 a[232:233], v204 offset:256\n\t"
    "v_cvt_pk_bf16_f32 v188, v208, v209\n\t"
    "v_mfma_f32_16x16x32_bf16 a[180:183], a[244:247], v[192:195], a[180:183]\n\t"
    "v_exp_f32 v210, v170\n\t"
    "v_mfma_f32_16x16x32_bf16 a[184:187], a[248:251], v[192:195], a[184:187]\n\t"
    "ds_read_b64_tr_b16 a[234:235], v204 offset:4352\n\t"
    "ds_read_b64_tr_b16 a[236:237], v205 offset:256\n\t"
    "v_mfma_f32_16x16x32_bf16 a[188:191], a[252:255], v[192:195], a[188:191]\n\t"
    "v_exp_f32 v211, v171\n\t"
    "v_mfma_f32_16x16x32_bf16 v[176:179], a[192:195], v[96:99], v[32:35]\n\t"
    "ds_read_b64_tr_b16 a[238:239], v205 offset:4352\n\t"
    "v_cmp_lt_f32 vcc, s71, v196\n\t"
    "v_cvt_pk_bf16_f32 v189, v210, v211\n\t"
    "v_mfma_f32_16x16x32_bf16 v[176:179], a[196:199], v[100:103], v[176:179]\n\t"
    "v_exp_f32 v212, v172\n\t"
    "v_mfma_f32_16x16x32_bf16 v[176:179], a[200:203], v[104:107], v[176:179]\n\t"
    "s_cbranch_vccnz Lstub_c9_0_%=\n\t"
    "Lret_c9_0_%=:\n\t"
    "ds_read_b64_tr_b16 a[240:241], v204 offset:512\n\t"
    "v_add_f32 v48, v48, v196\n\t"
    "v_mfma_f32_16x16x32_bf16 v[176:179], a[204:207], v[108:111], v[176:179]\n\t"
    "v_exp_f32 v213, v173\n\t"
    "v_mfma_f32_16x16x32_bf16 v[180:183], a[208:211], v[96:99], v[32:35]\n\t"
    "ds_read_b64_tr_b16 a[242:243], v204 offset:4608\n\t"
    "v_cvt_pk_bf16_f32 v190, v212, v213\n\t"
    "v_mfma_f32_16x16x32_bf16 v[180:183], a[212:215], v[100:103], v[180:183]\n\t"
    "v_exp_f32 v214, v174\n\t"
    "v_mfma_f32_16x16x32_bf16 v[180:183], a[216:219], v[104:107], v[180:183]\n\t"
    "ds_read_b64_tr_b16 a[244:245], v205 offset:512\n\t"
    "ds_read_b64_tr_b16 a[246:247], v205 offset:4608\n\t"
    "v_mfma_f32_16x16x32_bf16 v[180:183], a[220:223], v[108:111], v[180:183]\n\t"
    "s_waitcnt lgkmcnt(3)\n\t"
    "v_exp_f32 v215, v175\n\t"
    "v_mfma_f32_16x16x32_bf16 a[0:3], a[224:227], v[184:187], a[0:3]\n\t"
    "ds_read_b64_tr_b16 a[248:249], v204 offset:768\n\t"
    "v_cvt_pk_bf16_f32 v191, v214, v215\n\t"
    "v_mfma_f32_16x16x32_bf16 a[4:7], a[228:231], v[184:187], a[4:7]\n\t"
    "ds_read_b64_tr_b16 a[250:251], v204 offset:4864\n\t"
    "ds_read_b64_tr_b16 a[252:253], v205 offset:768\n\t"
    "v_mfma_f32_16x16x32_bf16 a[8:11], a[232:235], v[184:187], a[8:11]\n\t"
    "s_add_u32 s98, s98, 0x68\n\t"
    "v_exp_f32 v208, v176\n\t"
    "v_mfma_f32_16x16x32_bf16 v[196:199], v[54:57], v[188:191], 0\n\t"
    "ds_read_b64_tr_b16 a[254:255], v205 offset:4864\n\t"
    "v_mfma_f32_16x16x32_bf16 a[12:15], a[236:239], v[184:187], a[12:15]\n\t"
    "s_waitcnt lgkmcnt(4)\n\t"
    "s_max_u32 s18, s18, s98\n\t"
    "v_exp_f32 v209, v177\n\t"
    "v_mfma_f32_16x16x32_bf16 a[16:19], a[240:243], v[184:187], a[16:19]\n\t"
    "v_cvt_pk_bf16_f32 v192, v208, v209\n\t"
    "v_mfma_f32_16x16x32_bf16 a[20:23], a[244:247], v[184:187], a[20:23]\n\t"
    "s_waitcnt lgkmcnt(1)\n\t"
    "s_add_u32 m0, s66, 0x8000\n\t"
    "v_exp_f32 v210, v178\n\t"
    "v_mfma_f32_16x16x32_bf16 a[24:27], a[248:251], v[184:187], a[24:27]\n\t"
    "s_waitcnt lgkmcnt(0)\n\t"
    "buffer_load_dwordx4 v7, s[44:47], s69 offen nt lds\n\t"
    "v_mfma_f32_16x16x32_bf16 a[28:31], a[252:255], v[184:187], a[28:31]\n\t"
    "v_exp_f32 v211, v179\n\t"
    "v_mfma_f32_16x16x32_bf16 v[160:163], a[192:195], v[112:115], v[36:39]\n\t"
    "v_cmp_lt_f32 vcc, s71, v196\n\t"
    "v_cvt_pk_bf16_f32 v193, v210, v211\n\t"
    "v_mfma_f32_16x16x32_bf16 v[160:163], a[196:199], v[116:119], v[160:163]\n\t"
    "v_exp_f32 v212, v180\n\t"
    "v_mfma_f32_16x16x32_bf16 v[160:163], a[200:203], v[120:123], v[160:163]\n\t"
    "s_cbranch_vccnz Lstub_c9_1_%=\n\t"
    "Lret_c9_1_%=:\n\t"
    "v_add_f32 v49, v49, v196\n\t"
    "v_mfma_f32_16x16x32_bf16 v[160:163], a[204:207], v[124:127], v[160:163]\n\t"
    "v_exp_f32 v213, v181\n\t"
    "v_mfma_f32_16x16x32_bf16 v[164:167], a[208:211], v[112:115], v[36:39]\n\t"
    "v_cvt_pk_bf16_f32 v194, v212, v213\n\t"
    "v_mfma_f32_16x16x32_bf16 v[164:167], a[212:215], v[116:119], v[164:167]\n\t"
    "v_exp_f32 v214, v182\n\t"
    "v_mfma_f32_16x16x32_bf16 v[164:167], a[216:219], v[120:123], v[164:167]\n\t"
    "buffer_load_dwordx4 v8, s[44:47], s69 offen offset:1024 nt lds\n\t"
    "v_mfma_f32_16x16x32_bf16 v[164:167], a[220:223], v[124:127], v[164:167]\n\t"
    "v_exp_f32 v215, v183\n\t"
    "v_mfma_f32_16x16x32_bf16 a[32:35], a[224:227], v[188:191], a[32:35]\n\t"
    "v_cvt_pk_bf16_f32 v195, v214, v215\n\t"
    "v_mfma_f32_16x16x32_bf16 a[36:39], a[228:231], v[188:191], a[36:39]\n\t"
    "buffer_load_dwordx4 v9, s[44:47], s69 offen offset:2048 nt lds\n\t"
    "v_mfma_f32_16x16x32_bf16 a[40:43], a[232:235], v[188:191], a[40:43]\n\t"
    "v_exp_f32 v208, v160\n\t"
    "v_mfma_f32_16x16x32_bf16 v[196:199], v[54:57], v[192:195], 0\n\t"
    "v_mfma_f32_16x16x32_bf16 a[44:47], a[236:239], v[188:191], a[44:47]\n\t"
    "v_exp_f32 v209, v161\n\t"
    "v_mfma_f32_16x16x32_bf16 a[48:51], a[240:243], v[188:191], a[48:51]\n\t"
    "v_cvt_pk_bf16_f32 v184, v208, v209\n\t"
    "v_mfma_f32_16x16x32_bf16 a[52:55], a[244:247], v[188:191], a[52:55]\n\t"
    "v_exp_f32 v210, v162\n\t"
    "v_mfma_f32_16x16x32_bf16 a[56:59], a[248:251], v[188:191], a[56:59]\n\t"
    "buffer_load_dwordx4 v10, s[44:47], s69 offen offset:3072 nt lds\n\t"
    "v_mfma_f32_16x16x32_bf16 a[60:63], a[252:255], v[188:191], a[60:63]\n\t"
    "s_add_u32 s69, s69, 0x2000\n\t"
    "v_exp_f32 v211, v163\n\t"
    "v_mfma_f32_16x16x32_bf16 v[168:171], a[192:195], v[128:131], v[40:43]\n\t"
    "s_and_b32 s69, s69, 0x1ffff\n\t"
    "v_cmp_lt_f32 vcc, s71, v196\n\t"
    "v_cvt_pk_bf16_f32 v185, v210, v211\n\t"
    "v_mfma_f32_16x16x32_bf16 v[168:171], a[196:199], v[132:135], v[168:171]\n\t"
    "v_exp_f32 v212, v164\n\t"
    "v_mfma_f32_16x16x32_bf16 v[168:171], a[200:203], v[136:139], v[168:171]\n\t"
    "s_cbranch_vccnz Lstub_c9_2_%=\n\t"
    "Lret_c9_2_%=:\n\t"
    "v_add_f32 v50, v50, v196\n\t"
    "v_mfma_f32_16x16x32_bf16 v[168:171], a[204:207], v[140:143], v[168:171]\n\t"
    "v_exp_f32 v213, v165\n\t"
    "v_mfma_f32_16x16x32_bf16 v[172:175], a[208:211], v[128:131], v[40:43]\n\t"
    "v_cvt_pk_bf16_f32 v186, v212, v213\n\t"
    "v_mfma_f32_16x16x32_bf16 v[172:175], a[212:215], v[132:135], v[172:175]\n\t"
    "v_exp_f32 v214, v166\n\t"
    "v_mfma_f32_16x16x32_bf16 v[172:175], a[216:219], v[136:139], v[172:175]\n\t"
    "v_mfma_f32_16x16x32_bf16 v[172:175], a[220:223], v[140:143], v[172:175]\n\t"
    "v_exp_f32 v215, v167\n\t"
    "v_mfma_f32_16x16x32_bf16 a[64:67], a[224:227], v[192:195], a[64:67]\n\t"
    "v_cvt_pk_bf16_f32 v187, v214, v215\n\t"
    "v_mfma_f32_16x16x32_bf16 a[68:71], a[228:231], v[192:195], a[68:71]\n\t"
    "v_mfma_f32_16x16x32_bf16 a[72:75], a[232:235], v[192:195], a[72:75]\n\t"
    "s_add_u32 m0, s66, 0xa000\n\t"
    "v_exp_f32 v208, v168\n\t"
    "v_mfma_f32_16x16x32_bf16 v[196:199], v[54:57], v[184:187], 0\n\t"
    "buffer_load_dwordx4 v11, s[48:51], s70 offen nt lds\n\t"
    "v_mfma_f32_16x16x32_bf16 a[76:79], a[236:239], v[192:195], a[76:79]\n\t"
    "v_exp_f32 v209, v169\n\t"
    "v_mfma_f32_16x16x32_bf16 a[80:83], a[240:243], v[192:195], a[80:83]\n\t"
    "v_cvt_pk_bf16_f32 v188, v208, v209\n\t"
    "v_mfma_f32_16x16x32_bf16 a[84:87], a[244:247], v[192:195], a[84:87]\n\t"
    "v_exp_f32 v210, v170\n\t"
    "v_mfma_f32_16x16x32_bf16 a[88:91], a[248:251], v[192:195], a[88:91]\n\t"
    "v_mfma_f32_16x16x32_bf16 a[92:95], a[252:255], v[192:195], a[92:95]\n\t"
    "v_exp_f32 v211, v171\n\t"
    "v_mfma_f32_16x16x32_bf16 v[176:179], a[192:195], v[144:147], v[44:47]\n\t"
    "v_cmp_lt_f32 vcc, s71, v196\n\t"
    "v_cvt_pk_bf16_f32 v189, v210, v211\n\t"
    "v_mfma_f32_16x16x32_bf16 v[176:179], a[196:199], v[148:151], v[176:179]\n\t"
    "v_exp_f32 v212, v172\n\t"
    "v_mfma_f32_16x16x32_bf16 v[176:179], a[200:203], v[152:155], v[176:179]\n\t"
    "s_cbranch_vccnz Lstub_c9_3_%=\n\t"
    "Lret_c9_3_%=:\n\t"
    "ds_read_b128 a[192:195], v1 offset:0\n\t"
    "v_add_f32 v51, v51, v196\n\t"
    "v_mfma_f32_16x16x32_bf16 v[176:179], a[204:207], v[156:159], v[176:179]\n\t"
    "v_exp_f32 v213, v173\n\t"
    "v_mfma_f32_16x16x32_bf16 v[180:183], a[208:211], v[144:147], v[44:47]\n\t"
    "ds_read_b128 a[196:199], v2 offset:0\n\t"
    "v_cvt_pk_bf16_f32 v190, v212, v213\n\t"
    "v_mfma_f32_16x16x32_bf16 v[180:183], a[212:215], v[148:151], v[180:183]\n\t"
    "v_exp_f32 v214, v174\n\t"
    "v_mfma_f32_16x16x32_bf16 v[180:183], a[216:219], v[152:155], v[180:183]\n\t"
    "ds_read_b128 a[200:203], v3 offset:0\n\t"
    "ds_read_b128 a[204:207], v4 offset:0\n\t"
    "v_mfma_f32_16x16x32_bf16 v[180:183], a[220:223], v[156:159], v[180:183]\n\t"
    "v_exp_f32 v215, v175\n\t"
    "v_mfma_f32_16x16x32_bf16 a[96:99], a[224:227], v[184:187], a[96:99]\n\t"
    "ds_read_b128 a[208:211], v1 offset:4096\n\t"
    "v_cvt_pk_bf16_f32 v191, v214, v215\n\t"
    "v_mfma_f32_16x16x32_bf16 a[100:103], a[228:231], v[184:187], a[100:103]\n\t"
    "ds_read_b128 a[212:215], v2 offset:4096\n\t"
    "ds_read_b128 a[216:219], v3 offset:4096\n\t"
    "v_mfma_f32_16x16x32_bf16 a[104:107], a[232:235], v[184:187], a[104:107]\n\t"
    "v_exp_f32 v208, v176\n\t"
    "v_mfma_f32_16x16x32_bf16 v[196:199], v[54:57], v[188:191], 0\n\t"
    "ds_read_b128 a[220:223], v4 offset:4096\n\t"
    "v_mfma_f32_16x16x32_bf16 a[108:111], a[236:239], v[184:187], a[108:111]\n\t"
    "v_exp_f32 v209, v177\n\t"
    "v_mfma_f32_16x16x32_bf16 a[112:115], a[240:243], v[184:187], a[112:115]\n\t"
    "v_cvt_pk_bf16_f32 v192, v208, v209\n\t"
    "v_mfma_f32_16x16x32_bf16 a[116:119], a[244:247], v[184:187], a[116:119]\n\t"
    "v_exp_f32 v210, v178\n\t"
    "v_mfma_f32_16x16x32_bf16 a[120:123], a[248:251], v[184:187], a[120:123]\n\t"
    "buffer_load_dwordx4 v12, s[48:51], s70 offen offset:1024 nt lds\n\t"
    "v_mfma_f32_16x16x32_bf16 a[124:127], a[252:255], v[184:187], a[124:127]\n\t"
    "s_waitcnt lgkmcnt(0)\n\t"
    "v_exp_f32 v211, v179\n\t"
    "v_mfma_f32_16x16x32_bf16 v[160:163], a[192:195], v[64:67], v[24:27]\n\t"
    "v_cmp_lt_f32 vcc, s71, v196\n\t"
    "v_cvt_pk_bf16_f32 v193, v210, v211\n\t"
    "v_mfma_f32_16x16x32_bf16 v[160:163], a[196:199], v[68:71], v[160:163]\n\t"
    "v_exp_f32 v212, v180\n\t"
    "v_mfma_f32_16x16x32_bf16 v[160:163], a[200:203], v[72:75], v[160:163]\n\t"
    "s_cbranch_vccnz Lstub_c9_4_%=\n\t"
    "Lret_c9_4_%=:\n\t"
    "v_add_f32 v52, v52, v196\n\t"
    "v_mfma_f32_16x16x32_bf16 v[160:163], a[204:207], v[76:79], v[160:163]\n\t"
    "v_exp_f32 v213, v181\n\t"
    "v_mfma_f32_16x16x32_bf16 v[164:167], a[208:211], v[64:67], v[24:27]\n\t"
    "v_cvt_pk_bf16_f32 v194, v212, v213\n\t"
    "v_mfma_f32_16x16x32_bf16 v[164:167], a[212:215], v[68:71], v[164:167]\n\t"
    "v_exp_f32 v214, v182\n\t"
    "v_mfma_f32_16x16x32_bf16 v[164:167], a[216:219], v[72:75], v[164:167]\n\t"
    "buffer_load_dwordx4 v11, s[48:51], s70 offen offset:2048 nt lds\n\t"
    "v_mfma_f32_16x16x32_bf16 v[164:167], a[220:223], v[76:79], v[164:167]\n\t"
    "v_exp_f32 v215, v183\n\t"
    "v_mfma_f32_16x16x32_bf16 a[128:131], a[224:227], v[188:191], a[128:131]\n\t"
    "s_memrealtime s[98:99]\n\t"
    "v_cvt_pk_bf16_f32 v195, v214, v215\n\t"
    "v_mfma_f32_16x16x32_bf16 a[132:135], a[228:231], v[188:191], a[132:135]\n\t"
    "v_mfma_f32_16x16x32_bf16 a[136:139], a[232:235], v[188:191], a[136:139]\n\t"
    "v_exp_f32 v208, v160\n\t"
    "v_mfma_f32_16x16x32_bf16 v[196:199], v[54:57], v[192:195], 0\n\t"
    "v_mfma_f32_16x16x32_bf16 a[140:143], a[236:239], v[188:191], a[140:143]\n\t"
    "v_exp_f32 v209, v161\n\t"
    "v_mfma_f32_16x16x32_bf16 a[144:147], a[240:243], v[188:191], a[144:147]\n\t"
    "v_cvt_pk_bf16_f32 v184, v208, v209\n\t"
    "v_mfma_f32_16x16x32_bf16 a[148:151], a[244:247], v[188:191], a[148:151]\n\t"
    "v_exp_f32 v210, v162\n\t"
    "v_mfma_f32_16x16x32_bf16 a[152:155], a[248:251], v[188:191], a[152:155]\n\t"
    "v_mfma_f32_16x16x32_bf16 a[156:159], a[252:255], v[188:191], a[156:159]\n\t"
    "v_exp_f32 v211, v163\n\t"
    "v_mfma_f32_16x16x32_bf16 v[168:171], a[192:195], v[80:83], v[28:31]\n\t"
    "v_cmp_lt_f32 vcc, s71, v196\n\t"
    "v_cvt_pk_bf16_f32 v185, v210, v211\n\t"
    "v_mfma_f32_16x16x32_bf16 v[168:171], a[196:199], v[84:87], v[168:171]\n\t"
    "s_waitcnt lgkmcnt(0)\n\t"
    "v_exp_f32 v212, v164\n\t"
    "v_mfma_f32_16x16x32_bf16 v[168:171], a[200:203], v[88:91], v[168:171]\n\t"
    "s_cbranch_vccnz Lstub_c9_5_%=\n\t"
    "Lret_c9_5_%=:\n\t"
    "v_add_f32 v53, v53, v196\n\t"
    "v_mfma_f32_16x16x32_bf16 v[168:171], a[204:207], v[92:95], v[168:171]\n\t"
    "s_waitcnt vmcnt(11)\n\t"
    "v_exp_f32 v213, v165\n\t"
    "v_mfma_f32_16x16x32_bf16 v[172:175], a[208:211], v[80:83], v[28:31]\n\t"
    "s_sub_u32 s19, s18, s98\n\t"
    "v_cvt_pk_bf16_f32 v186, v212, v213\n\t"
    "v_mfma_f32_16x16x32_bf16 v[172:175], a[212:215], v[84:87], v[172:175]\n\t"
    "s_barrier\n\t"
    "v_exp_f32 v214, v166\n\t"
    "v_mfma_f32_16x16x32_bf16 v[172:175], a[216:219], v[88:91], v[172:175]\n\t"
    "s_cmp_gt_i32 s19, 0\n\t"
    "s_cbranch_scc1 Lpace10_%=\n\t"
    "Lpaced10_%=:\n\t"
    "v_mfma_f32_16x16x32_bf16 v[172:175], a[220:223], v[92:95], v[172:175]\n\t"
    "s_add_u32 s18, s18, 0x72\n\t"
    "v_exp_f32 v215, v167\n\t"
    "v_mfma_f32_16x16x32_bf16 a[160:163], a[224:227], v[192:195], a[160:163]\n\t"
    "v_cvt_pk_bf16_f32 v187, v214, v215\n\t"
    "v_mfma_f32_16x16x32_bf16 a[164:167], a[228:231], v[192:195], a[164:167]\n\t"
    "ds_read_b64_tr_b16 a[224:225], v5 offset:0\n\t"
    "ds_read_b64_tr_b16 a[226:227], v5 offset:4096\n\t"
    "v_mfma_f32_16x16x32_bf16 a[168:171], a[232:235], v[192:195], a[168:171]\n\t"
    "v_exp_f32 v208, v168\n\t"
    "v_mfma_f32_16x16x32_bf16 v[196:199], v[54:57], v[184:187], 0\n\t"
    "ds_read_b64_tr_b16 a[228:229], v6 offset:0\n\t"
    "ds_read_b64_tr_b16 a[230:231], v6 offset:4096\n\t"
    "v_mfma_f32_16x16x32_bf16 a[172:175], a[236:239], v[192:195], a[172:175]\n\t"
    "v_exp_f32 v209, v169\n\t"
    "v_mfma_f32_16x16x32_bf16 a[176:179], a[240:243], v[192:195], a[176:179]\n\t"
    "ds_read_b64_tr_b16 a[232:233], v5 offset:256\n\t"
    "v_cvt_pk_bf16_f32 v188, v208, v209\n\t"
    "v_mfma_f32_16x16x32_bf16 a[180:183], a[244:247], v[192:195], a[180:183]\n\t"
    "v_exp_f32 v210, v170\n\t"
    "v_mfma_f32_16x16x32_bf16 a[184:187], a[248:251], v[192:195], a[184:187]\n\t"
    "ds_read_b64_tr_b16 a[234:235], v5 offset:4352\n\t"
    "ds_read_b64_tr_b16 a[236:237], v6 offset:256\n\t"
    "v_mfma_f32_16x16x32_bf16 a[188:191], a[252:255], v[192:195], a[188:191]\n\t"
    "v_exp_f32 v211, v171\n\t"
    "v_mfma_f32_16x16x32_bf16 v[176:179], a[192:195], v[96:99], v[32:35]\n\t"
    "ds_read_b64_tr_b16 a[238:239], v6 offset:4352\n\t"
    "v_cmp_lt_f32 vcc, s71, v196\n\t"
    "v_cvt_pk_bf16_f32 v189, v210, v211\n\t"
    "v_mfma_f32_16x16x32_bf16 v[176:179], a[196:199], v[100:103], v[176:179]\n\t"
    "v_exp_f32 v212, v172\n\t"
    "v_mfma_f32_16x16x32_bf16 v[176:179], a[200:203], v[104:107], v[176:179]\n\t"
    "s_cbranch_vccnz Lstub_c10_0_%=\n\t"
    "Lret_c10_0_%=:\n\t"
    "ds_read_b64_tr_b16 a[240:241], v5 offset:512\n\t"
    "v_add_f32 v48, v48, v196\n\t"
    "v_mfma_f32_16x16x32_bf16 v[176:179], a[204:207], v[108:111], v[176:179]\n\t"
    "v_exp_f32 v213, v173\n\t"
    "v_mfma_f32_16x16x32_bf16 v[180:183], a[208:211], v[96:99], v[32:35]\n\t"
    "ds_read_b64_tr_b16 a[242:243], v5 offset:4608\n\t"
    "v_cvt_pk_bf16_f32 v190, v212, v213\n\t"
    "v_mfma_f32_16x16x32_bf16 v[180:183], a[212:215], v[100:103], v[180:183]\n\t"
    "v_exp_f32 v214, v174\n\t"
    "v_mfma_f32_16x16x32_bf16 v[180:183], a[216:219], v[104:107], v[180:183]\n\t"
    "ds_read_b64_tr_b16 a[244:245], v6 offset:512\n\t"
    "ds_read_b64_tr_b16 a[246:247], v6 offset:4608\n\t"
    "v_mfma_f32_16x16x32_bf16 v[180:183], a[220:223], v[108:111], v[180:183]\n\t"
    "s_waitcnt lgkmcnt(3)\n\t"
    "v_exp_f32 v215, v175\n\t"
    "v_mfma_f32_16x16x32_bf16 a[0:3], a[224:227], v[184:187], a[0:3]\n\t"
    "ds_read_b64_tr_b16 a[248:249], v5 offset:768\n\t"
    "v_cvt_pk_bf16_f32 v191, v214, v215\n\t"
    "v_mfma_f32_16x16x32_bf16 a[4:7], a[228:231], v[184:187], a[4:7]\n\t"
    "ds_read_b64_tr_b16 a[250:251], v5 offset:4864\n\t"
    "ds_read_b64_tr_b16 a[252:253], v6 offset:768\n\t"
    "v_mfma_f32_16x16x32_bf16 a[8:11], a[232:235], v[184:187], a[8:11]\n\t"
    "s_add_u32 s98, s98, 0x68\n\t"
    "v_exp_f32 v208, v176\n\t"
    "v_mfma_f32_16x16x32_bf16 v[196:199], v[54:57], v[188:191], 0\n\t"
    "ds_read_b64_tr_b16 a[254:255], v6 offset:4864\n\t"
    "v_mfma_f32_16x16x32_bf16 a[12:15], a[236:239], v[184:187], a[12:15]\n\t"
    "s_waitcnt lgkmcnt(4)\n\t"
    "s_max_u32 s18, s18, s98\n\t"
    "v_exp_f32 v209, v177\n\t"
    "v_mfma_f32_16x16x32_bf16 a[16:19], a[240:243], v[184:187], a[16:19]\n\t"
    "v_cvt_pk_bf16_f32 v192, v208, v209\n\t"
    "v_mfma_f32_16x16x32_bf16 a[20:23], a[244:247], v[184:187], a[20:23]\n\t"
    "s_waitcnt lgkmcnt(1)\n\t"
    "v_exp_f32 v210, v178\n\t"
    "v_mfma_f32_16x16x32_bf16 a[24:27], a[248:251], v[184:187], a[24:27]\n\t"
    "s_waitcnt lgkmcnt(0)\n\t"
    "buffer_load_dwordx4 v12, s[48:51], s70 offen offset:3072 nt lds\n\t"
    "v_mfma_f32_16x16x32_bf16 a[28:31], a[252:255], v[184:187], a[28:31]\n\t"
    "s_add_u32 s70, s70, 0x2000\n\t"
    "v_exp_f32 v211, v179\n\t"
    "v_mfma_f32_16x16x32_bf16 v[160:163], a[192:195], v[112:115], v[36:39]\n\t"
    "s_and_b32 s70, s70, 0x1ffff\n\t"
    "v_cmp_lt_f32 vcc, s71, v196\n\t"
    "v_cvt_pk_bf16_f32 v193, v210, v211\n\t"
    "v_mfma_f32_16x16x32_bf16 v[160:163], a[196:199], v[116:119], v[160:163]\n\t"
    "v_exp_f32 v212, v180\n\t"
    "v_mfma_f32_16x16x32_bf16 v[160:163], a[200:203], v[120:123], v[160:163]\n\t"
    "s_cbranch_vccnz Lstub_c10_1_%=\n\t"
    "Lret_c10_1_%=:\n\t"
    "v_add_f32 v49, v49, v196\n\t"
    "v_mfma_f32_16x16x32_bf16 v[160:163], a[204:207], v[124:127], v[160:163]\n\t"
    "v_exp_f32 v213, v181\n\t"
    "v_mfma_f32_16x16x32_bf16 v[164:167], a[208:211], v[112:115], v[36:39]\n\t"
    "v_cvt_pk_bf16_f32 v194, v212, v213\n\t"
    "v_mfma_f32_16x16x32_bf16 v[164:167], a[212:215], v[116:119], v[164:167]\n\t"
    "s_add_u32 m0, s66, 0xc000\n\t"
    "v_exp_f32 v214, v182\n\t"
    "v_mfma_f32_16x16x32_bf16 v[164:167], a[216:219], v[120:123], v[164:167]\n\t"
    "buffer_load_dwordx4 v7, s[44:47], s69 offen nt lds\n\t"
    "v_mfma_f32_16x16x32_bf16 v[164:167], a[220:223], v[124:127], v[164:167]\n\t"
    "v_exp_f32 v215, v183\n\t"
    "v_mfma_f32_16x16x32_bf16 a[32:35], a[224:227], v[188:191], a[32:35]\n\t"
    "v_cvt_pk_bf16_f32 v195, v214, v215\n\t"
    "v_mfma_f32_16x16x32_bf16 a[36:39], a[228:231], v[188:191], a[36:39]\n\t"
    "buffer_load_dwordx4 v8, s[44:47], s69 offen offset:1024 nt lds\n\t"
    "s_getpc_b64 s[4:5]\n\t"
    "Lrt10_%=:\n\t"
    "s_mov_b32 s6, Lend_%=-Lrt10_%=\n\t"
    "s_mov_b32 s7, 0x20000\n\t"
    "s_lshr_b32 s8, %1, 3\n\t"
    "s_lshl_b32 s8, s8, 2\n\t"
    "s_add_u32 s8, s8, s56\n\t"
    "s_lshl_b32 s8, s8, 9\n\t"
    "v_cmp_gt_u32 s[76:77], 4, v0\n\t"
    "v_lshlrev_b32 v229, 7, v0\n\t"
    "v_mov_b32 v230, 0x40000000\n\t"
    "v_cndmask_b32 v229, v230, v229, s[76:77]\n\t"
    "buffer_load_dword v23, v229, s[4:7], s8 offen\n\t"
    "v_mfma_f32_16x16x32_bf16 a[40:43], a[232:235], v[188:191], a[40:43]\n\t"
    "v_exp_f32 v208, v160\n\t"
    "v_mfma_f32_16x16x32_bf16 v[196:199], v[54:57], v[192:195], 0\n\t"
    "v_mfma_f32_16x16x32_bf16 a[44:47], a[236:239], v[188:191], a[44:47]\n\t"
    "v_exp_f32 v209, v161\n\t"
    "v_mfma_f32_16x16x32_bf16 a[48:51], a[240:243], v[188:191], a[48:51]\n\t"
    "v_cvt_pk_bf16_f32 v184, v208, v209\n\t"
    "v_mfma_f32_16x16x32_bf16 a[52:55], a[244:247], v[188:191], a[52:55]\n\t"
    "v_exp_f32 v210, v162\n\t"
    "v_mfma_f32_16x16x32_bf16 a[56:59], a[248:251], v[188:191], a[56:59]\n\t"
    "buffer_load_dwordx4 v9, s[44:47], s69 offen offset:2048 nt lds\n\t"
    "v_mfma_f32_16x16x32_bf16 a[60:63], a[252:255], v[188:191], a[60:63]\n\t"
    "v_exp_f32 v211, v163\n\t"
    "v_mfma_f32_16x16x32_bf16 v[168:171], a[192:195], v[128:131], v[40:43]\n\t"
    "v_cmp_lt_f32 vcc, s71, v196\n\t"
    "v_cvt_pk_bf16_f32 v185, v210, v211\n\t"
    "v_mfma_f32_16x16x32_bf16 v[168:171], a[196:199], v[132:135], v[168:171]\n\t"
    "v_exp_f32 v212, v164\n\t"
    "v_mfma_f32_16x16x32_bf16 v[168:171], a[200:203], v[136:139], v[168:171]\n\t"
    "s_cbranch_vccnz Lstub_c10_2_%=\n\t"
    "Lret_c10_2_%=:\n\t"
    "v_add_f32 v50, v50, v196\n\t"
    "v_mfma_f32_16x16x32_bf16 v[168:171], a[204:207], v[140:143], v[168:171]\n\t"
    "v_exp_f32 v213, v165\n\t"
    "v_mfma_f32_16x16x32_bf16 v[172:175], a[208:211], v[128:131], v[40:43]\n\t"
    "v_cvt_pk_bf16_f32 v186, v212, v213\n\t"
    "v_mfma_f32_16x16x32_bf16 v[172:175], a[212:215], v[132:135], v[172:175]\n\t"
    "v_exp_f32 v214, v166\n\t"
    "v_mfma_f32_16x16x32_bf16 v[172:175], a[216:219], v[136:139], v[172:175]\n\t"
    "v_mfma_f32_16x16x32_bf16 v[172:175], a[220:223], v[140:143], v[172:175]\n\t"
    "v_exp_f32 v215, v167\n\t"
    "v_mfma_f32_16x16x32_bf16 a[64:67], a[224:227], v[192:195], a[64:67]\n\t"
    "v_cvt_pk_bf16_f32 v187, v214, v215\n\t"
    "v_mfma_f32_16x16x32_bf16 a[68:71], a[228:231], v[192:195], a[68:71]\n\t"
    "v_mfma_f32_16x16x32_bf16 a[72:75], a[232:235], v[192:195], a[72:75]\n\t"
    "v_exp_f32 v208, v168\n\t"
    "v_mfma_f32_16x16x32_bf16 v[196:199], v[54:57], v[184:187], 0\n\t"
    "v_mfma_f32_16x16x32_bf16 a[76:79], a[236:239], v[192:195], a[76:79]\n\t"
    "v_exp_f32 v209, v169\n\t"
    "v_mfma_f32_16x16x32_bf16 a[80:83], a[240:243], v[192:195], a[80:83]\n\t"
    "v_cvt_pk_bf16_f32 v188, v208, v209\n\t"
    "v_mfma_f32_16x16x32_bf16 a[84:87], a[244:247], v[192:195], a[84:87]\n\t"
    "v_exp_f32 v210, v170\n\t"
    "v_mfma_f32_16x16x32_bf16 a[88:91], a[248:251], v[192:195], a[88:91]\n\t"
    "buffer_load_dwordx4 v10, s[44:47], s69 offen offset:3072 nt lds\n\t"
    "v_mfma_f32_16x16x32_bf16 a[92:95], a[252:255], v[192:195], a[92:95]\n\t"
    "s_add_u32 s69, s69, 0x2000\n\t"
    "v_exp_f32 v211, v171\n\t"
    "v_mfma_f32_16x16x32_bf16 v[176:179], a[192:195], v[144:147], v[44:47]\n\t"
    "s_and_b32 s69, s69, 0x1ffff\n\t"
    "v_cmp_lt_f32 vcc, s71, v196\n\t"
    "v_cvt_pk_bf16_f32 v189, v210, v211\n\t"
    "v_mfma_f32_16x16x32_bf16 v[176:179], a[196:199], v[148:151], v[176:179]\n\t"
    "v_exp_f32 v212, v172\n\t"
    "v_mfma_f32_16x16x32_bf16 v[176:179], a[200:203], v[152:155], v[176:179]\n\t"
    "s_cbranch_vccnz Lstub_c10_3_%=\n\t"
    "Lret_c10_3_%=:\n\t"
    "ds_read_b128 a[192:195], v1 offset:16384\n\t"
    "v_add_f32 v51, v51, v196\n\t"
    "v_mfma_f32_16x16x32_bf16 v[176:179], a[204:207], v[156:159], v[176:179]\n\t"
    "v_exp_f32 v213, v173\n\t"
    "v_mfma_f32_16x16x32_bf16 v[180:183], a[208:211], v[144:147], v[44:47]\n\t"
    "ds_read_b128 a[196:199], v2 offset:16384\n\t"
    "v_cvt_pk_bf16_f32 v190, v212, v213\n\t"
    "v_mfma_f32_16x16x32_bf16 v[180:183], a[212:215], v[148:151], v[180:183]\n\t"
    "v_exp_f32 v214, v174\n\t"
    "v_mfma_f32_16x16x32_bf16 v[180:183], a[216:219], v[152:155], v[180:183]\n\t"
    "ds_read_b128 a[200:203], v3 offset:16384\n\t"
    "ds_read_b128 a[204:207], v4 offset:16384\n\t"
    "v_mfma_f32_16x16x32_bf16 v[180:183], a[220:223], v[156:159], v[180:183]\n\t"
    "v_exp_f32 v215, v175\n\t"
    "v_mfma_f32_16x16x32_bf16 a[96:99], a[224:227], v[184:187], a[96:99]\n\t"
    "ds_read_b128 a[208:211], v1 offset:20480\n\t"
    "v_cvt_pk_bf16_f32 v191, v214, v215\n\t"
    "v_mfma_f32_16x16x32_bf16 a[100:103], a[228:231], v[184:187], a[100:103]\n\t"
    "ds_read_b128 a[212:215], v2 offset:20480\n\t"
    "ds_read_b128 a[216:219], v3 offset:20480\n\t"
    "v_mfma_f32_16x16x32_bf16 a[104:107], a[232:235], v[184:187], a[104:107]\n\t"
    "v_exp_f32 v208, v176\n\t"
    "v_mfma_f32_16x16x32_bf16 v[196:199], v[54:57], v[188:191], 0\n\t"
    "ds_read_b128 a[220:223], v4 offset:20480\n\t"
    "v_mfma_f32_16x16x32_bf16 a[108:111], a[236:239], v[184:187], a[108:111]\n\t"
    "v_exp_f32 v209, v177\n\t"
    "v_mfma_f32_16x16x32_bf16 a[112:115], a[240:243], v[184:187], a[112:115]\n\t"
    "v_cvt_pk_bf16_f32 v192, v208, v209\n\t"
    "v_mfma_f32_16x16x32_bf16 a[116:119], a[244:247], v[184:187], a[116:119]\n\t"
    "s_add_u32 m0, s66, 0xe000\n\t"
    "v_exp_f32 v210, v178\n\t"
    "v_mfma_f32_16x16x32_bf16 a[120:123], a[248:251], v[184:187], a[120:123]\n\t"
    "buffer_load_dwordx4 v11, s[48:51], s70 offen nt lds\n\t"
    "v_mfma_f32_16x16x32_bf16 a[124:127], a[252:255], v[184:187], a[124:127]\n\t"
    "s_waitcnt lgkmcnt(0)\n\t"
    "v_exp_f32 v211, v179\n\t"
    "v_mfma_f32_16x16x32_bf16 v[160:163], a[192:195], v[64:67], v[24:27]\n\t"
    "v_cmp_lt_f32 vcc, s71, v196\n\t"
    "v_cvt_pk_bf16_f32 v193, v210, v211\n\t"
    "v_mfma_f32_16x16x32_bf16 v[160:163], a[196:199], v[68:71], v[160:163]\n\t"
    "v_exp_f32 v212, v180\n\t"
    "v_mfma_f32_16x16x32_bf16 v[160:163], a[200:203], v[72:75], v[160:163]\n\t"
    "s_cbranch_vccnz Lstub_c10_4_%=\n\t"
    "Lret_c10_4_%=:\n\t"
    "v_add_f32 v52, v52, v196\n\t"
    "v_mfma_f32_16x16x32_bf16 v[160:163], a[204:207], v[76:79], v[160:163]\n\t"
    "v_exp_f32 v213, v181\n\t"
    "v_mfma_f32_16x16x32_bf16 v[164:167], a[208:211], v[64:67], v[24:27]\n\t"
    "v_cvt_pk_bf16_f32 v194, v212, v213\n\t"
    "v_mfma_f32_16x16x32_bf16 v[164:167], a[212:215], v[68:71], v[164:167]\n\t"
    "v_exp_f32 v214, v182\n\t"
    "v_mfma_f32_16x16x32_bf16 v[164:167], a[216:219], v[72:75], v[164:167]\n\t"
    "v_mfma_f32_16x16x32_bf16 v[164:167], a[220:223], v[76:79], v[164:167]\n\t"
    "v_exp_f32 v215, v183\n\t"
    "v_mfma_f32_16x16x32_bf16 a[128:131], a[224:227], v[188:191], a[128:131]\n\t"
    "s_memrealtime s[98:99]\n\t"
    "v_cvt_pk_bf16_f32 v195, v214, v215\n\t"
    "v_mfma_f32_16x16x32_bf16 a[132:135], a[228:231], v[188:191], a[132:135]\n\t"
    "buffer_load_dwordx4 v12, s[48:51], s70 offen offset:1024 nt lds\n\t"
    "v_mfma_f32_16x16x32_bf16 a[136:139], a[232:235], v[188:191], a[136:139]\n\t"
    "v_exp_f32 v208, v160\n\t"
    "v_mfma_f32_16x16x32_bf16 v[196:199], v[54:57], v[192:195], 0\n\t"
    "v_mfma_f32_16x16x32_bf16 a[140:143], a[236:239], v[188:191], a[140:143]\n\t"
    "v_exp_f32 v209, v161\n\t"
    "v_mfma_f32_16x16x32_bf16 a[144:147], a[240:243], v[188:191], a[144:147]\n\t"
    "v_cvt_pk_bf16_f32 v184, v208, v209\n\t"
    "v_mfma_f32_16x16x32_bf16 a[148:151], a[244:247], v[188:191], a[148:151]\n\t"
    "v_exp_f32 v210, v162\n\t"
    "v_mfma_f32_16x16x32_bf16 a[152:155], a[248:251], v[188:191], a[152:155]\n\t"
    "v_mfma_f32_16x16x32_bf16 a[156:159], a[252:255], v[188:191], a[156:159]\n\t"
    "v_exp_f32 v211, v163\n\t"
    "v_mfma_f32_16x16x32_bf16 v[168:171], a[192:195], v[80:83], v[28:31]\n\t"
    "v_cmp_lt_f32 vcc, s71, v196\n\t"
    "v_cvt_pk_bf16_f32 v185, v210, v211\n\t"
    "v_mfma_f32_16x16x32_bf16 v[168:171], a[196:199], v[84:87], v[168:171]\n\t"
    "s_waitcnt lgkmcnt(0)\n\t"
    "v_exp_f32 v212, v164\n\t"
    "v_mfma_f32_16x16x32_bf16 v[168:171], a[200:203], v[88:91], v[168:171]\n\t"
    "s_cbranch_vccnz Lstub_c10_5_%=\n\t"
    "Lret_c10_5_%=:\n\t"
    "v_add_f32 v53, v53, v196\n\t"
    "v_mfma_f32_16x16x32_bf16 v[168:171], a[204:207], v[92:95], v[168:171]\n\t"
    "s_waitcnt vmcnt(11)\n\t"
    "v_exp_f32 v213, v165\n\t"
    "v_mfma_f32_16x16x32_bf16 v[172:175], a[208:211], v[80:83], v[28:31]\n\t"
    "s_sub_u32 s19, s18, s98\n\t"
    "v_cvt_pk_bf16_f32 v186, v212, v213\n\t"
    "v_mfma_f32_16x16x32_bf16 v[172:175], a[212:215], v[84:87], v[172:175]\n\t"
    "s_barrier\n\t"
    "v_exp_f32 v214, v166\n\t"
    "v_mfma_f32_16x16x32_bf16 v[172:175], a[216:219], v[88:91], v[172:175]\n\t"
    "s_cmp_gt_i32 s19, 0\n\t"
    "s_cbranch_scc1 Lpace11_%=\n\t"
    "Lpaced11_%=:\n\t"
    "v_mfma_f32_16x16x32_bf16 v[172:175], a[220:223], v[92:95], v[172:175]\n\t"
    "s_add_u32 s18, s18, 0x72\n\t"
    "v_exp_f32 v215, v167\n\t"
    "v_mfma_f32_16x16x32_bf16 a[160:163], a[224:227], v[192:195], a[160:163]\n\t"
    "v_cvt_pk_bf16_f32 v187, v214, v215\n\t"
    "v_mfma_f32_16x16x32_bf16 a[164:167], a[228:231], v[192:195], a[164:167]\n\t"
    "ds_read_b64_tr_b16 a[224:225], v5 offset:16384\n\t"
    "ds_read_b64_tr_b16 a[226:227], v5 offset:20480\n\t"
    "v_mfma_f32_16x16x32_bf16 a[168:171], a[232:235], v[192:195], a[168:171]\n\t"
    "v_exp_f32 v208, v168\n\t"
    "v_mfma_f32_16x16x32_bf16 v[196:199], v[54:57], v[184:187], 0\n\t"
    "ds_read_b64_tr_b16 a[228:229], v6 offset:16384\n\t"
    "ds_read_b64_tr_b16 a[230:231], v6 offset:20480\n\t"
    "v_mfma_f32_16x16x32_bf16 a[172:175], a[236:239], v[192:195], a[172:175]\n\t"
    "v_exp_f32 v209, v169\n\t"
    "v_mfma_f32_16x16x32_bf16 a[176:179], a[240:243], v[192:195], a[176:179]\n\t"
    "ds_read_b64_tr_b16 a[232:233], v5 offset:16640\n\t"
    "v_cvt_pk_bf16_f32 v188, v208, v209\n\t"
    "v_mfma_f32_16x16x32_bf16 a[180:183], a[244:247], v[192:195], a[180:183]\n\t"
    "v_exp_f32 v210, v170\n\t"
    "v_mfma_f32_16x16x32_bf16 a[184:187], a[248:251], v[192:195], a[184:187]\n\t"
    "ds_read_b64_tr_b16 a[234:235], v5 offset:20736\n\t"
    "ds_read_b64_tr_b16 a[236:237], v6 offset:16640\n\t"
    "v_mfma_f32_16x16x32_bf16 a[188:191], a[252:255], v[192:195], a[188:191]\n\t"
    "v_exp_f32 v211, v171\n\t"
    "v_mfma_f32_16x16x32_bf16 v[176:179], a[192:195], v[96:99], v[32:35]\n\t"
    "ds_read_b64_tr_b16 a[238:239], v6 offset:20736\n\t"
    "v_cmp_lt_f32 vcc, s71, v196\n\t"
    "v_cvt_pk_bf16_f32 v189, v210, v211\n\t"
    "v_mfma_f32_16x16x32_bf16 v[176:179], a[196:199], v[100:103], v[176:179]\n\t"
    "v_exp_f32 v212, v172\n\t"
    "v_mfma_f32_16x16x32_bf16 v[176:179], a[200:203], v[104:107], v[176:179]\n\t"
    "s_cbranch_vccnz Lstub_c11_0_%=\n\t"
    "Lret_c11_0_%=:\n\t"
    "ds_read_b64_tr_b16 a[240:241], v5 offset:16896\n\t"
    "v_add_f32 v48, v48, v196\n\t"
    "v_mfma_f32_16x16x32_bf16 v[176:179], a[204:207], v[108:111], v[176:179]\n\t"
    "v_exp_f32 v213, v173\n\t"
    "v_mfma_f32_16x16x32_bf16 v[180:183], a[208:211], v[96:99], v[32:35]\n\t"
    "ds_read_b64_tr_b16 a[242:243], v5 offset:20992\n\t"
    "v_cvt_pk_bf16_f32 v190, v212, v213\n\t"
    "v_mfma_f32_16x16x32_bf16 v[180:183], a[212:215], v[100:103], v[180:183]\n\t"
    "v_exp_f32 v214, v174\n\t"
    "v_mfma_f32_16x16x32_bf16 v[180:183], a[216:219], v[104:107], v[180:183]\n\t"
    "ds_read_b64_tr_b16 a[244:245], v6 offset:16896\n\t"
    "ds_read_b64_tr_b16 a[246:247], v6 offset:20992\n\t"
    "v_mfma_f32_16x16x32_bf16 v[180:183], a[220:223], v[108:111], v[180:183]\n\t"
    "s_waitcnt lgkmcnt(3)\n\t"
    "v_exp_f32 v215, v175\n\t"
    "v_mfma_f32_16x16x32_bf16 a[0:3], a[224:227], v[184:187], a[0:3]\n\t"
    "ds_read_b64_tr_b16 a[248:249], v5 offset:17152\n\t"
    "v_cvt_pk_bf16_f32 v191, v214, v215\n\t"
    "v_mfma_f32_16x16x32_bf16 a[4:7], a[228:231], v[184:187], a[4:7]\n\t"
    "ds_read_b64_tr_b16 a[250:251], v5 offset:21248\n\t"
    "ds_read_b64_tr_b16 a[252:253], v6 offset:17152\n\t"
    "v_mfma_f32_16x16x32_bf16 a[8:11], a[232:235], v[184:187], a[8:11]\n\t"
    "s_add_u32 s98, s98, 0x68\n\t"
    "v_exp_f32 v208, v176\n\t"
    "v_mfma_f32_16x16x32_bf16 v[196:199], v[54:57], v[188:191], 0\n\t"
    "ds_read_b64_tr_b16 a[254:255], v6 offset:21248\n\t"
    "v_mfma_f32_16x16x32_bf16 a[12:15], a[236:239], v[184:187], a[12:15]\n\t"
    "s_waitcnt lgkmcnt(4)\n\t"
    "s_max_u32 s18, s18, s98\n\t"
    "v_exp_f32 v209, v177\n\t"
    "v_mfma_f32_16x16x32_bf16 a[16:19], a[240:243], v[184:187], a[16:19]\n\t"
    "v_cvt_pk_bf16_f32 v192, v208, v209\n\t"
    "v_mfma_f32_16x16x32_bf16 a[20:23], a[244:247], v[184:187], a[20:23]\n\t"
    "s_waitcnt lgkmcnt(1)\n\t"
    "v_exp_f32 v210, v178\n\t"
    "v_mfma_f32_16x16x32_bf16 a[24:27], a[248:251], v[184:187], a[24:27]\n\t"
    "s_waitcnt lgkmcnt(0)\n\t"
    "buffer_load_dwordx4 v11, s[48:51], s70 offen offset:2048 nt lds\n\t"
    "v_mfma_f32_16x16x32_bf16 a[28:31], a[252:255], v[184:187], a[28:31]\n\t"
    "v_exp_f32 v211, v179\n\t"
    "v_mfma_f32_16x16x32_bf16 v[160:163], a[192:195], v[112:115], v[36:39]\n\t"
    "v_cmp_lt_f32 vcc, s71, v196\n\t"
    "v_cvt_pk_bf16_f32 v193, v210, v211\n\t"
    "v_mfma_f32_16x16x32_bf16 v[160:163], a[196:199], v[116:119], v[160:163]\n\t"
    "v_exp_f32 v212, v180\n\t"
    "v_mfma_f32_16x16x32_bf16 v[160:163], a[200:203], v[120:123], v[160:163]\n\t"
    "s_cbranch_vccnz Lstub_c11_1_%=\n\t"
    "Lret_c11_1_%=:\n\t"
    "v_add_f32 v49, v49, v196\n\t"
    "v_mfma_f32_16x16x32_bf16 v[160:163], a[204:207], v[124:127], v[160:163]\n\t"
    "v_exp_f32 v213, v181\n\t"
    "v_mfma_f32_16x16x32_bf16 v[164:167], a[208:211], v[112:115], v[36:39]\n\t"
    "v_cvt_pk_bf16_f32 v194, v212, v213\n\t"
    "v_mfma_f32_16x16x32_bf16 v[164:167], a[212:215], v[116:119], v[164:167]\n\t"
    "v_exp_f32 v214, v182\n\t"
    "v_mfma_f32_16x16x32_bf16 v[164:167], a[216:219], v[120:123], v[164:167]\n\t"
    "buffer_load_dwordx4 v12, s[48:51], s70 offen offset:3072 nt lds\n\t"
    "v_mfma_f32_16x16x32_bf16 v[164:167], a[220:223], v[124:127], v[164:167]\n\t"
    "s_add_u32 s70, s70, 0x2000\n\t"
    "v_exp_f32 v215, v183\n\t"
    "v_mfma_f32_16x16x32_bf16 a[32:35], a[224:227], v[188:191], a[32:35]\n\t"
    "s_and_b32 s70, s70, 0x1ffff\n\t"
    "s_add_u32 m0, s97, 0\n\t"
    "v_cvt_pk_bf16_f32 v195, v214, v215\n\t"
    "v_mfma_f32_16x16x32_bf16 a[36:39], a[228:231], v[188:191], a[36:39]\n\t"
    "buffer_load_dwordx4 v7, s[44:47], s69 offen nt lds\n\t"
    "v_mfma_f32_16x16x32_bf16 a[40:43], a[232:235], v[188:191], a[40:43]\n\t"
    "s_add_u32 m0, s59, 0x4000\n\t"
    "v_exp_f32 v208, v160\n\t"
    "v_mfma_f32_16x16x32_bf16 v[196:199], v[54:57], v[192:195], 0\n\t"
    "s_add_u32 s78, s64, 0\n\t"
    "buffer_load_dwordx4 v62, s[36:39], s78 offen lds\n\t"
    "v_mfma_f32_16x16x32_bf16 a[44:47], a[236:239], v[188:191], a[44:47]\n\t"
    "v_exp_f32 v209, v161\n\t"
    "v_mfma_f32_16x16x32_bf16 a[48:51], a[240:243], v[188:191], a[48:51]\n\t"
    "v_cvt_pk_bf16_f32 v184, v208, v209\n\t"
    "v_mfma_f32_16x16x32_bf16 a[52:55], a[244:247], v[188:191], a[52:55]\n\t"
    "s_add_u32 m0, s59, 0x4400\n\t"
    "v_exp_f32 v210, v162\n\t"
    "v_mfma_f32_16x16x32_bf16 a[56:59], a[248:251], v[188:191], a[56:59]\n\t"
    "s_add_u32 s78, s64, 0x1000\n\t"
    "buffer_load_dwordx4 v63, s[36:39], s78 offen lds\n\t"
    "v_mfma_f32_16x16x32_bf16 a[60:63], a[252:255], v[188:191], a[60:63]\n\t"
    "v_exp_f32 v211, v163\n\t"
    "v_mfma_f32_16x16x32_bf16 v[168:171], a[192:195], v[128:131], v[40:43]\n\t"
    "v_cmp_lt_f32 vcc, s71, v196\n\t"
    "v_cvt_pk_bf16_f32 v185, v210, v211\n\t"
    "v_mfma_f32_16x16x32_bf16 v[168:171], a[196:199], v[132:135], v[168:171]\n\t"
    "v_exp_f32 v212, v164\n\t"
    "v_mfma_f32_16x16x32_bf16 v[168:171], a[200:203], v[136:139], v[168:171]\n\t"
    "s_cbranch_vccnz Lstub_c11_2_%=\n\t"
    "Lret_c11_2_%=:\n\t"
    "v_add_f32 v50, v50, v196\n\t"
    "v_mfma_f32_16x16x32_bf16 v[168:171], a[204:207], v[140:143], v[168:171]\n\t"
    "v_exp_f32 v213, v165\n\t"
    "v_mfma_f32_16x16x32_bf16 v[172:175], a[208:211], v[128:131], v[40:43]\n\t"
    "v_cvt_pk_bf16_f32 v186, v212, v213\n\t"
    "v_mfma_f32_16x16x32_bf16 v[172:175], a[212:215], v[132:135], v[172:175]\n\t"
    "s_add_u32 m0, s97, 0\n\t"
    "v_exp_f32 v214, v166\n\t"
    "v_mfma_f32_16x16x32_bf16 v[172:175], a[216:219], v[136:139], v[172:175]\n\t"
    "buffer_load_dwordx4 v8, s[44:47], s69 offen offset:1024 nt lds\n\t"
    "v_mfma_f32_16x16x32_bf16 v[172:175], a[220:223], v[140:143], v[172:175]\n\t"
    "v_exp_f32 v215, v167\n\t"
    "v_mfma_f32_16x16x32_bf16 a[64:67], a[224:227], v[192:195], a[64:67]\n\t"
    "s_add_u32 m0, s59, 0x14000\n\t"
    "v_cvt_pk_bf16_f32 v187, v214, v215\n\t"
    "v_mfma_f32_16x16x32_bf16 a[68:71], a[228:231], v[192:195], a[68:71]\n\t"
    "s_add_u32 s78, s64, 0x20000\n\t"
    "buffer_load_dwordx4 v62, s[36:39], s78 offen lds\n\t"
    "v_mfma_f32_16x16x32_bf16 a[72:75], a[232:235], v[192:195], a[72:75]\n\t"
    "s_add_u32 m0, s59, 0x14400\n\t"
    "v_exp_f32 v208, v168\n\t"
    "v_mfma_f32_16x16x32_bf16 v[196:199], v[54:57], v[184:187], 0\n\t"
    "s_add_u32 s78, s64, 0x21000\n\t"
    "buffer_load_dwordx4 v63, s[36:39], s78 offen lds\n\t"
    "v_mfma_f32_16x16x32_bf16 a[76:79], a[236:239], v[192:195], a[76:79]\n\t"
    "v_exp_f32 v209, v169\n\t"
    "v_mfma_f32_16x16x32_bf16 a[80:83], a[240:243], v[192:195], a[80:83]\n\t"
    "v_cvt_pk_bf16_f32 v188, v208, v209\n\t"
    "v_mfma_f32_16x16x32_bf16 a[84:87], a[244:247], v[192:195], a[84:87]\n\t"
    "s_add_u32 m0, s97, 0\n\t"
    "v_exp_f32 v210, v170\n\t"
    "v_mfma_f32_16x16x32_bf16 a[88:91], a[248:251], v[192:195], a[88:91]\n\t"
    "buffer_load_dwordx4 v9, s[44:47], s69 offen offset:2048 nt lds\n\t"
    "v_mfma_f32_16x16x32_bf16 a[92:95], a[252:255], v[192:195], a[92:95]\n\t"
    "v_exp_f32 v211, v171\n\t"
    "v_mfma_f32_16x16x32_bf16 v[176:179], a[192:195], v[144:147], v[44:47]\n\t"
    "v_cmp_lt_f32 vcc, s71, v196\n\t"
    "v_cvt_pk_bf16_f32 v189, v210, v211\n\t"
    "v_mfma_f32_16x16x32_bf16 v[176:179], a[196:199], v[148:151], v[176:179]\n\t"
    "v_exp_f32 v212, v172\n\t"
    "v_mfma_f32_16x16x32_bf16 v[176:179], a[200:203], v[152:155], v[176:179]\n\t"
    "s_cbranch_vccnz Lstub_c11_3_%=\n\t"
    "Lret_c11_3_%=:\n\t"
    "ds_read_b128 a[192:195], v1 offset:32768\n\t"
    "v_add_f32 v51, v51, v196\n\t"
    "v_mfma_f32_16x16x32_bf16 v[176:179], a[204:207], v[156:159], v[176:179]\n\t"
    "v_exp_f32 v213, v173\n\t"
    "v_mfma_f32_16x16x32_bf16 v[180:183], a[208:211], v[144:147], v[44:47]\n\t"
    "ds_read_b128 a[196:199], v2 offset:32768\n\t"
    "v_cvt_pk_bf16_f32 v190, v212, v213\n\t"
    "v_mfma_f32_16x16x32_bf16 v[180:183], a[212:215], v[148:151], v[180:183]\n\t"
    "v_exp_f32 v214, v174\n\t"
    "v_mfma_f32_16x16x32_bf16 v[180:183], a[216:219], v[152:155], v[180:183]\n\t"
    "ds_read_b128 a[200:203], v3 offset:32768\n\t"
    "ds_read_b128 a[204:207], v4 offset:32768\n\t"
    "v_mfma_f32_16x16x32_bf16 v[180:183], a[220:223], v[156:159], v[180:183]\n\t"
    "v_exp_f32 v215, v175\n\t"
    "v_mfma_f32_16x16x32_bf16 a[96:99], a[224:227], v[184:187], a[96:99]\n\t"
    "ds_read_b128 a[208:211], v1 offset:36864\n\t"
    "v_cvt_pk_bf16_f32 v191, v214, v215\n\t"
    "v_mfma_f32_16x16x32_bf16 a[100:103], a[228:231], v[184:187], a[100:103]\n\t"
    "ds_read_b128 a[212:215], v2 offset:36864\n\t"
    "ds_read_b128 a[216:219], v3 offset:36864\n\t"
    "v_mfma_f32_16x16x32_bf16 a[104:107], a[232:235], v[184:187], a[104:107]\n\t"
    "v_exp_f32 v208, v176\n\t"
    "v_mfma_f32_16x16x32_bf16 v[196:199], v[54:57], v[188:191], 0\n\t"
    "ds_read_b128 a[220:223], v4 offset:36864\n\t"
    "v_mfma_f32_16x16x32_bf16 a[108:111], a[236:239], v[184:187], a[108:111]\n\t"
    "v_exp_f32 v209, v177\n\t"
    "v_mfma_f32_16x16x32_bf16 a[112:115], a[240:243], v[184:187], a[112:115]\n\t"
    "v_cvt_pk_bf16_f32 v192, v208, v209\n\t"
    "v_mfma_f32_16x16x32_bf16 a[116:119], a[244:247], v[184:187], a[116:119]\n\t"
    "v_exp_f32 v210, v178\n\t"
    "v_mfma_f32_16x16x32_bf16 a[120:123], a[248:251], v[184:187], a[120:123]\n\t"
    "buffer_load_dwordx4 v10, s[44:47], s69 offen offset:3072 nt lds\n\t"
    "v_mfma_f32_16x16x32_bf16 a[124:127], a[252:255], v[184:187], a[124:127]\n\t"
    "s_waitcnt lgkmcnt(0)\n\t"
    "s_add_u32 s69, s69, 0x2000\n\t"
    "v_exp_f32 v211, v179\n\t"
    "v_mfma_f32_16x16x32_bf16 v[160:163], a[192:195], v[64:67], v[24:27]\n\t"
    "s_and_b32 s69, s69, 0x1ffff\n\t"
    "v_cmp_lt_f32 vcc, s71, v196\n\t"
    "v_cvt_pk_bf16_f32 v193, v210, v211\n\t"
    "v_mfma_f32_16x16x32_bf16 v[160:163], a[196:199], v[68:71], v[160:163]\n\t"
    "v_exp_f32 v212, v180\n\t"
    "v_mfma_f32_16x16x32_bf16 v[160:163], a[200:203], v[72:75], v[160:163]\n\t"
    "s_cbranch_vccnz Lstub_c11_4_%=\n\t"
    "Lret_c11_4_%=:\n\t"
    "v_add_f32 v52, v52, v196\n\t"
    "v_mfma_f32_16x16x32_bf16 v[160:163], a[204:207], v[76:79], v[160:163]\n\t"
    "v_exp_f32 v213, v181\n\t"
    "v_mfma_f32_16x16x32_bf16 v[164:167], a[208:211], v[64:67], v[24:27]\n\t"
    "v_cvt_pk_bf16_f32 v194, v212, v213\n\t"
    "v_mfma_f32_16x16x32_bf16 v[164:167], a[212:215], v[68:71], v[164:167]\n\t"
    "v_exp_f32 v214, v182\n\t"
    "v_mfma_f32_16x16x32_bf16 v[164:167], a[216:219], v[72:75], v[164:167]\n\t"
    "v_mfma_f32_16x16x32_bf16 v[164:167], a[220:223], v[76:79], v[164:167]\n\t"
    "v_exp_f32 v215, v183\n\t"
    "v_mfma_f32_16x16x32_bf16 a[128:131], a[224:227], v[188:191], a[128:131]\n\t"
    "s_memrealtime s[98:99]\n\t"
    "v_cvt_pk_bf16_f32 v195, v214, v215\n\t"
    "v_mfma_f32_16x16x32_bf16 a[132:135], a[228:231], v[188:191], a[132:135]\n\t"
    "v_mfma_f32_16x16x32_bf16 a[136:139], a[232:235], v[188:191], a[136:139]\n\t"
    "s_add_u32 m0, s97, 0x2000\n\t"
    "v_exp_f32 v208, v160\n\t"
    "v_mfma_f32_16x16x32_bf16 v[196:199], v[54:57], v[192:195], 0\n\t"
    "buffer_load_dwordx4 v11, s[48:51], s70 offen nt lds\n\t"
    "v_mfma_f32_16x16x32_bf16 a[140:143], a[236:239], v[188:191], a[140:143]\n\t"
    "v_exp_f32 v209, v161\n\t"
    "v_mfma_f32_16x16x32_bf16 a[144:147], a[240:243], v[188:191], a[144:147]\n\t"
    "v_cvt_pk_bf16_f32 v184, v208, v209\n\t"
    "v_mfma_f32_16x16x32_bf16 a[148:151], a[244:247], v[188:191], a[148:151]\n\t"
    "v_exp_f32 v210, v162\n\t"
    "v_mfma_f32_16x16x32_bf16 a[152:155], a[248:251], v[188:191], a[152:155]\n\t"
    "v_mfma_f32_16x16x32_bf16 a[156:159], a[252:255], v[188:191], a[156:159]\n\t"
    "v_exp_f32 v211, v163\n\t"
    "v_mfma_f32_16x16x32_bf16 v[168:171], a[192:195], v[80:83], v[28:31]\n\t"
    "v_cmp_lt_f32 vcc, s71, v196\n\t"
    "v_cvt_pk_bf16_f32 v185, v210, v211\n\t"
    "v_mfma_f32_16x16x32_bf16 v[168:171], a[196:199], v[84:87], v[168:171]\n\t"
    "s_waitcnt lgkmcnt(0)\n\t"
    "v_exp_f32 v212, v164\n\t"
    "v_mfma_f32_16x16x32_bf16 v[168:171], a[200:203], v[88:91], v[168:171]\n\t"
    "s_cbranch_vccnz Lstub_c11_5_%=\n\t"
    "Lret_c11_5_%=:\n\t"
    "v_add_f32 v53, v53, v196\n\t"
    "v_mfma_f32_16x16x32_bf16 v[168:171], a[204:207], v[92:95], v[168:171]\n\t"
    "s_waitcnt vmcnt(13)\n\t"
    "v_exp_f32 v213, v165\n\t"
    "v_mfma_f32_16x16x32_bf16 v[172:175], a[208:211], v[80:83], v[28:31]\n\t"
    "s_sub_u32 s19, s18, s98\n\t"
    "v_cvt_pk_bf16_f32 v186, v212, v213\n\t"
    "v_mfma_f32_16x16x32_bf16 v[172:175], a[212:215], v[84:87], v[172:175]\n\t"
    "s_barrier\n\t"
    "v_exp_f32 v214, v166\n\t"
    "v_mfma_f32_16x16x32_bf16 v[172:175], a[216:219], v[88:91], v[172:175]\n\t"
    "s_cmp_gt_i32 s19, 0\n\t"
    "s_cbranch_scc1 Lpace12_%=\n\t"
    "Lpaced12_%=:\n\t"
    "v_mfma_f32_16x16x32_bf16 v[172:175], a[220:223], v[92:95], v[172:175]\n\t"
    "s_add_u32 s18, s18, 0x72\n\t"
    "v_exp_f32 v215, v167\n\t"
    "v_mfma_f32_16x16x32_bf16 a[160:163], a[224:227], v[192:195], a[160:163]\n\t"
    "v_cvt_pk_bf16_f32 v187, v214, v215\n\t"
    "v_mfma_f32_16x16x32_bf16 a[164:167], a[228:231], v[192:195], a[164:167]\n\t"
    "ds_read_b64_tr_b16 a[224:225], v5 offset:32768\n\t"
    "ds_read_b64_tr_b16 a[226:227], v5 offset:36864\n\t"
    "v_mfma_f32_16x16x32_bf16 a[168:171], a[232:235], v[192:195], a[168:171]\n\t"
    "v_exp_f32 v208, v168\n\t"
    "v_mfma_f32_16x16x32_bf16 v[196:199], v[54:57], v[184:187], 0\n\t"
    "ds_read_b64_tr_b16 a[228:229], v6 offset:32768\n\t"
    "ds_read_b64_tr_b16 a[230:231], v6 offset:36864\n\t"
    "v_mfma_f32_16x16x32_bf16 a[172:175], a[236:239], v[192:195], a[172:175]\n\t"
    "v_exp_f32 v209, v169\n\t"
    "v_mfma_f32_16x16x32_bf16 a[176:179], a[240:243], v[192:195], a[176:179]\n\t"
    "ds_read_b64_tr_b16 a[232:233], v5 offset:33024\n\t"
    "v_cvt_pk_bf16_f32 v188, v208, v209\n\t"
    "v_mfma_f32_16x16x32_bf16 a[180:183], a[244:247], v[192:195], a[180:183]\n\t"
    "v_exp_f32 v210, v170\n\t"
    "v_mfma_f32_16x16x32_bf16 a[184:187], a[248:251], v[192:195], a[184:187]\n\t"
    "ds_read_b64_tr_b16 a[234:235], v5 offset:37120\n\t"
    "ds_read_b64_tr_b16 a[236:237], v6 offset:33024\n\t"
    "v_mfma_f32_16x16x32_bf16 a[188:191], a[252:255], v[192:195], a[188:191]\n\t"
    "v_exp_f32 v211, v171\n\t"
    "v_mfma_f32_16x16x32_bf16 v[176:179], a[192:195], v[96:99], v[32:35]\n\t"
    "ds_read_b64_tr_b16 a[238:239], v6 offset:37120\n\t"
    "v_cmp_lt_f32 vcc, s71, v196\n\t"
    "v_cvt_pk_bf16_f32 v189, v210, v211\n\t"
    "v_mfma_f32_16x16x32_bf16 v[176:179], a[196:199], v[100:103], v[176:179]\n\t"
    "v_exp_f32 v212, v172\n\t"
    "v_mfma_f32_16x16x32_bf16 v[176:179], a[200:203], v[104:107], v[176:179]\n\t"
    "s_cbranch_vccnz Lstub_c12_0_%=\n\t"
    "Lret_c12_0_%=:\n\t"
    "ds_read_b64_tr_b16 a[240:241], v5 offset:33280\n\t"
    "v_add_f32 v48, v48, v196\n\t"
    "v_mfma_f32_16x16x32_bf16 v[176:179], a[204:207], v[108:111], v[176:179]\n\t"
    "v_exp_f32 v213, v173\n\t"
    "v_mfma_f32_16x16x32_bf16 v[180:183], a[208:211], v[96:99], v[32:35]\n\t"
    "ds_read_b64_tr_b16 a[242:243], v5 offset:37376\n\t"
    "v_cvt_pk_bf16_f32 v190, v212, v213\n\t"
    "v_mfma_f32_16x16x32_bf16 v[180:183], a[212:215], v[100:103], v[180:183]\n\t"
    "v_exp_f32 v214, v174\n\t"
    "v_mfma_f32_16x16x32_bf16 v[180:183], a[216:219], v[104:107], v[180:183]\n\t"
    "ds_read_b64_tr_b16 a[244:245], v6 offset:33280\n\t"
    "ds_read_b64_tr_b16 a[246:247], v6 offset:37376\n\t"
    "v_mfma_f32_16x16x32_bf16 v[180:183], a[220:223], v[108:111], v[180:183]\n\t"
    "s_waitcnt lgkmcnt(3)\n\t"
    "v_exp_f32 v215, v175\n\t"
    "v_mfma_f32_16x16x32_bf16 a[0:3], a[224:227], v[184:187], a[0:3]\n\t"
    "ds_read_b64_tr_b16 a[248:249], v5 offset:33536\n\t"
    "v_cvt_pk_bf16_f32 v191, v214, v215\n\t"
    "v_mfma_f32_16x16x32_bf16 a[4:7], a[228:231], v[184:187], a[4:7]\n\t"
    "ds_read_b64_tr_b16 a[250:251], v5 offset:37632\n\t"
    "ds_read_b64_tr_b16 a[252:253], v6 offset:33536\n\t"
    "v_mfma_f32_16x16x32_bf16 a[8:11], a[232:235], v[184:187], a[8:11]\n\t"
    "s_add_u32 s98, s98, 0x68\n\t"
    "v_exp_f32 v208, v176\n\t"
    "v_mfma_f32_16x16x32_bf16 v[196:199], v[54:57], v[188:191], 0\n\t"
    "ds_read_b64_tr_b16 a[254:255], v6 offset:37632\n\t"
    "v_mfma_f32_16x16x32_bf16 a[12:15], a[236:239], v[184:187], a[12:15]\n\t"
    "s_waitcnt lgkmcnt(4)\n\t"
    "s_max_u32 s18, s18, s98\n\t"
    "v_exp_f32 v209, v177\n\t"
    "v_mfma_f32_16x16x32_bf16 a[16:19], a[240:243], v[184:187], a[16:19]\n\t"
    "v_cvt_pk_bf16_f32 v192, v208, v209\n\t"
    "v_mfma_f32_16x16x32_bf16 a[20:23], a[244:247], v[184:187], a[20:23]\n\t"
    "s_waitcnt lgkmcnt(1)\n\t"
    "v_exp_f32 v210, v178\n\t"
    "v_mfma_f32_16x16x32_bf16 a[24:27], a[248:251], v[184:187], a[24:27]\n\t"
    "s_waitcnt lgkmcnt(0)\n\t"
    "buffer_load_dwordx4 v12, s[48:51], s70 offen offset:1024 nt lds\n\t"
    "v_mfma_f32_16x16x32_bf16 a[28:31], a[252:255], v[184:187], a[28:31]\n\t"
    "v_exp_f32 v211, v179\n\t"
    "v_mfma_f32_16x16x32_bf16 v[160:163], a[192:195], v[112:115], v[36:39]\n\t"
    "v_cmp_lt_f32 vcc, s71, v196\n\t"
    "v_cvt_pk_bf16_f32 v193, v210, v211\n\t"
    "v_mfma_f32_16x16x32_bf16 v[160:163], a[196:199], v[116:119], v[160:163]\n\t"
    "v_exp_f32 v212, v180\n\t"
    "v_mfma_f32_16x16x32_bf16 v[160:163], a[200:203], v[120:123], v[160:163]\n\t"
    "s_cbranch_vccnz Lstub_c12_1_%=\n\t"
    "Lret_c12_1_%=:\n\t"
    "v_add_f32 v49, v49, v196\n\t"
    "v_mfma_f32_16x16x32_bf16 v[160:163], a[204:207], v[124:127], v[160:163]\n\t"
    "v_exp_f32 v213, v181\n\t"
    "v_mfma_f32_16x16x32_bf16 v[164:167], a[208:211], v[112:115], v[36:39]\n\t"
    "v_cvt_pk_bf16_f32 v194, v212, v213\n\t"
    "v_mfma_f32_16x16x32_bf16 v[164:167], a[212:215], v[116:119], v[164:167]\n\t"
    "v_exp_f32 v214, v182\n\t"
    "v_mfma_f32_16x16x32_bf16 v[164:167], a[216:219], v[120:123], v[164:167]\n\t"
    "buffer_load_dwordx4 v11, s[48:51], s70 offen offset:2048 nt lds\n\t"
    "v_mfma_f32_16x16x32_bf16 v[164:167], a[220:223], v[124:127], v[164:167]\n\t"
    "v_exp_f32 v215, v183\n\t"
    "v_mfma_f32_16x16x32_bf16 a[32:35], a[224:227], v[188:191], a[32:35]\n\t"
    "v_cvt_pk_bf16_f32 v195, v214, v215\n\t"
    "v_mfma_f32_16x16x32_bf16 a[36:39], a[228:231], v[188:191], a[36:39]\n\t"
    "buffer_load_dwordx4 v12, s[48:51], s70 offen offset:3072 nt lds\n\t"
    "v_mfma_f32_16x16x32_bf16 a[40:43], a[232:235], v[188:191], a[40:43]\n\t"
    "s_add_u32 s70, s70, 0x2000\n\t"
    "s_add_u32 m0, s59, 0x6000\n\t"
    "v_exp_f32 v208, v160\n\t"
    "v_mfma_f32_16x16x32_bf16 v[196:199], v[54:57], v[192:195], 0\n\t"
    "s_and_b32 s70, s70, 0x1ffff\n\t"
    "s_add_u32 s78, s64, 0x8000\n\t"
    "buffer_load_dwordx4 v62, s[36:39], s78 offen lds\n\t"
    "v_mfma_f32_16x16x32_bf16 a[44:47], a[236:239], v[188:191], a[44:47]\n\t"
    "v_exp_f32 v209, v161\n\t"
    "v_mfma_f32_16x16x32_bf16 a[48:51], a[240:243], v[188:191], a[48:51]\n\t"
    "v_cvt_pk_bf16_f32 v184, v208, v209\n\t"
    "v_mfma_f32_16x16x32_bf16 a[52:55], a[244:247], v[188:191], a[52:55]\n\t"
    "s_add_u32 m0, s59, 0x6400\n\t"
    "v_exp_f32 v210, v162\n\t"
    "v_mfma_f32_16x16x32_bf16 a[56:59], a[248:251], v[188:191], a[56:59]\n\t"
    "s_add_u32 s78, s64, 0x9000\n\t"
    "buffer_load_dwordx4 v63, s[36:39], s78 offen lds\n\t"
    "v_mfma_f32_16x16x32_bf16 a[60:63], a[252:255], v[188:191], a[60:63]\n\t"
    "v_exp_f32 v211, v163\n\t"
    "v_mfma_f32_16x16x32_bf16 v[168:171], a[192:195], v[128:131], v[40:43]\n\t"
    "v_cmp_lt_f32 vcc, s71, v196\n\t"
    "v_cvt_pk_bf16_f32 v185, v210, v211\n\t"
    "v_mfma_f32_16x16x32_bf16 v[168:171], a[196:199], v[132:135], v[168:171]\n\t"
    "v_exp_f32 v212, v164\n\t"
    "v_mfma_f32_16x16x32_bf16 v[168:171], a[200:203], v[136:139], v[168:171]\n\t"
    "s_cbranch_vccnz Lstub_c12_2_%=\n\t"
    "Lret_c12_2_%=:\n\t"
    "v_add_f32 v50, v50, v196\n\t"
    "v_mfma_f32_16x16x32_bf16 v[168:171], a[204:207], v[140:143], v[168:171]\n\t"
    "v_exp_f32 v213, v165\n\t"
    "v_mfma_f32_16x16x32_bf16 v[172:175], a[208:211], v[128:131], v[40:43]\n\t"
    "v_cvt_pk_bf16_f32 v186, v212, v213\n\t"
    "v_mfma_f32_16x16x32_bf16 v[172:175], a[212:215], v[132:135], v[172:175]\n\t"
    "s_add_u32 m0, s66, 0\n\t"
    "v_exp_f32 v214, v166\n\t"
    "v_mfma_f32_16x16x32_bf16 v[172:175], a[216:219], v[136:139], v[172:175]\n\t"
    "buffer_load_dwordx4 v7, s[44:47], s69 offen nt lds\n\t"
    "v_mfma_f32_16x16x32_bf16 v[172:175], a[220:223], v[140:143], v[172:175]\n\t"
    "v_exp_f32 v215, v167\n\t"
    "v_mfma_f32_16x16x32_bf16 a[64:67], a[224:227], v[192:195], a[64:67]\n\t"
    "s_add_u32 m0, s59, 0x16000\n\t"
    "v_cvt_pk_bf16_f32 v187, v214, v215\n\t"
    "v_mfma_f32_16x16x32_bf16 a[68:71], a[228:231], v[192:195], a[68:71]\n\t"
    "s_add_u32 s78, s64, 0x28000\n\t"
    "buffer_load_dwordx4 v62, s[36:39], s78 offen lds\n\t"
    "v_mfma_f32_16x16x32_bf16 a[72:75], a[232:235], v[192:195], a[72:75]\n\t"
    "s_add_u32 m0, s59, 0x16400\n\t"
    "v_exp_f32 v208, v168\n\t"
    "v_mfma_f32_16x16x32_bf16 v[196:199], v[54:57], v[184:187], 0\n\t"
    "s_add_u32 s78, s64, 0x29000\n\t"
    "buffer_load_dwordx4 v63, s[36:39], s78 offen lds\n\t"
    "v_mfma_f32_16x16x32_bf16 a[76:79], a[236:239], v[192:195], a[76:79]\n\t"
    "v_exp_f32 v209, v169\n\t"
    "v_mfma_f32_16x16x32_bf16 a[80:83], a[240:243], v[192:195], a[80:83]\n\t"
    "v_cvt_pk_bf16_f32 v188, v208, v209\n\t"
    "v_mfma_f32_16x16x32_bf16 a[84:87], a[244:247], v[192:195], a[84:87]\n\t"
    "s_add_u32 m0, s59, 0x8000\n\t"
    "v_exp_f32 v210, v170\n\t"
    "v_mfma_f32_16x16x32_bf16 a[88:91], a[248:251], v[192:195], a[88:91]\n\t"
    "s_add_u32 s78, s64, 0x10000\n\t"
    "buffer_load_dwordx4 v62, s[36:39], s78 offen lds\n\t"
    "v_mfma_f32_16x16x32_bf16 a[92:95], a[252:255], v[192:195], a[92:95]\n\t"
    "v_exp_f32 v211, v171\n\t"
    "v_mfma_f32_16x16x32_bf16 v[176:179], a[192:195], v[144:147], v[44:47]\n\t"
    "v_cmp_lt_f32 vcc, s71, v196\n\t"
    "v_cvt_pk_bf16_f32 v189, v210, v211\n\t"
    "v_mfma_f32_16x16x32_bf16 v[176:179], a[196:199], v[148:151], v[176:179]\n\t"
    "v_exp_f32 v212, v172\n\t"
    "v_mfma_f32_16x16x32_bf16 v[176:179], a[200:203], v[152:155], v[176:179]\n\t"
    "s_cbranch_vccnz Lstub_c12_3_%=\n\t"
    "Lret_c12_3_%=:\n\t"
    "ds_read_b128 a[192:195], v1 offset:49152\n\t"
    "v_add_f32 v51, v51, v196\n\t"
    "v_mfma_f32_16x16x32_bf16 v[176:179], a[204:207], v[156:159], v[176:179]\n\t"
    "v_exp_f32 v213, v173\n\t"
    "v_mfma_f32_16x16x32_bf16 v[180:183], a[208:211], v[144:147], v[44:47]\n\t"
    "ds_read_b128 a[196:199], v2 offset:49152\n\t"
    "v_cvt_pk_bf16_f32 v190, v212, v213\n\t"
    "v_mfma_f32_16x16x32_bf16 v[180:183], a[212:215], v[148:151], v[180:183]\n\t"
    "v_exp_f32 v214, v174\n\t"
    "v_mfma_f32_16x16x32_bf16 v[180:183], a[216:219], v[152:155], v[180:183]\n\t"
    "ds_read_b128 a[200:203], v3 offset:49152\n\t"
    "ds_read_b128 a[204:207], v4 offset:49152\n\t"
    "v_mfma_f32_16x16x32_bf16 v[180:183], a[220:223], v[156:159], v[180:183]\n\t"
    "v_exp_f32 v215, v175\n\t"
    "v_mfma_f32_16x16x32_bf16 a[96:99], a[224:227], v[184:187], a[96:99]\n\t"
    "ds_read_b128 a[208:211], v1 offset:53248\n\t"
    "v_cvt_pk_bf16_f32 v191, v214, v215\n\t"
    "v_mfma_f32_16x16x32_bf16 a[100:103], a[228:231], v[184:187], a[100:103]\n\t"
    "ds_read_b128 a[212:215], v2 offset:53248\n\t"
    "ds_read_b128 a[216:219], v3 offset:53248\n\t"
    "v_mfma_f32_16x16x32_bf16 a[104:107], a[232:235], v[184:187], a[104:107]\n\t"
    "v_exp_f32 v208, v176\n\t"
    "v_mfma_f32_16x16x32_bf16 v[196:199], v[54:57], v[188:191], 0\n\t"
    "ds_read_b128 a[220:223], v4 offset:53248\n\t"
    "v_mfma_f32_16x16x32_bf16 a[108:111], a[236:239], v[184:187], a[108:111]\n\t"
    "v_exp_f32 v209, v177\n\t"
    "v_mfma_f32_16x16x32_bf16 a[112:115], a[240:243], v[184:187], a[112:115]\n\t"
    "v_cvt_pk_bf16_f32 v192, v208, v209\n\t"
    "v_mfma_f32_16x16x32_bf16 a[116:119], a[244:247], v[184:187], a[116:119]\n\t"
    "s_add_u32 m0, s66, 0\n\t"
    "v_exp_f32 v210, v178\n\t"
    "v_mfma_f32_16x16x32_bf16 a[120:123], a[248:251], v[184:187], a[120:123]\n\t"
    "buffer_load_dwordx4 v8, s[44:47], s69 offen offset:1024 nt lds\n\t"
    "v_mfma_f32_16x16x32_bf16 a[124:127], a[252:255], v[184:187], a[124:127]\n\t"
    "s_waitcnt lgkmcnt(0)\n\t"
    "v_exp_f32 v211, v179\n\t"
    "v_mfma_f32_16x16x32_bf16 v[160:163], a[192:195], v[64:67], v[24:27]\n\t"
    "v_cmp_lt_f32 vcc, s71, v196\n\t"
    "v_cvt_pk_bf16_f32 v193, v210, v211\n\t"
    "v_mfma_f32_16x16x32_bf16 v[160:163], a[196:199], v[68:71], v[160:163]\n\t"
    "v_exp_f32 v212, v180\n\t"
    "v_mfma_f32_16x16x32_bf16 v[160:163], a[200:203], v[72:75], v[160:163]\n\t"
    "s_cbranch_vccnz Lstub_c12_4_%=\n\t"
    "Lret_c12_4_%=:\n\t"
    "v_add_f32 v52, v52, v196\n\t"
    "v_mfma_f32_16x16x32_bf16 v[160:163], a[204:207], v[76:79], v[160:163]\n\t"
    "v_exp_f32 v213, v181\n\t"
    "v_mfma_f32_16x16x32_bf16 v[164:167], a[208:211], v[64:67], v[24:27]\n\t"
    "v_cvt_pk_bf16_f32 v194, v212, v213\n\t"
    "v_mfma_f32_16x16x32_bf16 v[164:167], a[212:215], v[68:71], v[164:167]\n\t"
    "v_exp_f32 v214, v182\n\t"
    "v_mfma_f32_16x16x32_bf16 v[164:167], a[216:219], v[72:75], v[164:167]\n\t"
    "buffer_load_dwordx4 v9, s[44:47], s69 offen offset:2048 nt lds\n\t"
    "v_mfma_f32_16x16x32_bf16 v[164:167], a[220:223], v[76:79], v[164:167]\n\t"
    "v_exp_f32 v215, v183\n\t"
    "v_mfma_f32_16x16x32_bf16 a[128:131], a[224:227], v[188:191], a[128:131]\n\t"
    "s_memrealtime s[98:99]\n\t"
    "s_add_u32 m0, s59, 0x8400\n\t"
    "v_cvt_pk_bf16_f32 v195, v214, v215\n\t"
    "v_mfma_f32_16x16x32_bf16 a[132:135], a[228:231], v[188:191], a[132:135]\n\t"
    "s_add_u32 s78, s64, 0x11000\n\t"
    "buffer_load_dwordx4 v63, s[36:39], s78 offen lds\n\t"
    "v_mfma_f32_16x16x32_bf16 a[136:139], a[232:235], v[188:191], a[136:139]\n\t"
    "s_add_u32 m0, s59, 0x18000\n\t"
    "v_exp_f32 v208, v160\n\t"
    "v_mfma_f32_16x16x32_bf16 v[196:199], v[54:57], v[192:195], 0\n\t"
    "s_add_u32 s78, s64, 0x30000\n\t"
    "buffer_load_dwordx4 v62, s[36:39], s78 offen lds\n\t"
    "v_mfma_f32_16x16x32_bf16 a[140:143], a[236:239], v[188:191], a[140:143]\n\t"
    "v_exp_f32 v209, v161\n\t"
    "v_mfma_f32_16x16x32_bf16 a[144:147], a[240:243], v[188:191], a[144:147]\n\t"
    "v_cvt_pk_bf16_f32 v184, v208, v209\n\t"
    "v_mfma_f32_16x16x32_bf16 a[148:151], a[244:247], v[188:191], a[148:151]\n\t"
    "s_add_u32 m0, s66, 0\n\t"
    "v_exp_f32 v210, v162\n\t"
    "v_mfma_f32_16x16x32_bf16 a[152:155], a[248:251], v[188:191], a[152:155]\n\t"
    "buffer_load_dwordx4 v10, s[44:47], s69 offen offset:3072 nt lds\n\t"
    "v_mfma_f32_16x16x32_bf16 a[156:159], a[252:255], v[188:191], a[156:159]\n\t"
    "s_add_u32 s69, s69, 0x2000\n\t"
    "v_exp_f32 v211, v163\n\t"
    "v_mfma_f32_16x16x32_bf16 v[168:171], a[192:195], v[80:83], v[28:31]\n\t"
    "s_and_b32 s69, s69, 0x1ffff\n\t"
    "v_cmp_lt_f32 vcc, s71, v196\n\t"
    "v_cvt_pk_bf16_f32 v185, v210, v211\n\t"
    "v_mfma_f32_16x16x32_bf16 v[168:171], a[196:199], v[84:87], v[168:171]\n\t"
    "s_waitcnt lgkmcnt(0)\n\t"
    "v_exp_f32 v212, v164\n\t"
    "v_mfma_f32_16x16x32_bf16 v[168:171], a[200:203], v[88:91], v[168:171]\n\t"
    "s_cbranch_vccnz Lstub_c12_5_%=\n\t"
    "Lret_c12_5_%=:\n\t"
    "v_add_f32 v53, v53, v196\n\t"
    "v_mfma_f32_16x16x32_bf16 v[168:171], a[204:207], v[92:95], v[168:171]\n\t"
    "s_waitcnt vmcnt(15)\n\t"
    "v_exp_f32 v213, v165\n\t"
    "v_mfma_f32_16x16x32_bf16 v[172:175], a[208:211], v[80:83], v[28:31]\n\t"
    "s_sub_u32 s19, s18, s98\n\t"
    "v_cvt_pk_bf16_f32 v186, v212, v213\n\t"
    "v_mfma_f32_16x16x32_bf16 v[172:175], a[212:215], v[84:87], v[172:175]\n\t"
    "s_barrier\n\t"
    "v_exp_f32 v214, v166\n\t"
    "v_mfma_f32_16x16x32_bf16 v[172:175], a[216:219], v[88:91], v[172:175]\n\t"
    "s_cmp_gt_i32 s19, 0\n\t"
    "s_cbranch_scc1 Lpace13_%=\n\t"
    "Lpaced13_%=:\n\t"
    "v_mfma_f32_16x16x32_bf16 v[172:175], a[220:223], v[92:95], v[172:175]\n\t"
    "s_add_u32 s18, s18, 0x72\n\t"
    "v_exp_f32 v215, v167\n\t"
    "v_mfma_f32_16x16x32_bf16 a[160:163], a[224:227], v[192:195], a[160:163]\n\t"
    "v_cvt_pk_bf16_f32 v187, v214, v215\n\t"
    "v_mfma_f32_16x16x32_bf16 a[164:167], a[228:231], v[192:195], a[164:167]\n\t"
    "ds_read_b64_tr_b16 a[224:225], v5 offset:49152\n\t"
    "ds_read_b64_tr_b16 a[226:227], v5 offset:53248\n\t"
    "v_mfma_f32_16x16x32_bf16 a[168:171], a[232:235], v[192:195], a[168:171]\n\t"
    "v_exp_f32 v208, v168\n\t"
    "v_mfma_f32_16x16x32_bf16 v[196:199], v[54:57], v[184:187], 0\n\t"
    "ds_read_b64_tr_b16 a[228:229], v6 offset:49152\n\t"
    "ds_read_b64_tr_b16 a[230:231], v6 offset:53248\n\t"
    "v_mfma_f32_16x16x32_bf16 a[172:175], a[236:239], v[192:195], a[172:175]\n\t"
    "v_exp_f32 v209, v169\n\t"
    "v_mfma_f32_16x16x32_bf16 a[176:179], a[240:243], v[192:195], a[176:179]\n\t"
    "ds_read_b64_tr_b16 a[232:233], v5 offset:49408\n\t"
    "v_cvt_pk_bf16_f32 v188, v208, v209\n\t"
    "v_mfma_f32_16x16x32_bf16 a[180:183], a[244:247], v[192:195], a[180:183]\n\t"
    "v_exp_f32 v210, v170\n\t"
    "v_mfma_f32_16x16x32_bf16 a[184:187], a[248:251], v[192:195], a[184:187]\n\t"
    "ds_read_b64_tr_b16 a[234:235], v5 offset:53504\n\t"
    "ds_read_b64_tr_b16 a[236:237], v6 offset:49408\n\t"
    "v_mfma_f32_16x16x32_bf16 a[188:191], a[252:255], v[192:195], a[188:191]\n\t"
    "v_exp_f32 v211, v171\n\t"
    "v_mfma_f32_16x16x32_bf16 v[176:179], a[192:195], v[96:99], v[32:35]\n\t"
    "ds_read_b64_tr_b16 a[238:239], v6 offset:53504\n\t"
    "v_cmp_lt_f32 vcc, s71, v196\n\t"
    "v_cvt_pk_bf16_f32 v189, v210, v211\n\t"
    "v_mfma_f32_16x16x32_bf16 v[176:179], a[196:199], v[100:103], v[176:179]\n\t"
    "v_exp_f32 v212, v172\n\t"
    "v_mfma_f32_16x16x32_bf16 v[176:179], a[200:203], v[104:107], v[176:179]\n\t"
    "s_cbranch_vccnz Lstub_c13_0_%=\n\t"
    "Lret_c13_0_%=:\n\t"
    "ds_read_b64_tr_b16 a[240:241], v5 offset:49664\n\t"
    "v_add_f32 v48, v48, v196\n\t"
    "v_mfma_f32_16x16x32_bf16 v[176:179], a[204:207], v[108:111], v[176:179]\n\t"
    "v_exp_f32 v213, v173\n\t"
    "v_mfma_f32_16x16x32_bf16 v[180:183], a[208:211], v[96:99], v[32:35]\n\t"
    "ds_read_b64_tr_b16 a[242:243], v5 offset:53760\n\t"
    "v_cvt_pk_bf16_f32 v190, v212, v213\n\t"
    "v_mfma_f32_16x16x32_bf16 v[180:183], a[212:215], v[100:103], v[180:183]\n\t"
    "v_exp_f32 v214, v174\n\t"
    "v_mfma_f32_16x16x32_bf16 v[180:183], a[216:219], v[104:107], v[180:183]\n\t"
    "ds_read_b64_tr_b16 a[244:245], v6 offset:49664\n\t"
    "ds_read_b64_tr_b16 a[246:247], v6 offset:53760\n\t"
    "v_mfma_f32_16x16x32_bf16 v[180:183], a[220:223], v[108:111], v[180:183]\n\t"
    "s_waitcnt lgkmcnt(3)\n\t"
    "v_exp_f32 v215, v175\n\t"
    "v_mfma_f32_16x16x32_bf16 a[0:3], a[224:227], v[184:187], a[0:3]\n\t"
    "ds_read_b64_tr_b16 a[248:249], v5 offset:49920\n\t"
    "v_cvt_pk_bf16_f32 v191, v214, v215\n\t"
    "v_mfma_f32_16x16x32_bf16 a[4:7], a[228:231], v[184:187], a[4:7]\n\t"
    "ds_read_b64_tr_b16 a[250:251], v5 offset:54016\n\t"
    "ds_read_b64_tr_b16 a[252:253], v6 offset:49920\n\t"
    "v_mfma_f32_16x16x32_bf16 a[8:11], a[232:235], v[184:187], a[8:11]\n\t"
    "s_add_u32 s98, s98, 0x68\n\t"
    "v_exp_f32 v208, v176\n\t"
    "v_mfma_f32_16x16x32_bf16 v[196:199], v[54:57], v[188:191], 0\n\t"
    "ds_read_b64_tr_b16 a[254:255], v6 offset:54016\n\t"
    "v_mfma_f32_16x16x32_bf16 a[12:15], a[236:239], v[184:187], a[12:15]\n\t"
    "s_waitcnt lgkmcnt(4)\n\t"
    "s_max_u32 s18, s18, s98\n\t"
    "v_exp_f32 v209, v177\n\t"
    "v_mfma_f32_16x16x32_bf16 a[16:19], a[240:243], v[184:187], a[16:19]\n\t"
    "v_cvt_pk_bf16_f32 v192, v208, v209\n\t"
    "v_mfma_f32_16x16x32_bf16 a[20:23], a[244:247], v[184:187], a[20:23]\n\t"
    "s_waitcnt lgkmcnt(1)\n\t"
    "s_add_u32 m0, s66, 0x2000\n\t"
    "v_exp_f32 v210, v178\n\t"
    "v_mfma_f32_16x16x32_bf16 a[24:27], a[248:251], v[184:187], a[24:27]\n\t"
    "s_waitcnt lgkmcnt(0)\n\t"
    "buffer_load_dwordx4 v11, s[48:51], s70 offen nt lds\n\t"
    "v_mfma_f32_16x16x32_bf16 a[28:31], a[252:255], v[184:187], a[28:31]\n\t"
    "v_exp_f32 v211, v179\n\t"
    "v_mfma_f32_16x16x32_bf16 v[160:163], a[192:195], v[112:115], v[36:39]\n\t"
    "v_cmp_lt_f32 vcc, s71, v196\n\t"
    "v_cvt_pk_bf16_f32 v193, v210, v211\n\t"
    "v_mfma_f32_16x16x32_bf16 v[160:163], a[196:199], v[116:119], v[160:163]\n\t"
    "v_exp_f32 v212, v180\n\t"
    "v_mfma_f32_16x16x32_bf16 v[160:163], a[200:203], v[120:123], v[160:163]\n\t"
    "s_cbranch_vccnz Lstub_c13_1_%=\n\t"
    "Lret_c13_1_%=:\n\t"
    "v_add_f32 v49, v49, v196\n\t"
    "v_mfma_f32_16x16x32_bf16 v[160:163], a[204:207], v[124:127], v[160:163]\n\t"
    "v_exp_f32 v213, v181\n\t"
    "v_mfma_f32_16x16x32_bf16 v[164:167], a[208:211], v[112:115], v[36:39]\n\t"
    "v_cvt_pk_bf16_f32 v194, v212, v213\n\t"
    "v_mfma_f32_16x16x32_bf16 v[164:167], a[212:215], v[116:119], v[164:167]\n\t"
    "v_exp_f32 v214, v182\n\t"
    "v_mfma_f32_16x16x32_bf16 v[164:167], a[216:219], v[120:123], v[164:167]\n\t"
    "buffer_load_dwordx4 v12, s[48:51], s70 offen offset:1024 nt lds\n\t"
    "v_mfma_f32_16x16x32_bf16 v[164:167], a[220:223], v[124:127], v[164:167]\n\t"
    "v_exp_f32 v215, v183\n\t"
    "v_mfma_f32_16x16x32_bf16 a[32:35], a[224:227], v[188:191], a[32:35]\n\t"
    "v_cvt_pk_bf16_f32 v195, v214, v215\n\t"
    "v_mfma_f32_16x16x32_bf16 a[36:39], a[228:231], v[188:191], a[36:39]\n\t"
    "buffer_load_dwordx4 v11, s[48:51], s70 offen offset:2048 nt lds\n\t"
    "v_mfma_f32_16x16x32_bf16 a[40:43], a[232:235], v[188:191], a[40:43]\n\t"
    "s_add_u32 m0, s59, 0x18400\n\t"
    "v_exp_f32 v208, v160\n\t"
    "v_mfma_f32_16x16x32_bf16 v[196:199], v[54:57], v[192:195], 0\n\t"
    "s_add_u32 s78, s64, 0x31000\n\t"
    "buffer_load_dwordx4 v63, s[36:39], s78 offen lds\n\t"
    "v_mfma_f32_16x16x32_bf16 a[44:47], a[236:239], v[188:191], a[44:47]\n\t"
    "v_exp_f32 v209, v161\n\t"
    "v_mfma_f32_16x16x32_bf16 a[48:51], a[240:243], v[188:191], a[48:51]\n\t"
    "v_cvt_pk_bf16_f32 v184, v208, v209\n\t"
    "v_mfma_f32_16x16x32_bf16 a[52:55], a[244:247], v[188:191], a[52:55]\n\t"
    "s_add_u32 m0, s59, 0xa000\n\t"
    "v_exp_f32 v210, v162\n\t"
    "v_mfma_f32_16x16x32_bf16 a[56:59], a[248:251], v[188:191], a[56:59]\n\t"
    "s_add_u32 s78, s64, 0x18000\n\t"
    "buffer_load_dwordx4 v62, s[36:39], s78 offen lds\n\t"
    "v_mfma_f32_16x16x32_bf16 a[60:63], a[252:255], v[188:191], a[60:63]\n\t"
    "v_exp_f32 v211, v163\n\t"
    "v_mfma_f32_16x16x32_bf16 v[168:171], a[192:195], v[128:131], v[40:43]\n\t"
    "v_cmp_lt_f32 vcc, s71, v196\n\t"
    "v_cvt_pk_bf16_f32 v185, v210, v211\n\t"
    "v_mfma_f32_16x16x32_bf16 v[168:171], a[196:199], v[132:135], v[168:171]\n\t"
    "v_exp_f32 v212, v164\n\t"
    "v_mfma_f32_16x16x32_bf16 v[168:171], a[200:203], v[136:139], v[168:171]\n\t"
    "s_cbranch_vccnz Lstub_c13_2_%=\n\t"
    "Lret_c13_2_%=:\n\t"
    "v_add_f32 v50, v50, v196\n\t"
    "v_mfma_f32_16x16x32_bf16 v[168:171], a[204:207], v[140:143], v[168:171]\n\t"
    "v_exp_f32 v213, v165\n\t"
    "v_mfma_f32_16x16x32_bf16 v[172:175], a[208:211], v[128:131], v[40:43]\n\t"
    "v_cvt_pk_bf16_f32 v186, v212, v213\n\t"
    "v_mfma_f32_16x16x32_bf16 v[172:175], a[212:215], v[132:135], v[172:175]\n\t"
    "s_add_u32 m0, s66, 0x2000\n\t"
    "v_exp_f32 v214, v166\n\t"
    "v_mfma_f32_16x16x32_bf16 v[172:175], a[216:219], v[136:139], v[172:175]\n\t"
    "buffer_load_dwordx4 v12, s[48:51], s70 offen offset:3072 nt lds\n\t"
    "v_mfma_f32_16x16x32_bf16 v[172:175], a[220:223], v[140:143], v[172:175]\n\t"
    "s_add_u32 s70, s70, 0x2000\n\t"
    "v_exp_f32 v215, v167\n\t"
    "v_mfma_f32_16x16x32_bf16 a[64:67], a[224:227], v[192:195], a[64:67]\n\t"
    "s_and_b32 s70, s70, 0x1ffff\n\t"
    "s_add_u32 m0, s59, 0xa400\n\t"
    "v_cvt_pk_bf16_f32 v187, v214, v215\n\t"
    "v_mfma_f32_16x16x32_bf16 a[68:71], a[228:231], v[192:195], a[68:71]\n\t"
    "s_add_u32 s78, s64, 0x19000\n\t"
    "buffer_load_dwordx4 v63, s[36:39], s78 offen lds\n\t"
    "v_mfma_f32_16x16x32_bf16 a[72:75], a[232:235], v[192:195], a[72:75]\n\t"
    "s_add_u32 m0, s59, 0x1a000\n\t"
    "v_exp_f32 v208, v168\n\t"
    "v_mfma_f32_16x16x32_bf16 v[196:199], v[54:57], v[184:187], 0\n\t"
    "s_add_u32 s78, s64, 0x38000\n\t"
    "buffer_load_dwordx4 v62, s[36:39], s78 offen lds\n\t"
    "v_mfma_f32_16x16x32_bf16 a[76:79], a[236:239], v[192:195], a[76:79]\n\t"
    "v_exp_f32 v209, v169\n\t"
    "v_mfma_f32_16x16x32_bf16 a[80:83], a[240:243], v[192:195], a[80:83]\n\t"
    "v_cvt_pk_bf16_f32 v188, v208, v209\n\t"
    "v_mfma_f32_16x16x32_bf16 a[84:87], a[244:247], v[192:195], a[84:87]\n\t"
    "s_add_u32 m0, s59, 0x1a400\n\t"
    "v_exp_f32 v210, v170\n\t"
    "v_mfma_f32_16x16x32_bf16 a[88:91], a[248:251], v[192:195], a[88:91]\n\t"
    "s_add_u32 s78, s64, 0x39000\n\t"
    "buffer_load_dwordx4 v63, s[36:39], s78 offen lds\n\t"
    "v_mfma_f32_16x16x32_bf16 a[92:95], a[252:255], v[192:195], a[92:95]\n\t"
    "v_exp_f32 v211, v171\n\t"
    "v_mfma_f32_16x16x32_bf16 v[176:179], a[192:195], v[144:147], v[44:47]\n\t"
    "v_cmp_lt_f32 vcc, s71, v196\n\t"
    "v_cvt_pk_bf16_f32 v189, v210, v211\n\t"
    "v_mfma_f32_16x16x32_bf16 v[176:179], a[196:199], v[148:151], v[176:179]\n\t"
    "v_exp_f32 v212, v172\n\t"
    "v_mfma_f32_16x16x32_bf16 v[176:179], a[200:203], v[152:155], v[176:179]\n\t"
    "s_cbranch_vccnz Lstub_c13_3_%=\n\t"
    "Lret_c13_3_%=:\n\t"
    "ds_read_b128 a[192:195], v200 offset:0\n\t"
    "v_add_f32 v51, v51, v196\n\t"
    "v_mfma_f32_16x16x32_bf16 v[176:179], a[204:207], v[156:159], v[176:179]\n\t"
    "v_exp_f32 v213, v173\n\t"
    "v_mfma_f32_16x16x32_bf16 v[180:183], a[208:211], v[144:147], v[44:47]\n\t"
    "ds_read_b128 a[196:199], v201 offset:0\n\t"
    "v_cvt_pk_bf16_f32 v190, v212, v213\n\t"
    "v_mfma_f32_16x16x32_bf16 v[180:183], a[212:215], v[148:151], v[180:183]\n\t"
    "v_exp_f32 v214, v174\n\t"
    "v_mfma_f32_16x16x32_bf16 v[180:183], a[216:219], v[152:155], v[180:183]\n\t"
    "ds_read_b128 a[200:203], v202 offset:0\n\t"
    "ds_read_b128 a[204:207], v203 offset:0\n\t"
    "v_mfma_f32_16x16x32_bf16 v[180:183], a[220:223], v[156:159], v[180:183]\n\t"
    "v_exp_f32 v215, v175\n\t"
    "v_mfma_f32_16x16x32_bf16 a[96:99], a[224:227], v[184:187], a[96:99]\n\t"
    "ds_read_b128 a[208:211], v200 offset:4096\n\t"
    "v_cvt_pk_bf16_f32 v191, v214, v215\n\t"
    "v_mfma_f32_16x16x32_bf16 a[100:103], a[228:231], v[184:187], a[100:103]\n\t"
    "ds_read_b128 a[212:215], v201 offset:4096\n\t"
    "ds_read_b128 a[216:219], v202 offset:4096\n\t"
    "v_mfma_f32_16x16x32_bf16 a[104:107], a[232:235], v[184:187], a[104:107]\n\t"
    "v_exp_f32 v208, v176\n\t"
    "v_mfma_f32_16x16x32_bf16 v[196:199], v[54:57], v[188:191], 0\n\t"
    "ds_read_b128 a[220:223], v203 offset:4096\n\t"
    "v_mfma_f32_16x16x32_bf16 a[108:111], a[236:239], v[184:187], a[108:111]\n\t"
    "v_exp_f32 v209, v177\n\t"
    "v_mfma_f32_16x16x32_bf16 a[112:115], a[240:243], v[184:187], a[112:115]\n\t"
    "v_cvt_pk_bf16_f32 v192, v208, v209\n\t"
    "v_mfma_f32_16x16x32_bf16 a[116:119], a[244:247], v[184:187], a[116:119]\n\t"
    "v_exp_f32 v210, v178\n\t"
    "v_mfma_f32_16x16x32_bf16 a[120:123], a[248:251], v[184:187], a[120:123]\n\t"
    "v_mfma_f32_16x16x32_bf16 a[124:127], a[252:255], v[184:187], a[124:127]\n\t"
    "s_waitcnt lgkmcnt(0)\n\t"
    "v_exp_f32 v211, v179\n\t"
    "v_mfma_f32_16x16x32_bf16 v[160:163], a[192:195], v[64:67], v[24:27]\n\t"
    "v_cmp_lt_f32 vcc, s71, v196\n\t"
    "v_cvt_pk_bf16_f32 v193, v210, v211\n\t"
    "v_mfma_f32_16x16x32_bf16 v[160:163], a[196:199], v[68:71], v[160:163]\n\t"
    "v_exp_f32 v212, v180\n\t"
    "v_mfma_f32_16x16x32_bf16 v[160:163], a[200:203], v[72:75], v[160:163]\n\t"
    "s_cbranch_vccnz Lstub_c13_4_%=\n\t"
    "Lret_c13_4_%=:\n\t"
    "v_add_f32 v52, v52, v196\n\t"
    "v_mfma_f32_16x16x32_bf16 v[160:163], a[204:207], v[76:79], v[160:163]\n\t"
    "v_exp_f32 v213, v181\n\t"
    "v_mfma_f32_16x16x32_bf16 v[164:167], a[208:211], v[64:67], v[24:27]\n\t"
    "v_cvt_pk_bf16_f32 v194, v212, v213\n\t"
    "v_mfma_f32_16x16x32_bf16 v[164:167], a[212:215], v[68:71], v[164:167]\n\t"
    "v_exp_f32 v214, v182\n\t"
    "v_mfma_f32_16x16x32_bf16 v[164:167], a[216:219], v[72:75], v[164:167]\n\t"
    "v_mfma_f32_16x16x32_bf16 v[164:167], a[220:223], v[76:79], v[164:167]\n\t"
    "v_exp_f32 v215, v183\n\t"
    "v_mfma_f32_16x16x32_bf16 a[128:131], a[224:227], v[188:191], a[128:131]\n\t"
    "v_cvt_pk_bf16_f32 v195, v214, v215\n\t"
    "v_mfma_f32_16x16x32_bf16 a[132:135], a[228:231], v[188:191], a[132:135]\n\t"
    "v_mfma_f32_16x16x32_bf16 a[136:139], a[232:235], v[188:191], a[136:139]\n\t"
    "v_exp_f32 v208, v160\n\t"
    "v_mfma_f32_16x16x32_bf16 v[196:199], v[54:57], v[192:195], 0\n\t"
    "v_mfma_f32_16x16x32_bf16 a[140:143], a[236:239], v[188:191], a[140:143]\n\t"
    "v_exp_f32 v209, v161\n\t"
    "v_mfma_f32_16x16x32_bf16 a[144:147], a[240:243], v[188:191], a[144:147]\n\t"
    "v_cvt_pk_bf16_f32 v184, v208, v209\n\t"
    "v_mfma_f32_16x16x32_bf16 a[148:151], a[244:247], v[188:191], a[148:151]\n\t"
    "v_exp_f32 v210, v162\n\t"
    "v_mfma_f32_16x16x32_bf16 a[152:155], a[248:251], v[188:191], a[152:155]\n\t"
    "v_mfma_f32_16x16x32_bf16 a[156:159], a[252:255], v[188:191], a[156:159]\n\t"
    "v_exp_f32 v211, v163\n\t"
    "v_mfma_f32_16x16x32_bf16 v[168:171], a[192:195], v[80:83], v[28:31]\n\t"
    "v_cmp_lt_f32 vcc, s71, v196\n\t"
    "v_cvt_pk_bf16_f32 v185, v210, v211\n\t"
    "v_mfma_f32_16x16x32_bf16 v[168:171], a[196:199], v[84:87], v[168:171]\n\t"
    "v_exp_f32 v212, v164\n\t"
    "v_mfma_f32_16x16x32_bf16 v[168:171], a[200:203], v[88:91], v[168:171]\n\t"
    "s_cbranch_vccnz Lstub_c13_5_%=\n\t"
    "Lret_c13_5_%=:\n\t"
    "v_add_f32 v53, v53, v196\n\t"
    "v_mfma_f32_16x16x32_bf16 v[168:171], a[204:207], v[92:95], v[168:171]\n\t"
    "s_waitcnt vmcnt(9)\n\t"
    "v_exp_f32 v213, v165\n\t"
    "v_mfma_f32_16x16x32_bf16 v[172:175], a[208:211], v[80:83], v[28:31]\n\t"
    "v_cvt_pk_bf16_f32 v186, v212, v213\n\t"
    "v_mfma_f32_16x16x32_bf16 v[172:175], a[212:215], v[84:87], v[172:175]\n\t"
    "s_barrier\n\t"
    "v_exp_f32 v214, v166\n\t"
    "v_mfma_f32_16x16x32_bf16 v[172:175], a[216:219], v[88:91], v[172:175]\n\t"
    "v_mfma_f32_16x16x32_bf16 v[172:175], a[220:223], v[92:95], v[172:175]\n\t"
    "v_exp_f32 v215, v167\n\t"
    "v_mfma_f32_16x16x32_bf16 a[160:163], a[224:227], v[192:195], a[160:163]\n\t"
    "v_cvt_pk_bf16_f32 v187, v214, v215\n\t"
    "v_mfma_f32_16x16x32_bf16 a[164:167], a[228:231], v[192:195], a[164:167]\n\t"
    "ds_read_b64_tr_b16 a[224:225], v204 offset:0\n\t"
    "ds_read_b64_tr_b16 a[226:227], v204 offset:4096\n\t"
    "v_mfma_f32_16x16x32_bf16 a[168:171], a[232:235], v[192:195], a[168:171]\n\t"
    "v_exp_f32 v208, v168\n\t"
    "v_mfma_f32_16x16x32_bf16 v[196:199], v[54:57], v[184:187], 0\n\t"
    "ds_read_b64_tr_b16 a[228:229], v205 offset:0\n\t"
    "ds_read_b64_tr_b16 a[230:231], v205 offset:4096\n\t"
    "v_mfma_f32_16x16x32_bf16 a[172:175], a[236:239], v[192:195], a[172:175]\n\t"
    "v_exp_f32 v209, v169\n\t"
    "v_mfma_f32_16x16x32_bf16 a[176:179], a[240:243], v[192:195], a[176:179]\n\t"
    "ds_read_b64_tr_b16 a[232:233], v204 offset:256\n\t"
    "v_cvt_pk_bf16_f32 v188, v208, v209\n\t"
    "v_mfma_f32_16x16x32_bf16 a[180:183], a[244:247], v[192:195], a[180:183]\n\t"
    "v_exp_f32 v210, v170\n\t"
    "v_mfma_f32_16x16x32_bf16 a[184:187], a[248:251], v[192:195], a[184:187]\n\t"
    "ds_read_b64_tr_b16 a[234:235], v204 offset:4352\n\t"
    "ds_read_b64_tr_b16 a[236:237], v205 offset:256\n\t"
    "v_mfma_f32_16x16x32_bf16 a[188:191], a[252:255], v[192:195], a[188:191]\n\t"
    "v_exp_f32 v211, v171\n\t"
    "v_mfma_f32_16x16x32_bf16 v[176:179], a[192:195], v[96:99], v[32:35]\n\t"
    "ds_read_b64_tr_b16 a[238:239], v205 offset:4352\n\t"
    "v_cmp_lt_f32 vcc, s71, v196\n\t"
    "v_cvt_pk_bf16_f32 v189, v210, v211\n\t"
    "v_mfma_f32_16x16x32_bf16 v[176:179], a[196:199], v[100:103], v[176:179]\n\t"
    "v_exp_f32 v212, v172\n\t"
    "v_mfma_f32_16x16x32_bf16 v[176:179], a[200:203], v[104:107], v[176:179]\n\t"
    "s_cbranch_vccnz Lstub_c14_0_%=\n\t"
    "Lret_c14_0_%=:\n\t"
    "ds_read_b64_tr_b16 a[240:241], v204 offset:512\n\t"
    "v_add_f32 v48, v48, v196\n\t"
    "v_mfma_f32_16x16x32_bf16 v[176:179], a[204:207], v[108:111], v[176:179]\n\t"
    "v_exp_f32 v213, v173\n\t"
    "v_mfma_f32_16x16x32_bf16 v[180:183], a[208:211], v[96:99], v[32:35]\n\t"
    "ds_read_b64_tr_b16 a[242:243], v204 offset:4608\n\t"
    "v_cvt_pk_bf16_f32 v190, v212, v213\n\t"
    "v_mfma_f32_16x16x32_bf16 v[180:183], a[212:215], v[100:103], v[180:183]\n\t"
    "v_exp_f32 v214, v174\n\t"
    "v_mfma_f32_16x16x32_bf16 v[180:183], a[216:219], v[104:107], v[180:183]\n\t"
    "ds_read_b64_tr_b16 a[244:245], v205 offset:512\n\t"
    "ds_read_b64_tr_b16 a[246:247], v205 offset:4608\n\t"
    "v_mfma_f32_16x16x32_bf16 v[180:183], a[220:223], v[108:111], v[180:183]\n\t"
    "s_waitcnt lgkmcnt(3)\n\t"
    "v_exp_f32 v215, v175\n\t"
    "v_mfma_f32_16x16x32_bf16 a[0:3], a[224:227], v[184:187], a[0:3]\n\t"
    "ds_read_b64_tr_b16 a[248:249], v204 offset:768\n\t"
    "v_cvt_pk_bf16_f32 v191, v214, v215\n\t"
    "v_mfma_f32_16x16x32_bf16 a[4:7], a[228:231], v[184:187], a[4:7]\n\t"
    "ds_read_b64_tr_b16 a[250:251], v204 offset:4864\n\t"
    "ds_read_b64_tr_b16 a[252:253], v205 offset:768\n\t"
    "v_mfma_f32_16x16x32_bf16 a[8:11], a[232:235], v[184:187], a[8:11]\n\t"
    "v_exp_f32 v208, v176\n\t"
    "v_mfma_f32_16x16x32_bf16 v[196:199], v[54:57], v[188:191], 0\n\t"
    "ds_read_b64_tr_b16 a[254:255], v205 offset:4864\n\t"
    "v_mfma_f32_16x16x32_bf16 a[12:15], a[236:239], v[184:187], a[12:15]\n\t"
    "s_waitcnt lgkmcnt(4)\n\t"
    "v_exp_f32 v209, v177\n\t"
    "v_mfma_f32_16x16x32_bf16 a[16:19], a[240:243], v[184:187], a[16:19]\n\t"
    "v_cvt_pk_bf16_f32 v192, v208, v209\n\t"
    "v_mfma_f32_16x16x32_bf16 a[20:23], a[244:247], v[184:187], a[20:23]\n\t"
    "s_waitcnt lgkmcnt(1)\n\t"
    "v_exp_f32 v210, v178\n\t"
    "v_mfma_f32_16x16x32_bf16 a[24:27], a[248:251], v[184:187], a[24:27]\n\t"
    "s_waitcnt lgkmcnt(0)\n\t"
    "v_mfma_f32_16x16x32_bf16 a[28:31], a[252:255], v[184:187], a[28:31]\n\t"
    "v_exp_f32 v211, v179\n\t"
    "v_mfma_f32_16x16x32_bf16 v[160:163], a[192:195], v[112:115], v[36:39]\n\t"
    "v_cmp_lt_f32 vcc, s71, v196\n\t"
    "v_cvt_pk_bf16_f32 v193, v210, v211\n\t"
    "v_mfma_f32_16x16x32_bf16 v[160:163], a[196:199], v[116:119], v[160:163]\n\t"
    "v_exp_f32 v212, v180\n\t"
    "v_mfma_f32_16x16x32_bf16 v[160:163], a[200:203], v[120:123], v[160:163]\n\t"
    "s_cbranch_vccnz Lstub_c14_1_%=\n\t"
    "Lret_c14_1_%=:\n\t"
    "v_add_f32 v49, v49, v196\n\t"
    "v_mfma_f32_16x16x32_bf16 v[160:163], a[204:207], v[124:127], v[160:163]\n\t"
    "v_exp_f32 v213, v181\n\t"
    "v_mfma_f32_16x16x32_bf16 v[164:167], a[208:211], v[112:115], v[36:39]\n\t"
    "v_cvt_pk_bf16_f32 v194, v212, v213\n\t"
    "v_mfma_f32_16x16x32_bf16 v[164:167], a[212:215], v[116:119], v[164:167]\n\t"
    "v_exp_f32 v214, v182\n\t"
    "v_mfma_f32_16x16x32_bf16 v[164:167], a[216:219], v[120:123], v[164:167]\n\t"
    "v_mfma_f32_16x16x32_bf16 v[164:167], a[220:223], v[124:127], v[164:167]\n\t"
    "v_exp_f32 v215, v183\n\t"
    "v_mfma_f32_16x16x32_bf16 a[32:35], a[224:227], v[188:191], a[32:35]\n\t"
    "v_cvt_pk_bf16_f32 v195, v214, v215\n\t"
    "v_mfma_f32_16x16x32_bf16 a[36:39], a[228:231], v[188:191], a[36:39]\n\t"
    "s_getpc_b64 s[4:5]\n\t"
    "Lrt14_%=:\n\t"
    "s_mov_b32 s6, Lend_%=-Lrt14_%=\n\t"
    "s_mov_b32 s7, 0x20000\n\t"
    "s_lshr_b32 s8, %1, 3\n\t"
    "s_lshl_b32 s8, s8, 2\n\t"
    "s_add_u32 s8, s8, s56\n\t"
    "s_lshl_b32 s8, s8, 9\n\t"
    "v_cmp_gt_u32 s[76:77], 4, v0\n\t"
    "v_lshlrev_b32 v229, 7, v0\n\t"
    "v_mov_b32 v230, 0x40000000\n\t"
    "v_cndmask_b32 v229, v230, v229, s[76:77]\n\t"
    "buffer_load_dword v23, v229, s[4:7], s8 offen\n\t"
    "v_mfma_f32_16x16x32_bf16 a[40:43], a[232:235], v[188:191], a[40:43]\n\t"
    "v_exp_f32 v208, v160\n\t"
    "v_mfma_f32_16x16x32_bf16 v[196:199], v[54:57], v[192:195], 0\n\t"
    "v_mfma_f32_16x16x32_bf16 a[44:47], a[236:239], v[188:191], a[44:47]\n\t"
    "v_exp_f32 v209, v161\n\t"
    "v_mfma_f32_16x16x32_bf16 a[48:51], a[240:243], v[188:191], a[48:51]\n\t"
    "v_cvt_pk_bf16_f32 v184, v208, v209\n\t"
    "v_mfma_f32_16x16x32_bf16 a[52:55], a[244:247], v[188:191], a[52:55]\n\t"
    "v_exp_f32 v210, v162\n\t"
    "v_mfma_f32_16x16x32_bf16 a[56:59], a[248:251], v[188:191], a[56:59]\n\t"
    "v_mfma_f32_16x16x32_bf16 a[60:63], a[252:255], v[188:191], a[60:63]\n\t"
    "v_exp_f32 v211, v163\n\t"
    "v_mfma_f32_16x16x32_bf16 v[168:171], a[192:195], v[128:131], v[40:43]\n\t"
    "v_cmp_lt_f32 vcc, s71, v196\n\t"
    "v_cvt_pk_bf16_f32 v185, v210, v211\n\t"
    "v_mfma_f32_16x16x32_bf16 v[168:171], a[196:199], v[132:135], v[168:171]\n\t"
    "v_exp_f32 v212, v164\n\t"
    "v_mfma_f32_16x16x32_bf16 v[168:171], a[200:203], v[136:139], v[168:171]\n\t"
    "s_cbranch_vccnz Lstub_c14_2_%=\n\t"
    "Lret_c14_2_%=:\n\t"
    "v_add_f32 v50, v50, v196\n\t"
    "v_mfma_f32_16x16x32_bf16 v[168:171], a[204:207], v[140:143], v[168:171]\n\t"
    "v_exp_f32 v213, v165\n\t"
    "v_mfma_f32_16x16x32_bf16 v[172:175], a[208:211], v[128:131], v[40:43]\n\t"
    "v_cvt_pk_bf16_f32 v186, v212, v213\n\t"
    "v_mfma_f32_16x16x32_bf16 v[172:175], a[212:215], v[132:135], v[172:175]\n\t"
    "v_exp_f32 v214, v166\n\t"
    "v_mfma_f32_16x16x32_bf16 v[172:175], a[216:219], v[136:139], v[172:175]\n\t"
    "v_mfma_f32_16x16x32_bf16 v[172:175], a[220:223], v[140:143], v[172:175]\n\t"
    "v_exp_f32 v215, v167\n\t"
    "v_mfma_f32_16x16x32_bf16 a[64:67], a[224:227], v[192:195], a[64:67]\n\t"
    "v_cvt_pk_bf16_f32 v187, v214, v215\n\t"
    "v_mfma_f32_16x16x32_bf16 a[68:71], a[228:231], v[192:195], a[68:71]\n\t"
    "v_mfma_f32_16x16x32_bf16 a[72:75], a[232:235], v[192:195], a[72:75]\n\t"
    "v_exp_f32 v208, v168\n\t"
    "v_mfma_f32_16x16x32_bf16 v[196:199], v[54:57], v[184:187], 0\n\t"
    "v_mfma_f32_16x16x32_bf16 a[76:79], a[236:239], v[192:195], a[76:79]\n\t"
    "v_exp_f32 v209, v169\n\t"
    "v_mfma_f32_16x16x32_bf16 a[80:83], a[240:243], v[192:195], a[80:83]\n\t"
    "v_cvt_pk_bf16_f32 v188, v208, v209\n\t"
    "v_mfma_f32_16x16x32_bf16 a[84:87], a[244:247], v[192:195], a[84:87]\n\t"
    "v_exp_f32 v210, v170\n\t"
    "v_mfma_f32_16x16x32_bf16 a[88:91], a[248:251], v[192:195], a[88:91]\n\t"
    "v_mfma_f32_16x16x32_bf16 a[92:95], a[252:255], v[192:195], a[92:95]\n\t"
    "v_exp_f32 v211, v171\n\t"
    "v_mfma_f32_16x16x32_bf16 v[176:179], a[192:195], v[144:147], v[44:47]\n\t"
    "v_cmp_lt_f32 vcc, s71, v196\n\t"
    "v_cvt_pk_bf16_f32 v189, v210, v211\n\t"
    "v_mfma_f32_16x16x32_bf16 v[176:179], a[196:199], v[148:151], v[176:179]\n\t"
    "v_exp_f32 v212, v172\n\t"
    "v_mfma_f32_16x16x32_bf16 v[176:179], a[200:203], v[152:155], v[176:179]\n\t"
    "s_cbranch_vccnz Lstub_c14_3_%=\n\t"
    "Lret_c14_3_%=:\n\t"
    "ds_read_b128 a[192:195], v1 offset:0\n\t"
    "v_add_f32 v51, v51, v196\n\t"
    "v_mfma_f32_16x16x32_bf16 v[176:179], a[204:207], v[156:159], v[176:179]\n\t"
    "v_exp_f32 v213, v173\n\t"
    "v_mfma_f32_16x16x32_bf16 v[180:183], a[208:211], v[144:147], v[44:47]\n\t"
    "ds_read_b128 a[196:199], v2 offset:0\n\t"
    "v_cvt_pk_bf16_f32 v190, v212, v213\n\t"
    "v_mfma_f32_16x16x32_bf16 v[180:183], a[212:215], v[148:151], v[180:183]\n\t"
    "v_exp_f32 v214, v174\n\t"
    "v_mfma_f32_16x16x32_bf16 v[180:183], a[216:219], v[152:155], v[180:183]\n\t"
    "ds_read_b128 a[200:203], v3 offset:0\n\t"
    "ds_read_b128 a[204:207], v4 offset:0\n\t"
    "v_mfma_f32_16x16x32_bf16 v[180:183], a[220:223], v[156:159], v[180:183]\n\t"
    "v_exp_f32 v215, v175\n\t"
    "v_mfma_f32_16x16x32_bf16 a[96:99], a[224:227], v[184:187], a[96:99]\n\t"
    "ds_read_b128 a[208:211], v1 offset:4096\n\t"
    "v_cvt_pk_bf16_f32 v191, v214, v215\n\t"
    "v_mfma_f32_16x16x32_bf16 a[100:103], a[228:231], v[184:187], a[100:103]\n\t"
    "ds_read_b128 a[212:215], v2 offset:4096\n\t"
    "ds_read_b128 a[216:219], v3 offset:4096\n\t"
    "v_mfma_f32_16x16x32_bf16 a[104:107], a[232:235], v[184:187], a[104:107]\n\t"
    "v_exp_f32 v208, v176\n\t"
    "v_mfma_f32_16x16x32_bf16 v[196:199], v[54:57], v[188:191], 0\n\t"
    "ds_read_b128 a[220:223], v4 offset:4096\n\t"
    "v_mfma_f32_16x16x32_bf16 a[108:111], a[236:239], v[184:187], a[108:111]\n\t"
    "v_exp_f32 v209, v177\n\t"
    "v_mfma_f32_16x16x32_bf16 a[112:115], a[240:243], v[184:187], a[112:115]\n\t"
    "v_cvt_pk_bf16_f32 v192, v208, v209\n\t"
    "v_mfma_f32_16x16x32_bf16 a[116:119], a[244:247], v[184:187], a[116:119]\n\t"
    "v_exp_f32 v210, v178\n\t"
    "v_mfma_f32_16x16x32_bf16 a[120:123], a[248:251], v[184:187], a[120:123]\n\t"
    "v_mfma_f32_16x16x32_bf16 a[124:127], a[252:255], v[184:187], a[124:127]\n\t"
    "s_waitcnt lgkmcnt(0)\n\t"
    "v_exp_f32 v211, v179\n\t"
    "v_mfma_f32_16x16x32_bf16 v[160:163], a[192:195], v[64:67], v[24:27]\n\t"
    "v_cmp_lt_f32 vcc, s71, v196\n\t"
    "v_cvt_pk_bf16_f32 v193, v210, v211\n\t"
    "v_mfma_f32_16x16x32_bf16 v[160:163], a[196:199], v[68:71], v[160:163]\n\t"
    "v_exp_f32 v212, v180\n\t"
    "v_mfma_f32_16x16x32_bf16 v[160:163], a[200:203], v[72:75], v[160:163]\n\t"
    "s_cbranch_vccnz Lstub_c14_4_%=\n\t"
    "Lret_c14_4_%=:\n\t"
    "v_add_f32 v52, v52, v196\n\t"
    "v_mfma_f32_16x16x32_bf16 v[160:163], a[204:207], v[76:79], v[160:163]\n\t"
    "s_lshl_b32 s96, s57, 16\n\t"
    "v_exp_f32 v213, v181\n\t"
    "v_mfma_f32_16x16x32_bf16 v[164:167], a[208:211], v[64:67], v[24:27]\n\t"
    "s_add_u32 s96, s96, 0x2000\n\t"
    "v_cvt_pk_bf16_f32 v194, v212, v213\n\t"
    "v_mfma_f32_16x16x32_bf16 v[164:167], a[212:215], v[68:71], v[164:167]\n\t"
    "v_exp_f32 v214, v182\n\t"
    "v_mfma_f32_16x16x32_bf16 v[164:167], a[216:219], v[72:75], v[164:167]\n\t"
    "v_subrev_u32 v7, s96, v5\n\t"
    "v_mfma_f32_16x16x32_bf16 v[164:167], a[220:223], v[76:79], v[164:167]\n\t"
    "v_exp_f32 v215, v183\n\t"
    "v_mfma_f32_16x16x32_bf16 a[128:131], a[224:227], v[188:191], a[128:131]\n\t"
    "v_cvt_pk_bf16_f32 v195, v214, v215\n\t"
    "v_add_u32 v1, 0x2000, v5\n\t"
    "v_mfma_f32_16x16x32_bf16 a[132:135], a[228:231], v[188:191], a[132:135]\n\t"
    "v_subrev_u32 v8, s96, v6\n\t"
    "v_mfma_f32_16x16x32_bf16 a[136:139], a[232:235], v[188:191], a[136:139]\n\t"
    "v_exp_f32 v208, v160\n\t"
    "v_mfma_f32_16x16x32_bf16 v[196:199], v[54:57], v[192:195], 0\n\t"
    "v_add_u32 v2, 0x2000, v6\n\t"
    "v_mfma_f32_16x16x32_bf16 a[140:143], a[236:239], v[188:191], a[140:143]\n\t"
    "v_exp_f32 v209, v161\n\t"
    "v_mfma_f32_16x16x32_bf16 a[144:147], a[240:243], v[188:191], a[144:147]\n\t"
    "v_cvt_pk_bf16_f32 v184, v208, v209\n\t"
    "v_xor_b32 v3, 0x10000, v1\n\t"
    "v_mfma_f32_16x16x32_bf16 a[148:151], a[244:247], v[188:191], a[148:151]\n\t"
    "v_exp_f32 v210, v162\n\t"
    "v_mfma_f32_16x16x32_bf16 a[152:155], a[248:251], v[188:191], a[152:155]\n\t"
    "v_add_u32 v9, 0x18000, v7\n\t"
    "v_mfma_f32_16x16x32_bf16 a[156:159], a[252:255], v[188:191], a[156:159]\n\t"
    "v_exp_f32 v211, v163\n\t"
    "v_mfma_f32_16x16x32_bf16 v[168:171], a[192:195], v[80:83], v[28:31]\n\t"
    "v_cmp_lt_f32 vcc, s71, v196\n\t"
    "v_cvt_pk_bf16_f32 v185, v210, v211\n\t"
    "v_mfma_f32_16x16x32_bf16 v[168:171], a[196:199], v[84:87], v[168:171]\n\t"
    "v_exp_f32 v212, v164\n\t"
    "v_mfma_f32_16x16x32_bf16 v[168:171], a[200:203], v[88:91], v[168:171]\n\t"
    "s_cbranch_vccnz Lstub_c14_5_%=\n\t"
    "Lret_c14_5_%=:\n\t"
    "v_add_f32 v53, v53, v196\n\t"
    "v_add_u32 v15, 0x10000, v7\n\t"
    "v_mfma_f32_16x16x32_bf16 v[168:171], a[204:207], v[92:95], v[168:171]\n\t"
    "s_waitcnt vmcnt(4)\n\t"
    "v_exp_f32 v213, v165\n\t"
    "v_mfma_f32_16x16x32_bf16 v[172:175], a[208:211], v[80:83], v[28:31]\n\t"
    "v_cvt_pk_bf16_f32 v186, v212, v213\n\t"
    "v_xor_b32 v4, 0x10000, v2\n\t"
    "v_mfma_f32_16x16x32_bf16 v[172:175], a[212:215], v[84:87], v[172:175]\n\t"
    "s_barrier\n\t"
    "v_exp_f32 v214, v166\n\t"
    "v_mfma_f32_16x16x32_bf16 v[172:175], a[216:219], v[88:91], v[172:175]\n\t"
    "v_add_u32 v10, 0x18000, v8\n\t"
    "v_mfma_f32_16x16x32_bf16 v[172:175], a[220:223], v[92:95], v[172:175]\n\t"
    "v_exp_f32 v215, v167\n\t"
    "v_mfma_f32_16x16x32_bf16 a[160:163], a[224:227], v[192:195], a[160:163]\n\t"
    "v_cvt_pk_bf16_f32 v187, v214, v215\n\t"
    "v_add_u32 v16, 0x10000, v8\n\t"
    "v_mfma_f32_16x16x32_bf16 a[164:167], a[228:231], v[192:195], a[164:167]\n\t"
    "s_lshl_b32 s96, s57, 16\n\t"
    "ds_read_b64_tr_b16 a[224:225], v5 offset:0\n\t"
    "ds_read_b64_tr_b16 a[226:227], v5 offset:4096\n\t"
    "v_mfma_f32_16x16x32_bf16 a[168:171], a[232:235], v[192:195], a[168:171]\n\t"
    "s_mul_i32 s79, s58, 0xc000\n\t"
    "v_exp_f32 v208, v168\n\t"
    "v_mfma_f32_16x16x32_bf16 v[196:199], v[54:57], v[184:187], 0\n\t"
    "s_add_u32 s96, s96, s79\n\t"
    "ds_read_b64_tr_b16 a[228:229], v6 offset:0\n\t"
    "ds_read_b64_tr_b16 a[230:231], v6 offset:4096\n\t"
    "v_mfma_f32_16x16x32_bf16 a[172:175], a[236:239], v[192:195], a[172:175]\n\t"
    "s_add_u32 s96, s96, 0\n\t"
    "v_exp_f32 v209, v169\n\t"
    "v_mfma_f32_16x16x32_bf16 a[176:179], a[240:243], v[192:195], a[176:179]\n\t"
    "ds_read_b64_tr_b16 a[232:233], v5 offset:256\n\t"
    "v_cvt_pk_bf16_f32 v188, v208, v209\n\t"
    "v_mfma_f32_16x16x32_bf16 a[180:183], a[244:247], v[192:195], a[180:183]\n\t"
    "v_exp_f32 v210, v170\n\t"
    "v_mfma_f32_16x16x32_bf16 a[184:187], a[248:251], v[192:195], a[184:187]\n\t"
    "ds_read_b64_tr_b16 a[234:235], v5 offset:4352\n\t"
    "ds_read_b64_tr_b16 a[236:237], v6 offset:256\n\t"
    "v_mfma_f32_16x16x32_bf16 a[188:191], a[252:255], v[192:195], a[188:191]\n\t"
    "v_exp_f32 v211, v171\n\t"
    "v_mfma_f32_16x16x32_bf16 v[176:179], a[192:195], v[96:99], v[32:35]\n\t"
    "ds_read_b64_tr_b16 a[238:239], v6 offset:4352\n\t"
    "v_cmp_lt_f32 vcc, s71, v196\n\t"
    "v_cvt_pk_bf16_f32 v189, v210, v211\n\t"
    "v_mfma_f32_16x16x32_bf16 v[176:179], a[196:199], v[100:103], v[176:179]\n\t"
    "v_exp_f32 v212, v172\n\t"
    "v_mfma_f32_16x16x32_bf16 v[176:179], a[200:203], v[104:107], v[176:179]\n\t"
    "s_cbranch_vccnz Lstub_c15_0_%=\n\t"
    "Lret_c15_0_%=:\n\t"
    "ds_read_b64_tr_b16 a[240:241], v5 offset:512\n\t"
    "v_add_f32 v48, v48, v196\n\t"
    "v_mfma_f32_16x16x32_bf16 v[176:179], a[204:207], v[108:111], v[176:179]\n\t"
    "v_exp_f32 v213, v173\n\t"
    "v_mfma_f32_16x16x32_bf16 v[180:183], a[208:211], v[96:99], v[32:35]\n\t"
    "ds_read_b64_tr_b16 a[242:243], v5 offset:4608\n\t"
    "v_cvt_pk_bf16_f32 v190, v212, v213\n\t"
    "v_mfma_f32_16x16x32_bf16 v[180:183], a[212:215], v[100:103], v[180:183]\n\t"
    "v_exp_f32 v214, v174\n\t"
    "v_mfma_f32_16x16x32_bf16 v[180:183], a[216:219], v[104:107], v[180:183]\n\t"
    "ds_read_b64_tr_b16 a[244:245], v6 offset:512\n\t"
    "ds_read_b64_tr_b16 a[246:247], v6 offset:4608\n\t"
    "v_rcp_f32 v48, v48\n\t"
    "v_mfma_f32_16x16x32_bf16 v[180:183], a[220:223], v[108:111], v[180:183]\n\t"
    "s_waitcnt lgkmcnt(3)\n\t"
    "v_exp_f32 v215, v175\n\t"
    "v_mfma_f32_16x16x32_bf16 a[0:3], a[224:227], v[184:187], a[0:3]\n\t"
    "ds_read_b64_tr_b16 a[248:249], v5 offset:768\n\t"
    "v_cvt_pk_bf16_f32 v191, v214, v215\n\t"
    "v_mfma_f32_16x16x32_bf16 a[4:7], a[228:231], v[184:187], a[4:7]\n\t"
    "ds_read_b64_tr_b16 a[250:251], v5 offset:4864\n\t"
    "ds_read_b64_tr_b16 a[252:253], v6 offset:768\n\t"
    "v_mfma_f32_16x16x32_bf16 a[8:11], a[232:235], v[184:187], a[8:11]\n\t"
    "v_exp_f32 v208, v176\n\t"
    "v_mfma_f32_16x16x32_bf16 v[196:199], v[54:57], v[188:191], 0\n\t"
    "ds_read_b64_tr_b16 a[254:255], v6 offset:4864\n\t"
    "v_add_u32 v11, s96, v20\n\t"
    "v_mfma_f32_16x16x32_bf16 a[12:15], a[236:239], v[184:187], a[12:15]\n\t"
    "s_waitcnt lgkmcnt(4)\n\t"
    "v_exp_f32 v209, v177\n\t"
    "v_mfma_f32_16x16x32_bf16 a[16:19], a[240:243], v[184:187], a[16:19]\n\t"
    "v_cvt_pk_bf16_f32 v192, v208, v209\n\t"
    "v_xor_b32 v12, 0x10000, v11\n\t"
    "v_mfma_f32_16x16x32_bf16 a[20:23], a[244:247], v[184:187], a[20:23]\n\t"
    "s_waitcnt lgkmcnt(1)\n\t"
    "s_add_u32 m0, s59, 0x20000\n\t"
    "v_exp_f32 v210, v178\n\t"
    "v_mfma_f32_16x16x32_bf16 a[24:27], a[248:251], v[184:187], a[24:27]\n\t"
    "s_waitcnt lgkmcnt(0)\n\t"
    "s_add_u32 s78, s65, 0\n\t"
    "buffer_load_dwordx4 v62, s[36:39], s78 offen lds\n\t"
    "v_mfma_f32_16x16x32_bf16 a[28:31], a[252:255], v[184:187], a[28:31]\n\t"
    "s_add_u32 s96, s62, 1\n\t"
    "v_exp_f32 v211, v179\n\t"
    "v_mfma_f32_16x16x32_bf16 v[160:163], a[192:195], v[112:115], v[36:39]\n\t"
    "v_cmp_lt_f32 vcc, s71, v196\n\t"
    "v_cvt_pk_bf16_f32 v193, v210, v211\n\t"
    "v_mfma_f32_16x16x32_bf16 v[160:163], a[196:199], v[116:119], v[160:163]\n\t"
    "v_exp_f32 v212, v180\n\t"
    "v_mfma_f32_16x16x32_bf16 v[160:163], a[200:203], v[120:123], v[160:163]\n\t"
    "s_cbranch_vccnz Lstub_c15_1_%=\n\t"
    "Lret_c15_1_%=:\n\t"
    "v_add_f32 v49, v49, v196\n\t"
    "v_add_u32 v13, s96, v0\n\t"
    "v_mfma_f32_16x16x32_bf16 v[160:163], a[204:207], v[124:127], v[160:163]\n\t"
    "v_exp_f32 v213, v181\n\t"
    "v_mfma_f32_16x16x32_bf16 v[164:167], a[208:211], v[112:115], v[36:39]\n\t"
    "v_cvt_pk_bf16_f32 v194, v212, v213\n\t"
    "v_and_b32 v13, 3, v13\n\t"
    "v_mfma_f32_16x16x32_bf16 v[164:167], a[212:215], v[116:119], v[164:167]\n\t"
    "s_add_u32 m0, s59, 0x20400\n\t"
    "v_exp_f32 v214, v182\n\t"
    "v_mfma_f32_16x16x32_bf16 v[164:167], a[216:219], v[120:123], v[164:167]\n\t"
    "s_add_u32 s78, s65, 0x1000\n\t"
    "buffer_load_dwordx4 v63, s[36:39], s78 offen lds\n\t"
    "v_mfma_f32_16x16x32_bf16 v[164:167], a[220:223], v[124:127], v[164:167]\n\t"
    "s_and_b32 s79, s63, 0xfffffffc\n\t"
    "v_exp_f32 v215, v183\n\t"
    "v_accvgpr_read_b32 v216, a0\n\t"
    "v_accvgpr_read_b32 v217, a1\n\t"
    "v_accvgpr_read_b32 v218, a2\n\t"
    "v_accvgpr_read_b32 v219, a3\n\t"
    "v_mfma_f32_16x16x32_bf16 a[32:35], a[224:227], v[188:191], a[32:35]\n\t"
    "s_add_u32 m0, s59, 0x22000\n\t"
    "v_cvt_pk_bf16_f32 v195, v214, v215\n\t"
    "v_or_b32 v13, s79, v13\n\t"
    "v_accvgpr_read_b32 v220, a4\n\t"
    "v_accvgpr_read_b32 v221, a5\n\t"
    "v_accvgpr_read_b32 v222, a6\n\t"
    "v_accvgpr_read_b32 v223, a7\n\t"
    "v_mfma_f32_16x16x32_bf16 a[36:39], a[228:231], v[188:191], a[36:39]\n\t"
    "s_add_u32 s78, s65, 0x8000\n\t"
    "buffer_load_dwordx4 v62, s[36:39], s78 offen lds\n\t"
    "v_mul_f32 v216, v48, v216\n\t"
    "v_mul_f32 v217, v48, v217\n\t"
    "v_mul_f32 v218, v48, v218\n\t"
    "v_mfma_f32_16x16x32_bf16 a[40:43], a[232:235], v[188:191], a[40:43]\n\t"
    "s_add_u32 m0, s59, 0x22400\n\t"
    "v_exp_f32 v208, v160\n\t"
    "v_mul_f32 v219, v48, v219\n\t"
    "v_mul_f32 v220, v48, v220\n\t"
    "v_mul_f32 v221, v48, v221\n\t"
    "v_mul_f32 v222, v48, v222\n\t"
    "v_mfma_f32_16x16x32_bf16 v[196:199], v[54:57], v[192:195], 0\n\t"
    "s_add_u32 s78, s65, 0x9000\n\t"
    "buffer_load_dwordx4 v63, s[36:39], s78 offen lds\n\t"
    "v_mul_f32 v223, v48, v223\n\t"
    "v_cvt_pk_bf16_f32 v64, v216, v217\n\t"
    "v_cvt_pk_bf16_f32 v65, v218, v219\n\t"
    "v_mfma_f32_16x16x32_bf16 a[44:47], a[236:239], v[188:191], a[44:47]\n\t"
    "v_exp_f32 v209, v161\n\t"
    "v_cvt_pk_bf16_f32 v66, v220, v221\n\t"
    "v_cvt_pk_bf16_f32 v67, v222, v223\n\t"
    "v_mfma_f32_16x16x32_bf16 a[48:51], a[240:243], v[188:191], a[48:51]\n\t"
    "s_add_u32 s19, s80, 0\n\t"
    "buffer_store_dwordx4 v[64:67], v20, s[52:55], s19 offen sc1\n\t"
    "v_cvt_pk_bf16_f32 v184, v208, v209\n\t"
    "v_lshlrev_b32 v13, 7, v13\n\t"
    "v_accvgpr_read_b32 v224, a8\n\t"
    "v_mfma_f32_16x16x32_bf16 a[52:55], a[244:247], v[188:191], a[52:55]\n\t"
    "s_add_u32 m0, s59, 0x24000\n\t"
    "v_exp_f32 v210, v162\n\t"
    "v_accvgpr_read_b32 v225, a9\n\t"
    "v_accvgpr_read_b32 v226, a10\n\t"
    "v_accvgpr_read_b32 v227, a11\n\t"
    "v_accvgpr_read_b32 v228, a12\n\t"
    "v_mfma_f32_16x16x32_bf16 a[56:59], a[248:251], v[188:191], a[56:59]\n\t"
    "s_add_u32 s78, s67, 0\n\t"
    "buffer_load_dwordx4 v62, s[36:39], s78 offen lds\n\t"
    "v_accvgpr_read_b32 v229, a13\n\t"
    "v_accvgpr_read_b32 v230, a14\n\t"
    "v_accvgpr_read_b32 v231, a15\n\t"
    "v_mfma_f32_16x16x32_bf16 a[60:63], a[252:255], v[188:191], a[60:63]\n\t"
    "v_exp_f32 v211, v163\n\t"
    "v_mul_f32 v224, v48, v224\n\t"
    "v_mul_f32 v225, v48, v225\n\t"
    "v_mul_f32 v226, v48, v226\n\t"
    "v_mul_f32 v227, v48, v227\n\t"
    "v_mfma_f32_16x16x32_bf16 v[168:171], a[192:195], v[128:131], v[40:43]\n\t"
    "v_cmp_lt_f32 vcc, s71, v196\n\t"
    "v_cvt_pk_bf16_f32 v185, v210, v211\n\t"
    "v_mul_f32 v228, v48, v228\n\t"
    "v_mul_f32 v229, v48, v229\n\t"
    "v_mul_f32 v230, v48, v230\n\t"
    "v_mul_f32 v231, v48, v231\n\t"
    "v_mfma_f32_16x16x32_bf16 v[168:171], a[196:199], v[132:135], v[168:171]\n\t"
    "v_exp_f32 v212, v164\n\t"
    "v_cvt_pk_bf16_f32 v68, v224, v225\n\t"
    "v_cvt_pk_bf16_f32 v69, v226, v227\n\t"
    "v_cvt_pk_bf16_f32 v70, v228, v229\n\t"
    "v_cvt_pk_bf16_f32 v71, v230, v231\n\t"
    "v_mfma_f32_16x16x32_bf16 v[168:171], a[200:203], v[136:139], v[168:171]\n\t"
    "s_cbranch_vccnz Lstub_c15_2_%=\n\t"
    "Lret_c15_2_%=:\n\t"
    "s_add_u32 s19, s80, 0x3000\n\t"
    "buffer_store_dwordx4 v[68:71], v20, s[52:55], s19 offen sc1\n\t"
    "v_add_f32 v50, v50, v196\n\t"
    "v_add_u32 v13, 0x1000, v13\n\t"
    "v_accvgpr_read_b32 v216, a16\n\t"
    "v_mfma_f32_16x16x32_bf16 v[168:171], a[204:207], v[140:143], v[168:171]\n\t"
    "v_exp_f32 v213, v165\n\t"
    "v_accvgpr_read_b32 v217, a17\n\t"
    "v_accvgpr_read_b32 v218, a18\n\t"
    "v_accvgpr_read_b32 v219, a19\n\t"
    "v_accvgpr_read_b32 v220, a20\n\t"
    "v_mfma_f32_16x16x32_bf16 v[172:175], a[208:211], v[128:131], v[40:43]\n\t"
    "v_cvt_pk_bf16_f32 v186, v212, v213\n\t"
    "v_min_u32 v14, 2, v0\n\t"
    "v_accvgpr_read_b32 v221, a21\n\t"
    "v_accvgpr_read_b32 v222, a22\n\t"
    "v_accvgpr_read_b32 v223, a23\n\t"
    "v_mul_f32 v216, v48, v216\n\t"
    "v_mfma_f32_16x16x32_bf16 v[172:175], a[212:215], v[132:135], v[172:175]\n\t"
    "s_add_u32 m0, s59, 0x24400\n\t"
    "v_exp_f32 v214, v166\n\t"
    "v_mul_f32 v217, v48, v217\n\t"
    "v_mul_f32 v218, v48, v218\n\t"
    "v_mul_f32 v219, v48, v219\n\t"
    "v_mul_f32 v220, v48, v220\n\t"
    "v_mfma_f32_16x16x32_bf16 v[172:175], a[216:219], v[136:139], v[172:175]\n\t"
    "s_add_u32 s78, s67, 0x1000\n\t"
    "buffer_load_dwordx4 v63, s[36:39], s78 offen lds\n\t"
    "v_mul_f32 v221, v48, v221\n\t"
    "v_mul_f32 v222, v48, v222\n\t"
    "v_mul_f32 v223, v48, v223\n\t"
    "v_mfma_f32_16x16x32_bf16 v[172:175], a[220:223], v[140:143], v[172:175]\n\t"
    "v_exp_f32 v215, v167\n\t"
    "v_cvt_pk_bf16_f32 v72, v216, v217\n\t"
    "v_cvt_pk_bf16_f32 v73, v218, v219\n\t"
    "v_cvt_pk_bf16_f32 v74, v220, v221\n\t"
    "v_cvt_pk_bf16_f32 v75, v222, v223\n\t"
    "v_mfma_f32_16x16x32_bf16 a[64:67], a[224:227], v[192:195], a[64:67]\n\t"
    "s_add_u32 m0, s59, 0x26000\n\t"
    "s_add_u32 s19, s80, 0x6000\n\t"
    "buffer_store_dwordx4 v[72:75], v20, s[52:55], s19 offen sc1\n\t"
    "v_cvt_pk_bf16_f32 v187, v214, v215\n\t"
    "v_lshlrev_b32 v14, 8, v14\n\t"
    "v_accvgpr_read_b32 v224, a24\n\t"
    "v_mfma_f32_16x16x32_bf16 a[68:71], a[228:231], v[192:195], a[68:71]\n\t"
    "s_add_u32 s78, s67, 0x8000\n\t"
    "buffer_load_dwordx4 v62, s[36:39], s78 offen lds\n\t"
    "v_accvgpr_read_b32 v225, a25\n\t"
    "v_accvgpr_read_b32 v226, a26\n\t"
    "v_accvgpr_read_b32 v227, a27\n\t"
    "v_mfma_f32_16x16x32_bf16 a[72:75], a[232:235], v[192:195], a[72:75]\n\t"
    "s_add_u32 m0, s59, 0x26400\n\t"
    "v_exp_f32 v208, v168\n\t"
    "v_accvgpr_read_b32 v228, a28\n\t"
    "v_accvgpr_read_b32 v229, a29\n\t"
    "v_accvgpr_read_b32 v230, a30\n\t"
    "v_accvgpr_read_b32 v231, a31\n\t"
    "v_mfma_f32_16x16x32_bf16 v[196:199], v[54:57], v[184:187], 0\n\t"
    "s_add_u32 s78, s67, 0x9000\n\t"
    "buffer_load_dwordx4 v63, s[36:39], s78 offen lds\n\t"
    "v_mul_f32 v224, v48, v224\n\t"
    "v_mul_f32 v225, v48, v225\n\t"
    "v_mul_f32 v226, v48, v226\n\t"
    "v_mfma_f32_16x16x32_bf16 a[76:79], a[236:239], v[192:195], a[76:79]\n\t"
    "s_mul_i32 s96, s63, 0x300\n\t"
    "v_exp_f32 v209, v169\n\t"
    "v_mul_f32 v227, v48, v227\n\t"
    "v_mul_f32 v228, v48, v228\n\t"
    "v_mul_f32 v229, v48, v229\n\t"
    "v_mul_f32 v230, v48, v230\n\t"
    "v_mfma_f32_16x16x32_bf16 a[80:83], a[240:243], v[192:195], a[80:83]\n\t"
    "s_lshl_b32 s79, s56, 6\n\t"
    "v_cvt_pk_bf16_f32 v188, v208, v209\n\t"
    "v_mul_f32 v231, v48, v231\n\t"
    "v_cvt_pk_bf16_f32 v76, v224, v225\n\t"
    "v_cvt_pk_bf16_f32 v77, v226, v227\n\t"
    "v_cvt_pk_bf16_f32 v78, v228, v229\n\t"
    "v_cvt_pk_bf16_f32 v79, v230, v231\n\t"
    "v_mfma_f32_16x16x32_bf16 a[84:87], a[244:247], v[192:195], a[84:87]\n\t"
    "s_add_u32 s96, s96, s79\n\t"
    "s_add_u32 s19, s80, 0x9000\n\t"
    "buffer_store_dwordx4 v[76:79], v20, s[52:55], s19 offen sc1\n\t"
    "v_exp_f32 v210, v170\n\t"
    "v_rcp_f32 v49, v49\n\t"
    "v_mfma_f32_16x16x32_bf16 a[88:91], a[248:251], v[192:195], a[88:91]\n\t"
    "s_add_u32 s96, s96, 0x10000\n\t"
    "v_accvgpr_read_b32 v216, a32\n\t"
    "v_accvgpr_read_b32 v217, a33\n\t"
    "v_accvgpr_read_b32 v218, a34\n\t"
    "v_accvgpr_read_b32 v219, a35\n\t"
    "v_accvgpr_read_b32 v220, a36\n\t"
    "v_accvgpr_read_b32 v221, a37\n\t"
    "v_mfma_f32_16x16x32_bf16 a[92:95], a[252:255], v[192:195], a[92:95]\n\t"
    "v_exp_f32 v211, v171\n\t"
    "v_accvgpr_read_b32 v222, a38\n\t"
    "v_accvgpr_read_b32 v223, a39\n\t"
    "v_mul_f32 v216, v49, v216\n\t"
    "v_mul_f32 v217, v49, v217\n\t"
    "v_mfma_f32_16x16x32_bf16 v[176:179], a[192:195], v[144:147], v[44:47]\n\t"
    "v_cmp_lt_f32 vcc, s71, v196\n\t"
    "v_cvt_pk_bf16_f32 v189, v210, v211\n\t"
    "v_mul_f32 v218, v49, v218\n\t"
    "v_mul_f32 v219, v49, v219\n\t"
    "v_mul_f32 v220, v49, v220\n\t"
    "v_mul_f32 v221, v49, v221\n\t"
    "v_mfma_f32_16x16x32_bf16 v[176:179], a[196:199], v[148:151], v[176:179]\n\t"
    "v_exp_f32 v212, v172\n\t"
    "v_mul_f32 v222, v49, v222\n\t"
    "v_mul_f32 v223, v49, v223\n\t"
    "v_cvt_pk_bf16_f32 v80, v216, v217\n\t"
    "v_cvt_pk_bf16_f32 v81, v218, v219\n\t"
    "v_mfma_f32_16x16x32_bf16 v[176:179], a[200:203], v[152:155], v[176:179]\n\t"
    "s_cbranch_vccnz Lstub_c15_3_%=\n\t"
    "Lret_c15_3_%=:\n\t"
    "v_add_f32 v51, v51, v196\n\t"
    "v_add_u32 v14, s96, v14\n\t"
    "v_cvt_pk_bf16_f32 v82, v220, v221\n\t"
    "v_cvt_pk_bf16_f32 v83, v222, v223\n\t"
    "v_mfma_f32_16x16x32_bf16 v[176:179], a[204:207], v[156:159], v[176:179]\n\t"
    "s_add_u32 s19, s80, 0x400\n\t"
    "buffer_store_dwordx4 v[80:83], v20, s[52:55], s19 offen sc1\n\t"
    "v_exp_f32 v213, v173\n\t"
    "v_accvgpr_read_b32 v224, a40\n\t"
    "v_mfma_f32_16x16x32_bf16 v[180:183], a[208:211], v[144:147], v[44:47]\n\t"
    "v_cvt_pk_bf16_f32 v190, v212, v213\n\t"
    "v_and_b32 v17, 15, v0\n\t"
    "v_accvgpr_read_b32 v225, a41\n\t"
    "v_accvgpr_read_b32 v226, a42\n\t"
    "v_accvgpr_read_b32 v227, a43\n\t"
    "v_accvgpr_read_b32 v228, a44\n\t"
    "v_mfma_f32_16x16x32_bf16 v[180:183], a[212:215], v[148:151], v[180:183]\n\t"
    "v_exp_f32 v214, v174\n\t"
    "v_accvgpr_read_b32 v229, a45\n\t"
    "v_accvgpr_read_b32 v230, a46\n\t"
    "v_accvgpr_read_b32 v231, a47\n\t"
    "v_mul_f32 v224, v49, v224\n\t"
    "v_mfma_f32_16x16x32_bf16 v[180:183], a[216:219], v[152:155], v[180:183]\n\t"
    "v_lshlrev_b32 v17, 10, v17\n\t"
    "v_mul_f32 v225, v49, v225\n\t"
    "v_mul_f32 v226, v49, v226\n\t"
    "v_mul_f32 v227, v49, v227\n\t"
    "v_mul_f32 v228, v49, v228\n\t"
    "v_mul_f32 v229, v49, v229\n\t"
    "v_mfma_f32_16x16x32_bf16 v[180:183], a[220:223], v[156:159], v[180:183]\n\t"
    "v_exp_f32 v215, v175\n\t"
    "v_mul_f32 v230, v49, v230\n\t"
    "v_mul_f32 v231, v49, v231\n\t"
    "v_cvt_pk_bf16_f32 v84, v224, v225\n\t"
    "v_cvt_pk_bf16_f32 v85, v226, v227\n\t"
    "v_mfma_f32_16x16x32_bf16 a[96:99], a[224:227], v[184:187], a[96:99]\n\t"
    "v_cvt_pk_bf16_f32 v191, v214, v215\n\t"
    "v_lshrrev_b32 v18, 4, v0\n\t"
    "v_cvt_pk_bf16_f32 v86, v228, v229\n\t"
    "v_cvt_pk_bf16_f32 v87, v230, v231\n\t"
    "v_mfma_f32_16x16x32_bf16 a[100:103], a[228:231], v[184:187], a[100:103]\n\t"
    "s_add_u32 s19, s80, 0x3400\n\t"
    "buffer_store_dwordx4 v[84:87], v20, s[52:55], s19 offen sc1\n\t"
    "v_lshl_add_u32 v17, v18, 4, v17\n\t"
    "v_accvgpr_read_b32 v216, a48\n\t"
    "v_accvgpr_read_b32 v217, a49\n\t"
    "v_mfma_f32_16x16x32_bf16 a[104:107], a[232:235], v[184:187], a[104:107]\n\t"
    "s_mov_b32 s79, 0\n\t"
    "v_exp_f32 v208, v176\n\t"
    "v_accvgpr_read_b32 v218, a50\n\t"
    "v_accvgpr_read_b32 v219, a51\n\t"
    "v_accvgpr_read_b32 v220, a52\n\t"
    "v_accvgpr_read_b32 v221, a53\n\t"
    "v_mfma_f32_16x16x32_bf16 v[196:199], v[54:57], v[188:191], 0\n\t"
    "v_accvgpr_read_b32 v222, a54\n\t"
    "v_accvgpr_read_b32 v223, a55\n\t"
    "v_mul_f32 v216, v49, v216\n\t"
    "v_mul_f32 v217, v49, v217\n\t"
    "v_mul_f32 v218, v49, v218\n\t"
    "v_mul_f32 v219, v49, v219\n\t"
    "v_mfma_f32_16x16x32_bf16 a[108:111], a[236:239], v[184:187], a[108:111]\n\t"
    "v_exp_f32 v209, v177\n\t"
    "v_mul_f32 v220, v49, v220\n\t"
    "v_mul_f32 v221, v49, v221\n\t"
    "v_mul_f32 v222, v49, v222\n\t"
    "v_mul_f32 v223, v49, v223\n\t"
    "v_mfma_f32_16x16x32_bf16 a[112:115], a[240:243], v[184:187], a[112:115]\n\t"
    "v_cvt_pk_bf16_f32 v192, v208, v209\n\t"
    "v_cvt_pk_bf16_f32 v88, v216, v217\n\t"
    "v_cvt_pk_bf16_f32 v89, v218, v219\n\t"
    "v_cvt_pk_bf16_f32 v90, v220, v221\n\t"
    "v_cvt_pk_bf16_f32 v91, v222, v223\n\t"
    "v_mfma_f32_16x16x32_bf16 a[116:119], a[244:247], v[184:187], a[116:119]\n\t"
    "s_add_u32 s19, s80, 0x6400\n\t"
    "buffer_store_dwordx4 v[88:91], v20, s[52:55], s19 offen sc1\n\t"
    "v_exp_f32 v210, v178\n\t"
    "v_accvgpr_read_b32 v224, a56\n\t"
    "v_mfma_f32_16x16x32_bf16 a[120:123], a[248:251], v[184:187], a[120:123]\n\t"
    "v_accvgpr_read_b32 v225, a57\n\t"
    "v_accvgpr_read_b32 v226, a58\n\t"
    "v_accvgpr_read_b32 v227, a59\n\t"
    "v_accvgpr_read_b32 v228, a60\n\t"
    "v_accvgpr_read_b32 v229, a61\n\t"
    "v_accvgpr_read_b32 v230, a62\n\t"
    "v_mfma_f32_16x16x32_bf16 a[124:127], a[252:255], v[184:187], a[124:127]\n\t"
    "v_exp_f32 v211, v179\n\t"
    "v_accvgpr_read_b32 v231, a63\n\t"
    "v_mul_f32 v224, v49, v224\n\t"
    "v_mul_f32 v225, v49, v225\n\t"
    "v_mul_f32 v226, v49, v226\n\t"
    "v_cmp_lt_f32 vcc, s71, v196\n\t"
    "v_cvt_pk_bf16_f32 v193, v210, v211\n\t"
    "v_mul_f32 v227, v49, v227\n\t"
    "v_mul_f32 v228, v49, v228\n\t"
    "v_mul_f32 v229, v49, v229\n\t"
    "v_mul_f32 v230, v49, v230\n\t"
    "v_exp_f32 v212, v180\n\t"
    "v_mul_f32 v231, v49, v231\n\t"
    "v_cvt_pk_bf16_f32 v92, v224, v225\n\t"
    "v_cvt_pk_bf16_f32 v93, v226, v227\n\t"
    "v_cvt_pk_bf16_f32 v94, v228, v229\n\t"
    "s_cbranch_vccnz Lstub_c15_4_%=\n\t"
    "Lret_c15_4_%=:\n\t"
    "v_add_f32 v52, v52, v196\n\t"
    "v_cvt_pk_bf16_f32 v95, v230, v231\n\t"
    "s_add_u32 s19, s80, 0x9400\n\t"
    "buffer_store_dwordx4 v[92:95], v20, s[52:55], s19 offen sc1\n\t"
    "v_exp_f32 v213, v181\n\t"
    "v_rcp_f32 v50, v50\n\t"
    "v_cvt_pk_bf16_f32 v194, v212, v213\n\t"
    "v_accvgpr_read_b32 v216, a64\n\t"
    "v_accvgpr_read_b32 v217, a65\n\t"
    "v_accvgpr_read_b32 v218, a66\n\t"
    "v_accvgpr_read_b32 v219, a67\n\t"
    "v_accvgpr_read_b32 v220, a68\n\t"
    "v_exp_f32 v214, v182\n\t"
    "v_accvgpr_read_b32 v221, a69\n\t"
    "v_accvgpr_read_b32 v222, a70\n\t"
    "v_accvgpr_read_b32 v223, a71\n\t"
    "v_mul_f32 v216, v50, v216\n\t"
    "v_mul_f32 v217, v50, v217\n\t"
    "v_mul_f32 v218, v50, v218\n\t"
    "v_mul_f32 v219, v50, v219\n\t"
    "v_mul_f32 v220, v50, v220\n\t"
    "v_mul_f32 v221, v50, v221\n\t"
    "v_mul_f32 v222, v50, v222\n\t"
    "v_exp_f32 v215, v183\n\t"
    "v_mul_f32 v223, v50, v223\n\t"
    "v_cvt_pk_bf16_f32 v96, v216, v217\n\t"
    "v_cvt_pk_bf16_f32 v97, v218, v219\n\t"
    "v_cvt_pk_bf16_f32 v98, v220, v221\n\t"
    "v_mfma_f32_16x16x32_bf16 a[128:131], a[224:227], v[188:191], a[128:131]\n\t"
    "v_cvt_pk_bf16_f32 v195, v214, v215\n\t"
    "v_cvt_pk_bf16_f32 v99, v222, v223\n\t"
    "v_mfma_f32_16x16x32_bf16 a[132:135], a[228:231], v[188:191], a[132:135]\n\t"
    "s_add_u32 s19, s80, 0x800\n\t"
    "buffer_store_dwordx4 v[96:99], v20, s[52:55], s19 offen sc1\n\t"
    "v_accvgpr_read_b32 v224, a72\n\t"
    "v_accvgpr_read_b32 v225, a73\n\t"
    "v_accvgpr_read_b32 v226, a74\n\t"
    "v_mfma_f32_16x16x32_bf16 a[136:139], a[232:235], v[188:191], a[136:139]\n\t"
    "v_accvgpr_read_b32 v227, a75\n\t"
    "v_accvgpr_read_b32 v228, a76\n\t"
    "v_accvgpr_read_b32 v229, a77\n\t"
    "v_accvgpr_read_b32 v230, a78\n\t"
    "v_accvgpr_read_b32 v231, a79\n\t"
    "v_mul_f32 v224, v50, v224\n\t"
    "v_mfma_f32_16x16x32_bf16 v[196:199], v[54:57], v[192:195], 0\n\t"
    "v_mul_f32 v225, v50, v225\n\t"
    "v_mul_f32 v226, v50, v226\n\t"
    "v_mul_f32 v227, v50, v227\n\t"
    "v_mul_f32 v228, v50, v228\n\t"
    "v_mul_f32 v229, v50, v229\n\t"
    "v_mul_f32 v230, v50, v230\n\t"
    "v_mfma_f32_16x16x32_bf16 a[140:143], a[236:239], v[188:191], a[140:143]\n\t"
    "v_mul_f32 v231, v50, v231\n\t"
    "v_cvt_pk_bf16_f32 v100, v224, v225\n\t"
    "v_cvt_pk_bf16_f32 v101, v226, v227\n\t"
    "v_cvt_pk_bf16_f32 v102, v228, v229\n\t"
    "v_cvt_pk_bf16_f32 v103, v230, v231\n\t"
    "v_mfma_f32_16x16x32_bf16 a[144:147], a[240:243], v[188:191], a[144:147]\n\t"
    "s_add_u32 s19, s80, 0x3800\n\t"
    "buffer_store_dwordx4 v[100:103], v20, s[52:55], s19 offen sc1\n\t"
    "v_accvgpr_read_b32 v216, a80\n\t"
    "v_accvgpr_read_b32 v217, a81\n\t"
    "v_accvgpr_read_b32 v218, a82\n\t"
    "v_mfma_f32_16x16x32_bf16 a[148:151], a[244:247], v[188:191], a[148:151]\n\t"
    "v_accvgpr_read_b32 v219, a83\n\t"
    "v_accvgpr_read_b32 v220, a84\n\t"
    "v_accvgpr_read_b32 v221, a85\n\t"
    "v_accvgpr_read_b32 v222, a86\n\t"
    "v_accvgpr_read_b32 v223, a87\n\t"
    "v_mul_f32 v216, v50, v216\n\t"
    "v_mfma_f32_16x16x32_bf16 a[152:155], a[248:251], v[188:191], a[152:155]\n\t"
    "v_mul_f32 v217, v50, v217\n\t"
    "v_mul_f32 v218, v50, v218\n\t"
    "v_mul_f32 v219, v50, v219\n\t"
    "v_mul_f32 v220, v50, v220\n\t"
    "v_mul_f32 v221, v50, v221\n\t"
    "v_mul_f32 v222, v50, v222\n\t"
    "v_mfma_f32_16x16x32_bf16 a[156:159], a[252:255], v[188:191], a[156:159]\n\t"
    "v_mul_f32 v223, v50, v223\n\t"
    "v_cvt_pk_bf16_f32 v104, v216, v217\n\t"
    "v_cvt_pk_bf16_f32 v105, v218, v219\n\t"
    "v_cvt_pk_bf16_f32 v106, v220, v221\n\t"
    "v_cvt_pk_bf16_f32 v107, v222, v223\n\t"
    "s_add_u32 s19, s80, 0x6800\n\t"
    "buffer_store_dwordx4 v[104:107], v20, s[52:55], s19 offen sc1\n\t"
    "v_cmp_lt_f32 vcc, s71, v196\n\t"
    "v_accvgpr_read_b32 v224, a88\n\t"
    "v_accvgpr_read_b32 v225, a89\n\t"
    "v_accvgpr_read_b32 v226, a90\n\t"
    "v_accvgpr_read_b32 v227, a91\n\t"
    "v_accvgpr_read_b32 v228, a92\n\t"
    "v_accvgpr_read_b32 v229, a93\n\t"
    "v_accvgpr_read_b32 v230, a94\n\t"
    "v_accvgpr_read_b32 v231, a95\n\t"
    "s_cbranch_vccnz Lstub_c15_5_%=\n\t"
    "Lret_c15_5_%=:\n\t"
    "v_add_f32 v53, v53, v196\n\t"
    "v_mul_f32 v224, v50, v224\n\t"
    "v_mul_f32 v225, v50, v225\n\t"
    "v_mul_f32 v226, v50, v226\n\t"
    "v_mul_f32 v227, v50, v227\n\t"
    "v_mul_f32 v228, v50, v228\n\t"
    "v_mul_f32 v229, v50, v229\n\t"
    "v_mul_f32 v230, v50, v230\n\t"
    "v_mul_f32 v231, v50, v231\n\t"
    "v_cvt_pk_bf16_f32 v108, v224, v225\n\t"
    "v_cvt_pk_bf16_f32 v109, v226, v227\n\t"
    "v_cvt_pk_bf16_f32 v110, v228, v229\n\t"
    "v_cvt_pk_bf16_f32 v111, v230, v231\n\t"
    "s_add_u32 s19, s80, 0x9800\n\t"
    "buffer_store_dwordx4 v[108:111], v20, s[52:55], s19 offen sc1\n\t"
    "v_mfma_f32_16x16x32_bf16 a[160:163], a[224:227], v[192:195], a[160:163]\n\t"
    "v_mfma_f32_16x16x32_bf16 a[164:167], a[228:231], v[192:195], a[164:167]\n\t"
    "v_mfma_f32_16x16x32_bf16 a[168:171], a[232:235], v[192:195], a[168:171]\n\t"
    "v_mfma_f32_16x16x32_bf16 a[172:175], a[236:239], v[192:195], a[172:175]\n\t"
    "v_mfma_f32_16x16x32_bf16 a[176:179], a[240:243], v[192:195], a[176:179]\n\t"
    "v_mfma_f32_16x16x32_bf16 a[180:183], a[244:247], v[192:195], a[180:183]\n\t"
    "v_mfma_f32_16x16x32_bf16 a[184:187], a[248:251], v[192:195], a[184:187]\n\t"
    "v_mfma_f32_16x16x32_bf16 a[188:191], a[252:255], v[192:195], a[188:191]\n\t"
    "v_rcp_f32 v51, v51\n\t"
    "v_rcp_f32 v52, v52\n\t"
    "v_rcp_f32 v53, v53\n\t"
    "s_waitcnt vmcnt(21) lgkmcnt(0)\n\t"
    "s_barrier\n\t"
    "v_mov_b32 v24, v51\n\t"
    "v_mov_b32 v25, v51\n\t"
    "v_mov_b32 v26, v52\n\t"
    "v_mov_b32 v27, v52\n\t"
    "v_mov_b32 v28, v53\n\t"
    "v_mov_b32 v29, v53\n\t"
    "v_accvgpr_read_b32 v32, a96\n\t"
    "v_accvgpr_read_b32 v33, a97\n\t"
    "v_accvgpr_read_b32 v34, a98\n\t"
    "v_accvgpr_read_b32 v35, a99\n\t"
    "v_accvgpr_read_b32 v36, a100\n\t"
    "v_accvgpr_read_b32 v37, a101\n\t"
    "v_accvgpr_read_b32 v38, a102\n\t"
    "v_accvgpr_read_b32 v39, a103\n\t"
    "v_pk_mul_f32 v[32:33], v[32:33], v[24:25]\n\t"
    "v_pk_mul_f32 v[34:35], v[34:35], v[24:25]\n\t"
    "v_pk_mul_f32 v[36:37], v[36:37], v[24:25]\n\t"
    "v_pk_mul_f32 v[38:39], v[38:39], v[24:25]\n\t"
    "v_cvt_pk_bf16_f32 v112, v32, v33\n\t"
    "v_cvt_pk_bf16_f32 v113, v34, v35\n\t"
    "v_cvt_pk_bf16_f32 v114, v36, v37\n\t"
    "v_cvt_pk_bf16_f32 v115, v38, v39\n\t"
    "s_add_u32 s78, s81, 0\n\t"
    "buffer_store_dwordx4 v[112:115], v20, s[52:55], s78 offen sc1\n\t"
    "ds_write_b128 v11, v[112:115] offset:0\n\t"
    "v_accvgpr_read_b32 v40, a128\n\t"
    "v_accvgpr_read_b32 v41, a129\n\t"
    "v_accvgpr_read_b32 v42, a130\n\t"
    "v_accvgpr_read_b32 v43, a131\n\t"
    "v_accvgpr_read_b32 v44, a132\n\t"
    "v_accvgpr_read_b32 v45, a133\n\t"
    "v_accvgpr_read_b32 v46, a134\n\t"
    "v_accvgpr_read_b32 v47, a135\n\t"
    "v_pk_mul_f32 v[40:41], v[40:41], v[26:27]\n\t"
    "v_pk_mul_f32 v[42:43], v[42:43], v[26:27]\n\t"
    "v_pk_mul_f32 v[44:45], v[44:45], v[26:27]\n\t"
    "v_pk_mul_f32 v[46:47], v[46:47], v[26:27]\n\t"
    "v_cvt_pk_bf16_f32 v128, v40, v41\n\t"
    "v_cvt_pk_bf16_f32 v129, v42, v43\n\t"
    "v_cvt_pk_bf16_f32 v130, v44, v45\n\t"
    "v_cvt_pk_bf16_f32 v131, v46, v47\n\t"
    "s_add_u32 s78, s81, 0x400\n\t"
    "buffer_store_dwordx4 v[128:131], v20, s[52:55], s78 offen sc1\n\t"
    "ds_write_b128 v11, v[128:131] offset:1024\n\t"
    "v_accvgpr_read_b32 v32, a160\n\t"
    "v_accvgpr_read_b32 v33, a161\n\t"
    "v_accvgpr_read_b32 v34, a162\n\t"
    "v_accvgpr_read_b32 v35, a163\n\t"
    "v_accvgpr_read_b32 v36, a164\n\t"
    "v_accvgpr_read_b32 v37, a165\n\t"
    "v_accvgpr_read_b32 v38, a166\n\t"
    "v_accvgpr_read_b32 v39, a167\n\t"
    "v_pk_mul_f32 v[32:33], v[32:33], v[28:29]\n\t"
    "v_pk_mul_f32 v[34:35], v[34:35], v[28:29]\n\t"
    "v_pk_mul_f32 v[36:37], v[36:37], v[28:29]\n\t"
    "v_pk_mul_f32 v[38:39], v[38:39], v[28:29]\n\t"
    "v_cvt_pk_bf16_f32 v144, v32, v33\n\t"
    "v_cvt_pk_bf16_f32 v145, v34, v35\n\t"
    "v_cvt_pk_bf16_f32 v146, v36, v37\n\t"
    "v_cvt_pk_bf16_f32 v147, v38, v39\n\t"
    "s_add_u32 s78, s81, 0x800\n\t"
    "buffer_store_dwordx4 v[144:147], v20, s[52:55], s78 offen sc1\n\t"
    "ds_write_b128 v11, v[144:147] offset:2048\n\t"
    "v_accvgpr_read_b32 v40, a104\n\t"
    "v_accvgpr_read_b32 v41, a105\n\t"
    "v_accvgpr_read_b32 v42, a106\n\t"
    "v_accvgpr_read_b32 v43, a107\n\t"
    "v_accvgpr_read_b32 v44, a108\n\t"
    "v_accvgpr_read_b32 v45, a109\n\t"
    "v_accvgpr_read_b32 v46, a110\n\t"
    "v_accvgpr_read_b32 v47, a111\n\t"
    "v_pk_mul_f32 v[40:41], v[40:41], v[24:25]\n\t"
    "v_pk_mul_f32 v[42:43], v[42:43], v[24:25]\n\t"
    "v_pk_mul_f32 v[44:45], v[44:45], v[24:25]\n\t"
    "v_pk_mul_f32 v[46:47], v[46:47], v[24:25]\n\t"
    "v_cvt_pk_bf16_f32 v116, v40, v41\n\t"
    "v_cvt_pk_bf16_f32 v117, v42, v43\n\t"
    "v_cvt_pk_bf16_f32 v118, v44, v45\n\t"
    "v_cvt_pk_bf16_f32 v119, v46, v47\n\t"
    "s_add_u32 s78, s81, 0x3000\n\t"
    "buffer_store_dwordx4 v[116:119], v20, s[52:55], s78 offen sc1\n\t"
    "ds_write_b128 v11, v[116:119] offset:3072\n\t"
    "v_accvgpr_read_b32 v32, a136\n\t"
    "v_accvgpr_read_b32 v33, a137\n\t"
    "v_accvgpr_read_b32 v34, a138\n\t"
    "v_accvgpr_read_b32 v35, a139\n\t"
    "v_accvgpr_read_b32 v36, a140\n\t"
    "v_accvgpr_read_b32 v37, a141\n\t"
    "v_accvgpr_read_b32 v38, a142\n\t"
    "v_accvgpr_read_b32 v39, a143\n\t"
    "v_pk_mul_f32 v[32:33], v[32:33], v[26:27]\n\t"
    "v_pk_mul_f32 v[34:35], v[34:35], v[26:27]\n\t"
    "v_pk_mul_f32 v[36:37], v[36:37], v[26:27]\n\t"
    "v_pk_mul_f32 v[38:39], v[38:39], v[26:27]\n\t"
    "v_cvt_pk_bf16_f32 v132, v32, v33\n\t"
    "v_cvt_pk_bf16_f32 v133, v34, v35\n\t"
    "v_cvt_pk_bf16_f32 v134, v36, v37\n\t"
    "v_cvt_pk_bf16_f32 v135, v38, v39\n\t"
    "s_add_u32 s78, s81, 0x3400\n\t"
    "buffer_store_dwordx4 v[132:135], v20, s[52:55], s78 offen sc1\n\t"
    "ds_write_b128 v11, v[132:135] offset:4096\n\t"
    "v_accvgpr_read_b32 v40, a168\n\t"
    "v_accvgpr_read_b32 v41, a169\n\t"
    "v_accvgpr_read_b32 v42, a170\n\t"
    "v_accvgpr_read_b32 v43, a171\n\t"
    "v_accvgpr_read_b32 v44, a172\n\t"
    "v_accvgpr_read_b32 v45, a173\n\t"
    "v_accvgpr_read_b32 v46, a174\n\t"
    "v_accvgpr_read_b32 v47, a175\n\t"
    "v_pk_mul_f32 v[40:41], v[40:41], v[28:29]\n\t"
    "v_pk_mul_f32 v[42:43], v[42:43], v[28:29]\n\t"
    "v_pk_mul_f32 v[44:45], v[44:45], v[28:29]\n\t"
    "v_pk_mul_f32 v[46:47], v[46:47], v[28:29]\n\t"
    "v_cvt_pk_bf16_f32 v148, v40, v41\n\t"
    "v_cvt_pk_bf16_f32 v149, v42, v43\n\t"
    "v_cvt_pk_bf16_f32 v150, v44, v45\n\t"
    "v_cvt_pk_bf16_f32 v151, v46, v47\n\t"
    "s_add_u32 s78, s81, 0x3800\n\t"
    "buffer_store_dwordx4 v[148:151], v20, s[52:55], s78 offen sc1\n\t"
    "ds_write_b128 v11, v[148:151] offset:5120\n\t"
    "v_accvgpr_read_b32 v32, a112\n\t"
    "v_accvgpr_read_b32 v33, a113\n\t"
    "v_accvgpr_read_b32 v34, a114\n\t"
    "v_accvgpr_read_b32 v35, a115\n\t"
    "v_accvgpr_read_b32 v36, a116\n\t"
    "v_accvgpr_read_b32 v37, a117\n\t"
    "v_accvgpr_read_b32 v38, a118\n\t"
    "v_accvgpr_read_b32 v39, a119\n\t"
    "v_pk_mul_f32 v[32:33], v[32:33], v[24:25]\n\t"
    "v_pk_mul_f32 v[34:35], v[34:35], v[24:25]\n\t"
    "v_pk_mul_f32 v[36:37], v[36:37], v[24:25]\n\t"
    "v_pk_mul_f32 v[38:39], v[38:39], v[24:25]\n\t"
    "v_cvt_pk_bf16_f32 v120, v32, v33\n\t"
    "v_cvt_pk_bf16_f32 v121, v34, v35\n\t"
    "v_cvt_pk_bf16_f32 v122, v36, v37\n\t"
    "v_cvt_pk_bf16_f32 v123, v38, v39\n\t"
    "s_add_u32 s78, s81, 0x6000\n\t"
    "buffer_store_dwordx4 v[120:123], v20, s[52:55], s78 offen sc1\n\t"
    "ds_write_b128 v11, v[120:123] offset:6144\n\t"
    "v_accvgpr_read_b32 v40, a144\n\t"
    "v_accvgpr_read_b32 v41, a145\n\t"
    "v_accvgpr_read_b32 v42, a146\n\t"
    "v_accvgpr_read_b32 v43, a147\n\t"
    "v_accvgpr_read_b32 v44, a148\n\t"
    "v_accvgpr_read_b32 v45, a149\n\t"
    "v_accvgpr_read_b32 v46, a150\n\t"
    "v_accvgpr_read_b32 v47, a151\n\t"
    "v_pk_mul_f32 v[40:41], v[40:41], v[26:27]\n\t"
    "v_pk_mul_f32 v[42:43], v[42:43], v[26:27]\n\t"
    "v_pk_mul_f32 v[44:45], v[44:45], v[26:27]\n\t"
    "v_pk_mul_f32 v[46:47], v[46:47], v[26:27]\n\t"
    "v_cvt_pk_bf16_f32 v136, v40, v41\n\t"
    "v_cvt_pk_bf16_f32 v137, v42, v43\n\t"
    "v_cvt_pk_bf16_f32 v138, v44, v45\n\t"
    "v_cvt_pk_bf16_f32 v139, v46, v47\n\t"
    "s_add_u32 s78, s81, 0x6400\n\t"
    "buffer_store_dwordx4 v[136:139], v20, s[52:55], s78 offen sc1\n\t"
    "ds_write_b128 v11, v[136:139] offset:7168\n\t"
    "ds_read_b64_tr_b16 a[192:193], v1 offset:0\n\t"
    "ds_read_b64_tr_b16 a[194:195], v1 offset:4096\n\t"
    "ds_read_b64_tr_b16 a[196:197], v2 offset:0\n\t"
    "ds_read_b64_tr_b16 a[198:199], v2 offset:4096\n\t"
    "ds_read_b64_tr_b16 a[200:201], v1 offset:256\n\t"
    "ds_read_b64_tr_b16 a[202:203], v1 offset:4352\n\t"
    "ds_read_b64_tr_b16 a[204:205], v2 offset:256\n\t"
    "ds_read_b64_tr_b16 a[206:207], v2 offset:4352\n\t"
    "ds_read_b64_tr_b16 a[208:209], v1 offset:512\n\t"
    "ds_read_b64_tr_b16 a[210:211], v1 offset:4608\n\t"
    "ds_read_b64_tr_b16 a[212:213], v2 offset:512\n\t"
    "ds_read_b64_tr_b16 a[214:215], v2 offset:4608\n\t"
    "ds_read_b64_tr_b16 a[216:217], v1 offset:768\n\t"
    "ds_read_b64_tr_b16 a[218:219], v1 offset:4864\n\t"
    "ds_read_b64_tr_b16 a[220:221], v2 offset:768\n\t"
    "ds_read_b64_tr_b16 a[222:223], v2 offset:4864\n\t"
    "v_accvgpr_read_b32 v32, a176\n\t"
    "v_accvgpr_read_b32 v33, a177\n\t"
    "v_accvgpr_read_b32 v34, a178\n\t"
    "v_accvgpr_read_b32 v35, a179\n\t"
    "v_accvgpr_read_b32 v36, a180\n\t"
    "v_accvgpr_read_b32 v37, a181\n\t"
    "v_accvgpr_read_b32 v38, a182\n\t"
    "v_accvgpr_read_b32 v39, a183\n\t"
    "v_pk_mul_f32 v[32:33], v[32:33], v[28:29]\n\t"
    "v_pk_mul_f32 v[34:35], v[34:35], v[28:29]\n\t"
    "v_pk_mul_f32 v[36:37], v[36:37], v[28:29]\n\t"
    "v_pk_mul_f32 v[38:39], v[38:39], v[28:29]\n\t"
    "v_cvt_pk_bf16_f32 v152, v32, v33\n\t"
    "v_cvt_pk_bf16_f32 v153, v34, v35\n\t"
    "v_cvt_pk_bf16_f32 v154, v36, v37\n\t"
    "v_cvt_pk_bf16_f32 v155, v38, v39\n\t"
    "s_add_u32 s78, s81, 0x6800\n\t"
    "buffer_store_dwordx4 v[152:155], v20, s[52:55], s78 offen sc1\n\t"
    "ds_write_b128 v11, v[152:155] offset:8192\n\t"
    "v_accvgpr_read_b32 v40, a120\n\t"
    "v_accvgpr_read_b32 v41, a121\n\t"
    "v_accvgpr_read_b32 v42, a122\n\t"
    "v_accvgpr_read_b32 v43, a123\n\t"
    "v_accvgpr_read_b32 v44, a124\n\t"
    "v_accvgpr_read_b32 v45, a125\n\t"
    "v_accvgpr_read_b32 v46, a126\n\t"
    "v_accvgpr_read_b32 v47, a127\n\t"
    "v_pk_mul_f32 v[40:41], v[40:41], v[24:25]\n\t"
    "v_pk_mul_f32 v[42:43], v[42:43], v[24:25]\n\t"
    "v_pk_mul_f32 v[44:45], v[44:45], v[24:25]\n\t"
    "v_pk_mul_f32 v[46:47], v[46:47], v[24:25]\n\t"
    "v_cvt_pk_bf16_f32 v124, v40, v41\n\t"
    "v_cvt_pk_bf16_f32 v125, v42, v43\n\t"
    "v_cvt_pk_bf16_f32 v126, v44, v45\n\t"
    "v_cvt_pk_bf16_f32 v127, v46, v47\n\t"
    "s_add_u32 s78, s81, 0x9000\n\t"
    "buffer_store_dwordx4 v[124:127], v20, s[52:55], s78 offen sc1\n\t"
    "ds_write_b128 v11, v[124:127] offset:9216\n\t"
    "v_accvgpr_read_b32 v32, a152\n\t"
    "v_accvgpr_read_b32 v33, a153\n\t"
    "v_accvgpr_read_b32 v34, a154\n\t"
    "v_accvgpr_read_b32 v35, a155\n\t"
    "v_accvgpr_read_b32 v36, a156\n\t"
    "v_accvgpr_read_b32 v37, a157\n\t"
    "v_accvgpr_read_b32 v38, a158\n\t"
    "v_accvgpr_read_b32 v39, a159\n\t"
    "v_pk_mul_f32 v[32:33], v[32:33], v[26:27]\n\t"
    "v_pk_mul_f32 v[34:35], v[34:35], v[26:27]\n\t"
    "v_pk_mul_f32 v[36:37], v[36:37], v[26:27]\n\t"
    "v_pk_mul_f32 v[38:39], v[38:39], v[26:27]\n\t"
    "v_cvt_pk_bf16_f32 v140, v32, v33\n\t"
    "v_cvt_pk_bf16_f32 v141, v34, v35\n\t"
    "v_cvt_pk_bf16_f32 v142, v36, v37\n\t"
    "v_cvt_pk_bf16_f32 v143, v38, v39\n\t"
    "s_add_u32 s78, s81, 0x9400\n\t"
    "buffer_store_dwordx4 v[140:143], v20, s[52:55], s78 offen sc1\n\t"
    "ds_write_b128 v11, v[140:143] offset:10240\n\t"
    "v_accvgpr_read_b32 v40, a184\n\t"
    "v_accvgpr_read_b32 v41, a185\n\t"
    "v_accvgpr_read_b32 v42, a186\n\t"
    "v_accvgpr_read_b32 v43, a187\n\t"
    "v_accvgpr_read_b32 v44, a188\n\t"
    "v_accvgpr_read_b32 v45, a189\n\t"
    "v_accvgpr_read_b32 v46, a190\n\t"
    "v_accvgpr_read_b32 v47, a191\n\t"
    "v_pk_mul_f32 v[40:41], v[40:41], v[28:29]\n\t"
    "v_pk_mul_f32 v[42:43], v[42:43], v[28:29]\n\t"
    "v_pk_mul_f32 v[44:45], v[44:45], v[28:29]\n\t"
    "v_pk_mul_f32 v[46:47], v[46:47], v[28:29]\n\t"
    "v_cvt_pk_bf16_f32 v156, v40, v41\n\t"
    "v_cvt_pk_bf16_f32 v157, v42, v43\n\t"
    "v_cvt_pk_bf16_f32 v158, v44, v45\n\t"
    "v_cvt_pk_bf16_f32 v159, v46, v47\n\t"
    "s_add_u32 s78, s81, 0x9800\n\t"
    "buffer_store_dwordx4 v[156:159], v20, s[52:55], s78 offen sc1\n\t"
    "ds_write_b128 v11, v[156:159] offset:11264\n\t"
    "s_waitcnt lgkmcnt(0)\n\t"
    "s_barrier\n\t"
    "s_lshr_b32 s44, s65, 3\n\t"
    "s_mul_i32 s44, s44, 3\n\t"
    "s_lshr_b32 s45, s67, 3\n\t"
    "s_mul_i32 s45, s45, 3\n\t"
    "s_lshr_b32 s46, s68, 3\n\t"
    "s_mul_i32 s46, s46, 3\n\t"
    "v_mfma_f32_16x16x32_bf16 v[160:163], a[192:195], v[64:67], 0\n\t"
    "ds_read_b64_tr_b16 a[224:225], v1 offset:8192\n\t"
    "v_mfma_f32_16x16x32_bf16 v[192:195], a[192:195], v[80:83], 0\n\t"
    "ds_read_b64_tr_b16 a[226:227], v1 offset:12288\n\t"
    "v_mfma_f32_16x16x32_bf16 v[224:227], a[192:195], v[96:99], 0\n\t"
    "ds_read_b64_tr_b16 a[228:229], v2 offset:8192\n\t"
    "v_mfma_f32_16x16x32_bf16 v[164:167], a[196:199], v[64:67], 0\n\t"
    "ds_read_b64_tr_b16 a[230:231], v2 offset:12288\n\t"
    "v_mfma_f32_16x16x32_bf16 v[196:199], a[196:199], v[80:83], 0\n\t"
    "ds_read_b64_tr_b16 a[232:233], v1 offset:8448\n\t"
    "v_mfma_f32_16x16x32_bf16 v[228:231], a[196:199], v[96:99], 0\n\t"
    "ds_read_b64_tr_b16 a[234:235], v1 offset:12544\n\t"
    "v_mfma_f32_16x16x32_bf16 v[168:171], a[200:203], v[64:67], 0\n\t"
    "ds_read_b64_tr_b16 a[236:237], v2 offset:8448\n\t"
    "v_mfma_f32_16x16x32_bf16 v[200:203], a[200:203], v[80:83], 0\n\t"
    "ds_read_b64_tr_b16 a[238:239], v2 offset:12544\n\t"
    "v_mfma_f32_16x16x32_bf16 v[232:235], a[200:203], v[96:99], 0\n\t"
    "ds_read_b64_tr_b16 a[240:241], v1 offset:8704\n\t"
    "v_mfma_f32_16x16x32_bf16 v[172:175], a[204:207], v[64:67], 0\n\t"
    "ds_read_b64_tr_b16 a[242:243], v1 offset:12800\n\t"
    "v_mfma_f32_16x16x32_bf16 v[204:207], a[204:207], v[80:83], 0\n\t"
    "ds_read_b64_tr_b16 a[244:245], v2 offset:8704\n\t"
    "v_mfma_f32_16x16x32_bf16 v[236:239], a[204:207], v[96:99], 0\n\t"
    "ds_read_b64_tr_b16 a[246:247], v2 offset:12800\n\t"
    "v_mfma_f32_16x16x32_bf16 v[176:179], a[208:211], v[64:67], 0\n\t"
    "ds_read_b64_tr_b16 a[248:249], v1 offset:8960\n\t"
    "v_mfma_f32_16x16x32_bf16 v[208:211], a[208:211], v[80:83], 0\n\t"
    "ds_read_b64_tr_b16 a[250:251], v1 offset:13056\n\t"
    "v_mfma_f32_16x16x32_bf16 v[240:243], a[208:211], v[96:99], 0\n\t"
    "ds_read_b64_tr_b16 a[252:253], v2 offset:8960\n\t"
    "v_mfma_f32_16x16x32_bf16 v[180:183], a[212:215], v[64:67], 0\n\t"
    "ds_read_b64_tr_b16 a[254:255], v2 offset:13056\n\t"
    "v_mfma_f32_16x16x32_bf16 v[212:215], a[212:215], v[80:83], 0\n\t"
    "ds_read_b128 a[48:51], v12 offset:0\n\t"
    "v_mfma_f32_16x16x32_bf16 v[244:247], a[212:215], v[96:99], 0\n\t"
    "ds_read_b128 a[52:55], v12 offset:1024\n\t"
    "v_mfma_f32_16x16x32_bf16 v[184:187], a[216:219], v[64:67], 0\n\t"
    "ds_read_b128 a[56:59], v12 offset:2048\n\t"
    "v_mfma_f32_16x16x32_bf16 v[216:219], a[216:219], v[80:83], 0\n\t"
    "ds_read_b128 a[60:63], v12 offset:3072\n\t"
    "v_mfma_f32_16x16x32_bf16 v[248:251], a[216:219], v[96:99], 0\n\t"
    "v_mfma_f32_16x16x32_bf16 v[188:191], a[220:223], v[64:67], 0\n\t"
    "v_mfma_f32_16x16x32_bf16 v[220:223], a[220:223], v[80:83], 0\n\t"
    "v_mfma_f32_16x16x32_bf16 v[252:255], a[220:223], v[96:99], 0\n\t"
    "s_waitcnt lgkmcnt(1)\n\t"
    "v_mfma_f32_16x16x32_bf16 v[160:163], a[224:227], v[68:71], v[160:163]\n\t"
    "ds_read_b64_tr_b16 a[192:193], v1 offset:16384\n\t"
    "v_mfma_f32_16x16x32_bf16 v[192:195], a[224:227], v[84:87], v[192:195]\n\t"
    "ds_read_b64_tr_b16 a[194:195], v1 offset:20480\n\t"
    "v_mfma_f32_16x16x32_bf16 v[224:227], a[224:227], v[100:103], v[224:227]\n\t"
    "ds_read_b64_tr_b16 a[196:197], v2 offset:16384\n\t"
    "v_mfma_f32_16x16x32_bf16 v[164:167], a[228:231], v[68:71], v[164:167]\n\t"
    "ds_read_b64_tr_b16 a[198:199], v2 offset:20480\n\t"
    "v_mfma_f32_16x16x32_bf16 v[196:199], a[228:231], v[84:87], v[196:199]\n\t"
    "ds_read_b64_tr_b16 a[200:201], v1 offset:16640\n\t"
    "v_mfma_f32_16x16x32_bf16 v[228:231], a[228:231], v[100:103], v[228:231]\n\t"
    "ds_read_b64_tr_b16 a[202:203], v1 offset:20736\n\t"
    "v_mfma_f32_16x16x32_bf16 v[168:171], a[232:235], v[68:71], v[168:171]\n\t"
    "ds_read_b64_tr_b16 a[204:205], v2 offset:16640\n\t"
    "v_mfma_f32_16x16x32_bf16 v[200:203], a[232:235], v[84:87], v[200:203]\n\t"
    "ds_read_b64_tr_b16 a[206:207], v2 offset:20736\n\t"
    "v_mfma_f32_16x16x32_bf16 v[232:235], a[232:235], v[100:103], v[232:235]\n\t"
    "ds_read_b64_tr_b16 a[208:209], v1 offset:16896\n\t"
    "v_mfma_f32_16x16x32_bf16 v[172:175], a[236:239], v[68:71], v[172:175]\n\t"
    "ds_read_b64_tr_b16 a[210:211], v1 offset:20992\n\t"
    "v_mfma_f32_16x16x32_bf16 v[204:207], a[236:239], v[84:87], v[204:207]\n\t"
    "ds_read_b64_tr_b16 a[212:213], v2 offset:16896\n\t"
    "v_mfma_f32_16x16x32_bf16 v[236:239], a[236:239], v[100:103], v[236:239]\n\t"
    "ds_read_b64_tr_b16 a[214:215], v2 offset:20992\n\t"
    "v_mfma_f32_16x16x32_bf16 v[176:179], a[240:243], v[68:71], v[176:179]\n\t"
    "ds_read_b64_tr_b16 a[216:217], v1 offset:17152\n\t"
    "v_mfma_f32_16x16x32_bf16 v[208:211], a[240:243], v[84:87], v[208:211]\n\t"
    "ds_read_b64_tr_b16 a[218:219], v1 offset:21248\n\t"
    "v_mfma_f32_16x16x32_bf16 v[240:243], a[240:243], v[100:103], v[240:243]\n\t"
    "ds_read_b64_tr_b16 a[220:221], v2 offset:17152\n\t"
    "v_mfma_f32_16x16x32_bf16 v[180:183], a[244:247], v[68:71], v[180:183]\n\t"
    "ds_read_b64_tr_b16 a[222:223], v2 offset:21248\n\t"
    "v_mfma_f32_16x16x32_bf16 v[212:215], a[244:247], v[84:87], v[212:215]\n\t"
    "ds_read_b128 a[64:67], v12 offset:4096\n\t"
    "v_mfma_f32_16x16x32_bf16 v[244:247], a[244:247], v[100:103], v[244:247]\n\t"
    "ds_read_b128 a[68:71], v12 offset:5120\n\t"
    "v_mfma_f32_16x16x32_bf16 v[184:187], a[248:251], v[68:71], v[184:187]\n\t"
    "ds_read_b128 a[72:75], v12 offset:6144\n\t"
    "v_mfma_f32_16x16x32_bf16 v[216:219], a[248:251], v[84:87], v[216:219]\n\t"
    "ds_read_b128 a[76:79], v12 offset:7168\n\t"
    "v_mfma_f32_16x16x32_bf16 v[248:251], a[248:251], v[100:103], v[248:251]\n\t"
    "v_mfma_f32_16x16x32_bf16 v[188:191], a[252:255], v[68:71], v[188:191]\n\t"
    "v_mfma_f32_16x16x32_bf16 v[220:223], a[252:255], v[84:87], v[220:223]\n\t"
    "v_mfma_f32_16x16x32_bf16 v[252:255], a[252:255], v[100:103], v[252:255]\n\t"
    "s_waitcnt lgkmcnt(1)\n\t"
    "v_mfma_f32_16x16x32_bf16 v[160:163], a[192:195], v[72:75], v[160:163]\n\t"
    "ds_read_b64_tr_b16 a[224:225], v1 offset:24576\n\t"
    "v_mfma_f32_16x16x32_bf16 v[192:195], a[192:195], v[88:91], v[192:195]\n\t"
    "ds_read_b64_tr_b16 a[226:227], v1 offset:28672\n\t"
    "v_mfma_f32_16x16x32_bf16 v[224:227], a[192:195], v[104:107], v[224:227]\n\t"
    "ds_read_b64_tr_b16 a[228:229], v2 offset:24576\n\t"
    "v_mfma_f32_16x16x32_bf16 v[164:167], a[196:199], v[72:75], v[164:167]\n\t"
    "ds_read_b64_tr_b16 a[230:231], v2 offset:28672\n\t"
    "v_mfma_f32_16x16x32_bf16 v[196:199], a[196:199], v[88:91], v[196:199]\n\t"
    "ds_read_b64_tr_b16 a[232:233], v1 offset:24832\n\t"
    "v_mfma_f32_16x16x32_bf16 v[228:231], a[196:199], v[104:107], v[228:231]\n\t"
    "ds_read_b64_tr_b16 a[234:235], v1 offset:28928\n\t"
    "v_mfma_f32_16x16x32_bf16 v[168:171], a[200:203], v[72:75], v[168:171]\n\t"
    "ds_read_b64_tr_b16 a[236:237], v2 offset:24832\n\t"
    "v_mfma_f32_16x16x32_bf16 v[200:203], a[200:203], v[88:91], v[200:203]\n\t"
    "ds_read_b64_tr_b16 a[238:239], v2 offset:28928\n\t"
    "v_mfma_f32_16x16x32_bf16 v[232:235], a[200:203], v[104:107], v[232:235]\n\t"
    "ds_read_b64_tr_b16 a[240:241], v1 offset:25088\n\t"
    "v_mfma_f32_16x16x32_bf16 v[172:175], a[204:207], v[72:75], v[172:175]\n\t"
    "ds_read_b64_tr_b16 a[242:243], v1 offset:29184\n\t"
    "v_mfma_f32_16x16x32_bf16 v[204:207], a[204:207], v[88:91], v[204:207]\n\t"
    "ds_read_b64_tr_b16 a[244:245], v2 offset:25088\n\t"
    "v_mfma_f32_16x16x32_bf16 v[236:239], a[204:207], v[104:107], v[236:239]\n\t"
    "ds_read_b64_tr_b16 a[246:247], v2 offset:29184\n\t"
    "v_mfma_f32_16x16x32_bf16 v[176:179], a[208:211], v[72:75], v[176:179]\n\t"
    "ds_read_b64_tr_b16 a[248:249], v1 offset:25344\n\t"
    "v_mfma_f32_16x16x32_bf16 v[208:211], a[208:211], v[88:91], v[208:211]\n\t"
    "ds_read_b64_tr_b16 a[250:251], v1 offset:29440\n\t"
    "v_mfma_f32_16x16x32_bf16 v[240:243], a[208:211], v[104:107], v[240:243]\n\t"
    "ds_read_b64_tr_b16 a[252:253], v2 offset:25344\n\t"
    "v_mfma_f32_16x16x32_bf16 v[180:183], a[212:215], v[72:75], v[180:183]\n\t"
    "ds_read_b64_tr_b16 a[254:255], v2 offset:29440\n\t"
    "v_mfma_f32_16x16x32_bf16 v[212:215], a[212:215], v[88:91], v[212:215]\n\t"
    "ds_read_b128 a[80:83], v12 offset:8192\n\t"
    "v_mfma_f32_16x16x32_bf16 v[244:247], a[212:215], v[104:107], v[244:247]\n\t"
    "ds_read_b128 a[84:87], v12 offset:9216\n\t"
    "v_mfma_f32_16x16x32_bf16 v[184:187], a[216:219], v[72:75], v[184:187]\n\t"
    "ds_read_b128 a[88:91], v12 offset:10240\n\t"
    "v_mfma_f32_16x16x32_bf16 v[216:219], a[216:219], v[88:91], v[216:219]\n\t"
    "ds_read_b128 a[92:95], v12 offset:11264\n\t"
    "v_mfma_f32_16x16x32_bf16 v[248:251], a[216:219], v[104:107], v[248:251]\n\t"
    "v_mfma_f32_16x16x32_bf16 v[188:191], a[220:223], v[72:75], v[188:191]\n\t"
    "v_mfma_f32_16x16x32_bf16 v[220:223], a[220:223], v[88:91], v[220:223]\n\t"
    "v_mfma_f32_16x16x32_bf16 v[252:255], a[220:223], v[104:107], v[252:255]\n\t"
    "s_waitcnt lgkmcnt(1)\n\t"
    "v_mfma_f32_16x16x32_bf16 v[160:163], a[224:227], v[76:79], v[160:163]\n\t"
    "s_waitcnt lgkmcnt(0)\n\t"
    "s_barrier\n\t"
    "s_waitcnt vmcnt(0)\n\t"
    "s_barrier\n\t"
    "s_cmp_lg_u32 s56, 0\n\t"
    "s_cbranch_scc1 Lnp_o_%=\n\t"
    "v_mov_b32 v24, s82\n\t"
    "v_mov_b32 v25, s83\n\t"
    "s_lshl_b32 s78, s63, 7\n\t"
    "s_add_u32 s78, s78, 0x1000\n\t"
    "s_mov_b64 exec, 1\n\t"
    "buffer_store_dwordx2 v[24:25], v60, s[84:87], s78 offen sc1\n\t"
    "s_mov_b64 exec, -1\n\t"
    "Lnp_o_%=:\n\t"
    "ds_read_b64_tr_b16 a[192:193], v3 offset:0\n\t"
    "v_mfma_f32_16x16x32_bf16 v[192:195], a[224:227], v[92:95], v[192:195]\n\t"
    "ds_read_b64_tr_b16 a[194:195], v3 offset:4096\n\t"
    "v_mfma_f32_16x16x32_bf16 v[224:227], a[224:227], v[108:111], v[224:227]\n\t"
    "s_add_u32 m0, s59, 0\n\t"
    "ds_read_b64_tr_b16 a[196:197], v4 offset:0\n\t"
    "v_mfma_f32_16x16x32_bf16 v[164:167], a[228:231], v[76:79], v[164:167]\n\t"
    "s_add_u32 s78, s68, 0\n\t"
    "buffer_load_dwordx4 v62, s[36:39], s78 offen lds\n\t"
    "v_mfma_f32_16x16x32_bf16 v[196:199], a[228:231], v[92:95], v[196:199]\n\t"
    "ds_read_b64_tr_b16 a[198:199], v4 offset:4096\n\t"
    "v_mfma_f32_16x16x32_bf16 v[228:231], a[228:231], v[108:111], v[228:231]\n\t"
    "ds_read_b64_tr_b16 a[200:201], v3 offset:256\n\t"
    "v_mfma_f32_16x16x32_bf16 v[168:171], a[232:235], v[76:79], v[168:171]\n\t"
    "s_add_u32 m0, s59, 0x400\n\t"
    "ds_read_b64_tr_b16 a[202:203], v3 offset:4352\n\t"
    "v_mfma_f32_16x16x32_bf16 v[200:203], a[232:235], v[92:95], v[200:203]\n\t"
    "s_add_u32 s78, s68, 0x1000\n\t"
    "buffer_load_dwordx4 v63, s[36:39], s78 offen lds\n\t"
    "v_mfma_f32_16x16x32_bf16 v[232:235], a[232:235], v[108:111], v[232:235]\n\t"
    "ds_read_b64_tr_b16 a[204:205], v4 offset:256\n\t"
    "v_mfma_f32_16x16x32_bf16 v[172:175], a[236:239], v[76:79], v[172:175]\n\t"
    "ds_read_b64_tr_b16 a[206:207], v4 offset:4352\n\t"
    "v_mfma_f32_16x16x32_bf16 v[204:207], a[236:239], v[92:95], v[204:207]\n\t"
    "s_add_u32 m0, s59, 0x2000\n\t"
    "ds_read_b64_tr_b16 a[208:209], v3 offset:512\n\t"
    "v_mfma_f32_16x16x32_bf16 v[236:239], a[236:239], v[108:111], v[236:239]\n\t"
    "s_add_u32 s78, s68, 0x8000\n\t"
    "buffer_load_dwordx4 v62, s[36:39], s78 offen lds\n\t"
    "v_mfma_f32_16x16x32_bf16 v[176:179], a[240:243], v[76:79], v[176:179]\n\t"
    "ds_read_b64_tr_b16 a[210:211], v3 offset:4608\n\t"
    "v_mfma_f32_16x16x32_bf16 v[208:211], a[240:243], v[92:95], v[208:211]\n\t"
    "ds_read_b64_tr_b16 a[212:213], v4 offset:512\n\t"
    "v_mfma_f32_16x16x32_bf16 v[240:243], a[240:243], v[108:111], v[240:243]\n\t"
    "s_add_u32 m0, s59, 0x2400\n\t"
    "ds_read_b64_tr_b16 a[214:215], v4 offset:4608\n\t"
    "v_mfma_f32_16x16x32_bf16 v[180:183], a[244:247], v[76:79], v[180:183]\n\t"
    "s_add_u32 s78, s68, 0x9000\n\t"
    "buffer_load_dwordx4 v63, s[36:39], s78 offen lds\n\t"
    "v_mfma_f32_16x16x32_bf16 v[212:215], a[244:247], v[92:95], v[212:215]\n\t"
    "ds_read_b64_tr_b16 a[216:217], v3 offset:768\n\t"
    "v_mfma_f32_16x16x32_bf16 v[244:247], a[244:247], v[108:111], v[244:247]\n\t"
    "ds_read_b64_tr_b16 a[218:219], v3 offset:4864\n\t"
    "v_mfma_f32_16x16x32_bf16 v[184:187], a[248:251], v[76:79], v[184:187]\n\t"
    "s_add_u32 m0, s59, 0xc000\n\t"
    "ds_read_b64_tr_b16 a[220:221], v4 offset:768\n\t"
    "v_mfma_f32_16x16x32_bf16 v[216:219], a[248:251], v[92:95], v[216:219]\n\t"
    "s_add_u32 s78, s65, 0x10000\n\t"
    "buffer_load_dwordx4 v62, s[36:39], s78 offen lds\n\t"
    "v_mfma_f32_16x16x32_bf16 v[248:251], a[248:251], v[108:111], v[248:251]\n\t"
    "ds_read_b64_tr_b16 a[222:223], v4 offset:4864\n\t"
    "v_mfma_f32_16x16x32_bf16 v[188:191], a[252:255], v[76:79], v[188:191]\n\t"
    "v_mfma_f32_16x16x32_bf16 v[220:223], a[252:255], v[92:95], v[220:223]\n\t"
    "s_add_u32 m0, s59, 0xc400\n\t"
    "v_mfma_f32_16x16x32_bf16 v[252:255], a[252:255], v[108:111], v[252:255]\n\t"
    "s_waitcnt lgkmcnt(1)\n\t"
    "s_add_u32 s78, s65, 0x11000\n\t"
    "buffer_load_dwordx4 v63, s[36:39], s78 offen lds\n\t"
    "v_mfma_f32_16x16x32_bf16 v[160:163], a[192:195], a[48:51], v[160:163]\n\t"
    "ds_read_b64_tr_b16 a[224:225], v3 offset:8192\n\t"
    "v_mfma_f32_16x16x32_bf16 v[192:195], a[192:195], a[52:55], v[192:195]\n\t"
    "ds_read_b64_tr_b16 a[226:227], v3 offset:12288\n\t"
    "v_mfma_f32_16x16x32_bf16 v[224:227], a[192:195], a[56:59], v[224:227]\n\t"
    "s_add_u32 m0, s59, 0xe000\n\t"
    "ds_read_b64_tr_b16 a[228:229], v4 offset:8192\n\t"
    "v_mfma_f32_16x16x32_bf16 v[164:167], a[196:199], a[48:51], v[164:167]\n\t"
    "s_add_u32 s78, s65, 0x18000\n\t"
    "buffer_load_dwordx4 v62, s[36:39], s78 offen lds\n\t"
    "v_mfma_f32_16x16x32_bf16 v[196:199], a[196:199], a[52:55], v[196:199]\n\t"
    "ds_read_b64_tr_b16 a[230:231], v4 offset:12288\n\t"
    "v_mfma_f32_16x16x32_bf16 v[228:231], a[196:199], a[56:59], v[228:231]\n\t"
    "ds_read_b64_tr_b16 a[232:233], v3 offset:8448\n\t"
    "v_mfma_f32_16x16x32_bf16 v[168:171], a[200:203], a[48:51], v[168:171]\n\t"
    "s_add_u32 m0, s59, 0xe400\n\t"
    "ds_read_b64_tr_b16 a[234:235], v3 offset:12544\n\t"
    "v_mfma_f32_16x16x32_bf16 v[200:203], a[200:203], a[52:55], v[200:203]\n\t"
    "s_add_u32 s78, s65, 0x19000\n\t"
    "buffer_load_dwordx4 v63, s[36:39], s78 offen lds\n\t"
    "v_mfma_f32_16x16x32_bf16 v[232:235], a[200:203], a[56:59], v[232:235]\n\t"
    "ds_read_b64_tr_b16 a[236:237], v4 offset:8448\n\t"
    "v_mfma_f32_16x16x32_bf16 v[172:175], a[204:207], a[48:51], v[172:175]\n\t"
    "ds_read_b64_tr_b16 a[238:239], v4 offset:12544\n\t"
    "v_mfma_f32_16x16x32_bf16 v[204:207], a[204:207], a[52:55], v[204:207]\n\t"
    "s_add_u32 m0, s59, 0x10000\n\t"
    "ds_read_b64_tr_b16 a[240:241], v3 offset:8704\n\t"
    "v_mfma_f32_16x16x32_bf16 v[236:239], a[204:207], a[56:59], v[236:239]\n\t"
    "s_add_u32 s78, s67, 0x10000\n\t"
    "buffer_load_dwordx4 v62, s[36:39], s78 offen lds\n\t"
    "v_mfma_f32_16x16x32_bf16 v[176:179], a[208:211], a[48:51], v[176:179]\n\t"
    "ds_read_b64_tr_b16 a[242:243], v3 offset:12800\n\t"
    "v_mfma_f32_16x16x32_bf16 v[208:211], a[208:211], a[52:55], v[208:211]\n\t"
    "ds_read_b64_tr_b16 a[244:245], v4 offset:8704\n\t"
    "v_mfma_f32_16x16x32_bf16 v[240:243], a[208:211], a[56:59], v[240:243]\n\t"
    "s_add_u32 m0, s59, 0x10400\n\t"
    "ds_read_b64_tr_b16 a[246:247], v4 offset:12800\n\t"
    "v_mfma_f32_16x16x32_bf16 v[180:183], a[212:215], a[48:51], v[180:183]\n\t"
    "s_add_u32 s78, s67, 0x11000\n\t"
    "buffer_load_dwordx4 v63, s[36:39], s78 offen lds\n\t"
    "v_mfma_f32_16x16x32_bf16 v[212:215], a[212:215], a[52:55], v[212:215]\n\t"
    "ds_read_b64_tr_b16 a[248:249], v3 offset:8960\n\t"
    "v_mfma_f32_16x16x32_bf16 v[244:247], a[212:215], a[56:59], v[244:247]\n\t"
    "ds_read_b64_tr_b16 a[250:251], v3 offset:13056\n\t"
    "v_mfma_f32_16x16x32_bf16 v[184:187], a[216:219], a[48:51], v[184:187]\n\t"
    "s_add_u32 m0, s59, 0x12000\n\t"
    "ds_read_b64_tr_b16 a[252:253], v4 offset:8960\n\t"
    "v_mfma_f32_16x16x32_bf16 v[216:219], a[216:219], a[52:55], v[216:219]\n\t"
    "s_add_u32 s78, s67, 0x18000\n\t"
    "buffer_load_dwordx4 v62, s[36:39], s78 offen lds\n\t"
    "v_mfma_f32_16x16x32_bf16 v[248:251], a[216:219], a[56:59], v[248:251]\n\t"
    "ds_read_b64_tr_b16 a[254:255], v4 offset:13056\n\t"
    "s_waitcnt lgkmcnt(4)\n\t"
    "v_mfma_f32_16x16x32_bf16 v[188:191], a[220:223], a[48:51], v[188:191]\n\t"
    "v_mfma_f32_16x16x32_bf16 v[220:223], a[220:223], a[52:55], v[220:223]\n\t"
    "s_add_u32 m0, s59, 0x12400\n\t"
    "v_mfma_f32_16x16x32_bf16 v[252:255], a[220:223], a[56:59], v[252:255]\n\t"
    "s_add_u32 s78, s67, 0x19000\n\t"
    "buffer_load_dwordx4 v63, s[36:39], s78 offen lds\n\t"
    "v_mfma_f32_16x16x32_bf16 v[160:163], a[224:227], a[60:63], v[160:163]\n\t"
    "ds_read_b64_tr_b16 a[192:193], v3 offset:16384\n\t"
    "v_mfma_f32_16x16x32_bf16 v[192:195], a[224:227], a[64:67], v[192:195]\n\t"
    "ds_read_b64_tr_b16 a[194:195], v3 offset:20480\n\t"
    "v_mfma_f32_16x16x32_bf16 v[224:227], a[224:227], a[68:71], v[224:227]\n\t"
    "s_add_u32 m0, s59, 0x1c000\n\t"
    "ds_read_b64_tr_b16 a[196:197], v4 offset:16384\n\t"
    "v_mfma_f32_16x16x32_bf16 v[164:167], a[228:231], a[60:63], v[164:167]\n\t"
    "s_add_u32 s78, s68, 0x10000\n\t"
    "buffer_load_dwordx4 v62, s[36:39], s78 offen lds\n\t"
    "v_mfma_f32_16x16x32_bf16 v[196:199], a[228:231], a[64:67], v[196:199]\n\t"
    "ds_read_b64_tr_b16 a[198:199], v4 offset:20480\n\t"
    "v_mfma_f32_16x16x32_bf16 v[228:231], a[228:231], a[68:71], v[228:231]\n\t"
    "ds_read_b64_tr_b16 a[200:201], v3 offset:16640\n\t"
    "v_mfma_f32_16x16x32_bf16 v[168:171], a[232:235], a[60:63], v[168:171]\n\t"
    "s_add_u32 m0, s59, 0x1c400\n\t"
    "ds_read_b64_tr_b16 a[202:203], v3 offset:20736\n\t"
    "v_mfma_f32_16x16x32_bf16 v[200:203], a[232:235], a[64:67], v[200:203]\n\t"
    "s_add_u32 s78, s68, 0x11000\n\t"
    "buffer_load_dwordx4 v63, s[36:39], s78 offen lds\n\t"
    "v_mfma_f32_16x16x32_bf16 v[232:235], a[232:235], a[68:71], v[232:235]\n\t"
    "ds_read_b64_tr_b16 a[204:205], v4 offset:16640\n\t"
    "v_mfma_f32_16x16x32_bf16 v[172:175], a[236:239], a[60:63], v[172:175]\n\t"
    "ds_read_b64_tr_b16 a[206:207], v4 offset:20736\n\t"
    "v_mfma_f32_16x16x32_bf16 v[204:207], a[236:239], a[64:67], v[204:207]\n\t"
    "s_add_u32 m0, s59, 0x1e000\n\t"
    "ds_read_b64_tr_b16 a[208:209], v3 offset:16896\n\t"
    "v_mfma_f32_16x16x32_bf16 v[236:239], a[236:239], a[68:71], v[236:239]\n\t"
    "s_add_u32 s78, s68, 0x18000\n\t"
    "buffer_load_dwordx4 v62, s[36:39], s78 offen lds\n\t"
    "v_mfma_f32_16x16x32_bf16 v[176:179], a[240:243], a[60:63], v[176:179]\n\t"
    "ds_read_b64_tr_b16 a[210:211], v3 offset:20992\n\t"
    "v_mfma_f32_16x16x32_bf16 v[208:211], a[240:243], a[64:67], v[208:211]\n\t"
    "ds_read_b64_tr_b16 a[212:213], v4 offset:16896\n\t"
    "v_mfma_f32_16x16x32_bf16 v[240:243], a[240:243], a[68:71], v[240:243]\n\t"
    "s_add_u32 m0, s59, 0x1e400\n\t"
    "ds_read_b64_tr_b16 a[214:215], v4 offset:20992\n\t"
    "v_mfma_f32_16x16x32_bf16 v[180:183], a[244:247], a[60:63], v[180:183]\n\t"
    "s_add_u32 s78, s68, 0x19000\n\t"
    "buffer_load_dwordx4 v63, s[36:39], s78 offen lds\n\t"
    "v_mfma_f32_16x16x32_bf16 v[212:215], a[244:247], a[64:67], v[212:215]\n\t"
    "ds_read_b64_tr_b16 a[216:217], v3 offset:17152\n\t"
    "v_mfma_f32_16x16x32_bf16 v[244:247], a[244:247], a[68:71], v[244:247]\n\t"
    "ds_read_b64_tr_b16 a[218:219], v3 offset:21248\n\t"
    "s_waitcnt lgkmcnt(4)\n\t"
    "v_mfma_f32_16x16x32_bf16 v[184:187], a[248:251], a[60:63], v[184:187]\n\t"
    "ds_read_b64_tr_b16 a[220:221], v4 offset:17152\n\t"
    "v_mfma_f32_16x16x32_bf16 v[216:219], a[248:251], a[64:67], v[216:219]\n\t"
    "ds_read_b64_tr_b16 a[222:223], v4 offset:21248\n\t"
    "v_mfma_f32_16x16x32_bf16 v[248:251], a[248:251], a[68:71], v[248:251]\n\t"
    "v_mfma_f32_16x16x32_bf16 v[188:191], a[252:255], a[60:63], v[188:191]\n\t"
    "v_mfma_f32_16x16x32_bf16 v[220:223], a[252:255], a[64:67], v[220:223]\n\t"
    "v_mfma_f32_16x16x32_bf16 v[252:255], a[252:255], a[68:71], v[252:255]\n\t"
    "v_mfma_f32_16x16x32_bf16 v[160:163], a[192:195], a[72:75], v[160:163]\n\t"
    "s_and_b32 s78, s79, 1\n\t"
    "s_cmp_eq_u32 s78, 1\n\t"
    "s_cbranch_scc1 Lok_o1_%=\n\t"
    "s_mov_b32 s96, 0x400000\n\t"
    "Lpoll_o1_%=:\n\t"
    "buffer_load_dwordx2 v[26:27], v13, s[84:87], 0 offen sc1\n\t"
    "s_waitcnt vmcnt(0)\n\t"
    "v_cmp_eq_u32 vcc, s82, v26\n\t"
    "v_cmp_eq_u32 s[76:77], s83, v27\n\t"
    "s_nop 1\n\t"
    "s_and_b32 s79, vcc_lo, s76\n\t"
    "s_and_b32 s78, s79, 1\n\t"
    "s_cmp_eq_u32 s78, 1\n\t"
    "s_cbranch_scc1 Lok_o1_%=\n\t"
    "s_sleep 1\n\t"
    "s_sub_u32 s96, s96, 1\n\t"
    "s_cmp_lg_u32 s96, 0\n\t"
    "s_cbranch_scc1 Lpoll_o1_%=\n\t"
    "Lok_o1_%=:\n\t"
    "ds_read_b64_tr_b16 a[224:225], v3 offset:24576\n\t"
    "v_mfma_f32_16x16x32_bf16 v[192:195], a[192:195], a[76:79], v[192:195]\n\t"
    "ds_read_b64_tr_b16 a[226:227], v3 offset:28672\n\t"
    "v_mfma_f32_16x16x32_bf16 v[224:227], a[192:195], a[80:83], v[224:227]\n\t"
    "ds_read_b64_tr_b16 a[228:229], v4 offset:24576\n\t"
    "v_mfma_f32_16x16x32_bf16 v[164:167], a[196:199], a[72:75], v[164:167]\n\t"
    "s_add_u32 s78, s44, 0\n\t"
    "buffer_load_dwordx4 a[96:99], v20, s[40:43], s78 offen sc1\n\t"
    "v_mfma_f32_16x16x32_bf16 v[196:199], a[196:199], a[76:79], v[196:199]\n\t"
    "ds_read_b64_tr_b16 a[230:231], v4 offset:28672\n\t"
    "v_mfma_f32_16x16x32_bf16 v[228:231], a[196:199], a[80:83], v[228:231]\n\t"
    "ds_read_b64_tr_b16 a[232:233], v3 offset:24832\n\t"
    "v_mfma_f32_16x16x32_bf16 v[168:171], a[200:203], a[72:75], v[168:171]\n\t"
    "ds_read_b64_tr_b16 a[234:235], v3 offset:28928\n\t"
    "v_mfma_f32_16x16x32_bf16 v[200:203], a[200:203], a[76:79], v[200:203]\n\t"
    "s_add_u32 s78, s44, 0\n\t"
    "buffer_load_dwordx4 a[100:103], v20, s[40:43], s78 offen offset:1024 sc1\n\t"
    "v_mfma_f32_16x16x32_bf16 v[232:235], a[200:203], a[80:83], v[232:235]\n\t"
    "ds_read_b64_tr_b16 a[236:237], v4 offset:24832\n\t"
    "v_mfma_f32_16x16x32_bf16 v[172:175], a[204:207], a[72:75], v[172:175]\n\t"
    "ds_read_b64_tr_b16 a[238:239], v4 offset:28928\n\t"
    "v_mfma_f32_16x16x32_bf16 v[204:207], a[204:207], a[76:79], v[204:207]\n\t"
    "ds_read_b64_tr_b16 a[240:241], v3 offset:25088\n\t"
    "v_mfma_f32_16x16x32_bf16 v[236:239], a[204:207], a[80:83], v[236:239]\n\t"
    "s_add_u32 s78, s44, 0\n\t"
    "buffer_load_dwordx4 a[104:107], v20, s[40:43], s78 offen offset:2048 sc1\n\t"
    "v_mfma_f32_16x16x32_bf16 v[176:179], a[208:211], a[72:75], v[176:179]\n\t"
    "ds_read_b64_tr_b16 a[242:243], v3 offset:29184\n\t"
    "v_mfma_f32_16x16x32_bf16 v[208:211], a[208:211], a[76:79], v[208:211]\n\t"
    "ds_read_b64_tr_b16 a[244:245], v4 offset:25088\n\t"
    "v_mfma_f32_16x16x32_bf16 v[240:243], a[208:211], a[80:83], v[240:243]\n\t"
    "ds_read_b64_tr_b16 a[246:247], v4 offset:29184\n\t"
    "s_waitcnt lgkmcnt(4)\n\t"
    "v_mfma_f32_16x16x32_bf16 v[180:183], a[212:215], a[72:75], v[180:183]\n\t"
    "s_add_u32 s78, s44, 0x3000\n\t"
    "buffer_load_dwordx4 a[108:111], v20, s[40:43], s78 offen sc1\n\t"
    "v_mfma_f32_16x16x32_bf16 v[212:215], a[212:215], a[76:79], v[212:215]\n\t"
    "ds_read_b64_tr_b16 a[248:249], v3 offset:25344\n\t"
    "v_mfma_f32_16x16x32_bf16 v[244:247], a[212:215], a[80:83], v[244:247]\n\t"
    "ds_read_b64_tr_b16 a[250:251], v3 offset:29440\n\t"
    "v_mfma_f32_16x16x32_bf16 v[184:187], a[216:219], a[72:75], v[184:187]\n\t"
    "ds_read_b64_tr_b16 a[252:253], v4 offset:25344\n\t"
    "v_mfma_f32_16x16x32_bf16 v[216:219], a[216:219], a[76:79], v[216:219]\n\t"
    "s_add_u32 s78, s44, 0x3000\n\t"
    "buffer_load_dwordx4 a[112:115], v20, s[40:43], s78 offen offset:1024 sc1\n\t"
    "v_mfma_f32_16x16x32_bf16 v[248:251], a[216:219], a[80:83], v[248:251]\n\t"
    "ds_read_b64_tr_b16 a[254:255], v4 offset:29440\n\t"
    "v_mfma_f32_16x16x32_bf16 v[188:191], a[220:223], a[72:75], v[188:191]\n\t"
    "v_mfma_f32_16x16x32_bf16 v[220:223], a[220:223], a[76:79], v[220:223]\n\t"
    "v_mfma_f32_16x16x32_bf16 v[252:255], a[220:223], a[80:83], v[252:255]\n\t"
    "s_add_u32 s78, s44, 0x3000\n\t"
    "buffer_load_dwordx4 a[116:119], v20, s[40:43], s78 offen offset:2048 sc1\n\t"
    "v_mfma_f32_16x16x32_bf16 v[160:163], a[224:227], a[84:87], v[160:163]\n\t"
    "s_waitcnt lgkmcnt(0)\n\t"
    "s_barrier\n\t"
    "s_and_b32 s78, s79, 2\n\t"
    "s_cmp_eq_u32 s78, 2\n\t"
    "s_cbranch_scc1 Lok_o2_%=\n\t"
    "s_mov_b32 s96, 0x400000\n\t"
    "Lpoll_o2_%=:\n\t"
    "buffer_load_dwordx2 v[26:27], v13, s[84:87], 0 offen sc1\n\t"
    "s_waitcnt vmcnt(0)\n\t"
    "v_cmp_eq_u32 vcc, s82, v26\n\t"
    "v_cmp_eq_u32 s[76:77], s83, v27\n\t"
    "s_nop 1\n\t"
    "s_and_b32 s79, vcc_lo, s76\n\t"
    "s_and_b32 s78, s79, 2\n\t"
    "s_cmp_eq_u32 s78, 2\n\t"
    "s_cbranch_scc1 Lok_o2_%=\n\t"
    "s_sleep 1\n\t"
    "s_sub_u32 s96, s96, 1\n\t"
    "s_cmp_lg_u32 s96, 0\n\t"
    "s_cbranch_scc1 Lpoll_o2_%=\n\t"
    "Lok_o2_%=:\n\t"
    "v_mfma_f32_16x16x32_bf16 v[192:195], a[224:227], a[88:91], v[192:195]\n\t"
    "ds_read_b64_tr_b16 a[192:193], v9 offset:32768\n\t"
    "ds_read_b64_tr_b16 a[194:195], v9 offset:36864\n\t"
    "v_mfma_f32_16x16x32_bf16 v[224:227], a[224:227], a[92:95], v[224:227]\n\t"
    "ds_read_b64_tr_b16 a[196:197], v10 offset:32768\n\t"
    "v_mfma_f32_16x16x32_bf16 v[164:167], a[228:231], a[84:87], v[164:167]\n\t"
    "s_add_u32 s78, s45, 0\n\t"
    "buffer_load_dwordx4 a[120:123], v20, s[40:43], s78 offen sc1\n\t"
    "v_mfma_f32_16x16x32_bf16 v[196:199], a[228:231], a[88:91], v[196:199]\n\t"
    "ds_read_b64_tr_b16 a[198:199], v10 offset:36864\n\t"
    "v_mfma_f32_16x16x32_bf16 v[228:231], a[228:231], a[92:95], v[228:231]\n\t"
    "ds_read_b64_tr_b16 a[200:201], v9 offset:33024\n\t"
    "v_mfma_f32_16x16x32_bf16 v[168:171], a[232:235], a[84:87], v[168:171]\n\t"
    "ds_read_b64_tr_b16 a[202:203], v9 offset:37120\n\t"
    "v_mfma_f32_16x16x32_bf16 v[200:203], a[232:235], a[88:91], v[200:203]\n\t"
    "s_add_u32 s78, s45, 0\n\t"
    "buffer_load_dwordx4 a[124:127], v20, s[40:43], s78 offen offset:1024 sc1\n\t"
    "v_mfma_f32_16x16x32_bf16 v[232:235], a[232:235], a[92:95], v[232:235]\n\t"
    "ds_read_b64_tr_b16 a[204:205], v10 offset:33024\n\t"
    "v_mfma_f32_16x16x32_bf16 v[172:175], a[236:239], a[84:87], v[172:175]\n\t"
    "ds_read_b64_tr_b16 a[206:207], v10 offset:37120\n\t"
    "v_mfma_f32_16x16x32_bf16 v[204:207], a[236:239], a[88:91], v[204:207]\n\t"
    "ds_read_b64_tr_b16 a[208:209], v9 offset:33280\n\t"
    "v_mfma_f32_16x16x32_bf16 v[236:239], a[236:239], a[92:95], v[236:239]\n\t"
    "s_add_u32 s78, s45, 0\n\t"
    "buffer_load_dwordx4 a[128:131], v20, s[40:43], s78 offen offset:2048 sc1\n\t"
    "v_mfma_f32_16x16x32_bf16 v[176:179], a[240:243], a[84:87], v[176:179]\n\t"
    "ds_read_b64_tr_b16 a[210:211], v9 offset:37376\n\t"
    "v_mfma_f32_16x16x32_bf16 v[208:211], a[240:243], a[88:91], v[208:211]\n\t"
    "ds_read_b64_tr_b16 a[212:213], v10 offset:33280\n\t"
    "v_mfma_f32_16x16x32_bf16 v[240:243], a[240:243], a[92:95], v[240:243]\n\t"
    "ds_read_b64_tr_b16 a[214:215], v10 offset:37376\n\t"
    "v_mfma_f32_16x16x32_bf16 v[180:183], a[244:247], a[84:87], v[180:183]\n\t"
    "s_add_u32 s78, s45, 0x3000\n\t"
    "buffer_load_dwordx4 a[132:135], v20, s[40:43], s78 offen sc1\n\t"
    "v_mfma_f32_16x16x32_bf16 v[212:215], a[244:247], a[88:91], v[212:215]\n\t"
    "ds_read_b64_tr_b16 a[216:217], v9 offset:33536\n\t"
    "v_mfma_f32_16x16x32_bf16 v[244:247], a[244:247], a[92:95], v[244:247]\n\t"
    "ds_read_b64_tr_b16 a[218:219], v9 offset:37632\n\t"
    "v_mfma_f32_16x16x32_bf16 v[184:187], a[248:251], a[84:87], v[184:187]\n\t"
    "ds_read_b64_tr_b16 a[220:221], v10 offset:33536\n\t"
    "v_mfma_f32_16x16x32_bf16 v[216:219], a[248:251], a[88:91], v[216:219]\n\t"
    "s_add_u32 s78, s45, 0x3000\n\t"
    "buffer_load_dwordx4 a[136:139], v20, s[40:43], s78 offen offset:1024 sc1\n\t"
    "v_mfma_f32_16x16x32_bf16 v[248:251], a[248:251], a[92:95], v[248:251]\n\t"
    "ds_read_b64_tr_b16 a[222:223], v10 offset:37632\n\t"
    "v_mfma_f32_16x16x32_bf16 v[188:191], a[252:255], a[84:87], v[188:191]\n\t"
    "s_waitcnt vmcnt(8)\n\t"
    "v_mfma_f32_16x16x32_bf16 v[220:223], a[252:255], a[88:91], v[220:223]\n\t"
    "v_mfma_f32_16x16x32_bf16 v[252:255], a[252:255], a[92:95], v[252:255]\n\t"
    "s_waitcnt lgkmcnt(1)\n\t"
    "s_add_u32 s78, s45, 0x3000\n\t"
    "buffer_load_dwordx4 a[140:143], v20, s[40:43], s78 offen offset:2048 sc1\n\t"
    "v_mfma_f32_16x16x32_bf16 v[160:163], a[192:195], a[96:99], v[160:163]\n\t"
    "s_and_b32 s78, s79, 4\n\t"
    "s_cmp_eq_u32 s78, 4\n\t"
    "s_cbranch_scc1 Lok_o3_%=\n\t"
    "s_mov_b32 s96, 0x400000\n\t"
    "Lpoll_o3_%=:\n\t"
    "buffer_load_dwordx2 v[26:27], v13, s[84:87], 0 offen sc1\n\t"
    "s_waitcnt vmcnt(0)\n\t"
    "v_cmp_eq_u32 vcc, s82, v26\n\t"
    "v_cmp_eq_u32 s[76:77], s83, v27\n\t"
    "s_nop 1\n\t"
    "s_and_b32 s79, vcc_lo, s76\n\t"
    "s_and_b32 s78, s79, 4\n\t"
    "s_cmp_eq_u32 s78, 4\n\t"
    "s_cbranch_scc1 Lok_o3_%=\n\t"
    "s_sleep 1\n\t"
    "s_sub_u32 s96, s96, 1\n\t"
    "s_cmp_lg_u32 s96, 0\n\t"
    "s_cbranch_scc1 Lpoll_o3_%=\n\t"
    "Lok_o3_%=:\n\t"
    "ds_read_b64_tr_b16 a[224:225], v9 offset:40960\n\t"
    "v_mfma_f32_16x16x32_bf16 v[192:195], a[192:195], a[100:103], v[192:195]\n\t"
    "ds_read_b64_tr_b16 a[226:227], v9 offset:45056\n\t"
    "v_mfma_f32_16x16x32_bf16 v[224:227], a[192:195], a[104:107], v[224:227]\n\t"
    "ds_read_b64_tr_b16 a[228:229], v10 offset:40960\n\t"
    "v_mfma_f32_16x16x32_bf16 v[164:167], a[196:199], a[96:99], v[164:167]\n\t"
    "s_add_u32 s78, s46, 0\n\t"
    "buffer_load_dwordx4 a[144:147], v20, s[40:43], s78 offen sc1\n\t"
    "v_mfma_f32_16x16x32_bf16 v[196:199], a[196:199], a[100:103], v[196:199]\n\t"
    "ds_read_b64_tr_b16 a[230:231], v10 offset:45056\n\t"
    "v_mfma_f32_16x16x32_bf16 v[228:231], a[196:199], a[104:107], v[228:231]\n\t"
    "ds_read_b64_tr_b16 a[232:233], v9 offset:41216\n\t"
    "v_mfma_f32_16x16x32_bf16 v[168:171], a[200:203], a[96:99], v[168:171]\n\t"
    "ds_read_b64_tr_b16 a[234:235], v9 offset:45312\n\t"
    "v_mfma_f32_16x16x32_bf16 v[200:203], a[200:203], a[100:103], v[200:203]\n\t"
    "s_add_u32 s78, s46, 0\n\t"
    "buffer_load_dwordx4 a[148:151], v20, s[40:43], s78 offen offset:1024 sc1\n\t"
    "v_mfma_f32_16x16x32_bf16 v[232:235], a[200:203], a[104:107], v[232:235]\n\t"
    "ds_read_b64_tr_b16 a[236:237], v10 offset:41216\n\t"
    "v_mfma_f32_16x16x32_bf16 v[172:175], a[204:207], a[96:99], v[172:175]\n\t"
    "ds_read_b64_tr_b16 a[238:239], v10 offset:45312\n\t"
    "v_mfma_f32_16x16x32_bf16 v[204:207], a[204:207], a[100:103], v[204:207]\n\t"
    "ds_read_b64_tr_b16 a[240:241], v9 offset:41472\n\t"
    "v_mfma_f32_16x16x32_bf16 v[236:239], a[204:207], a[104:107], v[236:239]\n\t"
    "s_add_u32 s78, s46, 0\n\t"
    "buffer_load_dwordx4 a[152:155], v20, s[40:43], s78 offen offset:2048 sc1\n\t"
    "v_mfma_f32_16x16x32_bf16 v[176:179], a[208:211], a[96:99], v[176:179]\n\t"
    "ds_read_b64_tr_b16 a[242:243], v9 offset:45568\n\t"
    "v_mfma_f32_16x16x32_bf16 v[208:211], a[208:211], a[100:103], v[208:211]\n\t"
    "ds_read_b64_tr_b16 a[244:245], v10 offset:41472\n\t"
    "v_mfma_f32_16x16x32_bf16 v[240:243], a[208:211], a[104:107], v[240:243]\n\t"
    "ds_read_b64_tr_b16 a[246:247], v10 offset:45568\n\t"
    "v_mfma_f32_16x16x32_bf16 v[180:183], a[212:215], a[96:99], v[180:183]\n\t"
    "s_add_u32 s78, s46, 0x3000\n\t"
    "buffer_load_dwordx4 a[156:159], v20, s[40:43], s78 offen sc1\n\t"
    "v_mfma_f32_16x16x32_bf16 v[212:215], a[212:215], a[100:103], v[212:215]\n\t"
    "ds_read_b64_tr_b16 a[248:249], v9 offset:41728\n\t"
    "v_mfma_f32_16x16x32_bf16 v[244:247], a[212:215], a[104:107], v[244:247]\n\t"
    "ds_read_b64_tr_b16 a[250:251], v9 offset:45824\n\t"
    "v_mfma_f32_16x16x32_bf16 v[184:187], a[216:219], a[96:99], v[184:187]\n\t"
    "ds_read_b64_tr_b16 a[252:253], v10 offset:41728\n\t"
    "v_mfma_f32_16x16x32_bf16 v[216:219], a[216:219], a[100:103], v[216:219]\n\t"
    "s_add_u32 s78, s46, 0x3000\n\t"
    "buffer_load_dwordx4 a[160:163], v20, s[40:43], s78 offen offset:1024 sc1\n\t"
    "v_mfma_f32_16x16x32_bf16 v[248:251], a[216:219], a[104:107], v[248:251]\n\t"
    "ds_read_b64_tr_b16 a[254:255], v10 offset:45824\n\t"
    "s_waitcnt lgkmcnt(4)\n\t"
    "v_mfma_f32_16x16x32_bf16 v[188:191], a[220:223], a[96:99], v[188:191]\n\t"
    "s_waitcnt vmcnt(11)\n\t"
    "v_mfma_f32_16x16x32_bf16 v[220:223], a[220:223], a[100:103], v[220:223]\n\t"
    "v_mfma_f32_16x16x32_bf16 v[252:255], a[220:223], a[104:107], v[252:255]\n\t"
    "s_add_u32 s78, s46, 0x3000\n\t"
    "buffer_load_dwordx4 a[164:167], v20, s[40:43], s78 offen offset:2048 sc1\n\t"
    "v_mfma_f32_16x16x32_bf16 v[160:163], a[224:227], a[108:111], v[160:163]\n\t"
    "ds_read_b64_tr_b16 a[192:193], v9 offset:49152\n\t"
    "v_mfma_f32_16x16x32_bf16 v[192:195], a[224:227], a[112:115], v[192:195]\n\t"
    "ds_read_b64_tr_b16 a[194:195], v9 offset:53248\n\t"
    "v_mfma_f32_16x16x32_bf16 v[224:227], a[224:227], a[116:119], v[224:227]\n\t"
    "ds_read_b64_tr_b16 a[196:197], v10 offset:49152\n\t"
    "v_mfma_f32_16x16x32_bf16 v[164:167], a[228:231], a[108:111], v[164:167]\n\t"
    "s_add_u32 s78, s44, 0x6000\n\t"
    "buffer_load_dwordx4 a[168:171], v20, s[40:43], s78 offen sc1\n\t"
    "v_mfma_f32_16x16x32_bf16 v[196:199], a[228:231], a[112:115], v[196:199]\n\t"
    "ds_read_b64_tr_b16 a[198:199], v10 offset:53248\n\t"
    "v_mfma_f32_16x16x32_bf16 v[228:231], a[228:231], a[116:119], v[228:231]\n\t"
    "ds_read_b64_tr_b16 a[200:201], v9 offset:49408\n\t"
    "v_mfma_f32_16x16x32_bf16 v[168:171], a[232:235], a[108:111], v[168:171]\n\t"
    "ds_read_b64_tr_b16 a[202:203], v9 offset:53504\n\t"
    "v_mfma_f32_16x16x32_bf16 v[200:203], a[232:235], a[112:115], v[200:203]\n\t"
    "s_add_u32 s78, s44, 0x6000\n\t"
    "buffer_load_dwordx4 a[172:175], v20, s[40:43], s78 offen offset:1024 sc1\n\t"
    "v_mfma_f32_16x16x32_bf16 v[232:235], a[232:235], a[116:119], v[232:235]\n\t"
    "ds_read_b64_tr_b16 a[204:205], v10 offset:49408\n\t"
    "v_mfma_f32_16x16x32_bf16 v[172:175], a[236:239], a[108:111], v[172:175]\n\t"
    "ds_read_b64_tr_b16 a[206:207], v10 offset:53504\n\t"
    "v_mfma_f32_16x16x32_bf16 v[204:207], a[236:239], a[112:115], v[204:207]\n\t"
    "ds_read_b64_tr_b16 a[208:209], v9 offset:49664\n\t"
    "v_mfma_f32_16x16x32_bf16 v[236:239], a[236:239], a[116:119], v[236:239]\n\t"
    "s_add_u32 s78, s44, 0x6000\n\t"
    "buffer_load_dwordx4 a[176:179], v20, s[40:43], s78 offen offset:2048 sc1\n\t"
    "v_mfma_f32_16x16x32_bf16 v[176:179], a[240:243], a[108:111], v[176:179]\n\t"
    "ds_read_b64_tr_b16 a[210:211], v9 offset:53760\n\t"
    "v_mfma_f32_16x16x32_bf16 v[208:211], a[240:243], a[112:115], v[208:211]\n\t"
    "ds_read_b64_tr_b16 a[212:213], v10 offset:49664\n\t"
    "v_mfma_f32_16x16x32_bf16 v[240:243], a[240:243], a[116:119], v[240:243]\n\t"
    "ds_read_b64_tr_b16 a[214:215], v10 offset:53760\n\t"
    "v_mfma_f32_16x16x32_bf16 v[180:183], a[244:247], a[108:111], v[180:183]\n\t"
    "s_add_u32 s78, s44, 0x9000\n\t"
    "buffer_load_dwordx4 a[180:183], v20, s[40:43], s78 offen sc1\n\t"
    "v_mfma_f32_16x16x32_bf16 v[212:215], a[244:247], a[112:115], v[212:215]\n\t"
    "ds_read_b64_tr_b16 a[216:217], v9 offset:49920\n\t"
    "v_mfma_f32_16x16x32_bf16 v[244:247], a[244:247], a[116:119], v[244:247]\n\t"
    "ds_read_b64_tr_b16 a[218:219], v9 offset:54016\n\t"
    "s_waitcnt lgkmcnt(4)\n\t"
    "v_mfma_f32_16x16x32_bf16 v[184:187], a[248:251], a[108:111], v[184:187]\n\t"
    "ds_read_b64_tr_b16 a[220:221], v10 offset:49920\n\t"
    "v_mfma_f32_16x16x32_bf16 v[216:219], a[248:251], a[112:115], v[216:219]\n\t"
    "s_add_u32 s78, s44, 0x9000\n\t"
    "buffer_load_dwordx4 a[184:187], v20, s[40:43], s78 offen offset:1024 sc1\n\t"
    "v_mfma_f32_16x16x32_bf16 v[248:251], a[248:251], a[116:119], v[248:251]\n\t"
    "ds_read_b64_tr_b16 a[222:223], v10 offset:54016\n\t"
    "v_mfma_f32_16x16x32_bf16 v[188:191], a[252:255], a[108:111], v[188:191]\n\t"
    "s_waitcnt vmcnt(14)\n\t"
    "v_mfma_f32_16x16x32_bf16 v[220:223], a[252:255], a[112:115], v[220:223]\n\t"
    "v_mfma_f32_16x16x32_bf16 v[252:255], a[252:255], a[116:119], v[252:255]\n\t"
    "s_add_u32 s78, s44, 0x9000\n\t"
    "buffer_load_dwordx4 a[188:191], v20, s[40:43], s78 offen offset:2048 sc1\n\t"
    "v_mfma_f32_16x16x32_bf16 v[160:163], a[192:195], a[120:123], v[160:163]\n\t"
    "ds_read_b64_tr_b16 a[224:225], v9 offset:57344\n\t"
    "v_mfma_f32_16x16x32_bf16 v[192:195], a[192:195], a[124:127], v[192:195]\n\t"
    "ds_read_b64_tr_b16 a[226:227], v9 offset:61440\n\t"
    "v_mfma_f32_16x16x32_bf16 v[224:227], a[192:195], a[128:131], v[224:227]\n\t"
    "ds_read_b64_tr_b16 a[228:229], v10 offset:57344\n\t"
    "v_mfma_f32_16x16x32_bf16 v[164:167], a[196:199], a[120:123], v[164:167]\n\t"
    "s_add_u32 s78, s45, 0x6000\n\t"
    "buffer_load_dwordx4 a[0:3], v20, s[40:43], s78 offen sc1\n\t"
    "v_mfma_f32_16x16x32_bf16 v[196:199], a[196:199], a[124:127], v[196:199]\n\t"
    "ds_read_b64_tr_b16 a[230:231], v10 offset:61440\n\t"
    "v_mfma_f32_16x16x32_bf16 v[228:231], a[196:199], a[128:131], v[228:231]\n\t"
    "ds_read_b64_tr_b16 a[232:233], v9 offset:57600\n\t"
    "v_mfma_f32_16x16x32_bf16 v[168:171], a[200:203], a[120:123], v[168:171]\n\t"
    "ds_read_b64_tr_b16 a[234:235], v9 offset:61696\n\t"
    "v_mfma_f32_16x16x32_bf16 v[200:203], a[200:203], a[124:127], v[200:203]\n\t"
    "s_add_u32 s78, s45, 0x6000\n\t"
    "buffer_load_dwordx4 a[4:7], v20, s[40:43], s78 offen offset:1024 sc1\n\t"
    "v_mfma_f32_16x16x32_bf16 v[232:235], a[200:203], a[128:131], v[232:235]\n\t"
    "ds_read_b64_tr_b16 a[236:237], v10 offset:57600\n\t"
    "v_mfma_f32_16x16x32_bf16 v[172:175], a[204:207], a[120:123], v[172:175]\n\t"
    "ds_read_b64_tr_b16 a[238:239], v10 offset:61696\n\t"
    "v_mfma_f32_16x16x32_bf16 v[204:207], a[204:207], a[124:127], v[204:207]\n\t"
    "ds_read_b64_tr_b16 a[240:241], v9 offset:57856\n\t"
    "v_mfma_f32_16x16x32_bf16 v[236:239], a[204:207], a[128:131], v[236:239]\n\t"
    "s_add_u32 s78, s45, 0x6000\n\t"
    "buffer_load_dwordx4 a[8:11], v20, s[40:43], s78 offen offset:2048 sc1\n\t"
    "v_mfma_f32_16x16x32_bf16 v[176:179], a[208:211], a[120:123], v[176:179]\n\t"
    "ds_read_b64_tr_b16 a[242:243], v9 offset:61952\n\t"
    "v_mfma_f32_16x16x32_bf16 v[208:211], a[208:211], a[124:127], v[208:211]\n\t"
    "ds_read_b64_tr_b16 a[244:245], v10 offset:57856\n\t"
    "v_mfma_f32_16x16x32_bf16 v[240:243], a[208:211], a[128:131], v[240:243]\n\t"
    "ds_read_b64_tr_b16 a[246:247], v10 offset:61952\n\t"
    "s_waitcnt lgkmcnt(4)\n\t"
    "v_mfma_f32_16x16x32_bf16 v[180:183], a[212:215], a[120:123], v[180:183]\n\t"
    "s_add_u32 s78, s45, 0x9000\n\t"
    "buffer_load_dwordx4 a[12:15], v20, s[40:43], s78 offen sc1\n\t"
    "v_mfma_f32_16x16x32_bf16 v[212:215], a[212:215], a[124:127], v[212:215]\n\t"
    "ds_read_b64_tr_b16 a[248:249], v9 offset:58112\n\t"
    "v_mfma_f32_16x16x32_bf16 v[244:247], a[212:215], a[128:131], v[244:247]\n\t"
    "ds_read_b64_tr_b16 a[250:251], v9 offset:62208\n\t"
    "v_mfma_f32_16x16x32_bf16 v[184:187], a[216:219], a[120:123], v[184:187]\n\t"
    "s_add_u32 m0, s59, 0x4000\n\t"
    "ds_read_b64_tr_b16 a[252:253], v10 offset:58112\n\t"
    "v_mfma_f32_16x16x32_bf16 v[216:219], a[216:219], a[124:127], v[216:219]\n\t"
    "s_add_u32 s78, s65, 0x20000\n\t"
    "buffer_load_dwordx4 v62, s[36:39], s78 offen lds\n\t"
    "v_mfma_f32_16x16x32_bf16 v[248:251], a[216:219], a[128:131], v[248:251]\n\t"
    "ds_read_b64_tr_b16 a[254:255], v10 offset:62208\n\t"
    "v_mfma_f32_16x16x32_bf16 v[188:191], a[220:223], a[120:123], v[188:191]\n\t"
    "s_waitcnt vmcnt(17)\n\t"
    "v_mfma_f32_16x16x32_bf16 v[220:223], a[220:223], a[124:127], v[220:223]\n\t"
    "s_add_u32 m0, s59, 0x4400\n\t"
    "v_mfma_f32_16x16x32_bf16 v[252:255], a[220:223], a[128:131], v[252:255]\n\t"
    "s_add_u32 s78, s65, 0x21000\n\t"
    "buffer_load_dwordx4 v63, s[36:39], s78 offen lds\n\t"
    "v_mfma_f32_16x16x32_bf16 v[160:163], a[224:227], a[132:135], v[160:163]\n\t"
    "s_waitcnt lgkmcnt(0)\n\t"
    "s_barrier\n\t"
    "v_mfma_f32_16x16x32_bf16 v[192:195], a[224:227], a[136:139], v[192:195]\n\t"
    "ds_read_b64_tr_b16 a[192:193], v7 offset:0\n\t"
    "ds_read_b64_tr_b16 a[194:195], v7 offset:4096\n\t"
    "v_mfma_f32_16x16x32_bf16 v[224:227], a[224:227], a[140:143], v[224:227]\n\t"
    "ds_read_b64_tr_b16 a[196:197], v8 offset:0\n\t"
    "v_mfma_f32_16x16x32_bf16 v[164:167], a[228:231], a[132:135], v[164:167]\n\t"
    "s_add_u32 s78, s45, 0x9000\n\t"
    "buffer_load_dwordx4 a[16:19], v20, s[40:43], s78 offen offset:1024 sc1\n\t"
    "v_mfma_f32_16x16x32_bf16 v[196:199], a[228:231], a[136:139], v[196:199]\n\t"
    "ds_read_b64_tr_b16 a[198:199], v8 offset:4096\n\t"
    "v_mfma_f32_16x16x32_bf16 v[228:231], a[228:231], a[140:143], v[228:231]\n\t"
    "ds_read_b64_tr_b16 a[200:201], v7 offset:256\n\t"
    "v_mfma_f32_16x16x32_bf16 v[168:171], a[232:235], a[132:135], v[168:171]\n\t"
    "ds_read_b64_tr_b16 a[202:203], v7 offset:4352\n\t"
    "v_mfma_f32_16x16x32_bf16 v[200:203], a[232:235], a[136:139], v[200:203]\n\t"
    "s_add_u32 s78, s45, 0x9000\n\t"
    "buffer_load_dwordx4 a[20:23], v20, s[40:43], s78 offen offset:2048 sc1\n\t"
    "v_mfma_f32_16x16x32_bf16 v[232:235], a[232:235], a[140:143], v[232:235]\n\t"
    "ds_read_b64_tr_b16 a[204:205], v8 offset:256\n\t"
    "v_mfma_f32_16x16x32_bf16 v[172:175], a[236:239], a[132:135], v[172:175]\n\t"
    "ds_read_b64_tr_b16 a[206:207], v8 offset:4352\n\t"
    "v_mfma_f32_16x16x32_bf16 v[204:207], a[236:239], a[136:139], v[204:207]\n\t"
    "ds_read_b64_tr_b16 a[208:209], v7 offset:512\n\t"
    "v_mfma_f32_16x16x32_bf16 v[236:239], a[236:239], a[140:143], v[236:239]\n\t"
    "s_add_u32 s78, s46, 0x6000\n\t"
    "buffer_load_dwordx4 a[24:27], v20, s[40:43], s78 offen sc1\n\t"
    "v_mfma_f32_16x16x32_bf16 v[176:179], a[240:243], a[132:135], v[176:179]\n\t"
    "ds_read_b64_tr_b16 a[210:211], v7 offset:4608\n\t"
    "v_mfma_f32_16x16x32_bf16 v[208:211], a[240:243], a[136:139], v[208:211]\n\t"
    "ds_read_b64_tr_b16 a[212:213], v8 offset:512\n\t"
    "v_mfma_f32_16x16x32_bf16 v[240:243], a[240:243], a[140:143], v[240:243]\n\t"
    "ds_read_b64_tr_b16 a[214:215], v8 offset:4608\n\t"
    "v_mfma_f32_16x16x32_bf16 v[180:183], a[244:247], a[132:135], v[180:183]\n\t"
    "s_add_u32 s78, s46, 0x6000\n\t"
    "buffer_load_dwordx4 a[28:31], v20, s[40:43], s78 offen offset:1024 sc1\n\t"
    "v_mfma_f32_16x16x32_bf16 v[212:215], a[244:247], a[136:139], v[212:215]\n\t"
    "ds_read_b64_tr_b16 a[216:217], v7 offset:768\n\t"
    "v_mfma_f32_16x16x32_bf16 v[244:247], a[244:247], a[140:143], v[244:247]\n\t"
    "ds_read_b64_tr_b16 a[218:219], v7 offset:4864\n\t"
    "v_mfma_f32_16x16x32_bf16 v[184:187], a[248:251], a[132:135], v[184:187]\n\t"
    "s_add_u32 m0, s59, 0x6000\n\t"
    "ds_read_b64_tr_b16 a[220:221], v8 offset:768\n\t"
    "v_mfma_f32_16x16x32_bf16 v[216:219], a[248:251], a[136:139], v[216:219]\n\t"
    "s_add_u32 s78, s65, 0x28000\n\t"
    "buffer_load_dwordx4 v62, s[36:39], s78 offen lds\n\t"
    "v_mfma_f32_16x16x32_bf16 v[248:251], a[248:251], a[140:143], v[248:251]\n\t"
    "ds_read_b64_tr_b16 a[222:223], v8 offset:4864\n\t"
    "v_mfma_f32_16x16x32_bf16 v[188:191], a[252:255], a[132:135], v[188:191]\n\t"
    "s_waitcnt vmcnt(20)\n\t"
    "v_mfma_f32_16x16x32_bf16 v[220:223], a[252:255], a[136:139], v[220:223]\n\t"
    "s_add_u32 m0, s59, 0x6400\n\t"
    "v_mfma_f32_16x16x32_bf16 v[252:255], a[252:255], a[140:143], v[252:255]\n\t"
    "s_waitcnt lgkmcnt(1)\n\t"
    "s_add_u32 s78, s65, 0x29000\n\t"
    "buffer_load_dwordx4 v63, s[36:39], s78 offen lds\n\t"
    "v_mfma_f32_16x16x32_bf16 v[160:163], a[192:195], a[144:147], v[160:163]\n\t"
    "ds_read_b64_tr_b16 a[224:225], v7 offset:8192\n\t"
    "v_mfma_f32_16x16x32_bf16 v[192:195], a[192:195], a[148:151], v[192:195]\n\t"
    "ds_read_b64_tr_b16 a[226:227], v7 offset:12288\n\t"
    "v_mfma_f32_16x16x32_bf16 v[224:227], a[192:195], a[152:155], v[224:227]\n\t"
    "ds_read_b64_tr_b16 a[228:229], v8 offset:8192\n\t"
    "v_mfma_f32_16x16x32_bf16 v[164:167], a[196:199], a[144:147], v[164:167]\n\t"
    "s_add_u32 s78, s46, 0x6000\n\t"
    "buffer_load_dwordx4 a[32:35], v20, s[40:43], s78 offen offset:2048 sc1\n\t"
    "v_mfma_f32_16x16x32_bf16 v[196:199], a[196:199], a[148:151], v[196:199]\n\t"
    "ds_read_b64_tr_b16 a[230:231], v8 offset:12288\n\t"
    "v_mfma_f32_16x16x32_bf16 v[228:231], a[196:199], a[152:155], v[228:231]\n\t"
    "ds_read_b64_tr_b16 a[232:233], v7 offset:8448\n\t"
    "v_mfma_f32_16x16x32_bf16 v[168:171], a[200:203], a[144:147], v[168:171]\n\t"
    "ds_read_b64_tr_b16 a[234:235], v7 offset:12544\n\t"
    "v_mfma_f32_16x16x32_bf16 v[200:203], a[200:203], a[148:151], v[200:203]\n\t"
    "s_add_u32 s78, s46, 0x9000\n\t"
    "buffer_load_dwordx4 a[36:39], v20, s[40:43], s78 offen sc1\n\t"
    "v_mfma_f32_16x16x32_bf16 v[232:235], a[200:203], a[152:155], v[232:235]\n\t"
    "ds_read_b64_tr_b16 a[236:237], v8 offset:8448\n\t"
    "v_mfma_f32_16x16x32_bf16 v[172:175], a[204:207], a[144:147], v[172:175]\n\t"
    "ds_read_b64_tr_b16 a[238:239], v8 offset:12544\n\t"
    "v_mfma_f32_16x16x32_bf16 v[204:207], a[204:207], a[148:151], v[204:207]\n\t"
    "ds_read_b64_tr_b16 a[240:241], v7 offset:8704\n\t"
    "v_mfma_f32_16x16x32_bf16 v[236:239], a[204:207], a[152:155], v[236:239]\n\t"
    "s_add_u32 s78, s46, 0x9000\n\t"
    "buffer_load_dwordx4 a[40:43], v20, s[40:43], s78 offen offset:1024 sc1\n\t"
    "v_mfma_f32_16x16x32_bf16 v[176:179], a[208:211], a[144:147], v[176:179]\n\t"
    "ds_read_b64_tr_b16 a[242:243], v7 offset:12800\n\t"
    "v_mfma_f32_16x16x32_bf16 v[208:211], a[208:211], a[148:151], v[208:211]\n\t"
    "ds_read_b64_tr_b16 a[244:245], v8 offset:8704\n\t"
    "v_mfma_f32_16x16x32_bf16 v[240:243], a[208:211], a[152:155], v[240:243]\n\t"
    "ds_read_b64_tr_b16 a[246:247], v8 offset:12800\n\t"
    "v_mfma_f32_16x16x32_bf16 v[180:183], a[212:215], a[144:147], v[180:183]\n\t"
    "s_add_u32 s78, s46, 0x9000\n\t"
    "buffer_load_dwordx4 a[44:47], v20, s[40:43], s78 offen offset:2048 sc1\n\t"
    "v_mfma_f32_16x16x32_bf16 v[212:215], a[212:215], a[148:151], v[212:215]\n\t"
    "ds_read_b64_tr_b16 a[248:249], v7 offset:8960\n\t"
    "v_mfma_f32_16x16x32_bf16 v[244:247], a[212:215], a[152:155], v[244:247]\n\t"
    "ds_read_b64_tr_b16 a[250:251], v7 offset:13056\n\t"
    "v_mfma_f32_16x16x32_bf16 v[184:187], a[216:219], a[144:147], v[184:187]\n\t"
    "s_add_u32 m0, s59, 0x8000\n\t"
    "ds_read_b64_tr_b16 a[252:253], v8 offset:8960\n\t"
    "v_mfma_f32_16x16x32_bf16 v[216:219], a[216:219], a[148:151], v[216:219]\n\t"
    "s_add_u32 s78, s67, 0x20000\n\t"
    "buffer_load_dwordx4 v62, s[36:39], s78 offen lds\n\t"
    "v_mfma_f32_16x16x32_bf16 v[248:251], a[216:219], a[152:155], v[248:251]\n\t"
    "ds_read_b64_tr_b16 a[254:255], v8 offset:13056\n\t"
    "s_waitcnt lgkmcnt(4)\n\t"
    "v_mfma_f32_16x16x32_bf16 v[188:191], a[220:223], a[144:147], v[188:191]\n\t"
    "s_waitcnt vmcnt(23)\n\t"
    "v_mfma_f32_16x16x32_bf16 v[220:223], a[220:223], a[148:151], v[220:223]\n\t"
    "s_add_u32 m0, s59, 0x8400\n\t"
    "v_mfma_f32_16x16x32_bf16 v[252:255], a[220:223], a[152:155], v[252:255]\n\t"
    "s_add_u32 s78, s67, 0x21000\n\t"
    "buffer_load_dwordx4 v63, s[36:39], s78 offen lds\n\t"
    "s_getpc_b64 s[4:5]\n\t"
    "Lort12_%=:\n\t"
    "s_mov_b32 s6, Lend_%=-Lort12_%=\n\t"
    "s_mov_b32 s7, 0x20000\n\t"
    "s_lshr_b32 s8, %1, 3\n\t"
    "s_lshl_b32 s8, s8, 2\n\t"
    "s_add_u32 s8, s8, s56\n\t"
    "s_lshl_b32 s8, s8, 8\n\t"
    "v_cmp_gt_u32 s[76:77], 2, v0\n\t"
    "v_lshlrev_b32 v44, 7, v0\n\t"
    "v_mov_b32 v45, 0x40000000\n\t"
    "v_cndmask_b32 v44, v45, v44, s[76:77]\n\t"
    "buffer_load_dword v23, v44, s[4:7], s8 offen\n\t"
    "v_mfma_f32_16x16x32_bf16 v[160:163], a[224:227], a[156:159], v[160:163]\n\t"
    "ds_read_b64_tr_b16 a[192:193], v7 offset:49152\n\t"
    "v_mfma_f32_16x16x32_bf16 v[192:195], a[224:227], a[160:163], v[192:195]\n\t"
    "ds_read_b64_tr_b16 a[194:195], v7 offset:53248\n\t"
    "v_mfma_f32_16x16x32_bf16 v[224:227], a[224:227], a[164:167], v[224:227]\n\t"
    "ds_read_b64_tr_b16 a[196:197], v8 offset:49152\n\t"
    "v_mfma_f32_16x16x32_bf16 v[164:167], a[228:231], a[156:159], v[164:167]\n\t"
    "s_add_u32 s78, s44, 0xc000\n\t"
    "buffer_load_dwordx4 a[48:51], v20, s[40:43], s78 offen sc1\n\t"
    "v_mfma_f32_16x16x32_bf16 v[196:199], a[228:231], a[160:163], v[196:199]\n\t"
    "ds_read_b64_tr_b16 a[198:199], v8 offset:53248\n\t"
    "v_mfma_f32_16x16x32_bf16 v[228:231], a[228:231], a[164:167], v[228:231]\n\t"
    "ds_read_b64_tr_b16 a[200:201], v7 offset:49408\n\t"
    "v_mfma_f32_16x16x32_bf16 v[168:171], a[232:235], a[156:159], v[168:171]\n\t"
    "ds_read_b64_tr_b16 a[202:203], v7 offset:53504\n\t"
    "v_mfma_f32_16x16x32_bf16 v[200:203], a[232:235], a[160:163], v[200:203]\n\t"
    "s_add_u32 s78, s44, 0xc000\n\t"
    "buffer_load_dwordx4 a[52:55], v20, s[40:43], s78 offen offset:1024 sc1\n\t"
    "v_mfma_f32_16x16x32_bf16 v[232:235], a[232:235], a[164:167], v[232:235]\n\t"
    "ds_read_b64_tr_b16 a[204:205], v8 offset:49408\n\t"
    "v_mfma_f32_16x16x32_bf16 v[172:175], a[236:239], a[156:159], v[172:175]\n\t"
    "ds_read_b64_tr_b16 a[206:207], v8 offset:53504\n\t"
    "v_mfma_f32_16x16x32_bf16 v[204:207], a[236:239], a[160:163], v[204:207]\n\t"
    "ds_read_b64_tr_b16 a[208:209], v7 offset:49664\n\t"
    "v_mfma_f32_16x16x32_bf16 v[236:239], a[236:239], a[164:167], v[236:239]\n\t"
    "s_add_u32 s78, s44, 0xc000\n\t"
    "buffer_load_dwordx4 a[56:59], v20, s[40:43], s78 offen offset:2048 sc1\n\t"
    "v_mfma_f32_16x16x32_bf16 v[176:179], a[240:243], a[156:159], v[176:179]\n\t"
    "ds_read_b64_tr_b16 a[210:211], v7 offset:53760\n\t"
    "v_mfma_f32_16x16x32_bf16 v[208:211], a[240:243], a[160:163], v[208:211]\n\t"
    "ds_read_b64_tr_b16 a[212:213], v8 offset:49664\n\t"
    "v_mfma_f32_16x16x32_bf16 v[240:243], a[240:243], a[164:167], v[240:243]\n\t"
    "ds_read_b64_tr_b16 a[214:215], v8 offset:53760\n\t"
    "v_mfma_f32_16x16x32_bf16 v[180:183], a[244:247], a[156:159], v[180:183]\n\t"
    "s_add_u32 s78, s44, 0xf000\n\t"
    "buffer_load_dwordx4 a[60:63], v20, s[40:43], s78 offen sc1\n\t"
    "v_mfma_f32_16x16x32_bf16 v[212:215], a[244:247], a[160:163], v[212:215]\n\t"
    "ds_read_b64_tr_b16 a[216:217], v7 offset:49920\n\t"
    "v_mfma_f32_16x16x32_bf16 v[244:247], a[244:247], a[164:167], v[244:247]\n\t"
    "ds_read_b64_tr_b16 a[218:219], v7 offset:54016\n\t"
    "s_waitcnt lgkmcnt(4)\n\t"
    "v_mfma_f32_16x16x32_bf16 v[184:187], a[248:251], a[156:159], v[184:187]\n\t"
    "s_add_u32 m0, s59, 0xa000\n\t"
    "ds_read_b64_tr_b16 a[220:221], v8 offset:49920\n\t"
    "v_mfma_f32_16x16x32_bf16 v[216:219], a[248:251], a[160:163], v[216:219]\n\t"
    "s_add_u32 s78, s67, 0x28000\n\t"
    "buffer_load_dwordx4 v62, s[36:39], s78 offen lds\n\t"
    "v_mfma_f32_16x16x32_bf16 v[248:251], a[248:251], a[164:167], v[248:251]\n\t"
    "ds_read_b64_tr_b16 a[222:223], v8 offset:54016\n\t"
    "v_mfma_f32_16x16x32_bf16 v[188:191], a[252:255], a[156:159], v[188:191]\n\t"
    "s_waitcnt vmcnt(27)\n\t"
    "v_mfma_f32_16x16x32_bf16 v[220:223], a[252:255], a[160:163], v[220:223]\n\t"
    "s_add_u32 m0, s59, 0xa400\n\t"
    "v_mfma_f32_16x16x32_bf16 v[252:255], a[252:255], a[164:167], v[252:255]\n\t"
    "s_add_u32 s78, s67, 0x29000\n\t"
    "buffer_load_dwordx4 v63, s[36:39], s78 offen lds\n\t"
    "v_mfma_f32_16x16x32_bf16 v[160:163], a[192:195], a[168:171], v[160:163]\n\t"
    "ds_read_b64_tr_b16 a[224:225], v7 offset:57344\n\t"
    "v_mfma_f32_16x16x32_bf16 v[192:195], a[192:195], a[172:175], v[192:195]\n\t"
    "ds_read_b64_tr_b16 a[226:227], v7 offset:61440\n\t"
    "v_mfma_f32_16x16x32_bf16 v[224:227], a[192:195], a[176:179], v[224:227]\n\t"
    "ds_read_b64_tr_b16 a[228:229], v8 offset:57344\n\t"
    "v_mfma_f32_16x16x32_bf16 v[164:167], a[196:199], a[168:171], v[164:167]\n\t"
    "s_add_u32 s78, s44, 0xf000\n\t"
    "buffer_load_dwordx4 a[64:67], v20, s[40:43], s78 offen offset:1024 sc1\n\t"
    "v_mfma_f32_16x16x32_bf16 v[196:199], a[196:199], a[172:175], v[196:199]\n\t"
    "ds_read_b64_tr_b16 a[230:231], v8 offset:61440\n\t"
    "v_mfma_f32_16x16x32_bf16 v[228:231], a[196:199], a[176:179], v[228:231]\n\t"
    "ds_read_b64_tr_b16 a[232:233], v7 offset:57600\n\t"
    "v_mfma_f32_16x16x32_bf16 v[168:171], a[200:203], a[168:171], v[168:171]\n\t"
    "ds_read_b64_tr_b16 a[234:235], v7 offset:61696\n\t"
    "v_mfma_f32_16x16x32_bf16 v[200:203], a[200:203], a[172:175], v[200:203]\n\t"
    "s_add_u32 s78, s44, 0xf000\n\t"
    "buffer_load_dwordx4 a[68:71], v20, s[40:43], s78 offen offset:2048 sc1\n\t"
    "v_mfma_f32_16x16x32_bf16 v[232:235], a[200:203], a[176:179], v[232:235]\n\t"
    "ds_read_b64_tr_b16 a[236:237], v8 offset:57600\n\t"
    "v_mfma_f32_16x16x32_bf16 v[172:175], a[204:207], a[168:171], v[172:175]\n\t"
    "ds_read_b64_tr_b16 a[238:239], v8 offset:61696\n\t"
    "v_mfma_f32_16x16x32_bf16 v[204:207], a[204:207], a[172:175], v[204:207]\n\t"
    "ds_read_b64_tr_b16 a[240:241], v7 offset:57856\n\t"
    "v_mfma_f32_16x16x32_bf16 v[236:239], a[204:207], a[176:179], v[236:239]\n\t"
    "s_add_u32 s78, s45, 0xc000\n\t"
    "buffer_load_dwordx4 a[72:75], v20, s[40:43], s78 offen sc1\n\t"
    "v_mfma_f32_16x16x32_bf16 v[176:179], a[208:211], a[168:171], v[176:179]\n\t"
    "ds_read_b64_tr_b16 a[242:243], v7 offset:61952\n\t"
    "v_mfma_f32_16x16x32_bf16 v[208:211], a[208:211], a[172:175], v[208:211]\n\t"
    "ds_read_b64_tr_b16 a[244:245], v8 offset:57856\n\t"
    "v_mfma_f32_16x16x32_bf16 v[240:243], a[208:211], a[176:179], v[240:243]\n\t"
    "ds_read_b64_tr_b16 a[246:247], v8 offset:61952\n\t"
    "s_waitcnt lgkmcnt(4)\n\t"
    "v_mfma_f32_16x16x32_bf16 v[180:183], a[212:215], a[168:171], v[180:183]\n\t"
    "s_add_u32 s78, s45, 0xc000\n\t"
    "buffer_load_dwordx4 a[76:79], v20, s[40:43], s78 offen offset:1024 sc1\n\t"
    "v_mfma_f32_16x16x32_bf16 v[212:215], a[212:215], a[172:175], v[212:215]\n\t"
    "ds_read_b64_tr_b16 a[248:249], v7 offset:58112\n\t"
    "v_mfma_f32_16x16x32_bf16 v[244:247], a[212:215], a[176:179], v[244:247]\n\t"
    "ds_read_b64_tr_b16 a[250:251], v7 offset:62208\n\t"
    "v_mfma_f32_16x16x32_bf16 v[184:187], a[216:219], a[168:171], v[184:187]\n\t"
    "s_add_u32 m0, s59, 0x14000\n\t"
    "ds_read_b64_tr_b16 a[252:253], v8 offset:58112\n\t"
    "v_mfma_f32_16x16x32_bf16 v[216:219], a[216:219], a[172:175], v[216:219]\n\t"
    "s_add_u32 s78, s68, 0x20000\n\t"
    "buffer_load_dwordx4 v62, s[36:39], s78 offen lds\n\t"
    "v_mfma_f32_16x16x32_bf16 v[248:251], a[216:219], a[176:179], v[248:251]\n\t"
    "ds_read_b64_tr_b16 a[254:255], v8 offset:62208\n\t"
    "v_mfma_f32_16x16x32_bf16 v[188:191], a[220:223], a[168:171], v[188:191]\n\t"
    "s_waitcnt vmcnt(30)\n\t"
    "v_mfma_f32_16x16x32_bf16 v[220:223], a[220:223], a[172:175], v[220:223]\n\t"
    "s_add_u32 m0, s59, 0x14400\n\t"
    "v_mfma_f32_16x16x32_bf16 v[252:255], a[220:223], a[176:179], v[252:255]\n\t"
    "s_add_u32 s78, s68, 0x21000\n\t"
    "buffer_load_dwordx4 v63, s[36:39], s78 offen lds\n\t"
    "v_mfma_f32_16x16x32_bf16 v[160:163], a[224:227], a[180:183], v[160:163]\n\t"
    "s_waitcnt lgkmcnt(0)\n\t"
    "s_barrier\n\t"
    "v_mfma_f32_16x16x32_bf16 v[192:195], a[224:227], a[184:187], v[192:195]\n\t"
    "ds_read_b64_tr_b16 a[192:193], v15 offset:0\n\t"
    "v_mfma_f32_16x16x32_bf16 v[224:227], a[224:227], a[188:191], v[224:227]\n\t"
    "ds_read_b64_tr_b16 a[194:195], v15 offset:4096\n\t"
    "v_mfma_f32_16x16x32_bf16 v[164:167], a[228:231], a[180:183], v[164:167]\n\t"
    "s_add_u32 s78, s45, 0xc000\n\t"
    "buffer_load_dwordx4 a[80:83], v20, s[40:43], s78 offen offset:2048 sc1\n\t"
    "v_mfma_f32_16x16x32_bf16 v[196:199], a[228:231], a[184:187], v[196:199]\n\t"
    "ds_read_b64_tr_b16 a[196:197], v16 offset:0\n\t"
    "v_mfma_f32_16x16x32_bf16 v[228:231], a[228:231], a[188:191], v[228:231]\n\t"
    "ds_read_b64_tr_b16 a[198:199], v16 offset:4096\n\t"
    "v_mfma_f32_16x16x32_bf16 v[168:171], a[232:235], a[180:183], v[168:171]\n\t"
    "ds_read_b64_tr_b16 a[200:201], v15 offset:256\n\t"
    "v_mfma_f32_16x16x32_bf16 v[200:203], a[232:235], a[184:187], v[200:203]\n\t"
    "s_add_u32 s78, s45, 0xf000\n\t"
    "buffer_load_dwordx4 a[84:87], v20, s[40:43], s78 offen sc1\n\t"
    "v_mfma_f32_16x16x32_bf16 v[232:235], a[232:235], a[188:191], v[232:235]\n\t"
    "ds_read_b64_tr_b16 a[202:203], v15 offset:4352\n\t"
    "v_mfma_f32_16x16x32_bf16 v[172:175], a[236:239], a[180:183], v[172:175]\n\t"
    "ds_read_b64_tr_b16 a[204:205], v16 offset:256\n\t"
    "v_mfma_f32_16x16x32_bf16 v[204:207], a[236:239], a[184:187], v[204:207]\n\t"
    "ds_read_b64_tr_b16 a[206:207], v16 offset:4352\n\t"
    "v_mfma_f32_16x16x32_bf16 v[236:239], a[236:239], a[188:191], v[236:239]\n\t"
    "s_add_u32 s78, s45, 0xf000\n\t"
    "buffer_load_dwordx4 a[88:91], v20, s[40:43], s78 offen offset:1024 sc1\n\t"
    "v_mfma_f32_16x16x32_bf16 v[176:179], a[240:243], a[180:183], v[176:179]\n\t"
    "ds_read_b64_tr_b16 a[208:209], v15 offset:512\n\t"
    "v_mfma_f32_16x16x32_bf16 v[208:211], a[240:243], a[184:187], v[208:211]\n\t"
    "ds_read_b64_tr_b16 a[210:211], v15 offset:4608\n\t"
    "v_mfma_f32_16x16x32_bf16 v[240:243], a[240:243], a[188:191], v[240:243]\n\t"
    "ds_read_b64_tr_b16 a[212:213], v16 offset:512\n\t"
    "v_mfma_f32_16x16x32_bf16 v[180:183], a[244:247], a[180:183], v[180:183]\n\t"
    "s_add_u32 s78, s45, 0xf000\n\t"
    "buffer_load_dwordx4 a[92:95], v20, s[40:43], s78 offen offset:2048 sc1\n\t"
    "v_mfma_f32_16x16x32_bf16 v[212:215], a[244:247], a[184:187], v[212:215]\n\t"
    "ds_read_b64_tr_b16 a[214:215], v16 offset:4608\n\t"
    "v_mfma_f32_16x16x32_bf16 v[244:247], a[244:247], a[188:191], v[244:247]\n\t"
    "v_mfma_f32_16x16x32_bf16 v[184:187], a[248:251], a[180:183], v[184:187]\n\t"
    "s_add_u32 m0, s59, 0x16000\n\t"
    "v_mfma_f32_16x16x32_bf16 v[216:219], a[248:251], a[184:187], v[216:219]\n\t"
    "s_add_u32 s78, s68, 0x28000\n\t"
    "buffer_load_dwordx4 v62, s[36:39], s78 offen lds\n\t"
    "v_mfma_f32_16x16x32_bf16 v[248:251], a[248:251], a[188:191], v[248:251]\n\t"
    "v_mfma_f32_16x16x32_bf16 v[188:191], a[252:255], a[180:183], v[188:191]\n\t"
    "s_waitcnt vmcnt(33)\n\t"
    "v_mfma_f32_16x16x32_bf16 v[220:223], a[252:255], a[184:187], v[220:223]\n\t"
    "s_add_u32 m0, s59, 0x16400\n\t"
    "v_mfma_f32_16x16x32_bf16 v[252:255], a[252:255], a[188:191], v[252:255]\n\t"
    "s_waitcnt lgkmcnt(0)\n\t"
    "s_add_u32 s78, s68, 0x29000\n\t"
    "buffer_load_dwordx4 v63, s[36:39], s78 offen lds\n\t"
    "v_mfma_f32_16x16x32_bf16 v[160:163], a[192:195], a[0:3], v[160:163]\n\t"
    "ds_read_b64_tr_b16 a[224:225], v15 offset:8192\n\t"
    "ds_read_b64_tr_b16 a[226:227], v15 offset:12288\n\t"
    "v_mfma_f32_16x16x32_bf16 v[192:195], a[192:195], a[4:7], v[192:195]\n\t"
    "ds_read_b64_tr_b16 a[228:229], v16 offset:8192\n\t"
    "ds_read_b64_tr_b16 a[230:231], v16 offset:12288\n\t"
    "v_mfma_f32_16x16x32_bf16 v[224:227], a[192:195], a[8:11], v[224:227]\n\t"
    "s_add_u32 s78, s46, 0xc000\n\t"
    "buffer_load_dwordx4 a[96:99], v20, s[40:43], s78 offen sc1\n\t"
    "v_mfma_f32_16x16x32_bf16 v[164:167], a[196:199], a[0:3], v[164:167]\n\t"
    "ds_read_b64_tr_b16 a[232:233], v15 offset:8448\n\t"
    "v_mfma_f32_16x16x32_bf16 v[196:199], a[196:199], a[4:7], v[196:199]\n\t"
    "ds_read_b64_tr_b16 a[234:235], v15 offset:12544\n\t"
    "v_mfma_f32_16x16x32_bf16 v[228:231], a[196:199], a[8:11], v[228:231]\n\t"
    "s_add_u32 s78, s46, 0xc000\n\t"
    "buffer_load_dwordx4 a[100:103], v20, s[40:43], s78 offen offset:1024 sc1\n\t"
    "v_mfma_f32_16x16x32_bf16 v[168:171], a[200:203], a[0:3], v[168:171]\n\t"
    "ds_read_b64_tr_b16 a[236:237], v16 offset:8448\n\t"
    "v_mfma_f32_16x16x32_bf16 v[200:203], a[200:203], a[4:7], v[200:203]\n\t"
    "ds_read_b64_tr_b16 a[238:239], v16 offset:12544\n\t"
    "v_mfma_f32_16x16x32_bf16 v[232:235], a[200:203], a[8:11], v[232:235]\n\t"
    "s_add_u32 s78, s46, 0xc000\n\t"
    "buffer_load_dwordx4 a[104:107], v20, s[40:43], s78 offen offset:2048 sc1\n\t"
    "v_mfma_f32_16x16x32_bf16 v[172:175], a[204:207], a[0:3], v[172:175]\n\t"
    "ds_read_b64_tr_b16 a[240:241], v15 offset:8704\n\t"
    "v_mfma_f32_16x16x32_bf16 v[204:207], a[204:207], a[4:7], v[204:207]\n\t"
    "ds_read_b64_tr_b16 a[242:243], v15 offset:12800\n\t"
    "v_mfma_f32_16x16x32_bf16 v[236:239], a[204:207], a[8:11], v[236:239]\n\t"
    "s_add_u32 s78, s46, 0xf000\n\t"
    "buffer_load_dwordx4 a[108:111], v20, s[40:43], s78 offen sc1\n\t"
    "v_mfma_f32_16x16x32_bf16 v[176:179], a[208:211], a[0:3], v[176:179]\n\t"
    "ds_read_b64_tr_b16 a[244:245], v16 offset:8704\n\t"
    "v_mfma_f32_16x16x32_bf16 v[208:211], a[208:211], a[4:7], v[208:211]\n\t"
    "s_add_u32 m0, s59, 0x18000\n\t"
    "ds_read_b64_tr_b16 a[246:247], v16 offset:12800\n\t"
    "v_mfma_f32_16x16x32_bf16 v[240:243], a[208:211], a[8:11], v[240:243]\n\t"
    "s_add_u32 s78, s65, 0x30000\n\t"
    "buffer_load_dwordx4 v62, s[36:39], s78 offen lds\n\t"
    "v_mfma_f32_16x16x32_bf16 v[180:183], a[212:215], a[0:3], v[180:183]\n\t"
    "s_waitcnt vmcnt(34)\n\t"
    "v_mfma_f32_16x16x32_bf16 v[212:215], a[212:215], a[4:7], v[212:215]\n\t"
    "s_add_u32 m0, s59, 0x18400\n\t"
    "v_mfma_f32_16x16x32_bf16 v[244:247], a[212:215], a[8:11], v[244:247]\n\t"
    "s_waitcnt lgkmcnt(1)\n\t"
    "s_add_u32 s78, s65, 0x31000\n\t"
    "buffer_load_dwordx4 v63, s[36:39], s78 offen lds\n\t"
    "v_mfma_f32_16x16x32_bf16 v[160:163], a[224:227], a[12:15], v[160:163]\n\t"
    "ds_read_b64_tr_b16 a[192:193], v15 offset:49152\n\t"
    "ds_read_b64_tr_b16 a[194:195], v15 offset:53248\n\t"
    "v_mfma_f32_16x16x32_bf16 v[192:195], a[224:227], a[16:19], v[192:195]\n\t"
    "ds_read_b64_tr_b16 a[196:197], v16 offset:49152\n\t"
    "ds_read_b64_tr_b16 a[198:199], v16 offset:53248\n\t"
    "v_mfma_f32_16x16x32_bf16 v[224:227], a[224:227], a[20:23], v[224:227]\n\t"
    "s_add_u32 s78, s46, 0xf000\n\t"
    "buffer_load_dwordx4 a[112:115], v20, s[40:43], s78 offen offset:1024 sc1\n\t"
    "v_mfma_f32_16x16x32_bf16 v[164:167], a[228:231], a[12:15], v[164:167]\n\t"
    "ds_read_b64_tr_b16 a[200:201], v15 offset:49408\n\t"
    "v_mfma_f32_16x16x32_bf16 v[196:199], a[228:231], a[16:19], v[196:199]\n\t"
    "ds_read_b64_tr_b16 a[202:203], v15 offset:53504\n\t"
    "v_mfma_f32_16x16x32_bf16 v[228:231], a[228:231], a[20:23], v[228:231]\n\t"
    "s_add_u32 s78, s46, 0xf000\n\t"
    "buffer_load_dwordx4 a[116:119], v20, s[40:43], s78 offen offset:2048 sc1\n\t"
    "v_mfma_f32_16x16x32_bf16 v[168:171], a[232:235], a[12:15], v[168:171]\n\t"
    "ds_read_b64_tr_b16 a[204:205], v16 offset:49408\n\t"
    "v_mfma_f32_16x16x32_bf16 v[200:203], a[232:235], a[16:19], v[200:203]\n\t"
    "ds_read_b64_tr_b16 a[206:207], v16 offset:53504\n\t"
    "v_mfma_f32_16x16x32_bf16 v[232:235], a[232:235], a[20:23], v[232:235]\n\t"
    "s_add_u32 s78, s44, 0x12000\n\t"
    "buffer_load_dwordx4 a[120:123], v20, s[40:43], s78 offen sc1\n\t"
    "v_mfma_f32_16x16x32_bf16 v[172:175], a[236:239], a[12:15], v[172:175]\n\t"
    "ds_read_b64_tr_b16 a[208:209], v15 offset:49664\n\t"
    "v_mfma_f32_16x16x32_bf16 v[204:207], a[236:239], a[16:19], v[204:207]\n\t"
    "ds_read_b64_tr_b16 a[210:211], v15 offset:53760\n\t"
    "v_mfma_f32_16x16x32_bf16 v[236:239], a[236:239], a[20:23], v[236:239]\n\t"
    "s_add_u32 s78, s44, 0x12000\n\t"
    "buffer_load_dwordx4 a[124:127], v20, s[40:43], s78 offen offset:1024 sc1\n\t"
    "v_mfma_f32_16x16x32_bf16 v[176:179], a[240:243], a[12:15], v[176:179]\n\t"
    "ds_read_b64_tr_b16 a[212:213], v16 offset:49664\n\t"
    "v_mfma_f32_16x16x32_bf16 v[208:211], a[240:243], a[16:19], v[208:211]\n\t"
    "s_add_u32 m0, s59, 0x1a000\n\t"
    "ds_read_b64_tr_b16 a[214:215], v16 offset:53760\n\t"
    "v_mfma_f32_16x16x32_bf16 v[240:243], a[240:243], a[20:23], v[240:243]\n\t"
    "s_waitcnt lgkmcnt(3)\n\t"
    "s_add_u32 s78, s65, 0x38000\n\t"
    "buffer_load_dwordx4 v62, s[36:39], s78 offen lds\n\t"
    "v_mfma_f32_16x16x32_bf16 v[180:183], a[244:247], a[12:15], v[180:183]\n\t"
    "s_waitcnt vmcnt(35)\n\t"
    "v_mfma_f32_16x16x32_bf16 v[212:215], a[244:247], a[16:19], v[212:215]\n\t"
    "s_add_u32 m0, s59, 0x1a400\n\t"
    "v_mfma_f32_16x16x32_bf16 v[244:247], a[244:247], a[20:23], v[244:247]\n\t"
    "s_add_u32 s78, s65, 0x39000\n\t"
    "buffer_load_dwordx4 v63, s[36:39], s78 offen lds\n\t"
    "v_mfma_f32_16x16x32_bf16 v[160:163], a[192:195], a[24:27], v[160:163]\n\t"
    "ds_read_b64_tr_b16 a[224:225], v15 offset:57344\n\t"
    "ds_read_b64_tr_b16 a[226:227], v15 offset:61440\n\t"
    "v_mfma_f32_16x16x32_bf16 v[192:195], a[192:195], a[28:31], v[192:195]\n\t"
    "ds_read_b64_tr_b16 a[228:229], v16 offset:57344\n\t"
    "ds_read_b64_tr_b16 a[230:231], v16 offset:61440\n\t"
    "v_mfma_f32_16x16x32_bf16 v[224:227], a[192:195], a[32:35], v[224:227]\n\t"
    "s_add_u32 s78, s44, 0x12000\n\t"
    "buffer_load_dwordx4 a[128:131], v20, s[40:43], s78 offen offset:2048 sc1\n\t"
    "v_mfma_f32_16x16x32_bf16 v[164:167], a[196:199], a[24:27], v[164:167]\n\t"
    "ds_read_b64_tr_b16 a[232:233], v15 offset:57600\n\t"
    "v_mfma_f32_16x16x32_bf16 v[196:199], a[196:199], a[28:31], v[196:199]\n\t"
    "ds_read_b64_tr_b16 a[234:235], v15 offset:61696\n\t"
    "v_mfma_f32_16x16x32_bf16 v[228:231], a[196:199], a[32:35], v[228:231]\n\t"
    "s_add_u32 s78, s44, 0x15000\n\t"
    "buffer_load_dwordx4 a[132:135], v20, s[40:43], s78 offen sc1\n\t"
    "v_mfma_f32_16x16x32_bf16 v[168:171], a[200:203], a[24:27], v[168:171]\n\t"
    "ds_read_b64_tr_b16 a[236:237], v16 offset:57600\n\t"
    "v_mfma_f32_16x16x32_bf16 v[200:203], a[200:203], a[28:31], v[200:203]\n\t"
    "ds_read_b64_tr_b16 a[238:239], v16 offset:61696\n\t"
    "v_mfma_f32_16x16x32_bf16 v[232:235], a[200:203], a[32:35], v[232:235]\n\t"
    "s_add_u32 s78, s44, 0x15000\n\t"
    "buffer_load_dwordx4 a[136:139], v20, s[40:43], s78 offen offset:1024 sc1\n\t"
    "v_mfma_f32_16x16x32_bf16 v[172:175], a[204:207], a[24:27], v[172:175]\n\t"
    "ds_read_b64_tr_b16 a[240:241], v15 offset:57856\n\t"
    "v_mfma_f32_16x16x32_bf16 v[204:207], a[204:207], a[28:31], v[204:207]\n\t"
    "ds_read_b64_tr_b16 a[242:243], v15 offset:61952\n\t"
    "v_mfma_f32_16x16x32_bf16 v[236:239], a[204:207], a[32:35], v[236:239]\n\t"
    "s_waitcnt lgkmcnt(3)\n\t"
    "s_add_u32 s78, s44, 0x15000\n\t"
    "buffer_load_dwordx4 a[140:143], v20, s[40:43], s78 offen offset:2048 sc1\n\t"
    "v_mfma_f32_16x16x32_bf16 v[176:179], a[208:211], a[24:27], v[176:179]\n\t"
    "ds_read_b64_tr_b16 a[244:245], v16 offset:57856\n\t"
    "v_mfma_f32_16x16x32_bf16 v[208:211], a[208:211], a[28:31], v[208:211]\n\t"
    "s_add_u32 m0, s59, 0x20000\n\t"
    "ds_read_b64_tr_b16 a[246:247], v16 offset:61952\n\t"
    "v_mfma_f32_16x16x32_bf16 v[240:243], a[208:211], a[32:35], v[240:243]\n\t"
    "s_add_u32 s78, s67, 0x30000\n\t"
    "buffer_load_dwordx4 v62, s[36:39], s78 offen lds\n\t"
    "v_mfma_f32_16x16x32_bf16 v[180:183], a[212:215], a[24:27], v[180:183]\n\t"
    "s_waitcnt vmcnt(38)\n\t"
    "v_mfma_f32_16x16x32_bf16 v[212:215], a[212:215], a[28:31], v[212:215]\n\t"
    "s_add_u32 m0, s59, 0x20400\n\t"
    "v_mfma_f32_16x16x32_bf16 v[244:247], a[212:215], a[32:35], v[244:247]\n\t"
    "s_add_u32 s78, s67, 0x31000\n\t"
    "buffer_load_dwordx4 v63, s[36:39], s78 offen lds\n\t"
    "v_mfma_f32_16x16x32_bf16 v[160:163], a[224:227], a[36:39], v[160:163]\n\t"
    "s_waitcnt vmcnt(30) lgkmcnt(0)\n\t"
    "s_barrier\n\t"
    "v_mfma_f32_16x16x32_bf16 v[192:195], a[224:227], a[40:43], v[192:195]\n\t"
    "ds_read_b64_tr_b16 a[192:193], v7 offset:16384\n\t"
    "ds_read_b64_tr_b16 a[194:195], v7 offset:20480\n\t"
    "v_mfma_f32_16x16x32_bf16 v[224:227], a[224:227], a[44:47], v[224:227]\n\t"
    "s_add_u32 s78, s45, 0x12000\n\t"
    "buffer_load_dwordx4 a[144:147], v20, s[40:43], s78 offen sc1\n\t"
    "v_mfma_f32_16x16x32_bf16 v[164:167], a[228:231], a[36:39], v[164:167]\n\t"
    "ds_read_b64_tr_b16 a[196:197], v8 offset:16384\n\t"
    "ds_read_b64_tr_b16 a[198:199], v8 offset:20480\n\t"
    "v_mfma_f32_16x16x32_bf16 v[196:199], a[228:231], a[40:43], v[196:199]\n\t"
    "ds_read_b64_tr_b16 a[200:201], v7 offset:16640\n\t"
    "ds_read_b64_tr_b16 a[202:203], v7 offset:20736\n\t"
    "v_mfma_f32_16x16x32_bf16 v[228:231], a[228:231], a[44:47], v[228:231]\n\t"
    "s_add_u32 s78, s45, 0x12000\n\t"
    "buffer_load_dwordx4 a[148:151], v20, s[40:43], s78 offen offset:1024 sc1\n\t"
    "v_mfma_f32_16x16x32_bf16 v[168:171], a[232:235], a[36:39], v[168:171]\n\t"
    "ds_read_b64_tr_b16 a[204:205], v8 offset:16640\n\t"
    "v_mfma_f32_16x16x32_bf16 v[200:203], a[232:235], a[40:43], v[200:203]\n\t"
    "ds_read_b64_tr_b16 a[206:207], v8 offset:20736\n\t"
    "v_mfma_f32_16x16x32_bf16 v[232:235], a[232:235], a[44:47], v[232:235]\n\t"
    "s_add_u32 s78, s45, 0x12000\n\t"
    "buffer_load_dwordx4 a[152:155], v20, s[40:43], s78 offen offset:2048 sc1\n\t"
    "v_mfma_f32_16x16x32_bf16 v[172:175], a[236:239], a[36:39], v[172:175]\n\t"
    "ds_read_b64_tr_b16 a[208:209], v7 offset:16896\n\t"
    "v_mfma_f32_16x16x32_bf16 v[204:207], a[236:239], a[40:43], v[204:207]\n\t"
    "ds_read_b64_tr_b16 a[210:211], v7 offset:20992\n\t"
    "v_mfma_f32_16x16x32_bf16 v[236:239], a[236:239], a[44:47], v[236:239]\n\t"
    "s_add_u32 s78, s45, 0x15000\n\t"
    "buffer_load_dwordx4 a[156:159], v20, s[40:43], s78 offen sc1\n\t"
    "v_mfma_f32_16x16x32_bf16 v[176:179], a[240:243], a[36:39], v[176:179]\n\t"
    "ds_read_b64_tr_b16 a[212:213], v8 offset:16896\n\t"
    "v_mfma_f32_16x16x32_bf16 v[208:211], a[240:243], a[40:43], v[208:211]\n\t"
    "s_add_u32 m0, s59, 0x22000\n\t"
    "ds_read_b64_tr_b16 a[214:215], v8 offset:20992\n\t"
    "v_mfma_f32_16x16x32_bf16 v[240:243], a[240:243], a[44:47], v[240:243]\n\t"
    "s_add_u32 s78, s67, 0x38000\n\t"
    "buffer_load_dwordx4 v62, s[36:39], s78 offen lds\n\t"
    "v_mfma_f32_16x16x32_bf16 v[180:183], a[244:247], a[36:39], v[180:183]\n\t"
    "v_mfma_f32_16x16x32_bf16 v[212:215], a[244:247], a[40:43], v[212:215]\n\t"
    "s_add_u32 m0, s59, 0x22400\n\t"
    "v_mfma_f32_16x16x32_bf16 v[244:247], a[244:247], a[44:47], v[244:247]\n\t"
    "s_waitcnt lgkmcnt(1)\n\t"
    "s_add_u32 s78, s67, 0x39000\n\t"
    "buffer_load_dwordx4 v63, s[36:39], s78 offen lds\n\t"
    "v_mfma_f32_16x16x32_bf16 v[160:163], a[192:195], a[48:51], v[160:163]\n\t"
    "ds_read_b64_tr_b16 a[224:225], v7 offset:24576\n\t"
    "ds_read_b64_tr_b16 a[226:227], v7 offset:28672\n\t"
    "v_mfma_f32_16x16x32_bf16 v[192:195], a[192:195], a[52:55], v[192:195]\n\t"
    "ds_read_b64_tr_b16 a[228:229], v8 offset:24576\n\t"
    "ds_read_b64_tr_b16 a[230:231], v8 offset:28672\n\t"
    "v_mfma_f32_16x16x32_bf16 v[224:227], a[192:195], a[56:59], v[224:227]\n\t"
    "s_add_u32 s78, s45, 0x15000\n\t"
    "buffer_load_dwordx4 a[160:163], v20, s[40:43], s78 offen offset:1024 sc1\n\t"
    "v_mfma_f32_16x16x32_bf16 v[164:167], a[196:199], a[48:51], v[164:167]\n\t"
    "ds_read_b64_tr_b16 a[232:233], v7 offset:24832\n\t"
    "v_mfma_f32_16x16x32_bf16 v[196:199], a[196:199], a[52:55], v[196:199]\n\t"
    "ds_read_b64_tr_b16 a[234:235], v7 offset:28928\n\t"
    "v_mfma_f32_16x16x32_bf16 v[228:231], a[196:199], a[56:59], v[228:231]\n\t"
    "s_add_u32 s78, s45, 0x15000\n\t"
    "buffer_load_dwordx4 a[164:167], v20, s[40:43], s78 offen offset:2048 sc1\n\t"
    "v_mfma_f32_16x16x32_bf16 v[168:171], a[200:203], a[48:51], v[168:171]\n\t"
    "ds_read_b64_tr_b16 a[236:237], v8 offset:24832\n\t"
    "v_mfma_f32_16x16x32_bf16 v[200:203], a[200:203], a[52:55], v[200:203]\n\t"
    "ds_read_b64_tr_b16 a[238:239], v8 offset:28928\n\t"
    "v_mfma_f32_16x16x32_bf16 v[232:235], a[200:203], a[56:59], v[232:235]\n\t"
    "s_add_u32 s78, s46, 0x12000\n\t"
    "buffer_load_dwordx4 a[168:171], v20, s[40:43], s78 offen sc1\n\t"
    "v_mfma_f32_16x16x32_bf16 v[172:175], a[204:207], a[48:51], v[172:175]\n\t"
    "ds_read_b64_tr_b16 a[240:241], v7 offset:25088\n\t"
    "v_mfma_f32_16x16x32_bf16 v[204:207], a[204:207], a[52:55], v[204:207]\n\t"
    "ds_read_b64_tr_b16 a[242:243], v7 offset:29184\n\t"
    "v_mfma_f32_16x16x32_bf16 v[236:239], a[204:207], a[56:59], v[236:239]\n\t"
    "s_add_u32 s78, s46, 0x12000\n\t"
    "buffer_load_dwordx4 a[172:175], v20, s[40:43], s78 offen offset:1024 sc1\n\t"
    "v_mfma_f32_16x16x32_bf16 v[176:179], a[208:211], a[48:51], v[176:179]\n\t"
    "ds_read_b64_tr_b16 a[244:245], v8 offset:25088\n\t"
    "v_mfma_f32_16x16x32_bf16 v[208:211], a[208:211], a[52:55], v[208:211]\n\t"
    "s_add_u32 m0, s59, 0x24000\n\t"
    "ds_read_b64_tr_b16 a[246:247], v8 offset:29184\n\t"
    "v_mfma_f32_16x16x32_bf16 v[240:243], a[208:211], a[56:59], v[240:243]\n\t"
    "s_waitcnt lgkmcnt(3)\n\t"
    "s_add_u32 s78, s68, 0x30000\n\t"
    "buffer_load_dwordx4 v62, s[36:39], s78 offen lds\n\t"
    "v_mfma_f32_16x16x32_bf16 v[180:183], a[212:215], a[48:51], v[180:183]\n\t"
    "s_waitcnt vmcnt(39)\n\t"
    "v_mfma_f32_16x16x32_bf16 v[212:215], a[212:215], a[52:55], v[212:215]\n\t"
    "s_add_u32 m0, s59, 0x24400\n\t"
    "v_mfma_f32_16x16x32_bf16 v[244:247], a[212:215], a[56:59], v[244:247]\n\t"
    "s_add_u32 s78, s68, 0x31000\n\t"
    "buffer_load_dwordx4 v63, s[36:39], s78 offen lds\n\t"
    "v_mfma_f32_16x16x32_bf16 v[160:163], a[224:227], a[60:63], v[160:163]\n\t"
    "ds_read_b64_tr_b16 a[192:193], v7 offset:32768\n\t"
    "ds_read_b64_tr_b16 a[194:195], v7 offset:36864\n\t"
    "v_mfma_f32_16x16x32_bf16 v[192:195], a[224:227], a[64:67], v[192:195]\n\t"
    "ds_read_b64_tr_b16 a[196:197], v8 offset:32768\n\t"
    "ds_read_b64_tr_b16 a[198:199], v8 offset:36864\n\t"
    "v_mfma_f32_16x16x32_bf16 v[224:227], a[224:227], a[68:71], v[224:227]\n\t"
    "s_add_u32 s78, s46, 0x12000\n\t"
    "buffer_load_dwordx4 a[176:179], v20, s[40:43], s78 offen offset:2048 sc1\n\t"
    "v_mfma_f32_16x16x32_bf16 v[164:167], a[228:231], a[60:63], v[164:167]\n\t"
    "ds_read_b64_tr_b16 a[200:201], v7 offset:33024\n\t"
    "v_mfma_f32_16x16x32_bf16 v[196:199], a[228:231], a[64:67], v[196:199]\n\t"
    "ds_read_b64_tr_b16 a[202:203], v7 offset:37120\n\t"
    "v_mfma_f32_16x16x32_bf16 v[228:231], a[228:231], a[68:71], v[228:231]\n\t"
    "s_add_u32 s78, s46, 0x15000\n\t"
    "buffer_load_dwordx4 a[180:183], v20, s[40:43], s78 offen sc1\n\t"
    "v_mfma_f32_16x16x32_bf16 v[168:171], a[232:235], a[60:63], v[168:171]\n\t"
    "ds_read_b64_tr_b16 a[204:205], v8 offset:33024\n\t"
    "v_mfma_f32_16x16x32_bf16 v[200:203], a[232:235], a[64:67], v[200:203]\n\t"
    "ds_read_b64_tr_b16 a[206:207], v8 offset:37120\n\t"
    "v_mfma_f32_16x16x32_bf16 v[232:235], a[232:235], a[68:71], v[232:235]\n\t"
    "s_add_u32 s78, s46, 0x15000\n\t"
    "buffer_load_dwordx4 a[184:187], v20, s[40:43], s78 offen offset:1024 sc1\n\t"
    "v_mfma_f32_16x16x32_bf16 v[172:175], a[236:239], a[60:63], v[172:175]\n\t"
    "ds_read_b64_tr_b16 a[208:209], v7 offset:33280\n\t"
    "v_mfma_f32_16x16x32_bf16 v[204:207], a[236:239], a[64:67], v[204:207]\n\t"
    "ds_read_b64_tr_b16 a[210:211], v7 offset:37376\n\t"
    "v_mfma_f32_16x16x32_bf16 v[236:239], a[236:239], a[68:71], v[236:239]\n\t"
    "s_waitcnt lgkmcnt(3)\n\t"
    "s_add_u32 s78, s46, 0x15000\n\t"
    "buffer_load_dwordx4 a[188:191], v20, s[40:43], s78 offen offset:2048 sc1\n\t"
    "v_mfma_f32_16x16x32_bf16 v[176:179], a[240:243], a[60:63], v[176:179]\n\t"
    "ds_read_b64_tr_b16 a[212:213], v8 offset:33280\n\t"
    "v_mfma_f32_16x16x32_bf16 v[208:211], a[240:243], a[64:67], v[208:211]\n\t"
    "s_add_u32 m0, s59, 0x26000\n\t"
    "ds_read_b64_tr_b16 a[214:215], v8 offset:37376\n\t"
    "v_mfma_f32_16x16x32_bf16 v[240:243], a[240:243], a[68:71], v[240:243]\n\t"
    "s_add_u32 s78, s68, 0x38000\n\t"
    "buffer_load_dwordx4 v62, s[36:39], s78 offen lds\n\t"
    "v_mfma_f32_16x16x32_bf16 v[180:183], a[244:247], a[60:63], v[180:183]\n\t"
    "s_waitcnt vmcnt(40)\n\t"
    "v_mfma_f32_16x16x32_bf16 v[212:215], a[244:247], a[64:67], v[212:215]\n\t"
    "s_add_u32 m0, s59, 0x26400\n\t"
    "v_mfma_f32_16x16x32_bf16 v[244:247], a[244:247], a[68:71], v[244:247]\n\t"
    "s_add_u32 s78, s68, 0x39000\n\t"
    "buffer_load_dwordx4 v63, s[36:39], s78 offen lds\n\t"
    "v_mfma_f32_16x16x32_bf16 v[160:163], a[192:195], a[72:75], v[160:163]\n\t"
    "ds_read_b64_tr_b16 a[224:225], v7 offset:40960\n\t"
    "v_mfma_f32_16x16x32_bf16 v[192:195], a[192:195], a[76:79], v[192:195]\n\t"
    "ds_read_b64_tr_b16 a[226:227], v7 offset:45056\n\t"
    "v_mfma_f32_16x16x32_bf16 v[224:227], a[192:195], a[80:83], v[224:227]\n\t"
    "ds_read_b64_tr_b16 a[228:229], v8 offset:40960\n\t"
    "v_mfma_f32_16x16x32_bf16 v[164:167], a[196:199], a[72:75], v[164:167]\n\t"
    "ds_read_b64_tr_b16 a[230:231], v8 offset:45056\n\t"
    "v_mfma_f32_16x16x32_bf16 v[196:199], a[196:199], a[76:79], v[196:199]\n\t"
    "ds_read_b64_tr_b16 a[232:233], v7 offset:41216\n\t"
    "v_mfma_f32_16x16x32_bf16 v[228:231], a[196:199], a[80:83], v[228:231]\n\t"
    "ds_read_b64_tr_b16 a[234:235], v7 offset:45312\n\t"
    "v_mfma_f32_16x16x32_bf16 v[168:171], a[200:203], a[72:75], v[168:171]\n\t"
    "ds_read_b64_tr_b16 a[236:237], v8 offset:41216\n\t"
    "v_mfma_f32_16x16x32_bf16 v[200:203], a[200:203], a[76:79], v[200:203]\n\t"
    "ds_read_b64_tr_b16 a[238:239], v8 offset:45312\n\t"
    "v_mfma_f32_16x16x32_bf16 v[232:235], a[200:203], a[80:83], v[232:235]\n\t"
    "ds_read_b64_tr_b16 a[240:241], v7 offset:41472\n\t"
    "s_waitcnt lgkmcnt(5)\n\t"
    "v_mfma_f32_16x16x32_bf16 v[172:175], a[204:207], a[72:75], v[172:175]\n\t"
    "ds_read_b64_tr_b16 a[242:243], v7 offset:45568\n\t"
    "v_mfma_f32_16x16x32_bf16 v[204:207], a[204:207], a[76:79], v[204:207]\n\t"
    "ds_read_b64_tr_b16 a[244:245], v8 offset:41472\n\t"
    "v_mfma_f32_16x16x32_bf16 v[236:239], a[204:207], a[80:83], v[236:239]\n\t"
    "ds_read_b64_tr_b16 a[246:247], v8 offset:45568\n\t"
    "v_mfma_f32_16x16x32_bf16 v[176:179], a[208:211], a[72:75], v[176:179]\n\t"
    "v_mfma_f32_16x16x32_bf16 v[208:211], a[208:211], a[76:79], v[208:211]\n\t"
    "v_mfma_f32_16x16x32_bf16 v[240:243], a[208:211], a[80:83], v[240:243]\n\t"
    "v_mfma_f32_16x16x32_bf16 v[180:183], a[212:215], a[72:75], v[180:183]\n\t"
    "s_waitcnt vmcnt(38)\n\t"
    "v_mfma_f32_16x16x32_bf16 v[212:215], a[212:215], a[76:79], v[212:215]\n\t"
    "v_mfma_f32_16x16x32_bf16 v[244:247], a[212:215], a[80:83], v[244:247]\n\t"
    "v_mfma_f32_16x16x32_bf16 v[160:163], a[224:227], a[84:87], v[160:163]\n\t"
    "s_waitcnt vmcnt(24) lgkmcnt(0)\n\t"
    "s_barrier\n\t"
    "v_mfma_f32_16x16x32_bf16 v[192:195], a[224:227], a[88:91], v[192:195]\n\t"
    "ds_read_b64_tr_b16 a[192:193], v15 offset:16384\n\t"
    "ds_read_b64_tr_b16 a[194:195], v15 offset:20480\n\t"
    "v_mfma_f32_16x16x32_bf16 v[224:227], a[224:227], a[92:95], v[224:227]\n\t"
    "ds_read_b64_tr_b16 a[196:197], v16 offset:16384\n\t"
    "ds_read_b64_tr_b16 a[198:199], v16 offset:20480\n\t"
    "v_mfma_f32_16x16x32_bf16 v[164:167], a[228:231], a[84:87], v[164:167]\n\t"
    "ds_read_b64_tr_b16 a[200:201], v15 offset:24576\n\t"
    "v_mfma_f32_16x16x32_bf16 v[196:199], a[228:231], a[88:91], v[196:199]\n\t"
    "ds_read_b64_tr_b16 a[202:203], v15 offset:28672\n\t"
    "v_mfma_f32_16x16x32_bf16 v[228:231], a[228:231], a[92:95], v[228:231]\n\t"
    "ds_read_b64_tr_b16 a[204:205], v16 offset:24576\n\t"
    "v_mfma_f32_16x16x32_bf16 v[168:171], a[232:235], a[84:87], v[168:171]\n\t"
    "ds_read_b64_tr_b16 a[206:207], v16 offset:28672\n\t"
    "v_mfma_f32_16x16x32_bf16 v[200:203], a[232:235], a[88:91], v[200:203]\n\t"
    "ds_read_b64_tr_b16 a[208:209], v15 offset:32768\n\t"
    "v_mfma_f32_16x16x32_bf16 v[232:235], a[232:235], a[92:95], v[232:235]\n\t"
    "ds_read_b64_tr_b16 a[210:211], v15 offset:36864\n\t"
    "v_mfma_f32_16x16x32_bf16 v[172:175], a[236:239], a[84:87], v[172:175]\n\t"
    "ds_read_b64_tr_b16 a[212:213], v16 offset:32768\n\t"
    "v_mfma_f32_16x16x32_bf16 v[204:207], a[236:239], a[88:91], v[204:207]\n\t"
    "ds_read_b64_tr_b16 a[214:215], v16 offset:36864\n\t"
    "v_mfma_f32_16x16x32_bf16 v[236:239], a[236:239], a[92:95], v[236:239]\n\t"
    "ds_read_b64_tr_b16 a[216:217], v15 offset:40960\n\t"
    "v_mfma_f32_16x16x32_bf16 v[176:179], a[240:243], a[84:87], v[176:179]\n\t"
    "ds_read_b64_tr_b16 a[218:219], v15 offset:45056\n\t"
    "v_mfma_f32_16x16x32_bf16 v[208:211], a[240:243], a[88:91], v[208:211]\n\t"
    "ds_read_b64_tr_b16 a[220:221], v16 offset:40960\n\t"
    "v_mfma_f32_16x16x32_bf16 v[240:243], a[240:243], a[92:95], v[240:243]\n\t"
    "ds_read_b64_tr_b16 a[222:223], v16 offset:45056\n\t"
    "v_mfma_f32_16x16x32_bf16 v[180:183], a[244:247], a[84:87], v[180:183]\n\t"
    "v_mfma_f32_16x16x32_bf16 v[212:215], a[244:247], a[88:91], v[212:215]\n\t"
    "v_mfma_f32_16x16x32_bf16 v[244:247], a[244:247], a[92:95], v[244:247]\n\t"
    "s_waitcnt lgkmcnt(2)\n\t"
    "v_mov_b32 v24, s82\n\t"
    "v_mov_b32 v25, s83\n\t"
    "s_mov_b32 s79, 0\n\t"
    "s_mul_i32 s7, s63, 0x9000\n\t"
    "v_mfma_f32_16x16x32_bf16 v[160:163], a[192:195], a[96:99], v[160:163]\n\t"
    "s_waitcnt vmcnt(0) lgkmcnt(0)\n\t"
    "s_barrier\n\t"
    "v_mfma_f32_16x16x32_bf16 v[192:195], a[192:195], a[100:103], v[192:195]\n\t"
    "ds_read_b64_tr_b16 a[224:225], v9 offset:32768\n\t"
    "v_mfma_f32_16x16x32_bf16 v[224:227], a[192:195], a[104:107], v[224:227]\n\t"
    "ds_read_b64_tr_b16 a[226:227], v9 offset:36864\n\t"
    "v_mfma_f32_16x16x32_bf16 v[164:167], a[196:199], a[96:99], v[164:167]\n\t"
    "ds_read_b64_tr_b16 a[232:233], v9 offset:40960\n\t"
    "v_mfma_f32_16x16x32_bf16 v[196:199], a[196:199], a[100:103], v[196:199]\n\t"
    "ds_read_b64_tr_b16 a[234:235], v9 offset:45056\n\t"
    "v_mfma_f32_16x16x32_bf16 v[228:231], a[196:199], a[104:107], v[228:231]\n\t"
    "ds_read_b64_tr_b16 a[240:241], v9 offset:49152\n\t"
    "v_mfma_f32_16x16x32_bf16 v[160:163], a[200:203], a[108:111], v[160:163]\n\t"
    "ds_read_b64_tr_b16 a[242:243], v9 offset:53248\n\t"
    "v_mfma_f32_16x16x32_bf16 v[192:195], a[200:203], a[112:115], v[192:195]\n\t"
    "ds_read_b64_tr_b16 a[248:249], v9 offset:57344\n\t"
    "v_mfma_f32_16x16x32_bf16 v[224:227], a[200:203], a[116:119], v[224:227]\n\t"
    "ds_read_b64_tr_b16 a[250:251], v9 offset:61440\n\t"
    "v_mfma_f32_16x16x32_bf16 v[164:167], a[204:207], a[108:111], v[164:167]\n\t"
    "ds_read_b64_tr_b16 a[228:229], v10 offset:32768\n\t"
    "v_mfma_f32_16x16x32_bf16 v[196:199], a[204:207], a[112:115], v[196:199]\n\t"
    "ds_read_b64_tr_b16 a[230:231], v10 offset:36864\n\t"
    "v_mfma_f32_16x16x32_bf16 v[228:231], a[204:207], a[116:119], v[228:231]\n\t"
    "ds_read_b64_tr_b16 a[236:237], v10 offset:40960\n\t"
    "v_mfma_f32_16x16x32_bf16 v[160:163], a[208:211], a[120:123], v[160:163]\n\t"
    "ds_read_b64_tr_b16 a[238:239], v10 offset:45056\n\t"
    "v_mfma_f32_16x16x32_bf16 v[192:195], a[208:211], a[124:127], v[192:195]\n\t"
    "ds_read_b64_tr_b16 a[244:245], v10 offset:49152\n\t"
    "v_mfma_f32_16x16x32_bf16 v[224:227], a[208:211], a[128:131], v[224:227]\n\t"
    "ds_read_b64_tr_b16 a[246:247], v10 offset:53248\n\t"
    "v_mfma_f32_16x16x32_bf16 v[164:167], a[212:215], a[120:123], v[164:167]\n\t"
    "ds_read_b64_tr_b16 a[252:253], v10 offset:57344\n\t"
    "v_mfma_f32_16x16x32_bf16 v[196:199], a[212:215], a[124:127], v[196:199]\n\t"
    "ds_read_b64_tr_b16 a[254:255], v10 offset:61440\n\t"
    "v_mfma_f32_16x16x32_bf16 v[228:231], a[212:215], a[128:131], v[228:231]\n\t"
    "v_mfma_f32_16x16x32_bf16 v[160:163], a[216:219], a[132:135], v[160:163]\n\t"
    "v_mfma_f32_16x16x32_bf16 v[192:195], a[216:219], a[136:139], v[192:195]\n\t"
    "v_mfma_f32_16x16x32_bf16 v[224:227], a[216:219], a[140:143], v[224:227]\n\t"
    "v_mfma_f32_16x16x32_bf16 v[164:167], a[220:223], a[132:135], v[164:167]\n\t"
    "v_mfma_f32_16x16x32_bf16 v[196:199], a[220:223], a[136:139], v[196:199]\n\t"
    "v_mfma_f32_16x16x32_bf16 v[228:231], a[220:223], a[140:143], v[228:231]\n\t"
    "s_waitcnt lgkmcnt(0)\n\t"
    "v_mfma_f32_16x16x32_bf16 v[160:163], a[224:227], a[144:147], v[160:163]\n\t"
    "ds_read_b64_tr_b16 a[192:193], v15 offset:16640\n\t"
    "v_mfma_f32_16x16x32_bf16 v[192:195], a[224:227], a[148:151], v[192:195]\n\t"
    "ds_read_b64_tr_b16 a[194:195], v15 offset:20736\n\t"
    "v_mfma_f32_16x16x32_bf16 v[224:227], a[224:227], a[152:155], v[224:227]\n\t"
    "ds_read_b64_tr_b16 a[200:201], v15 offset:24832\n\t"
    "v_mfma_f32_16x16x32_bf16 v[164:167], a[228:231], a[144:147], v[164:167]\n\t"
    "ds_read_b64_tr_b16 a[202:203], v15 offset:28928\n\t"
    "v_mfma_f32_16x16x32_bf16 v[196:199], a[228:231], a[148:151], v[196:199]\n\t"
    "ds_read_b64_tr_b16 a[208:209], v15 offset:33024\n\t"
    "v_mfma_f32_16x16x32_bf16 v[228:231], a[228:231], a[152:155], v[228:231]\n\t"
    "ds_read_b64_tr_b16 a[210:211], v15 offset:37120\n\t"
    "v_mfma_f32_16x16x32_bf16 v[160:163], a[232:235], a[156:159], v[160:163]\n\t"
    "ds_read_b64_tr_b16 a[216:217], v15 offset:41216\n\t"
    "v_mfma_f32_16x16x32_bf16 v[192:195], a[232:235], a[160:163], v[192:195]\n\t"
    "ds_read_b64_tr_b16 a[218:219], v15 offset:45312\n\t"
    "v_mfma_f32_16x16x32_bf16 v[224:227], a[232:235], a[164:167], v[224:227]\n\t"
    "ds_read_b64_tr_b16 a[196:197], v16 offset:16640\n\t"
    "v_mfma_f32_16x16x32_bf16 v[164:167], a[236:239], a[156:159], v[164:167]\n\t"
    "ds_read_b64_tr_b16 a[198:199], v16 offset:20736\n\t"
    "v_mfma_f32_16x16x32_bf16 v[196:199], a[236:239], a[160:163], v[196:199]\n\t"
    "ds_read_b64_tr_b16 a[204:205], v16 offset:24832\n\t"
    "v_mfma_f32_16x16x32_bf16 v[228:231], a[236:239], a[164:167], v[228:231]\n\t"
    "ds_read_b64_tr_b16 a[206:207], v16 offset:28928\n\t"
    "v_mfma_f32_16x16x32_bf16 v[160:163], a[240:243], a[168:171], v[160:163]\n\t"
    "ds_read_b64_tr_b16 a[212:213], v16 offset:33024\n\t"
    "v_mfma_f32_16x16x32_bf16 v[192:195], a[240:243], a[172:175], v[192:195]\n\t"
    "ds_read_b64_tr_b16 a[214:215], v16 offset:37120\n\t"
    "v_mfma_f32_16x16x32_bf16 v[224:227], a[240:243], a[176:179], v[224:227]\n\t"
    "ds_read_b64_tr_b16 a[220:221], v16 offset:41216\n\t"
    "v_mfma_f32_16x16x32_bf16 v[164:167], a[244:247], a[168:171], v[164:167]\n\t"
    "ds_read_b64_tr_b16 a[222:223], v16 offset:45312\n\t"
    "v_mfma_f32_16x16x32_bf16 v[196:199], a[244:247], a[172:175], v[196:199]\n\t"
    "v_mfma_f32_16x16x32_bf16 v[228:231], a[244:247], a[176:179], v[228:231]\n\t"
    "v_mfma_f32_16x16x32_bf16 v[160:163], a[248:251], a[180:183], v[160:163]\n\t"
    "v_mfma_f32_16x16x32_bf16 v[192:195], a[248:251], a[184:187], v[192:195]\n\t"
    "v_mfma_f32_16x16x32_bf16 v[224:227], a[248:251], a[188:191], v[224:227]\n\t"
    "v_mfma_f32_16x16x32_bf16 v[164:167], a[252:255], a[180:183], v[164:167]\n\t"
    "v_mfma_f32_16x16x32_bf16 v[196:199], a[252:255], a[184:187], v[196:199]\n\t"
    "v_mfma_f32_16x16x32_bf16 v[228:231], a[252:255], a[188:191], v[228:231]\n\t"
    "s_waitcnt lgkmcnt(0)\n\t"
    "s_add_u32 s96, s61, 1\n\t"
    "s_and_b32 s96, s96, 3\n\t"
    "s_lshl_b32 s96, s96, 2\n\t"
    "s_and_b32 s78, s63, 0xfffffff3\n\t"
    "s_or_b32 s96, s96, s78\n\t"
    "s_mul_i32 s4, s96, 0x300\n\t"
    "s_lshl_b32 s78, s56, 6\n\t"
    "s_add_u32 s4, s4, s78\n\t"
    "s_add_u32 s4, s4, 0x10000\n\t"
    "s_mul_i32 s96, s96, 0x9000\n\t"
    "s_add_u32 s96, s96, 0\n\t"
    "v_mfma_f32_16x16x32_bf16 v[168:171], a[192:195], a[96:99], v[168:171]\n\t"
    "ds_read_b64_tr_b16 a[224:225], v9 offset:33024\n\t"
    "v_mfma_f32_16x16x32_bf16 v[168:171], a[200:203], a[108:111], v[168:171]\n\t"
    "ds_read_b64_tr_b16 a[226:227], v9 offset:37120\n\t"
    "v_mfma_f32_16x16x32_bf16 v[168:171], a[208:211], a[120:123], v[168:171]\n\t"
    "ds_read_b64_tr_b16 a[232:233], v9 offset:41216\n\t"
    "v_mfma_f32_16x16x32_bf16 v[168:171], a[216:219], a[132:135], v[168:171]\n\t"
    "ds_read_b64_tr_b16 a[234:235], v9 offset:45312\n\t"
    "v_cvt_pk_bf16_f32 v64, v160, v161\n\t"
    "v_mfma_f32_16x16x32_bf16 v[200:203], a[192:195], a[100:103], v[200:203]\n\t"
    "ds_read_b64_tr_b16 a[240:241], v9 offset:49408\n\t"
    "v_cvt_pk_bf16_f32 v65, v162, v163\n\t"
    "v_mfma_f32_16x16x32_bf16 v[200:203], a[200:203], a[112:115], v[200:203]\n\t"
    "ds_read_b64_tr_b16 a[242:243], v9 offset:53504\n\t"
    "v_cvt_pk_bf16_f32 v66, v164, v165\n\t"
    "v_mfma_f32_16x16x32_bf16 v[200:203], a[208:211], a[124:127], v[200:203]\n\t"
    "ds_read_b64_tr_b16 a[248:249], v9 offset:57600\n\t"
    "v_cvt_pk_bf16_f32 v67, v166, v167\n\t"
    "v_mfma_f32_16x16x32_bf16 v[200:203], a[216:219], a[136:139], v[200:203]\n\t"
    "ds_read_b64_tr_b16 a[250:251], v9 offset:61696\n\t"
    "v_cvt_pk_bf16_f32 v68, v192, v193\n\t"
    "v_mfma_f32_16x16x32_bf16 v[232:235], a[192:195], a[104:107], v[232:235]\n\t"
    "ds_read_b64_tr_b16 a[228:229], v10 offset:33024\n\t"
    "v_cvt_pk_bf16_f32 v69, v194, v195\n\t"
    "v_mfma_f32_16x16x32_bf16 v[232:235], a[200:203], a[116:119], v[232:235]\n\t"
    "ds_read_b64_tr_b16 a[230:231], v10 offset:37120\n\t"
    "v_cvt_pk_bf16_f32 v70, v196, v197\n\t"
    "v_mfma_f32_16x16x32_bf16 v[232:235], a[208:211], a[128:131], v[232:235]\n\t"
    "ds_read_b64_tr_b16 a[236:237], v10 offset:41216\n\t"
    "v_cvt_pk_bf16_f32 v71, v198, v199\n\t"
    "v_mfma_f32_16x16x32_bf16 v[232:235], a[216:219], a[140:143], v[232:235]\n\t"
    "s_add_u32 s78, s96, 0\n\t"
    "buffer_store_dwordx4 v[64:67], v20, s[88:91], s78 offen sc1\n\t"
    "v_cvt_pk_bf16_f32 v72, v224, v225\n\t"
    "v_mfma_f32_16x16x32_bf16 v[172:175], a[196:199], a[96:99], v[172:175]\n\t"
    "ds_read_b64_tr_b16 a[238:239], v10 offset:45312\n\t"
    "v_cvt_pk_bf16_f32 v73, v226, v227\n\t"
    "v_mfma_f32_16x16x32_bf16 v[172:175], a[204:207], a[108:111], v[172:175]\n\t"
    "ds_read_b64_tr_b16 a[244:245], v10 offset:49408\n\t"
    "v_cvt_pk_bf16_f32 v74, v228, v229\n\t"
    "v_mfma_f32_16x16x32_bf16 v[172:175], a[212:215], a[120:123], v[172:175]\n\t"
    "ds_read_b64_tr_b16 a[246:247], v10 offset:53504\n\t"
    "v_cvt_pk_bf16_f32 v75, v230, v231\n\t"
    "v_mfma_f32_16x16x32_bf16 v[172:175], a[220:223], a[132:135], v[172:175]\n\t"
    "s_add_u32 s78, s96, 0x400\n\t"
    "buffer_store_dwordx4 v[68:71], v20, s[88:91], s78 offen sc1\n\t"
    "v_mfma_f32_16x16x32_bf16 v[204:207], a[196:199], a[100:103], v[204:207]\n\t"
    "ds_read_b64_tr_b16 a[252:253], v10 offset:57600\n\t"
    "v_mfma_f32_16x16x32_bf16 v[204:207], a[204:207], a[112:115], v[204:207]\n\t"
    "ds_read_b64_tr_b16 a[254:255], v10 offset:61696\n\t"
    "v_mfma_f32_16x16x32_bf16 v[204:207], a[212:215], a[124:127], v[204:207]\n\t"
    "v_mfma_f32_16x16x32_bf16 v[204:207], a[220:223], a[136:139], v[204:207]\n\t"
    "s_add_u32 s78, s96, 0x800\n\t"
    "buffer_store_dwordx4 v[72:75], v20, s[88:91], s78 offen sc1\n\t"
    "v_mfma_f32_16x16x32_bf16 v[236:239], a[196:199], a[104:107], v[236:239]\n\t"
    "v_mfma_f32_16x16x32_bf16 v[236:239], a[204:207], a[116:119], v[236:239]\n\t"
    "v_mfma_f32_16x16x32_bf16 v[236:239], a[212:215], a[128:131], v[236:239]\n\t"
    "v_mfma_f32_16x16x32_bf16 v[236:239], a[220:223], a[140:143], v[236:239]\n\t"
    "s_waitcnt lgkmcnt(0)\n\t"
    "v_mfma_f32_16x16x32_bf16 v[168:171], a[224:227], a[144:147], v[168:171]\n\t"
    "ds_read_b64_tr_b16 a[192:193], v15 offset:16896\n\t"
    "v_mfma_f32_16x16x32_bf16 v[168:171], a[232:235], a[156:159], v[168:171]\n\t"
    "ds_read_b64_tr_b16 a[194:195], v15 offset:20992\n\t"
    "v_mfma_f32_16x16x32_bf16 v[168:171], a[240:243], a[168:171], v[168:171]\n\t"
    "ds_read_b64_tr_b16 a[200:201], v15 offset:25088\n\t"
    "v_mfma_f32_16x16x32_bf16 v[168:171], a[248:251], a[180:183], v[168:171]\n\t"
    "ds_read_b64_tr_b16 a[202:203], v15 offset:29184\n\t"
    "v_mfma_f32_16x16x32_bf16 v[200:203], a[224:227], a[148:151], v[200:203]\n\t"
    "ds_read_b64_tr_b16 a[208:209], v15 offset:33280\n\t"
    "v_mfma_f32_16x16x32_bf16 v[200:203], a[232:235], a[160:163], v[200:203]\n\t"
    "ds_read_b64_tr_b16 a[210:211], v15 offset:37376\n\t"
    "v_mfma_f32_16x16x32_bf16 v[200:203], a[240:243], a[172:175], v[200:203]\n\t"
    "ds_read_b64_tr_b16 a[216:217], v15 offset:41472\n\t"
    "v_mfma_f32_16x16x32_bf16 v[200:203], a[248:251], a[184:187], v[200:203]\n\t"
    "ds_read_b64_tr_b16 a[218:219], v15 offset:45568\n\t"
    "v_mfma_f32_16x16x32_bf16 v[232:235], a[224:227], a[152:155], v[232:235]\n\t"
    "ds_read_b64_tr_b16 a[196:197], v16 offset:16896\n\t"
    "v_mfma_f32_16x16x32_bf16 v[232:235], a[232:235], a[164:167], v[232:235]\n\t"
    "ds_read_b64_tr_b16 a[198:199], v16 offset:20992\n\t"
    "v_mfma_f32_16x16x32_bf16 v[232:235], a[240:243], a[176:179], v[232:235]\n\t"
    "ds_read_b64_tr_b16 a[204:205], v16 offset:25088\n\t"
    "v_mfma_f32_16x16x32_bf16 v[232:235], a[248:251], a[188:191], v[232:235]\n\t"
    "ds_read_b64_tr_b16 a[206:207], v16 offset:29184\n\t"
    "v_mfma_f32_16x16x32_bf16 v[172:175], a[228:231], a[144:147], v[172:175]\n\t"
    "ds_read_b64_tr_b16 a[212:213], v16 offset:33280\n\t"
    "v_mfma_f32_16x16x32_bf16 v[172:175], a[236:239], a[156:159], v[172:175]\n\t"
    "ds_read_b64_tr_b16 a[214:215], v16 offset:37376\n\t"
    "v_mfma_f32_16x16x32_bf16 v[172:175], a[244:247], a[168:171], v[172:175]\n\t"
    "ds_read_b64_tr_b16 a[220:221], v16 offset:41472\n\t"
    "v_mfma_f32_16x16x32_bf16 v[172:175], a[252:255], a[180:183], v[172:175]\n\t"
    "ds_read_b64_tr_b16 a[222:223], v16 offset:45568\n\t"
    "v_mfma_f32_16x16x32_bf16 v[204:207], a[228:231], a[148:151], v[204:207]\n\t"
    "v_mfma_f32_16x16x32_bf16 v[204:207], a[236:239], a[160:163], v[204:207]\n\t"
    "v_mfma_f32_16x16x32_bf16 v[204:207], a[244:247], a[172:175], v[204:207]\n\t"
    "v_mfma_f32_16x16x32_bf16 v[204:207], a[252:255], a[184:187], v[204:207]\n\t"
    "v_mfma_f32_16x16x32_bf16 v[236:239], a[228:231], a[152:155], v[236:239]\n\t"
    "v_mfma_f32_16x16x32_bf16 v[236:239], a[236:239], a[164:167], v[236:239]\n\t"
    "v_mfma_f32_16x16x32_bf16 v[236:239], a[244:247], a[176:179], v[236:239]\n\t"
    "v_mfma_f32_16x16x32_bf16 v[236:239], a[252:255], a[188:191], v[236:239]\n\t"
    "s_waitcnt lgkmcnt(0)\n\t"
    "s_add_u32 s96, s61, 2\n\t"
    "s_and_b32 s96, s96, 3\n\t"
    "s_lshl_b32 s96, s96, 2\n\t"
    "s_and_b32 s78, s63, 0xfffffff3\n\t"
    "s_or_b32 s96, s96, s78\n\t"
    "s_mul_i32 s5, s96, 0x300\n\t"
    "s_lshl_b32 s78, s56, 6\n\t"
    "s_add_u32 s5, s5, s78\n\t"
    "s_add_u32 s5, s5, 0x10100\n\t"
    "s_mul_i32 s96, s96, 0x9000\n\t"
    "s_add_u32 s96, s96, 0x3000\n\t"
    "v_mfma_f32_16x16x32_bf16 v[176:179], a[192:195], a[96:99], v[176:179]\n\t"
    "ds_read_b64_tr_b16 a[224:225], v9 offset:33280\n\t"
    "v_mfma_f32_16x16x32_bf16 v[176:179], a[200:203], a[108:111], v[176:179]\n\t"
    "s_waitcnt vmcnt(0)\n\t"
    "s_mov_b64 exec, 1\n\t"
    "buffer_store_dwordx2 v[24:25], v60, s[84:87], s4 offen sc1\n\t"
    "s_mov_b64 exec, -1\n\t"
    "ds_read_b64_tr_b16 a[226:227], v9 offset:37376\n\t"
    "v_mfma_f32_16x16x32_bf16 v[176:179], a[208:211], a[120:123], v[176:179]\n\t"
    "ds_read_b64_tr_b16 a[232:233], v9 offset:41472\n\t"
    "v_mfma_f32_16x16x32_bf16 v[176:179], a[216:219], a[132:135], v[176:179]\n\t"
    "ds_read_b64_tr_b16 a[234:235], v9 offset:45568\n\t"
    "v_cvt_pk_bf16_f32 v76, v168, v169\n\t"
    "v_mfma_f32_16x16x32_bf16 v[208:211], a[192:195], a[100:103], v[208:211]\n\t"
    "ds_read_b64_tr_b16 a[240:241], v9 offset:49664\n\t"
    "v_cvt_pk_bf16_f32 v77, v170, v171\n\t"
    "v_mfma_f32_16x16x32_bf16 v[208:211], a[200:203], a[112:115], v[208:211]\n\t"
    "ds_read_b64_tr_b16 a[242:243], v9 offset:53760\n\t"
    "v_cvt_pk_bf16_f32 v78, v172, v173\n\t"
    "v_mfma_f32_16x16x32_bf16 v[208:211], a[208:211], a[124:127], v[208:211]\n\t"
    "ds_read_b64_tr_b16 a[248:249], v9 offset:57856\n\t"
    "v_cvt_pk_bf16_f32 v79, v174, v175\n\t"
    "v_mfma_f32_16x16x32_bf16 v[208:211], a[216:219], a[136:139], v[208:211]\n\t"
    "ds_read_b64_tr_b16 a[250:251], v9 offset:61952\n\t"
    "v_cvt_pk_bf16_f32 v80, v200, v201\n\t"
    "v_mfma_f32_16x16x32_bf16 v[240:243], a[192:195], a[104:107], v[240:243]\n\t"
    "ds_read_b64_tr_b16 a[228:229], v10 offset:33280\n\t"
    "v_cvt_pk_bf16_f32 v81, v202, v203\n\t"
    "v_mfma_f32_16x16x32_bf16 v[240:243], a[200:203], a[116:119], v[240:243]\n\t"
    "ds_read_b64_tr_b16 a[230:231], v10 offset:37376\n\t"
    "v_cvt_pk_bf16_f32 v82, v204, v205\n\t"
    "v_mfma_f32_16x16x32_bf16 v[240:243], a[208:211], a[128:131], v[240:243]\n\t"
    "ds_read_b64_tr_b16 a[236:237], v10 offset:41472\n\t"
    "v_cvt_pk_bf16_f32 v83, v206, v207\n\t"
    "v_mfma_f32_16x16x32_bf16 v[240:243], a[216:219], a[140:143], v[240:243]\n\t"
    "s_add_u32 s78, s96, 0\n\t"
    "buffer_store_dwordx4 v[76:79], v20, s[88:91], s78 offen sc1\n\t"
    "v_cvt_pk_bf16_f32 v84, v232, v233\n\t"
    "v_mfma_f32_16x16x32_bf16 v[180:183], a[196:199], a[96:99], v[180:183]\n\t"
    "ds_read_b64_tr_b16 a[238:239], v10 offset:45568\n\t"
    "v_cvt_pk_bf16_f32 v85, v234, v235\n\t"
    "v_mfma_f32_16x16x32_bf16 v[180:183], a[204:207], a[108:111], v[180:183]\n\t"
    "ds_read_b64_tr_b16 a[244:245], v10 offset:49664\n\t"
    "v_cvt_pk_bf16_f32 v86, v236, v237\n\t"
    "v_mfma_f32_16x16x32_bf16 v[180:183], a[212:215], a[120:123], v[180:183]\n\t"
    "ds_read_b64_tr_b16 a[246:247], v10 offset:53760\n\t"
    "v_cvt_pk_bf16_f32 v87, v238, v239\n\t"
    "v_mfma_f32_16x16x32_bf16 v[180:183], a[220:223], a[132:135], v[180:183]\n\t"
    "s_add_u32 s78, s96, 0x400\n\t"
    "buffer_store_dwordx4 v[80:83], v20, s[88:91], s78 offen sc1\n\t"
    "v_mfma_f32_16x16x32_bf16 v[212:215], a[196:199], a[100:103], v[212:215]\n\t"
    "ds_read_b64_tr_b16 a[252:253], v10 offset:57856\n\t"
    "v_mfma_f32_16x16x32_bf16 v[212:215], a[204:207], a[112:115], v[212:215]\n\t"
    "ds_read_b64_tr_b16 a[254:255], v10 offset:61952\n\t"
    "v_mfma_f32_16x16x32_bf16 v[212:215], a[212:215], a[124:127], v[212:215]\n\t"
    "v_mfma_f32_16x16x32_bf16 v[212:215], a[220:223], a[136:139], v[212:215]\n\t"
    "s_add_u32 s78, s96, 0x800\n\t"
    "buffer_store_dwordx4 v[84:87], v20, s[88:91], s78 offen sc1\n\t"
    "v_mfma_f32_16x16x32_bf16 v[244:247], a[196:199], a[104:107], v[244:247]\n\t"
    "v_mfma_f32_16x16x32_bf16 v[244:247], a[204:207], a[116:119], v[244:247]\n\t"
    "v_mfma_f32_16x16x32_bf16 v[244:247], a[212:215], a[128:131], v[244:247]\n\t"
    "v_mfma_f32_16x16x32_bf16 v[244:247], a[220:223], a[140:143], v[244:247]\n\t"
    "s_waitcnt lgkmcnt(0)\n\t"
    "v_mfma_f32_16x16x32_bf16 v[176:179], a[224:227], a[144:147], v[176:179]\n\t"
    "ds_read_b64_tr_b16 a[192:193], v15 offset:768\n\t"
    "v_mfma_f32_16x16x32_bf16 v[176:179], a[232:235], a[156:159], v[176:179]\n\t"
    "ds_read_b64_tr_b16 a[194:195], v15 offset:4864\n\t"
    "v_mfma_f32_16x16x32_bf16 v[176:179], a[240:243], a[168:171], v[176:179]\n\t"
    "ds_read_b64_tr_b16 a[200:201], v15 offset:8960\n\t"
    "v_mfma_f32_16x16x32_bf16 v[176:179], a[248:251], a[180:183], v[176:179]\n\t"
    "ds_read_b64_tr_b16 a[202:203], v15 offset:13056\n\t"
    "v_mfma_f32_16x16x32_bf16 v[208:211], a[224:227], a[148:151], v[208:211]\n\t"
    "ds_read_b64_tr_b16 a[208:209], v15 offset:49920\n\t"
    "v_mfma_f32_16x16x32_bf16 v[208:211], a[232:235], a[160:163], v[208:211]\n\t"
    "ds_read_b64_tr_b16 a[210:211], v15 offset:54016\n\t"
    "v_mfma_f32_16x16x32_bf16 v[208:211], a[240:243], a[172:175], v[208:211]\n\t"
    "ds_read_b64_tr_b16 a[216:217], v15 offset:58112\n\t"
    "v_mfma_f32_16x16x32_bf16 v[208:211], a[248:251], a[184:187], v[208:211]\n\t"
    "ds_read_b64_tr_b16 a[218:219], v15 offset:62208\n\t"
    "v_mfma_f32_16x16x32_bf16 v[240:243], a[224:227], a[152:155], v[240:243]\n\t"
    "ds_read_b64_tr_b16 a[196:197], v16 offset:768\n\t"
    "v_mfma_f32_16x16x32_bf16 v[240:243], a[232:235], a[164:167], v[240:243]\n\t"
    "ds_read_b64_tr_b16 a[198:199], v16 offset:4864\n\t"
    "v_mfma_f32_16x16x32_bf16 v[240:243], a[240:243], a[176:179], v[240:243]\n\t"
    "ds_read_b64_tr_b16 a[204:205], v16 offset:8960\n\t"
    "v_mfma_f32_16x16x32_bf16 v[240:243], a[248:251], a[188:191], v[240:243]\n\t"
    "ds_read_b64_tr_b16 a[206:207], v16 offset:13056\n\t"
    "v_mfma_f32_16x16x32_bf16 v[180:183], a[228:231], a[144:147], v[180:183]\n\t"
    "ds_read_b64_tr_b16 a[212:213], v16 offset:49920\n\t"
    "v_mfma_f32_16x16x32_bf16 v[180:183], a[236:239], a[156:159], v[180:183]\n\t"
    "ds_read_b64_tr_b16 a[214:215], v16 offset:54016\n\t"
    "v_mfma_f32_16x16x32_bf16 v[180:183], a[244:247], a[168:171], v[180:183]\n\t"
    "ds_read_b64_tr_b16 a[220:221], v16 offset:58112\n\t"
    "v_mfma_f32_16x16x32_bf16 v[180:183], a[252:255], a[180:183], v[180:183]\n\t"
    "ds_read_b64_tr_b16 a[222:223], v16 offset:62208\n\t"
    "v_mfma_f32_16x16x32_bf16 v[212:215], a[228:231], a[148:151], v[212:215]\n\t"
    "v_mfma_f32_16x16x32_bf16 v[212:215], a[236:239], a[160:163], v[212:215]\n\t"
    "v_mfma_f32_16x16x32_bf16 v[212:215], a[244:247], a[172:175], v[212:215]\n\t"
    "v_mfma_f32_16x16x32_bf16 v[212:215], a[252:255], a[184:187], v[212:215]\n\t"
    "v_mfma_f32_16x16x32_bf16 v[244:247], a[228:231], a[152:155], v[244:247]\n\t"
    "v_mfma_f32_16x16x32_bf16 v[244:247], a[236:239], a[164:167], v[244:247]\n\t"
    "v_mfma_f32_16x16x32_bf16 v[244:247], a[244:247], a[176:179], v[244:247]\n\t"
    "v_mfma_f32_16x16x32_bf16 v[244:247], a[252:255], a[188:191], v[244:247]\n\t"
    "s_waitcnt lgkmcnt(0)\n\t"
    "s_add_u32 s96, s61, 3\n\t"
    "s_and_b32 s96, s96, 3\n\t"
    "s_lshl_b32 s96, s96, 2\n\t"
    "s_and_b32 s78, s63, 0xfffffff3\n\t"
    "s_or_b32 s96, s96, s78\n\t"
    "s_mul_i32 s6, s96, 0x300\n\t"
    "s_lshl_b32 s78, s56, 6\n\t"
    "s_add_u32 s6, s6, s78\n\t"
    "s_add_u32 s6, s6, 0x10200\n\t"
    "s_mul_i32 s96, s96, 0x9000\n\t"
    "s_add_u32 s96, s96, 0x6000\n\t"
    "v_mfma_f32_16x16x32_bf16 v[184:187], a[192:195], a[0:3], v[184:187]\n\t"
    "ds_read_b64_tr_b16 a[224:225], v7 offset:17152\n\t"
    "v_mfma_f32_16x16x32_bf16 v[184:187], a[200:203], a[12:15], v[184:187]\n\t"
    "s_waitcnt vmcnt(0)\n\t"
    "s_mov_b64 exec, 1\n\t"
    "buffer_store_dwordx2 v[24:25], v60, s[84:87], s5 offen sc1\n\t"
    "s_mov_b64 exec, -1\n\t"
    "ds_read_b64_tr_b16 a[226:227], v7 offset:21248\n\t"
    "v_mfma_f32_16x16x32_bf16 v[184:187], a[208:211], a[24:27], v[184:187]\n\t"
    "ds_read_b64_tr_b16 a[232:233], v7 offset:25344\n\t"
    "v_mfma_f32_16x16x32_bf16 v[184:187], a[216:219], a[36:39], v[184:187]\n\t"
    "ds_read_b64_tr_b16 a[234:235], v7 offset:29440\n\t"
    "v_cvt_pk_bf16_f32 v88, v176, v177\n\t"
    "v_mfma_f32_16x16x32_bf16 v[216:219], a[192:195], a[4:7], v[216:219]\n\t"
    "ds_read_b64_tr_b16 a[240:241], v7 offset:33536\n\t"
    "v_cvt_pk_bf16_f32 v89, v178, v179\n\t"
    "v_mfma_f32_16x16x32_bf16 v[216:219], a[200:203], a[16:19], v[216:219]\n\t"
    "ds_read_b64_tr_b16 a[242:243], v7 offset:37632\n\t"
    "v_cvt_pk_bf16_f32 v90, v180, v181\n\t"
    "v_mfma_f32_16x16x32_bf16 v[216:219], a[208:211], a[28:31], v[216:219]\n\t"
    "ds_read_b64_tr_b16 a[248:249], v7 offset:41728\n\t"
    "v_cvt_pk_bf16_f32 v91, v182, v183\n\t"
    "v_mfma_f32_16x16x32_bf16 v[216:219], a[216:219], a[40:43], v[216:219]\n\t"
    "ds_read_b64_tr_b16 a[250:251], v7 offset:45824\n\t"
    "v_cvt_pk_bf16_f32 v92, v208, v209\n\t"
    "v_mfma_f32_16x16x32_bf16 v[248:251], a[192:195], a[8:11], v[248:251]\n\t"
    "ds_read_b64_tr_b16 a[228:229], v8 offset:17152\n\t"
    "v_cvt_pk_bf16_f32 v93, v210, v211\n\t"
    "v_mfma_f32_16x16x32_bf16 v[248:251], a[200:203], a[20:23], v[248:251]\n\t"
    "ds_read_b64_tr_b16 a[230:231], v8 offset:21248\n\t"
    "v_cvt_pk_bf16_f32 v94, v212, v213\n\t"
    "v_mfma_f32_16x16x32_bf16 v[248:251], a[208:211], a[32:35], v[248:251]\n\t"
    "ds_read_b64_tr_b16 a[236:237], v8 offset:25344\n\t"
    "v_cvt_pk_bf16_f32 v95, v214, v215\n\t"
    "v_mfma_f32_16x16x32_bf16 v[248:251], a[216:219], a[44:47], v[248:251]\n\t"
    "s_add_u32 s78, s96, 0\n\t"
    "buffer_store_dwordx4 v[88:91], v20, s[88:91], s78 offen sc1\n\t"
    "v_cvt_pk_bf16_f32 v96, v240, v241\n\t"
    "v_mfma_f32_16x16x32_bf16 v[188:191], a[196:199], a[0:3], v[188:191]\n\t"
    "ds_read_b64_tr_b16 a[238:239], v8 offset:29440\n\t"
    "v_cvt_pk_bf16_f32 v97, v242, v243\n\t"
    "v_mfma_f32_16x16x32_bf16 v[188:191], a[204:207], a[12:15], v[188:191]\n\t"
    "ds_read_b64_tr_b16 a[244:245], v8 offset:33536\n\t"
    "v_cvt_pk_bf16_f32 v98, v244, v245\n\t"
    "v_mfma_f32_16x16x32_bf16 v[188:191], a[212:215], a[24:27], v[188:191]\n\t"
    "ds_read_b64_tr_b16 a[246:247], v8 offset:37632\n\t"
    "v_cvt_pk_bf16_f32 v99, v246, v247\n\t"
    "v_mfma_f32_16x16x32_bf16 v[188:191], a[220:223], a[36:39], v[188:191]\n\t"
    "s_add_u32 s78, s96, 0x400\n\t"
    "buffer_store_dwordx4 v[92:95], v20, s[88:91], s78 offen sc1\n\t"
    "v_mfma_f32_16x16x32_bf16 v[220:223], a[196:199], a[4:7], v[220:223]\n\t"
    "ds_read_b64_tr_b16 a[252:253], v8 offset:41728\n\t"
    "v_mfma_f32_16x16x32_bf16 v[220:223], a[204:207], a[16:19], v[220:223]\n\t"
    "ds_read_b64_tr_b16 a[254:255], v8 offset:45824\n\t"
    "v_mfma_f32_16x16x32_bf16 v[220:223], a[212:215], a[28:31], v[220:223]\n\t"
    "v_mfma_f32_16x16x32_bf16 v[220:223], a[220:223], a[40:43], v[220:223]\n\t"
    "s_add_u32 s78, s96, 0x800\n\t"
    "buffer_store_dwordx4 v[96:99], v20, s[88:91], s78 offen sc1\n\t"
    "v_mfma_f32_16x16x32_bf16 v[252:255], a[196:199], a[8:11], v[252:255]\n\t"
    "v_mfma_f32_16x16x32_bf16 v[252:255], a[204:207], a[20:23], v[252:255]\n\t"
    "v_mfma_f32_16x16x32_bf16 v[252:255], a[212:215], a[32:35], v[252:255]\n\t"
    "v_mfma_f32_16x16x32_bf16 v[252:255], a[220:223], a[44:47], v[252:255]\n\t"
    "s_waitcnt lgkmcnt(0)\n\t"
    "v_mfma_f32_16x16x32_bf16 v[184:187], a[224:227], a[48:51], v[184:187]\n\t"
    "ds_read_b64_tr_b16 a[192:193], v15 offset:17152\n\t"
    "v_mfma_f32_16x16x32_bf16 v[184:187], a[232:235], a[60:63], v[184:187]\n\t"
    "ds_read_b64_tr_b16 a[194:195], v15 offset:21248\n\t"
    "v_mfma_f32_16x16x32_bf16 v[184:187], a[240:243], a[72:75], v[184:187]\n\t"
    "ds_read_b64_tr_b16 a[200:201], v15 offset:25344\n\t"
    "v_mfma_f32_16x16x32_bf16 v[184:187], a[248:251], a[84:87], v[184:187]\n\t"
    "ds_read_b64_tr_b16 a[202:203], v15 offset:29440\n\t"
    "v_mfma_f32_16x16x32_bf16 v[216:219], a[224:227], a[52:55], v[216:219]\n\t"
    "ds_read_b64_tr_b16 a[208:209], v15 offset:33536\n\t"
    "v_mfma_f32_16x16x32_bf16 v[216:219], a[232:235], a[64:67], v[216:219]\n\t"
    "ds_read_b64_tr_b16 a[210:211], v15 offset:37632\n\t"
    "v_mfma_f32_16x16x32_bf16 v[216:219], a[240:243], a[76:79], v[216:219]\n\t"
    "ds_read_b64_tr_b16 a[216:217], v15 offset:41728\n\t"
    "v_mfma_f32_16x16x32_bf16 v[216:219], a[248:251], a[88:91], v[216:219]\n\t"
    "ds_read_b64_tr_b16 a[218:219], v15 offset:45824\n\t"
    "v_mfma_f32_16x16x32_bf16 v[248:251], a[224:227], a[56:59], v[248:251]\n\t"
    "ds_read_b64_tr_b16 a[196:197], v16 offset:17152\n\t"
    "v_mfma_f32_16x16x32_bf16 v[248:251], a[232:235], a[68:71], v[248:251]\n\t"
    "ds_read_b64_tr_b16 a[198:199], v16 offset:21248\n\t"
    "v_mfma_f32_16x16x32_bf16 v[248:251], a[240:243], a[80:83], v[248:251]\n\t"
    "ds_read_b64_tr_b16 a[204:205], v16 offset:25344\n\t"
    "v_mfma_f32_16x16x32_bf16 v[248:251], a[248:251], a[92:95], v[248:251]\n\t"
    "ds_read_b64_tr_b16 a[206:207], v16 offset:29440\n\t"
    "v_mfma_f32_16x16x32_bf16 v[188:191], a[228:231], a[48:51], v[188:191]\n\t"
    "ds_read_b64_tr_b16 a[212:213], v16 offset:33536\n\t"
    "v_mfma_f32_16x16x32_bf16 v[188:191], a[236:239], a[60:63], v[188:191]\n\t"
    "ds_read_b64_tr_b16 a[214:215], v16 offset:37632\n\t"
    "v_mfma_f32_16x16x32_bf16 v[188:191], a[244:247], a[72:75], v[188:191]\n\t"
    "ds_read_b64_tr_b16 a[220:221], v16 offset:41728\n\t"
    "v_mfma_f32_16x16x32_bf16 v[188:191], a[252:255], a[84:87], v[188:191]\n\t"
    "ds_read_b64_tr_b16 a[222:223], v16 offset:45824\n\t"
    "v_mfma_f32_16x16x32_bf16 v[220:223], a[228:231], a[52:55], v[220:223]\n\t"
    "v_mfma_f32_16x16x32_bf16 v[220:223], a[236:239], a[64:67], v[220:223]\n\t"
    "v_mfma_f32_16x16x32_bf16 v[220:223], a[244:247], a[76:79], v[220:223]\n\t"
    "v_mfma_f32_16x16x32_bf16 v[220:223], a[252:255], a[88:91], v[220:223]\n\t"
    "v_mfma_f32_16x16x32_bf16 v[252:255], a[228:231], a[56:59], v[252:255]\n\t"
    "v_mfma_f32_16x16x32_bf16 v[252:255], a[236:239], a[68:71], v[252:255]\n\t"
    "v_mfma_f32_16x16x32_bf16 v[252:255], a[244:247], a[80:83], v[252:255]\n\t"
    "buffer_load_dwordx2 v[26:27], v14, s[84:87], 0 offen sc1\n\t"
    "v_mfma_f32_16x16x32_bf16 v[252:255], a[252:255], a[92:95], v[252:255]\n\t"
    "s_waitcnt lgkmcnt(0)\n\t"
    "v_mfma_f32_16x16x32_bf16 v[184:187], a[192:195], a[96:99], v[184:187]\n\t"
    "ds_read_b64_tr_b16 a[224:225], v9 offset:33536\n\t"
    "v_mfma_f32_16x16x32_bf16 v[184:187], a[200:203], a[108:111], v[184:187]\n\t"
    "s_waitcnt vmcnt(1)\n\t"
    "s_mov_b64 exec, 1\n\t"
    "buffer_store_dwordx2 v[24:25], v60, s[84:87], s6 offen sc1\n\t"
    "s_mov_b64 exec, -1\n\t"
    "ds_read_b64_tr_b16 a[226:227], v9 offset:37632\n\t"
    "v_mfma_f32_16x16x32_bf16 v[184:187], a[208:211], a[120:123], v[184:187]\n\t"
    "s_or_b32 s78, %1, s56\n\t"
    "s_cbranch_scc1 Lnoep_%=\n\t"
    "v_lshlrev_b32 v28, 7, v0\n\t"
    "v_add_u32 v28, 0x1000, v28\n\t"
    "s_mov_b32 s96, 0x400000\n\t"
    "Lep_%=:\n\t"
    "s_mov_b32 s78, 0\n\t"
    "buffer_load_dwordx2 v[32:33], v28, s[84:87], s78 offen sc1\n\t"
    "s_mov_b32 s78, 0x2000\n\t"
    "buffer_load_dwordx2 v[34:35], v28, s[84:87], s78 offen sc1\n\t"
    "s_mov_b32 s78, 0x4000\n\t"
    "buffer_load_dwordx2 v[36:37], v28, s[84:87], s78 offen sc1\n\t"
    "s_mov_b32 s78, 0x6000\n\t"
    "buffer_load_dwordx2 v[38:39], v28, s[84:87], s78 offen sc1\n\t"
    "s_waitcnt vmcnt(0)\n\t"
    "v_xor_b32 v32, s82, v32\n\t"
    "v_xor_b32 v33, s83, v33\n\t"
    "v_xor_b32 v34, s82, v34\n\t"
    "v_xor_b32 v35, s83, v35\n\t"
    "v_xor_b32 v36, s82, v36\n\t"
    "v_xor_b32 v37, s83, v37\n\t"
    "v_xor_b32 v38, s82, v38\n\t"
    "v_xor_b32 v39, s83, v39\n\t"
    "v_or_b32 v32, v32, v33\n\t"
    "v_or_b32 v32, v32, v34\n\t"
    "v_or_b32 v32, v32, v35\n\t"
    "v_or_b32 v32, v32, v36\n\t"
    "v_or_b32 v32, v32, v37\n\t"
    "v_or_b32 v32, v32, v38\n\t"
    "v_or_b32 v32, v32, v39\n\t"
    "v_cmp_ne_u32 vcc, 0, v32\n\t"
    "s_nop 1\n\t"
    "s_cbranch_vccz Lepok_%=\n\t"
    "s_sub_u32 s96, s96, 1\n\t"
    "s_cmp_lg_u32 s96, 0\n\t"
    "s_cbranch_scc1 Lep_%=\n\t"
    "Lepok_%=:\n\t"
    "v_mov_b32 v32, s82\n\t"
    "v_mov_b32 v33, s83\n\t"
    "s_mov_b64 exec, 1\n\t"
    "buffer_store_dwordx2 v[32:33], v60, s[84:87], 0 offen sc1\n\t"
    "s_mov_b64 exec, -1\n\t"
    "Lnoep_%=:\n\t"
    "ds_read_b64_tr_b16 a[232:233], v9 offset:41728\n\t"
    "v_mfma_f32_16x16x32_bf16 v[184:187], a[216:219], a[132:135], v[184:187]\n\t"
    "ds_read_b64_tr_b16 a[234:235], v9 offset:45824\n\t"
    "v_mfma_f32_16x16x32_bf16 v[216:219], a[192:195], a[100:103], v[216:219]\n\t"
    "ds_read_b64_tr_b16 a[240:241], v9 offset:49920\n\t"
    "v_mfma_f32_16x16x32_bf16 v[216:219], a[200:203], a[112:115], v[216:219]\n\t"
    "ds_read_b64_tr_b16 a[242:243], v9 offset:54016\n\t"
    "v_mfma_f32_16x16x32_bf16 v[216:219], a[208:211], a[124:127], v[216:219]\n\t"
    "ds_read_b64_tr_b16 a[248:249], v9 offset:58112\n\t"
    "v_mfma_f32_16x16x32_bf16 v[216:219], a[216:219], a[136:139], v[216:219]\n\t"
    "ds_read_b64_tr_b16 a[250:251], v9 offset:62208\n\t"
    "v_mfma_f32_16x16x32_bf16 v[248:251], a[192:195], a[104:107], v[248:251]\n\t"
    "ds_read_b64_tr_b16 a[228:229], v10 offset:33536\n\t"
    "s_waitcnt vmcnt(1)\n\t"
    "v_cmp_eq_u32 vcc, s82, v26\n\t"
    "v_cmp_eq_u32 s[76:77], s83, v27\n\t"
    "v_mfma_f32_16x16x32_bf16 v[248:251], a[200:203], a[116:119], v[248:251]\n\t"
    "ds_read_b64_tr_b16 a[230:231], v10 offset:37632\n\t"
    "v_mfma_f32_16x16x32_bf16 v[248:251], a[208:211], a[128:131], v[248:251]\n\t"
    "s_and_b32 s79, vcc_lo, s76\n\t"
    "s_and_b32 s78, s79, 1\n\t"
    "s_cmp_eq_u32 s78, 1\n\t"
    "s_cbranch_scc1 Lok_p0_%=\n\t"
    "s_mov_b32 s96, 0x400000\n\t"
    "Lpoll_p0_%=:\n\t"
    "buffer_load_dwordx2 v[26:27], v14, s[84:87], 0 offen sc1\n\t"
    "s_waitcnt vmcnt(0)\n\t"
    "v_cmp_eq_u32 vcc, s82, v26\n\t"
    "v_cmp_eq_u32 s[76:77], s83, v27\n\t"
    "s_nop 1\n\t"
    "s_and_b32 s79, vcc_lo, s76\n\t"
    "s_and_b32 s78, s79, 1\n\t"
    "s_cmp_eq_u32 s78, 1\n\t"
    "s_cbranch_scc1 Lok_p0_%=\n\t"
    "s_sleep 1\n\t"
    "s_sub_u32 s96, s96, 1\n\t"
    "s_cmp_lg_u32 s96, 0\n\t"
    "s_cbranch_scc1 Lpoll_p0_%=\n\t"
    "Lok_p0_%=:\n\t"
    "buffer_load_dwordx2 v[26:27], v14, s[84:87], 0 offen sc1\n\t"
    "ds_read_b64_tr_b16 a[236:237], v10 offset:41728\n\t"
    "v_mfma_f32_16x16x32_bf16 v[248:251], a[216:219], a[140:143], v[248:251]\n\t"
    "ds_read_b64_tr_b16 a[238:239], v10 offset:45824\n\t"
    "v_mfma_f32_16x16x32_bf16 v[188:191], a[196:199], a[96:99], v[188:191]\n\t"
    "ds_read_b64_tr_b16 a[244:245], v10 offset:49920\n\t"
    "v_mfma_f32_16x16x32_bf16 v[188:191], a[204:207], a[108:111], v[188:191]\n\t"
    "s_add_u32 s78, s7, 0\n\t"
    "buffer_load_dwordx4 v[104:107], v20, s[88:91], s78 offen sc1\n\t"
    "ds_read_b64_tr_b16 a[246:247], v10 offset:54016\n\t"
    "v_mfma_f32_16x16x32_bf16 v[188:191], a[212:215], a[120:123], v[188:191]\n\t"
    "ds_read_b64_tr_b16 a[252:253], v10 offset:58112\n\t"
    "v_mfma_f32_16x16x32_bf16 v[188:191], a[220:223], a[132:135], v[188:191]\n\t"
    "ds_read_b64_tr_b16 a[254:255], v10 offset:62208\n\t"
    "v_mfma_f32_16x16x32_bf16 v[220:223], a[196:199], a[100:103], v[220:223]\n\t"
    "v_mfma_f32_16x16x32_bf16 v[220:223], a[204:207], a[112:115], v[220:223]\n\t"
    "s_add_u32 s78, s7, 0x400\n\t"
    "buffer_load_dwordx4 v[116:119], v20, s[88:91], s78 offen sc1\n\t"
    "v_mfma_f32_16x16x32_bf16 v[220:223], a[212:215], a[124:127], v[220:223]\n\t"
    "v_mfma_f32_16x16x32_bf16 v[220:223], a[220:223], a[136:139], v[220:223]\n\t"
    "v_mfma_f32_16x16x32_bf16 v[252:255], a[196:199], a[104:107], v[252:255]\n\t"
    "v_mfma_f32_16x16x32_bf16 v[252:255], a[204:207], a[116:119], v[252:255]\n\t"
    "s_add_u32 s78, s7, 0x800\n\t"
    "buffer_load_dwordx4 v[128:131], v20, s[88:91], s78 offen sc1\n\t"
    "v_mfma_f32_16x16x32_bf16 v[252:255], a[212:215], a[128:131], v[252:255]\n\t"
    "v_mfma_f32_16x16x32_bf16 v[252:255], a[220:223], a[140:143], v[252:255]\n\t"
    "s_waitcnt lgkmcnt(0)\n\t"
    "v_mfma_f32_16x16x32_bf16 v[184:187], a[224:227], a[144:147], v[184:187]\n\t"
    "s_waitcnt vmcnt(3)\n\t"
    "v_cmp_eq_u32 vcc, s82, v26\n\t"
    "v_cmp_eq_u32 s[76:77], s83, v27\n\t"
    "v_mfma_f32_16x16x32_bf16 v[184:187], a[232:235], a[156:159], v[184:187]\n\t"
    "v_mfma_f32_16x16x32_bf16 v[184:187], a[240:243], a[168:171], v[184:187]\n\t"
    "s_and_b32 s79, vcc_lo, s76\n\t"
    "s_and_b32 s78, s79, 2\n\t"
    "s_cmp_eq_u32 s78, 2\n\t"
    "s_cbranch_scc1 Lok_p1_%=\n\t"
    "s_mov_b32 s96, 0x400000\n\t"
    "Lpoll_p1_%=:\n\t"
    "buffer_load_dwordx2 v[26:27], v14, s[84:87], 0 offen sc1\n\t"
    "s_waitcnt vmcnt(0)\n\t"
    "v_cmp_eq_u32 vcc, s82, v26\n\t"
    "v_cmp_eq_u32 s[76:77], s83, v27\n\t"
    "s_nop 1\n\t"
    "s_and_b32 s79, vcc_lo, s76\n\t"
    "s_and_b32 s78, s79, 2\n\t"
    "s_cmp_eq_u32 s78, 2\n\t"
    "s_cbranch_scc1 Lok_p1_%=\n\t"
    "s_sleep 1\n\t"
    "s_sub_u32 s96, s96, 1\n\t"
    "s_cmp_lg_u32 s96, 0\n\t"
    "s_cbranch_scc1 Lpoll_p1_%=\n\t"
    "Lok_p1_%=:\n\t"
    "v_mfma_f32_16x16x32_bf16 v[184:187], a[248:251], a[180:183], v[184:187]\n\t"
    "v_mfma_f32_16x16x32_bf16 v[216:219], a[224:227], a[148:151], v[216:219]\n\t"
    "s_add_u32 s78, s7, 0x3000\n\t"
    "buffer_load_dwordx4 v[108:111], v20, s[88:91], s78 offen sc1\n\t"
    "v_mfma_f32_16x16x32_bf16 v[216:219], a[232:235], a[160:163], v[216:219]\n\t"
    "v_mfma_f32_16x16x32_bf16 v[216:219], a[240:243], a[172:175], v[216:219]\n\t"
    "v_mfma_f32_16x16x32_bf16 v[216:219], a[248:251], a[184:187], v[216:219]\n\t"
    "v_mfma_f32_16x16x32_bf16 v[248:251], a[224:227], a[152:155], v[248:251]\n\t"
    "s_add_u32 s78, s7, 0x3400\n\t"
    "buffer_load_dwordx4 v[120:123], v20, s[88:91], s78 offen sc1\n\t"
    "v_mfma_f32_16x16x32_bf16 v[248:251], a[232:235], a[164:167], v[248:251]\n\t"
    "v_mfma_f32_16x16x32_bf16 v[248:251], a[240:243], a[176:179], v[248:251]\n\t"
    "v_mfma_f32_16x16x32_bf16 v[248:251], a[248:251], a[188:191], v[248:251]\n\t"
    "v_mfma_f32_16x16x32_bf16 v[188:191], a[228:231], a[144:147], v[188:191]\n\t"
    "s_add_u32 s78, s7, 0x3800\n\t"
    "buffer_load_dwordx4 v[132:135], v20, s[88:91], s78 offen sc1\n\t"
    "v_mfma_f32_16x16x32_bf16 v[188:191], a[236:239], a[156:159], v[188:191]\n\t"
    "v_mfma_f32_16x16x32_bf16 v[188:191], a[244:247], a[168:171], v[188:191]\n\t"
    "v_mfma_f32_16x16x32_bf16 v[188:191], a[252:255], a[180:183], v[188:191]\n\t"
    "v_mfma_f32_16x16x32_bf16 v[220:223], a[228:231], a[148:151], v[220:223]\n\t"
    "buffer_load_dwordx2 v[26:27], v14, s[84:87], 0 offen sc1\n\t"
    "v_mfma_f32_16x16x32_bf16 v[220:223], a[236:239], a[160:163], v[220:223]\n\t"
    "v_mfma_f32_16x16x32_bf16 v[220:223], a[244:247], a[172:175], v[220:223]\n\t"
    "v_mfma_f32_16x16x32_bf16 v[220:223], a[252:255], a[184:187], v[220:223]\n\t"
    "v_mfma_f32_16x16x32_bf16 v[252:255], a[228:231], a[152:155], v[252:255]\n\t"
    "v_mfma_f32_16x16x32_bf16 v[252:255], a[236:239], a[164:167], v[252:255]\n\t"
    "v_mfma_f32_16x16x32_bf16 v[252:255], a[244:247], a[176:179], v[252:255]\n\t"
    "v_mfma_f32_16x16x32_bf16 v[252:255], a[252:255], a[188:191], v[252:255]\n\t"
    "s_waitcnt vmcnt(0)\n\t"
    "v_cmp_eq_u32 vcc, s82, v26\n\t"
    "v_cmp_eq_u32 s[76:77], s83, v27\n\t"
    "s_nop 1\n\t"
    "s_and_b32 s79, vcc_lo, s76\n\t"
    "s_and_b32 s78, s79, 4\n\t"
    "s_cmp_eq_u32 s78, 4\n\t"
    "s_cbranch_scc1 Lok_p2_%=\n\t"
    "s_mov_b32 s96, 0x400000\n\t"
    "Lpoll_p2_%=:\n\t"
    "buffer_load_dwordx2 v[26:27], v14, s[84:87], 0 offen sc1\n\t"
    "s_waitcnt vmcnt(0)\n\t"
    "v_cmp_eq_u32 vcc, s82, v26\n\t"
    "v_cmp_eq_u32 s[76:77], s83, v27\n\t"
    "s_nop 1\n\t"
    "s_and_b32 s79, vcc_lo, s76\n\t"
    "s_and_b32 s78, s79, 4\n\t"
    "s_cmp_eq_u32 s78, 4\n\t"
    "s_cbranch_scc1 Lok_p2_%=\n\t"
    "s_sleep 1\n\t"
    "s_sub_u32 s96, s96, 1\n\t"
    "s_cmp_lg_u32 s96, 0\n\t"
    "s_cbranch_scc1 Lpoll_p2_%=\n\t"
    "Lok_p2_%=:\n\t"
    "s_add_u32 s78, s7, 0x6000\n\t"
    "buffer_load_dwordx4 v[112:115], v20, s[88:91], s78 offen sc1\n\t"
    "s_add_u32 s78, s7, 0x6400\n\t"
    "buffer_load_dwordx4 v[124:127], v20, s[88:91], s78 offen sc1\n\t"
    "s_add_u32 s78, s7, 0x6800\n\t"
    "buffer_load_dwordx4 v[136:139], v20, s[88:91], s78 offen sc1\n\t"
    "v_lshlrev_b32 v28, 16, v104\n\t"
    "v_and_b32 v104, 0xffff0000, v104\n\t"
    "v_lshlrev_b32 v29, 16, v105\n\t"
    "v_and_b32 v105, 0xffff0000, v105\n\t"
    "v_lshlrev_b32 v30, 16, v106\n\t"
    "v_and_b32 v106, 0xffff0000, v106\n\t"
    "v_lshlrev_b32 v31, 16, v107\n\t"
    "v_and_b32 v107, 0xffff0000, v107\n\t"
    "v_add_f32 v184, v184, v28\n\t"
    "v_add_f32 v185, v185, v104\n\t"
    "v_add_f32 v186, v186, v29\n\t"
    "v_add_f32 v187, v187, v105\n\t"
    "v_add_f32 v188, v188, v30\n\t"
    "v_add_f32 v189, v189, v106\n\t"
    "v_add_f32 v190, v190, v31\n\t"
    "v_add_f32 v191, v191, v107\n\t"
    "v_lshlrev_b32 v28, 16, v108\n\t"
    "v_and_b32 v108, 0xffff0000, v108\n\t"
    "v_lshlrev_b32 v29, 16, v109\n\t"
    "v_and_b32 v109, 0xffff0000, v109\n\t"
    "v_lshlrev_b32 v30, 16, v110\n\t"
    "v_and_b32 v110, 0xffff0000, v110\n\t"
    "v_lshlrev_b32 v31, 16, v111\n\t"
    "v_and_b32 v111, 0xffff0000, v111\n\t"
    "v_add_f32 v184, v184, v28\n\t"
    "v_add_f32 v185, v185, v108\n\t"
    "v_add_f32 v186, v186, v29\n\t"
    "v_add_f32 v187, v187, v109\n\t"
    "v_add_f32 v188, v188, v30\n\t"
    "v_add_f32 v189, v189, v110\n\t"
    "v_add_f32 v190, v190, v31\n\t"
    "v_add_f32 v191, v191, v111\n\t"
    "v_lshlrev_b32 v28, 16, v116\n\t"
    "v_and_b32 v116, 0xffff0000, v116\n\t"
    "v_lshlrev_b32 v29, 16, v117\n\t"
    "v_and_b32 v117, 0xffff0000, v117\n\t"
    "v_lshlrev_b32 v30, 16, v118\n\t"
    "v_and_b32 v118, 0xffff0000, v118\n\t"
    "v_lshlrev_b32 v31, 16, v119\n\t"
    "v_and_b32 v119, 0xffff0000, v119\n\t"
    "v_add_f32 v216, v216, v28\n\t"
    "v_add_f32 v217, v217, v116\n\t"
    "v_add_f32 v218, v218, v29\n\t"
    "v_add_f32 v219, v219, v117\n\t"
    "v_add_f32 v220, v220, v30\n\t"
    "v_add_f32 v221, v221, v118\n\t"
    "v_add_f32 v222, v222, v31\n\t"
    "v_add_f32 v223, v223, v119\n\t"
    "v_lshlrev_b32 v28, 16, v120\n\t"
    "v_and_b32 v120, 0xffff0000, v120\n\t"
    "v_lshlrev_b32 v29, 16, v121\n\t"
    "v_and_b32 v121, 0xffff0000, v121\n\t"
    "v_lshlrev_b32 v30, 16, v122\n\t"
    "v_and_b32 v122, 0xffff0000, v122\n\t"
    "v_lshlrev_b32 v31, 16, v123\n\t"
    "v_and_b32 v123, 0xffff0000, v123\n\t"
    "v_add_f32 v216, v216, v28\n\t"
    "v_add_f32 v217, v217, v120\n\t"
    "v_add_f32 v218, v218, v29\n\t"
    "v_add_f32 v219, v219, v121\n\t"
    "v_add_f32 v220, v220, v30\n\t"
    "v_add_f32 v221, v221, v122\n\t"
    "v_add_f32 v222, v222, v31\n\t"
    "v_add_f32 v223, v223, v123\n\t"
    "v_lshlrev_b32 v28, 16, v128\n\t"
    "v_and_b32 v128, 0xffff0000, v128\n\t"
    "v_lshlrev_b32 v29, 16, v129\n\t"
    "v_and_b32 v129, 0xffff0000, v129\n\t"
    "v_lshlrev_b32 v30, 16, v130\n\t"
    "v_and_b32 v130, 0xffff0000, v130\n\t"
    "v_lshlrev_b32 v31, 16, v131\n\t"
    "v_and_b32 v131, 0xffff0000, v131\n\t"
    "v_add_f32 v248, v248, v28\n\t"
    "v_add_f32 v249, v249, v128\n\t"
    "v_add_f32 v250, v250, v29\n\t"
    "v_add_f32 v251, v251, v129\n\t"
    "v_add_f32 v252, v252, v30\n\t"
    "v_add_f32 v253, v253, v130\n\t"
    "v_add_f32 v254, v254, v31\n\t"
    "v_add_f32 v255, v255, v131\n\t"
    "v_lshlrev_b32 v28, 16, v132\n\t"
    "v_and_b32 v132, 0xffff0000, v132\n\t"
    "v_lshlrev_b32 v29, 16, v133\n\t"
    "v_and_b32 v133, 0xffff0000, v133\n\t"
    "v_lshlrev_b32 v30, 16, v134\n\t"
    "v_and_b32 v134, 0xffff0000, v134\n\t"
    "v_lshlrev_b32 v31, 16, v135\n\t"
    "v_and_b32 v135, 0xffff0000, v135\n\t"
    "v_add_f32 v248, v248, v28\n\t"
    "v_add_f32 v249, v249, v132\n\t"
    "v_add_f32 v250, v250, v29\n\t"
    "v_add_f32 v251, v251, v133\n\t"
    "v_add_f32 v252, v252, v30\n\t"
    "v_add_f32 v253, v253, v134\n\t"
    "v_add_f32 v254, v254, v31\n\t"
    "v_add_f32 v255, v255, v135\n\t"
    "s_waitcnt vmcnt(0)\n\t"
    "v_lshlrev_b32 v28, 16, v112\n\t"
    "v_and_b32 v112, 0xffff0000, v112\n\t"
    "v_lshlrev_b32 v29, 16, v113\n\t"
    "v_and_b32 v113, 0xffff0000, v113\n\t"
    "v_lshlrev_b32 v30, 16, v114\n\t"
    "v_and_b32 v114, 0xffff0000, v114\n\t"
    "v_lshlrev_b32 v31, 16, v115\n\t"
    "v_and_b32 v115, 0xffff0000, v115\n\t"
    "v_add_f32 v184, v184, v28\n\t"
    "v_add_f32 v185, v185, v112\n\t"
    "v_add_f32 v186, v186, v29\n\t"
    "v_add_f32 v187, v187, v113\n\t"
    "v_add_f32 v188, v188, v30\n\t"
    "v_add_f32 v189, v189, v114\n\t"
    "v_add_f32 v190, v190, v31\n\t"
    "v_add_f32 v191, v191, v115\n\t"
    "v_cvt_pk_bf16_f32 v184, v184, v185\n\t"
    "v_cvt_pk_bf16_f32 v185, v186, v187\n\t"
    "v_cvt_pk_bf16_f32 v186, v188, v189\n\t"
    "v_cvt_pk_bf16_f32 v187, v190, v191\n\t"
    "s_nop 0\n\t"
    "v_permlane32_swap_b32 v184, v186\n\t"
    "v_permlane32_swap_b32 v185, v187\n\t"
    "s_nop 0\n\t"
    "v_permlane16_swap_b32 v184, v186\n\t"
    "v_permlane16_swap_b32 v185, v187\n\t"
    "s_mov_b32 s78, 0\n\t"
    "buffer_store_dwordx4 v[184:187], v17, s[92:95], s78 offen sc1\n\t"
    "v_lshlrev_b32 v28, 16, v124\n\t"
    "v_and_b32 v124, 0xffff0000, v124\n\t"
    "v_lshlrev_b32 v29, 16, v125\n\t"
    "v_and_b32 v125, 0xffff0000, v125\n\t"
    "v_lshlrev_b32 v30, 16, v126\n\t"
    "v_and_b32 v126, 0xffff0000, v126\n\t"
    "v_lshlrev_b32 v31, 16, v127\n\t"
    "v_and_b32 v127, 0xffff0000, v127\n\t"
    "v_add_f32 v216, v216, v28\n\t"
    "v_add_f32 v217, v217, v124\n\t"
    "v_add_f32 v218, v218, v29\n\t"
    "v_add_f32 v219, v219, v125\n\t"
    "v_add_f32 v220, v220, v30\n\t"
    "v_add_f32 v221, v221, v126\n\t"
    "v_add_f32 v222, v222, v31\n\t"
    "v_add_f32 v223, v223, v127\n\t"
    "v_cvt_pk_bf16_f32 v216, v216, v217\n\t"
    "v_cvt_pk_bf16_f32 v217, v218, v219\n\t"
    "v_cvt_pk_bf16_f32 v218, v220, v221\n\t"
    "v_cvt_pk_bf16_f32 v219, v222, v223\n\t"
    "s_nop 0\n\t"
    "v_permlane32_swap_b32 v216, v218\n\t"
    "v_permlane32_swap_b32 v217, v219\n\t"
    "s_nop 0\n\t"
    "v_permlane16_swap_b32 v216, v218\n\t"
    "v_permlane16_swap_b32 v217, v219\n\t"
    "s_mov_b32 s78, 0x4000\n\t"
    "buffer_store_dwordx4 v[216:219], v17, s[92:95], s78 offen sc1\n\t"
    "v_lshlrev_b32 v28, 16, v136\n\t"
    "v_and_b32 v136, 0xffff0000, v136\n\t"
    "v_lshlrev_b32 v29, 16, v137\n\t"
    "v_and_b32 v137, 0xffff0000, v137\n\t"
    "v_lshlrev_b32 v30, 16, v138\n\t"
    "v_and_b32 v138, 0xffff0000, v138\n\t"
    "v_lshlrev_b32 v31, 16, v139\n\t"
    "v_and_b32 v139, 0xffff0000, v139\n\t"
    "v_add_f32 v248, v248, v28\n\t"
    "v_add_f32 v249, v249, v136\n\t"
    "v_add_f32 v250, v250, v29\n\t"
    "v_add_f32 v251, v251, v137\n\t"
    "v_add_f32 v252, v252, v30\n\t"
    "v_add_f32 v253, v253, v138\n\t"
    "v_add_f32 v254, v254, v31\n\t"
    "v_add_f32 v255, v255, v139\n\t"
    "v_cvt_pk_bf16_f32 v248, v248, v249\n\t"
    "v_cvt_pk_bf16_f32 v249, v250, v251\n\t"
    "v_cvt_pk_bf16_f32 v250, v252, v253\n\t"
    "v_cvt_pk_bf16_f32 v251, v254, v255\n\t"
    "s_nop 0\n\t"
    "v_permlane32_swap_b32 v248, v250\n\t"
    "v_permlane32_swap_b32 v249, v251\n\t"
    "s_nop 0\n\t"
    "v_permlane16_swap_b32 v248, v250\n\t"
    "v_permlane16_swap_b32 v249, v251\n\t"
    "s_mov_b32 s78, 0x8000\n\t"
    "buffer_store_dwordx4 v[248:251], v17, s[92:95], s78 offen sc1\n\t"
    "s_branch Lend_%=\n\t"
    "Lstub_c0_0_%=:\n\t"
    "s_call_b64 s[76:77], Lrare0_%=\n\t"
    "s_branch Lret_c0_0_%=\n\t"
    "Lstub_c0_1_%=:\n\t"
    "s_call_b64 s[76:77], Lrare1_%=\n\t"
    "s_branch Lret_c0_1_%=\n\t"
    "Lstub_c0_2_%=:\n\t"
    "s_call_b64 s[76:77], Lrare2_%=\n\t"
    "s_branch Lret_c0_2_%=\n\t"
    "Lstub_c0_3_%=:\n\t"
    "s_call_b64 s[76:77], Lrare3_%=\n\t"
    "s_branch Lret_c0_3_%=\n\t"
    "Lstub_c0_4_%=:\n\t"
    "s_call_b64 s[76:77], Lrare4_%=\n\t"
    "s_branch Lret_c0_4_%=\n\t"
    "Lstub_c0_5_%=:\n\t"
    "s_call_b64 s[76:77], Lrare5_%=\n\t"
    "s_branch Lret_c0_5_%=\n\t"
    "Lstub_c1_0_%=:\n\t"
    "s_call_b64 s[76:77], Lrare0_%=\n\t"
    "s_branch Lret_c1_0_%=\n\t"
    "Lstub_c1_1_%=:\n\t"
    "s_call_b64 s[76:77], Lrare1_%=\n\t"
    "s_branch Lret_c1_1_%=\n\t"
    "Lstub_c1_2_%=:\n\t"
    "s_call_b64 s[76:77], Lrare2_%=\n\t"
    "s_branch Lret_c1_2_%=\n\t"
    "Lstub_c1_3_%=:\n\t"
    "s_call_b64 s[76:77], Lrare3_%=\n\t"
    "s_branch Lret_c1_3_%=\n\t"
    "Lstub_c1_4_%=:\n\t"
    "s_call_b64 s[76:77], Lrare4_%=\n\t"
    "s_branch Lret_c1_4_%=\n\t"
    "Lstub_c1_5_%=:\n\t"
    "s_call_b64 s[76:77], Lrare5_%=\n\t"
    "s_branch Lret_c1_5_%=\n\t"
    "Lstub_c2_0_%=:\n\t"
    "s_call_b64 s[76:77], Lrare0_%=\n\t"
    "s_branch Lret_c2_0_%=\n\t"
    "Lstub_c2_1_%=:\n\t"
    "s_call_b64 s[76:77], Lrare1_%=\n\t"
    "s_branch Lret_c2_1_%=\n\t"
    "Lstub_c2_2_%=:\n\t"
    "s_call_b64 s[76:77], Lrare2_%=\n\t"
    "s_branch Lret_c2_2_%=\n\t"
    "Lstub_c2_3_%=:\n\t"
    "s_call_b64 s[76:77], Lrare3_%=\n\t"
    "s_branch Lret_c2_3_%=\n\t"
    "Lstub_c2_4_%=:\n\t"
    "s_call_b64 s[76:77], Lrare4_%=\n\t"
    "s_branch Lret_c2_4_%=\n\t"
    "Lstub_c2_5_%=:\n\t"
    "s_call_b64 s[76:77], Lrare5_%=\n\t"
    "s_branch Lret_c2_5_%=\n\t"
    "Lstub_c3_0_%=:\n\t"
    "s_call_b64 s[76:77], Lrare0_%=\n\t"
    "s_branch Lret_c3_0_%=\n\t"
    "Lstub_c3_1_%=:\n\t"
    "s_call_b64 s[76:77], Lrare1_%=\n\t"
    "s_branch Lret_c3_1_%=\n\t"
    "Lstub_c3_2_%=:\n\t"
    "s_call_b64 s[76:77], Lrare2_%=\n\t"
    "s_branch Lret_c3_2_%=\n\t"
    "Lstub_c3_3_%=:\n\t"
    "s_call_b64 s[76:77], Lrare3_%=\n\t"
    "s_branch Lret_c3_3_%=\n\t"
    "Lstub_c3_4_%=:\n\t"
    "s_call_b64 s[76:77], Lrare4_%=\n\t"
    "s_branch Lret_c3_4_%=\n\t"
    "Lstub_c3_5_%=:\n\t"
    "s_call_b64 s[76:77], Lrare5_%=\n\t"
    "s_branch Lret_c3_5_%=\n\t"
    "Lstub_c4_0_%=:\n\t"
    "s_call_b64 s[76:77], Lrare0_%=\n\t"
    "s_branch Lret_c4_0_%=\n\t"
    "Lstub_c4_1_%=:\n\t"
    "s_call_b64 s[76:77], Lrare1_%=\n\t"
    "s_branch Lret_c4_1_%=\n\t"
    "Lstub_c4_2_%=:\n\t"
    "s_call_b64 s[76:77], Lrare2_%=\n\t"
    "s_branch Lret_c4_2_%=\n\t"
    "Lstub_c4_3_%=:\n\t"
    "s_call_b64 s[76:77], Lrare3_%=\n\t"
    "s_branch Lret_c4_3_%=\n\t"
    "Lstub_c4_4_%=:\n\t"
    "s_call_b64 s[76:77], Lrare4_%=\n\t"
    "s_branch Lret_c4_4_%=\n\t"
    "Lstub_c4_5_%=:\n\t"
    "s_call_b64 s[76:77], Lrare5_%=\n\t"
    "s_branch Lret_c4_5_%=\n\t"
    "Lstub_c5_0_%=:\n\t"
    "s_call_b64 s[76:77], Lrare0_%=\n\t"
    "s_branch Lret_c5_0_%=\n\t"
    "Lstub_c5_1_%=:\n\t"
    "s_call_b64 s[76:77], Lrare1_%=\n\t"
    "s_branch Lret_c5_1_%=\n\t"
    "Lstub_c5_2_%=:\n\t"
    "s_call_b64 s[76:77], Lrare2_%=\n\t"
    "s_branch Lret_c5_2_%=\n\t"
    "Lstub_c5_3_%=:\n\t"
    "s_call_b64 s[76:77], Lrare3_%=\n\t"
    "s_branch Lret_c5_3_%=\n\t"
    "Lstub_c5_4_%=:\n\t"
    "s_call_b64 s[76:77], Lrare4_%=\n\t"
    "s_branch Lret_c5_4_%=\n\t"
    "Lstub_c5_5_%=:\n\t"
    "s_call_b64 s[76:77], Lrare5_%=\n\t"
    "s_branch Lret_c5_5_%=\n\t"
    "Lstub_c6_0_%=:\n\t"
    "s_call_b64 s[76:77], Lrare0_%=\n\t"
    "s_branch Lret_c6_0_%=\n\t"
    "Lstub_c6_1_%=:\n\t"
    "s_call_b64 s[76:77], Lrare1_%=\n\t"
    "s_branch Lret_c6_1_%=\n\t"
    "Lstub_c6_2_%=:\n\t"
    "s_call_b64 s[76:77], Lrare2_%=\n\t"
    "s_branch Lret_c6_2_%=\n\t"
    "Lstub_c6_3_%=:\n\t"
    "s_call_b64 s[76:77], Lrare3_%=\n\t"
    "s_branch Lret_c6_3_%=\n\t"
    "Lstub_c6_4_%=:\n\t"
    "s_call_b64 s[76:77], Lrare4_%=\n\t"
    "s_branch Lret_c6_4_%=\n\t"
    "Lstub_c6_5_%=:\n\t"
    "s_call_b64 s[76:77], Lrare5_%=\n\t"
    "s_branch Lret_c6_5_%=\n\t"
    "Lstub_c7_0_%=:\n\t"
    "s_call_b64 s[76:77], Lrare0_%=\n\t"
    "s_branch Lret_c7_0_%=\n\t"
    "Lstub_c7_1_%=:\n\t"
    "s_call_b64 s[76:77], Lrare1_%=\n\t"
    "s_branch Lret_c7_1_%=\n\t"
    "Lstub_c7_2_%=:\n\t"
    "s_call_b64 s[76:77], Lrare2_%=\n\t"
    "s_branch Lret_c7_2_%=\n\t"
    "Lstub_c7_3_%=:\n\t"
    "s_call_b64 s[76:77], Lrare3_%=\n\t"
    "s_branch Lret_c7_3_%=\n\t"
    "Lstub_c7_4_%=:\n\t"
    "s_call_b64 s[76:77], Lrare4_%=\n\t"
    "s_branch Lret_c7_4_%=\n\t"
    "Lstub_c7_5_%=:\n\t"
    "s_call_b64 s[76:77], Lrare5_%=\n\t"
    "s_branch Lret_c7_5_%=\n\t"
    "Lstub_c8_0_%=:\n\t"
    "s_call_b64 s[76:77], Lrare0_%=\n\t"
    "s_branch Lret_c8_0_%=\n\t"
    "Lstub_c8_1_%=:\n\t"
    "s_call_b64 s[76:77], Lrare1_%=\n\t"
    "s_branch Lret_c8_1_%=\n\t"
    "Lstub_c8_2_%=:\n\t"
    "s_call_b64 s[76:77], Lrare2_%=\n\t"
    "s_branch Lret_c8_2_%=\n\t"
    "Lstub_c8_3_%=:\n\t"
    "s_call_b64 s[76:77], Lrare3_%=\n\t"
    "s_branch Lret_c8_3_%=\n\t"
    "Lstub_c8_4_%=:\n\t"
    "s_call_b64 s[76:77], Lrare4_%=\n\t"
    "s_branch Lret_c8_4_%=\n\t"
    "Lstub_c8_5_%=:\n\t"
    "s_call_b64 s[76:77], Lrare5_%=\n\t"
    "s_branch Lret_c8_5_%=\n\t"
    "Lstub_c9_0_%=:\n\t"
    "s_call_b64 s[76:77], Lrare0_%=\n\t"
    "s_branch Lret_c9_0_%=\n\t"
    "Lstub_c9_1_%=:\n\t"
    "s_call_b64 s[76:77], Lrare1_%=\n\t"
    "s_branch Lret_c9_1_%=\n\t"
    "Lstub_c9_2_%=:\n\t"
    "s_call_b64 s[76:77], Lrare2_%=\n\t"
    "s_branch Lret_c9_2_%=\n\t"
    "Lstub_c9_3_%=:\n\t"
    "s_call_b64 s[76:77], Lrare3_%=\n\t"
    "s_branch Lret_c9_3_%=\n\t"
    "Lstub_c9_4_%=:\n\t"
    "s_call_b64 s[76:77], Lrare4_%=\n\t"
    "s_branch Lret_c9_4_%=\n\t"
    "Lstub_c9_5_%=:\n\t"
    "s_call_b64 s[76:77], Lrare5_%=\n\t"
    "s_branch Lret_c9_5_%=\n\t"
    "Lstub_c10_0_%=:\n\t"
    "s_call_b64 s[76:77], Lrare0_%=\n\t"
    "s_branch Lret_c10_0_%=\n\t"
    "Lstub_c10_1_%=:\n\t"
    "s_call_b64 s[76:77], Lrare1_%=\n\t"
    "s_branch Lret_c10_1_%=\n\t"
    "Lstub_c10_2_%=:\n\t"
    "s_call_b64 s[76:77], Lrare2_%=\n\t"
    "s_branch Lret_c10_2_%=\n\t"
    "Lstub_c10_3_%=:\n\t"
    "s_call_b64 s[76:77], Lrare3_%=\n\t"
    "s_branch Lret_c10_3_%=\n\t"
    "Lstub_c10_4_%=:\n\t"
    "s_call_b64 s[76:77], Lrare4_%=\n\t"
    "s_branch Lret_c10_4_%=\n\t"
    "Lstub_c10_5_%=:\n\t"
    "s_call_b64 s[76:77], Lrare5_%=\n\t"
    "s_branch Lret_c10_5_%=\n\t"
    "Lstub_c11_0_%=:\n\t"
    "s_call_b64 s[76:77], Lrare0_%=\n\t"
    "s_branch Lret_c11_0_%=\n\t"
    "Lstub_c11_1_%=:\n\t"
    "s_call_b64 s[76:77], Lrare1_%=\n\t"
    "s_branch Lret_c11_1_%=\n\t"
    "Lstub_c11_2_%=:\n\t"
    "s_call_b64 s[76:77], Lrare2_%=\n\t"
    "s_branch Lret_c11_2_%=\n\t"
    "Lstub_c11_3_%=:\n\t"
    "s_call_b64 s[76:77], Lrare3_%=\n\t"
    "s_branch Lret_c11_3_%=\n\t"
    "Lstub_c11_4_%=:\n\t"
    "s_call_b64 s[76:77], Lrare4_%=\n\t"
    "s_branch Lret_c11_4_%=\n\t"
    "Lstub_c11_5_%=:\n\t"
    "s_call_b64 s[76:77], Lrare5_%=\n\t"
    "s_branch Lret_c11_5_%=\n\t"
    "Lstub_c12_0_%=:\n\t"
    "s_call_b64 s[76:77], Lrare0_%=\n\t"
    "s_branch Lret_c12_0_%=\n\t"
    "Lstub_c12_1_%=:\n\t"
    "s_call_b64 s[76:77], Lrare1_%=\n\t"
    "s_branch Lret_c12_1_%=\n\t"
    "Lstub_c12_2_%=:\n\t"
    "s_call_b64 s[76:77], Lrare2_%=\n\t"
    "s_branch Lret_c12_2_%=\n\t"
    "Lstub_c12_3_%=:\n\t"
    "s_call_b64 s[76:77], Lrare3_%=\n\t"
    "s_branch Lret_c12_3_%=\n\t"
    "Lstub_c12_4_%=:\n\t"
    "s_call_b64 s[76:77], Lrare4_%=\n\t"
    "s_branch Lret_c12_4_%=\n\t"
    "Lstub_c12_5_%=:\n\t"
    "s_call_b64 s[76:77], Lrare5_%=\n\t"
    "s_branch Lret_c12_5_%=\n\t"
    "Lstub_c13_0_%=:\n\t"
    "s_call_b64 s[76:77], Lrare0_%=\n\t"
    "s_branch Lret_c13_0_%=\n\t"
    "Lstub_c13_1_%=:\n\t"
    "s_call_b64 s[76:77], Lrare1_%=\n\t"
    "s_branch Lret_c13_1_%=\n\t"
    "Lstub_c13_2_%=:\n\t"
    "s_call_b64 s[76:77], Lrare2_%=\n\t"
    "s_branch Lret_c13_2_%=\n\t"
    "Lstub_c13_3_%=:\n\t"
    "s_call_b64 s[76:77], Lrare3_%=\n\t"
    "s_branch Lret_c13_3_%=\n\t"
    "Lstub_c13_4_%=:\n\t"
    "s_call_b64 s[76:77], Lrare4_%=\n\t"
    "s_branch Lret_c13_4_%=\n\t"
    "Lstub_c13_5_%=:\n\t"
    "s_call_b64 s[76:77], Lrare5_%=\n\t"
    "s_branch Lret_c13_5_%=\n\t"
    "Lstub_c14_0_%=:\n\t"
    "s_call_b64 s[76:77], Lrare0_%=\n\t"
    "s_branch Lret_c14_0_%=\n\t"
    "Lstub_c14_1_%=:\n\t"
    "s_call_b64 s[76:77], Lrare1_%=\n\t"
    "s_branch Lret_c14_1_%=\n\t"
    "Lstub_c14_2_%=:\n\t"
    "s_call_b64 s[76:77], Lrare2_%=\n\t"
    "s_branch Lret_c14_2_%=\n\t"
    "Lstub_c14_3_%=:\n\t"
    "s_call_b64 s[76:77], Lrare3_%=\n\t"
    "s_branch Lret_c14_3_%=\n\t"
    "Lstub_c14_4_%=:\n\t"
    "s_call_b64 s[76:77], Lrare4_%=\n\t"
    "s_branch Lret_c14_4_%=\n\t"
    "Lstub_c14_5_%=:\n\t"
    "s_call_b64 s[76:77], Lrare5_%=\n\t"
    "s_branch Lret_c14_5_%=\n\t"
    "Lstub_c15_0_%=:\n\t"
    "s_call_b64 s[76:77], Lrare0_%=\n\t"
    "s_branch Lret_c15_0_%=\n\t"
    "Lstub_c15_1_%=:\n\t"
    "s_call_b64 s[76:77], Lrare1_%=\n\t"
    "s_branch Lret_c15_1_%=\n\t"
    "Lstub_c15_2_%=:\n\t"
    "s_call_b64 s[76:77], Lrare2_%=\n\t"
    "s_branch Lret_c15_2_%=\n\t"
    "Lstub_c15_3_%=:\n\t"
    "s_call_b64 s[76:77], Lrare3_%=\n\t"
    "s_branch Lret_c15_3_%=\n\t"
    "Lstub_c15_4_%=:\n\t"
    "s_call_b64 s[76:77], Lrare4_%=\n\t"
    "s_branch Lret_c15_4_%=\n\t"
    "Lstub_c15_5_%=:\n\t"
    "s_call_b64 s[76:77], Lrare5_%=\n\t"
    "s_branch Lret_c15_5_%=\n\t"
    "Lpace1_%=:\n\t"
    "s_memrealtime s[98:99]\n\t"
    "s_waitcnt lgkmcnt(0)\n\t"
    "s_sub_u32 s19, s18, s98\n\t"
    "s_cmp_gt_i32 s19, 0\n\t"
    "s_cbranch_scc1 Lpace1_%=\n\t"
    "s_branch Lpaced1_%=\n\t"
    "Lpace2_%=:\n\t"
    "s_memrealtime s[98:99]\n\t"
    "s_waitcnt lgkmcnt(0)\n\t"
    "s_sub_u32 s19, s18, s98\n\t"
    "s_cmp_gt_i32 s19, 0\n\t"
    "s_cbranch_scc1 Lpace2_%=\n\t"
    "s_branch Lpaced2_%=\n\t"
    "Lpace3_%=:\n\t"
    "s_memrealtime s[98:99]\n\t"
    "s_waitcnt lgkmcnt(0)\n\t"
    "s_sub_u32 s19, s18, s98\n\t"
    "s_cmp_gt_i32 s19, 0\n\t"
    "s_cbranch_scc1 Lpace3_%=\n\t"
    "s_branch Lpaced3_%=\n\t"
    "Lpace4_%=:\n\t"
    "s_memrealtime s[98:99]\n\t"
    "s_waitcnt lgkmcnt(0)\n\t"
    "s_sub_u32 s19, s18, s98\n\t"
    "s_cmp_gt_i32 s19, 0\n\t"
    "s_cbranch_scc1 Lpace4_%=\n\t"
    "s_branch Lpaced4_%=\n\t"
    "Lpace5_%=:\n\t"
    "s_memrealtime s[98:99]\n\t"
    "s_waitcnt lgkmcnt(0)\n\t"
    "s_sub_u32 s19, s18, s98\n\t"
    "s_cmp_gt_i32 s19, 0\n\t"
    "s_cbranch_scc1 Lpace5_%=\n\t"
    "s_branch Lpaced5_%=\n\t"
    "Lpace6_%=:\n\t"
    "s_memrealtime s[98:99]\n\t"
    "s_waitcnt lgkmcnt(0)\n\t"
    "s_sub_u32 s19, s18, s98\n\t"
    "s_cmp_gt_i32 s19, 0\n\t"
    "s_cbranch_scc1 Lpace6_%=\n\t"
    "s_branch Lpaced6_%=\n\t"
    "Lpace7_%=:\n\t"
    "s_memrealtime s[98:99]\n\t"
    "s_waitcnt lgkmcnt(0)\n\t"
    "s_sub_u32 s19, s18, s98\n\t"
    "s_cmp_gt_i32 s19, 0\n\t"
    "s_cbranch_scc1 Lpace7_%=\n\t"
    "s_branch Lpaced7_%=\n\t"
    "Lpace8_%=:\n\t"
    "s_memrealtime s[98:99]\n\t"
    "s_waitcnt lgkmcnt(0)\n\t"
    "s_sub_u32 s19, s18, s98\n\t"
    "s_cmp_gt_i32 s19, 0\n\t"
    "s_cbranch_scc1 Lpace8_%=\n\t"
    "s_branch Lpaced8_%=\n\t"
    "Lpace9_%=:\n\t"
    "s_memrealtime s[98:99]\n\t"
    "s_waitcnt lgkmcnt(0)\n\t"
    "s_sub_u32 s19, s18, s98\n\t"
    "s_cmp_gt_i32 s19, 0\n\t"
    "s_cbranch_scc1 Lpace9_%=\n\t"
    "s_branch Lpaced9_%=\n\t"
    "Lpace10_%=:\n\t"
    "s_memrealtime s[98:99]\n\t"
    "s_waitcnt lgkmcnt(0)\n\t"
    "s_sub_u32 s19, s18, s98\n\t"
    "s_cmp_gt_i32 s19, 0\n\t"
    "s_cbranch_scc1 Lpace10_%=\n\t"
    "s_branch Lpaced10_%=\n\t"
    "Lpace11_%=:\n\t"
    "s_memrealtime s[98:99]\n\t"
    "s_waitcnt lgkmcnt(0)\n\t"
    "s_sub_u32 s19, s18, s98\n\t"
    "s_cmp_gt_i32 s19, 0\n\t"
    "s_cbranch_scc1 Lpace11_%=\n\t"
    "s_branch Lpaced11_%=\n\t"
    "Lpace12_%=:\n\t"
    "s_memrealtime s[98:99]\n\t"
    "s_waitcnt lgkmcnt(0)\n\t"
    "s_sub_u32 s19, s18, s98\n\t"
    "s_cmp_gt_i32 s19, 0\n\t"
    "s_cbranch_scc1 Lpace12_%=\n\t"
    "s_branch Lpaced12_%=\n\t"
    "Lpace13_%=:\n\t"
    "s_memrealtime s[98:99]\n\t"
    "s_waitcnt lgkmcnt(0)\n\t"
    "s_sub_u32 s19, s18, s98\n\t"
    "s_cmp_gt_i32 s19, 0\n\t"
    "s_cbranch_scc1 Lpace13_%=\n\t"
    "s_branch Lpaced13_%=\n\t"
    "Lrare0_%=:\n\t"
    "s_nop 7\n\t"
    "v_max3_f32 v232, v160, v161, v162\n\t"
    "v_max3_f32 v232, v232, v163, v164\n\t"
    "v_max3_f32 v232, v232, v165, v166\n\t"
    "v_max_f32 v232, v232, v167\n\t"
    "v_mov_b32 v233, v232\n\t"
    "s_nop 1\n\t"
    "v_permlane32_swap_b32 v233, v232\n\t"
    "v_max_f32 v232, v232, v233\n\t"
    "v_mov_b32 v233, v232\n\t"
    "s_nop 1\n\t"
    "v_permlane16_swap_b32 v233, v232\n\t"
    "v_max_f32 v232, v232, v233\n\t"
    "v_max_f32 v232, s74, v232\n\t"
    "v_sub_f32 v234, 0, v232\n\t"
    "v_min_f32 v234, 0x42c80000, v234\n\t"
    "v_exp_f32 v234, v234\n\t"
    "v_sub_f32 v24, v24, v232\n\t"
    "v_sub_f32 v25, v25, v232\n\t"
    "v_sub_f32 v26, v26, v232\n\t"
    "v_sub_f32 v27, v27, v232\n\t"
    "v_mul_f32 v48, v48, v234\n\t"
    "v_sub_f32 v160, v160, v232\n\t"
    "v_sub_f32 v161, v161, v232\n\t"
    "v_sub_f32 v162, v162, v232\n\t"
    "v_sub_f32 v163, v163, v232\n\t"
    "v_sub_f32 v164, v164, v232\n\t"
    "v_sub_f32 v165, v165, v232\n\t"
    "v_sub_f32 v166, v166, v232\n\t"
    "v_sub_f32 v167, v167, v232\n\t"
    "v_exp_f32 v236, v160\n\t"
    "v_exp_f32 v237, v161\n\t"
    "v_exp_f32 v238, v162\n\t"
    "v_exp_f32 v239, v163\n\t"
    "v_exp_f32 v240, v164\n\t"
    "v_exp_f32 v241, v165\n\t"
    "v_exp_f32 v242, v166\n\t"
    "v_exp_f32 v243, v167\n\t"
    "v_cvt_pk_bf16_f32 v184, v236, v237\n\t"
    "v_cvt_pk_bf16_f32 v185, v238, v239\n\t"
    "v_cvt_pk_bf16_f32 v186, v240, v241\n\t"
    "v_cvt_pk_bf16_f32 v187, v242, v243\n\t"
    "s_nop 1\n\t"
    "v_mfma_f32_16x16x32_bf16 v[196:199], v[54:57], v[184:187], 0\n\t"
    "v_accvgpr_read_b32 v244, a0\n\t"
    "v_mul_f32 v244, v244, v234\n\t"
    "v_accvgpr_write_b32 a0, v244\n\t"
    "v_accvgpr_read_b32 v245, a1\n\t"
    "v_mul_f32 v245, v245, v234\n\t"
    "v_accvgpr_write_b32 a1, v245\n\t"
    "v_accvgpr_read_b32 v246, a2\n\t"
    "v_mul_f32 v246, v246, v234\n\t"
    "v_accvgpr_write_b32 a2, v246\n\t"
    "v_accvgpr_read_b32 v247, a3\n\t"
    "v_mul_f32 v247, v247, v234\n\t"
    "v_accvgpr_write_b32 a3, v247\n\t"
    "v_accvgpr_read_b32 v248, a4\n\t"
    "v_mul_f32 v248, v248, v234\n\t"
    "v_accvgpr_write_b32 a4, v248\n\t"
    "v_accvgpr_read_b32 v249, a5\n\t"
    "v_mul_f32 v249, v249, v234\n\t"
    "v_accvgpr_write_b32 a5, v249\n\t"
    "v_accvgpr_read_b32 v250, a6\n\t"
    "v_mul_f32 v250, v250, v234\n\t"
    "v_accvgpr_write_b32 a6, v250\n\t"
    "v_accvgpr_read_b32 v251, a7\n\t"
    "v_mul_f32 v251, v251, v234\n\t"
    "v_accvgpr_write_b32 a7, v251\n\t"
    "v_accvgpr_read_b32 v244, a8\n\t"
    "v_mul_f32 v244, v244, v234\n\t"
    "v_accvgpr_write_b32 a8, v244\n\t"
    "v_accvgpr_read_b32 v245, a9\n\t"
    "v_mul_f32 v245, v245, v234\n\t"
    "v_accvgpr_write_b32 a9, v245\n\t"
    "v_accvgpr_read_b32 v246, a10\n\t"
    "v_mul_f32 v246, v246, v234\n\t"
    "v_accvgpr_write_b32 a10, v246\n\t"
    "v_accvgpr_read_b32 v247, a11\n\t"
    "v_mul_f32 v247, v247, v234\n\t"
    "v_accvgpr_write_b32 a11, v247\n\t"
    "v_accvgpr_read_b32 v248, a12\n\t"
    "v_mul_f32 v248, v248, v234\n\t"
    "v_accvgpr_write_b32 a12, v248\n\t"
    "v_accvgpr_read_b32 v249, a13\n\t"
    "v_mul_f32 v249, v249, v234\n\t"
    "v_accvgpr_write_b32 a13, v249\n\t"
    "v_accvgpr_read_b32 v250, a14\n\t"
    "v_mul_f32 v250, v250, v234\n\t"
    "v_accvgpr_write_b32 a14, v250\n\t"
    "v_accvgpr_read_b32 v251, a15\n\t"
    "v_mul_f32 v251, v251, v234\n\t"
    "v_accvgpr_write_b32 a15, v251\n\t"
    "v_accvgpr_read_b32 v244, a16\n\t"
    "v_mul_f32 v244, v244, v234\n\t"
    "v_accvgpr_write_b32 a16, v244\n\t"
    "v_accvgpr_read_b32 v245, a17\n\t"
    "v_mul_f32 v245, v245, v234\n\t"
    "v_accvgpr_write_b32 a17, v245\n\t"
    "v_accvgpr_read_b32 v246, a18\n\t"
    "v_mul_f32 v246, v246, v234\n\t"
    "v_accvgpr_write_b32 a18, v246\n\t"
    "v_accvgpr_read_b32 v247, a19\n\t"
    "v_mul_f32 v247, v247, v234\n\t"
    "v_accvgpr_write_b32 a19, v247\n\t"
    "v_accvgpr_read_b32 v248, a20\n\t"
    "v_mul_f32 v248, v248, v234\n\t"
    "v_accvgpr_write_b32 a20, v248\n\t"
    "v_accvgpr_read_b32 v249, a21\n\t"
    "v_mul_f32 v249, v249, v234\n\t"
    "v_accvgpr_write_b32 a21, v249\n\t"
    "v_accvgpr_read_b32 v250, a22\n\t"
    "v_mul_f32 v250, v250, v234\n\t"
    "v_accvgpr_write_b32 a22, v250\n\t"
    "v_accvgpr_read_b32 v251, a23\n\t"
    "v_mul_f32 v251, v251, v234\n\t"
    "v_accvgpr_write_b32 a23, v251\n\t"
    "v_accvgpr_read_b32 v244, a24\n\t"
    "v_mul_f32 v244, v244, v234\n\t"
    "v_accvgpr_write_b32 a24, v244\n\t"
    "v_accvgpr_read_b32 v245, a25\n\t"
    "v_mul_f32 v245, v245, v234\n\t"
    "v_accvgpr_write_b32 a25, v245\n\t"
    "v_accvgpr_read_b32 v246, a26\n\t"
    "v_mul_f32 v246, v246, v234\n\t"
    "v_accvgpr_write_b32 a26, v246\n\t"
    "v_accvgpr_read_b32 v247, a27\n\t"
    "v_mul_f32 v247, v247, v234\n\t"
    "v_accvgpr_write_b32 a27, v247\n\t"
    "v_accvgpr_read_b32 v248, a28\n\t"
    "v_mul_f32 v248, v248, v234\n\t"
    "v_accvgpr_write_b32 a28, v248\n\t"
    "v_accvgpr_read_b32 v249, a29\n\t"
    "v_mul_f32 v249, v249, v234\n\t"
    "v_accvgpr_write_b32 a29, v249\n\t"
    "v_accvgpr_read_b32 v250, a30\n\t"
    "v_mul_f32 v250, v250, v234\n\t"
    "v_accvgpr_write_b32 a30, v250\n\t"
    "v_accvgpr_read_b32 v251, a31\n\t"
    "v_mul_f32 v251, v251, v234\n\t"
    "v_accvgpr_write_b32 a31, v251\n\t"
    "s_nop 3\n\t"
    "s_setpc_b64 s[76:77]\n\t"
    "Lrare1_%=:\n\t"
    "s_nop 7\n\t"
    "v_max3_f32 v232, v168, v169, v170\n\t"
    "v_max3_f32 v232, v232, v171, v172\n\t"
    "v_max3_f32 v232, v232, v173, v174\n\t"
    "v_max_f32 v232, v232, v175\n\t"
    "v_mov_b32 v233, v232\n\t"
    "s_nop 1\n\t"
    "v_permlane32_swap_b32 v233, v232\n\t"
    "v_max_f32 v232, v232, v233\n\t"
    "v_mov_b32 v233, v232\n\t"
    "s_nop 1\n\t"
    "v_permlane16_swap_b32 v233, v232\n\t"
    "v_max_f32 v232, v232, v233\n\t"
    "v_max_f32 v232, s74, v232\n\t"
    "v_sub_f32 v234, 0, v232\n\t"
    "v_min_f32 v234, 0x42c80000, v234\n\t"
    "v_exp_f32 v234, v234\n\t"
    "v_sub_f32 v28, v28, v232\n\t"
    "v_sub_f32 v29, v29, v232\n\t"
    "v_sub_f32 v30, v30, v232\n\t"
    "v_sub_f32 v31, v31, v232\n\t"
    "v_mul_f32 v49, v49, v234\n\t"
    "v_sub_f32 v168, v168, v232\n\t"
    "v_sub_f32 v169, v169, v232\n\t"
    "v_sub_f32 v170, v170, v232\n\t"
    "v_sub_f32 v171, v171, v232\n\t"
    "v_sub_f32 v172, v172, v232\n\t"
    "v_sub_f32 v173, v173, v232\n\t"
    "v_sub_f32 v174, v174, v232\n\t"
    "v_sub_f32 v175, v175, v232\n\t"
    "v_exp_f32 v236, v168\n\t"
    "v_exp_f32 v237, v169\n\t"
    "v_exp_f32 v238, v170\n\t"
    "v_exp_f32 v239, v171\n\t"
    "v_exp_f32 v240, v172\n\t"
    "v_exp_f32 v241, v173\n\t"
    "v_exp_f32 v242, v174\n\t"
    "v_exp_f32 v243, v175\n\t"
    "v_cvt_pk_bf16_f32 v188, v236, v237\n\t"
    "v_cvt_pk_bf16_f32 v189, v238, v239\n\t"
    "v_cvt_pk_bf16_f32 v190, v240, v241\n\t"
    "v_cvt_pk_bf16_f32 v191, v242, v243\n\t"
    "s_nop 1\n\t"
    "v_mfma_f32_16x16x32_bf16 v[196:199], v[54:57], v[188:191], 0\n\t"
    "v_accvgpr_read_b32 v244, a32\n\t"
    "v_mul_f32 v244, v244, v234\n\t"
    "v_accvgpr_write_b32 a32, v244\n\t"
    "v_accvgpr_read_b32 v245, a33\n\t"
    "v_mul_f32 v245, v245, v234\n\t"
    "v_accvgpr_write_b32 a33, v245\n\t"
    "v_accvgpr_read_b32 v246, a34\n\t"
    "v_mul_f32 v246, v246, v234\n\t"
    "v_accvgpr_write_b32 a34, v246\n\t"
    "v_accvgpr_read_b32 v247, a35\n\t"
    "v_mul_f32 v247, v247, v234\n\t"
    "v_accvgpr_write_b32 a35, v247\n\t"
    "v_accvgpr_read_b32 v248, a36\n\t"
    "v_mul_f32 v248, v248, v234\n\t"
    "v_accvgpr_write_b32 a36, v248\n\t"
    "v_accvgpr_read_b32 v249, a37\n\t"
    "v_mul_f32 v249, v249, v234\n\t"
    "v_accvgpr_write_b32 a37, v249\n\t"
    "v_accvgpr_read_b32 v250, a38\n\t"
    "v_mul_f32 v250, v250, v234\n\t"
    "v_accvgpr_write_b32 a38, v250\n\t"
    "v_accvgpr_read_b32 v251, a39\n\t"
    "v_mul_f32 v251, v251, v234\n\t"
    "v_accvgpr_write_b32 a39, v251\n\t"
    "v_accvgpr_read_b32 v244, a40\n\t"
    "v_mul_f32 v244, v244, v234\n\t"
    "v_accvgpr_write_b32 a40, v244\n\t"
    "v_accvgpr_read_b32 v245, a41\n\t"
    "v_mul_f32 v245, v245, v234\n\t"
    "v_accvgpr_write_b32 a41, v245\n\t"
    "v_accvgpr_read_b32 v246, a42\n\t"
    "v_mul_f32 v246, v246, v234\n\t"
    "v_accvgpr_write_b32 a42, v246\n\t"
    "v_accvgpr_read_b32 v247, a43\n\t"
    "v_mul_f32 v247, v247, v234\n\t"
    "v_accvgpr_write_b32 a43, v247\n\t"
    "v_accvgpr_read_b32 v248, a44\n\t"
    "v_mul_f32 v248, v248, v234\n\t"
    "v_accvgpr_write_b32 a44, v248\n\t"
    "v_accvgpr_read_b32 v249, a45\n\t"
    "v_mul_f32 v249, v249, v234\n\t"
    "v_accvgpr_write_b32 a45, v249\n\t"
    "v_accvgpr_read_b32 v250, a46\n\t"
    "v_mul_f32 v250, v250, v234\n\t"
    "v_accvgpr_write_b32 a46, v250\n\t"
    "v_accvgpr_read_b32 v251, a47\n\t"
    "v_mul_f32 v251, v251, v234\n\t"
    "v_accvgpr_write_b32 a47, v251\n\t"
    "v_accvgpr_read_b32 v244, a48\n\t"
    "v_mul_f32 v244, v244, v234\n\t"
    "v_accvgpr_write_b32 a48, v244\n\t"
    "v_accvgpr_read_b32 v245, a49\n\t"
    "v_mul_f32 v245, v245, v234\n\t"
    "v_accvgpr_write_b32 a49, v245\n\t"
    "v_accvgpr_read_b32 v246, a50\n\t"
    "v_mul_f32 v246, v246, v234\n\t"
    "v_accvgpr_write_b32 a50, v246\n\t"
    "v_accvgpr_read_b32 v247, a51\n\t"
    "v_mul_f32 v247, v247, v234\n\t"
    "v_accvgpr_write_b32 a51, v247\n\t"
    "v_accvgpr_read_b32 v248, a52\n\t"
    "v_mul_f32 v248, v248, v234\n\t"
    "v_accvgpr_write_b32 a52, v248\n\t"
    "v_accvgpr_read_b32 v249, a53\n\t"
    "v_mul_f32 v249, v249, v234\n\t"
    "v_accvgpr_write_b32 a53, v249\n\t"
    "v_accvgpr_read_b32 v250, a54\n\t"
    "v_mul_f32 v250, v250, v234\n\t"
    "v_accvgpr_write_b32 a54, v250\n\t"
    "v_accvgpr_read_b32 v251, a55\n\t"
    "v_mul_f32 v251, v251, v234\n\t"
    "v_accvgpr_write_b32 a55, v251\n\t"
    "v_accvgpr_read_b32 v244, a56\n\t"
    "v_mul_f32 v244, v244, v234\n\t"
    "v_accvgpr_write_b32 a56, v244\n\t"
    "v_accvgpr_read_b32 v245, a57\n\t"
    "v_mul_f32 v245, v245, v234\n\t"
    "v_accvgpr_write_b32 a57, v245\n\t"
    "v_accvgpr_read_b32 v246, a58\n\t"
    "v_mul_f32 v246, v246, v234\n\t"
    "v_accvgpr_write_b32 a58, v246\n\t"
    "v_accvgpr_read_b32 v247, a59\n\t"
    "v_mul_f32 v247, v247, v234\n\t"
    "v_accvgpr_write_b32 a59, v247\n\t"
    "v_accvgpr_read_b32 v248, a60\n\t"
    "v_mul_f32 v248, v248, v234\n\t"
    "v_accvgpr_write_b32 a60, v248\n\t"
    "v_accvgpr_read_b32 v249, a61\n\t"
    "v_mul_f32 v249, v249, v234\n\t"
    "v_accvgpr_write_b32 a61, v249\n\t"
    "v_accvgpr_read_b32 v250, a62\n\t"
    "v_mul_f32 v250, v250, v234\n\t"
    "v_accvgpr_write_b32 a62, v250\n\t"
    "v_accvgpr_read_b32 v251, a63\n\t"
    "v_mul_f32 v251, v251, v234\n\t"
    "v_accvgpr_write_b32 a63, v251\n\t"
    "s_nop 3\n\t"
    "s_setpc_b64 s[76:77]\n\t"
    "Lrare2_%=:\n\t"
    "s_nop 7\n\t"
    "v_max3_f32 v232, v176, v177, v178\n\t"
    "v_max3_f32 v232, v232, v179, v180\n\t"
    "v_max3_f32 v232, v232, v181, v182\n\t"
    "v_max_f32 v232, v232, v183\n\t"
    "v_mov_b32 v233, v232\n\t"
    "s_nop 1\n\t"
    "v_permlane32_swap_b32 v233, v232\n\t"
    "v_max_f32 v232, v232, v233\n\t"
    "v_mov_b32 v233, v232\n\t"
    "s_nop 1\n\t"
    "v_permlane16_swap_b32 v233, v232\n\t"
    "v_max_f32 v232, v232, v233\n\t"
    "v_max_f32 v232, s74, v232\n\t"
    "v_sub_f32 v234, 0, v232\n\t"
    "v_min_f32 v234, 0x42c80000, v234\n\t"
    "v_exp_f32 v234, v234\n\t"
    "v_sub_f32 v32, v32, v232\n\t"
    "v_sub_f32 v33, v33, v232\n\t"
    "v_sub_f32 v34, v34, v232\n\t"
    "v_sub_f32 v35, v35, v232\n\t"
    "v_mul_f32 v50, v50, v234\n\t"
    "v_sub_f32 v176, v176, v232\n\t"
    "v_sub_f32 v177, v177, v232\n\t"
    "v_sub_f32 v178, v178, v232\n\t"
    "v_sub_f32 v179, v179, v232\n\t"
    "v_sub_f32 v180, v180, v232\n\t"
    "v_sub_f32 v181, v181, v232\n\t"
    "v_sub_f32 v182, v182, v232\n\t"
    "v_sub_f32 v183, v183, v232\n\t"
    "v_exp_f32 v236, v176\n\t"
    "v_exp_f32 v237, v177\n\t"
    "v_exp_f32 v238, v178\n\t"
    "v_exp_f32 v239, v179\n\t"
    "v_exp_f32 v240, v180\n\t"
    "v_exp_f32 v241, v181\n\t"
    "v_exp_f32 v242, v182\n\t"
    "v_exp_f32 v243, v183\n\t"
    "v_cvt_pk_bf16_f32 v192, v236, v237\n\t"
    "v_cvt_pk_bf16_f32 v193, v238, v239\n\t"
    "v_cvt_pk_bf16_f32 v194, v240, v241\n\t"
    "v_cvt_pk_bf16_f32 v195, v242, v243\n\t"
    "s_nop 1\n\t"
    "v_mfma_f32_16x16x32_bf16 v[196:199], v[54:57], v[192:195], 0\n\t"
    "v_accvgpr_read_b32 v244, a64\n\t"
    "v_mul_f32 v244, v244, v234\n\t"
    "v_accvgpr_write_b32 a64, v244\n\t"
    "v_accvgpr_read_b32 v245, a65\n\t"
    "v_mul_f32 v245, v245, v234\n\t"
    "v_accvgpr_write_b32 a65, v245\n\t"
    "v_accvgpr_read_b32 v246, a66\n\t"
    "v_mul_f32 v246, v246, v234\n\t"
    "v_accvgpr_write_b32 a66, v246\n\t"
    "v_accvgpr_read_b32 v247, a67\n\t"
    "v_mul_f32 v247, v247, v234\n\t"
    "v_accvgpr_write_b32 a67, v247\n\t"
    "v_accvgpr_read_b32 v248, a68\n\t"
    "v_mul_f32 v248, v248, v234\n\t"
    "v_accvgpr_write_b32 a68, v248\n\t"
    "v_accvgpr_read_b32 v249, a69\n\t"
    "v_mul_f32 v249, v249, v234\n\t"
    "v_accvgpr_write_b32 a69, v249\n\t"
    "v_accvgpr_read_b32 v250, a70\n\t"
    "v_mul_f32 v250, v250, v234\n\t"
    "v_accvgpr_write_b32 a70, v250\n\t"
    "v_accvgpr_read_b32 v251, a71\n\t"
    "v_mul_f32 v251, v251, v234\n\t"
    "v_accvgpr_write_b32 a71, v251\n\t"
    "v_accvgpr_read_b32 v244, a72\n\t"
    "v_mul_f32 v244, v244, v234\n\t"
    "v_accvgpr_write_b32 a72, v244\n\t"
    "v_accvgpr_read_b32 v245, a73\n\t"
    "v_mul_f32 v245, v245, v234\n\t"
    "v_accvgpr_write_b32 a73, v245\n\t"
    "v_accvgpr_read_b32 v246, a74\n\t"
    "v_mul_f32 v246, v246, v234\n\t"
    "v_accvgpr_write_b32 a74, v246\n\t"
    "v_accvgpr_read_b32 v247, a75\n\t"
    "v_mul_f32 v247, v247, v234\n\t"
    "v_accvgpr_write_b32 a75, v247\n\t"
    "v_accvgpr_read_b32 v248, a76\n\t"
    "v_mul_f32 v248, v248, v234\n\t"
    "v_accvgpr_write_b32 a76, v248\n\t"
    "v_accvgpr_read_b32 v249, a77\n\t"
    "v_mul_f32 v249, v249, v234\n\t"
    "v_accvgpr_write_b32 a77, v249\n\t"
    "v_accvgpr_read_b32 v250, a78\n\t"
    "v_mul_f32 v250, v250, v234\n\t"
    "v_accvgpr_write_b32 a78, v250\n\t"
    "v_accvgpr_read_b32 v251, a79\n\t"
    "v_mul_f32 v251, v251, v234\n\t"
    "v_accvgpr_write_b32 a79, v251\n\t"
    "v_accvgpr_read_b32 v244, a80\n\t"
    "v_mul_f32 v244, v244, v234\n\t"
    "v_accvgpr_write_b32 a80, v244\n\t"
    "v_accvgpr_read_b32 v245, a81\n\t"
    "v_mul_f32 v245, v245, v234\n\t"
    "v_accvgpr_write_b32 a81, v245\n\t"
    "v_accvgpr_read_b32 v246, a82\n\t"
    "v_mul_f32 v246, v246, v234\n\t"
    "v_accvgpr_write_b32 a82, v246\n\t"
    "v_accvgpr_read_b32 v247, a83\n\t"
    "v_mul_f32 v247, v247, v234\n\t"
    "v_accvgpr_write_b32 a83, v247\n\t"
    "v_accvgpr_read_b32 v248, a84\n\t"
    "v_mul_f32 v248, v248, v234\n\t"
    "v_accvgpr_write_b32 a84, v248\n\t"
    "v_accvgpr_read_b32 v249, a85\n\t"
    "v_mul_f32 v249, v249, v234\n\t"
    "v_accvgpr_write_b32 a85, v249\n\t"
    "v_accvgpr_read_b32 v250, a86\n\t"
    "v_mul_f32 v250, v250, v234\n\t"
    "v_accvgpr_write_b32 a86, v250\n\t"
    "v_accvgpr_read_b32 v251, a87\n\t"
    "v_mul_f32 v251, v251, v234\n\t"
    "v_accvgpr_write_b32 a87, v251\n\t"
    "v_accvgpr_read_b32 v244, a88\n\t"
    "v_mul_f32 v244, v244, v234\n\t"
    "v_accvgpr_write_b32 a88, v244\n\t"
    "v_accvgpr_read_b32 v245, a89\n\t"
    "v_mul_f32 v245, v245, v234\n\t"
    "v_accvgpr_write_b32 a89, v245\n\t"
    "v_accvgpr_read_b32 v246, a90\n\t"
    "v_mul_f32 v246, v246, v234\n\t"
    "v_accvgpr_write_b32 a90, v246\n\t"
    "v_accvgpr_read_b32 v247, a91\n\t"
    "v_mul_f32 v247, v247, v234\n\t"
    "v_accvgpr_write_b32 a91, v247\n\t"
    "v_accvgpr_read_b32 v248, a92\n\t"
    "v_mul_f32 v248, v248, v234\n\t"
    "v_accvgpr_write_b32 a92, v248\n\t"
    "v_accvgpr_read_b32 v249, a93\n\t"
    "v_mul_f32 v249, v249, v234\n\t"
    "v_accvgpr_write_b32 a93, v249\n\t"
    "v_accvgpr_read_b32 v250, a94\n\t"
    "v_mul_f32 v250, v250, v234\n\t"
    "v_accvgpr_write_b32 a94, v250\n\t"
    "v_accvgpr_read_b32 v251, a95\n\t"
    "v_mul_f32 v251, v251, v234\n\t"
    "v_accvgpr_write_b32 a95, v251\n\t"
    "s_nop 3\n\t"
    "s_setpc_b64 s[76:77]\n\t"
    "Lrare3_%=:\n\t"
    "s_nop 7\n\t"
    "v_max3_f32 v232, v160, v161, v162\n\t"
    "v_max3_f32 v232, v232, v163, v164\n\t"
    "v_max3_f32 v232, v232, v165, v166\n\t"
    "v_max_f32 v232, v232, v167\n\t"
    "v_mov_b32 v233, v232\n\t"
    "s_nop 1\n\t"
    "v_permlane32_swap_b32 v233, v232\n\t"
    "v_max_f32 v232, v232, v233\n\t"
    "v_mov_b32 v233, v232\n\t"
    "s_nop 1\n\t"
    "v_permlane16_swap_b32 v233, v232\n\t"
    "v_max_f32 v232, v232, v233\n\t"
    "v_max_f32 v232, s74, v232\n\t"
    "v_sub_f32 v234, 0, v232\n\t"
    "v_min_f32 v234, 0x42c80000, v234\n\t"
    "v_exp_f32 v234, v234\n\t"
    "v_sub_f32 v36, v36, v232\n\t"
    "v_sub_f32 v37, v37, v232\n\t"
    "v_sub_f32 v38, v38, v232\n\t"
    "v_sub_f32 v39, v39, v232\n\t"
    "v_mul_f32 v51, v51, v234\n\t"
    "v_sub_f32 v160, v160, v232\n\t"
    "v_sub_f32 v161, v161, v232\n\t"
    "v_sub_f32 v162, v162, v232\n\t"
    "v_sub_f32 v163, v163, v232\n\t"
    "v_sub_f32 v164, v164, v232\n\t"
    "v_sub_f32 v165, v165, v232\n\t"
    "v_sub_f32 v166, v166, v232\n\t"
    "v_sub_f32 v167, v167, v232\n\t"
    "v_exp_f32 v236, v160\n\t"
    "v_exp_f32 v237, v161\n\t"
    "v_exp_f32 v238, v162\n\t"
    "v_exp_f32 v239, v163\n\t"
    "v_exp_f32 v240, v164\n\t"
    "v_exp_f32 v241, v165\n\t"
    "v_exp_f32 v242, v166\n\t"
    "v_exp_f32 v243, v167\n\t"
    "v_cvt_pk_bf16_f32 v184, v236, v237\n\t"
    "v_cvt_pk_bf16_f32 v185, v238, v239\n\t"
    "v_cvt_pk_bf16_f32 v186, v240, v241\n\t"
    "v_cvt_pk_bf16_f32 v187, v242, v243\n\t"
    "s_nop 1\n\t"
    "v_mfma_f32_16x16x32_bf16 v[196:199], v[54:57], v[184:187], 0\n\t"
    "v_accvgpr_read_b32 v244, a96\n\t"
    "v_mul_f32 v244, v244, v234\n\t"
    "v_accvgpr_write_b32 a96, v244\n\t"
    "v_accvgpr_read_b32 v245, a97\n\t"
    "v_mul_f32 v245, v245, v234\n\t"
    "v_accvgpr_write_b32 a97, v245\n\t"
    "v_accvgpr_read_b32 v246, a98\n\t"
    "v_mul_f32 v246, v246, v234\n\t"
    "v_accvgpr_write_b32 a98, v246\n\t"
    "v_accvgpr_read_b32 v247, a99\n\t"
    "v_mul_f32 v247, v247, v234\n\t"
    "v_accvgpr_write_b32 a99, v247\n\t"
    "v_accvgpr_read_b32 v248, a100\n\t"
    "v_mul_f32 v248, v248, v234\n\t"
    "v_accvgpr_write_b32 a100, v248\n\t"
    "v_accvgpr_read_b32 v249, a101\n\t"
    "v_mul_f32 v249, v249, v234\n\t"
    "v_accvgpr_write_b32 a101, v249\n\t"
    "v_accvgpr_read_b32 v250, a102\n\t"
    "v_mul_f32 v250, v250, v234\n\t"
    "v_accvgpr_write_b32 a102, v250\n\t"
    "v_accvgpr_read_b32 v251, a103\n\t"
    "v_mul_f32 v251, v251, v234\n\t"
    "v_accvgpr_write_b32 a103, v251\n\t"
    "v_accvgpr_read_b32 v244, a104\n\t"
    "v_mul_f32 v244, v244, v234\n\t"
    "v_accvgpr_write_b32 a104, v244\n\t"
    "v_accvgpr_read_b32 v245, a105\n\t"
    "v_mul_f32 v245, v245, v234\n\t"
    "v_accvgpr_write_b32 a105, v245\n\t"
    "v_accvgpr_read_b32 v246, a106\n\t"
    "v_mul_f32 v246, v246, v234\n\t"
    "v_accvgpr_write_b32 a106, v246\n\t"
    "v_accvgpr_read_b32 v247, a107\n\t"
    "v_mul_f32 v247, v247, v234\n\t"
    "v_accvgpr_write_b32 a107, v247\n\t"
    "v_accvgpr_read_b32 v248, a108\n\t"
    "v_mul_f32 v248, v248, v234\n\t"
    "v_accvgpr_write_b32 a108, v248\n\t"
    "v_accvgpr_read_b32 v249, a109\n\t"
    "v_mul_f32 v249, v249, v234\n\t"
    "v_accvgpr_write_b32 a109, v249\n\t"
    "v_accvgpr_read_b32 v250, a110\n\t"
    "v_mul_f32 v250, v250, v234\n\t"
    "v_accvgpr_write_b32 a110, v250\n\t"
    "v_accvgpr_read_b32 v251, a111\n\t"
    "v_mul_f32 v251, v251, v234\n\t"
    "v_accvgpr_write_b32 a111, v251\n\t"
    "v_accvgpr_read_b32 v244, a112\n\t"
    "v_mul_f32 v244, v244, v234\n\t"
    "v_accvgpr_write_b32 a112, v244\n\t"
    "v_accvgpr_read_b32 v245, a113\n\t"
    "v_mul_f32 v245, v245, v234\n\t"
    "v_accvgpr_write_b32 a113, v245\n\t"
    "v_accvgpr_read_b32 v246, a114\n\t"
    "v_mul_f32 v246, v246, v234\n\t"
    "v_accvgpr_write_b32 a114, v246\n\t"
    "v_accvgpr_read_b32 v247, a115\n\t"
    "v_mul_f32 v247, v247, v234\n\t"
    "v_accvgpr_write_b32 a115, v247\n\t"
    "v_accvgpr_read_b32 v248, a116\n\t"
    "v_mul_f32 v248, v248, v234\n\t"
    "v_accvgpr_write_b32 a116, v248\n\t"
    "v_accvgpr_read_b32 v249, a117\n\t"
    "v_mul_f32 v249, v249, v234\n\t"
    "v_accvgpr_write_b32 a117, v249\n\t"
    "v_accvgpr_read_b32 v250, a118\n\t"
    "v_mul_f32 v250, v250, v234\n\t"
    "v_accvgpr_write_b32 a118, v250\n\t"
    "v_accvgpr_read_b32 v251, a119\n\t"
    "v_mul_f32 v251, v251, v234\n\t"
    "v_accvgpr_write_b32 a119, v251\n\t"
    "v_accvgpr_read_b32 v244, a120\n\t"
    "v_mul_f32 v244, v244, v234\n\t"
    "v_accvgpr_write_b32 a120, v244\n\t"
    "v_accvgpr_read_b32 v245, a121\n\t"
    "v_mul_f32 v245, v245, v234\n\t"
    "v_accvgpr_write_b32 a121, v245\n\t"
    "v_accvgpr_read_b32 v246, a122\n\t"
    "v_mul_f32 v246, v246, v234\n\t"
    "v_accvgpr_write_b32 a122, v246\n\t"
    "v_accvgpr_read_b32 v247, a123\n\t"
    "v_mul_f32 v247, v247, v234\n\t"
    "v_accvgpr_write_b32 a123, v247\n\t"
    "v_accvgpr_read_b32 v248, a124\n\t"
    "v_mul_f32 v248, v248, v234\n\t"
    "v_accvgpr_write_b32 a124, v248\n\t"
    "v_accvgpr_read_b32 v249, a125\n\t"
    "v_mul_f32 v249, v249, v234\n\t"
    "v_accvgpr_write_b32 a125, v249\n\t"
    "v_accvgpr_read_b32 v250, a126\n\t"
    "v_mul_f32 v250, v250, v234\n\t"
    "v_accvgpr_write_b32 a126, v250\n\t"
    "v_accvgpr_read_b32 v251, a127\n\t"
    "v_mul_f32 v251, v251, v234\n\t"
    "v_accvgpr_write_b32 a127, v251\n\t"
    "s_nop 3\n\t"
    "s_setpc_b64 s[76:77]\n\t"
    "Lrare4_%=:\n\t"
    "s_nop 7\n\t"
    "v_max3_f32 v232, v168, v169, v170\n\t"
    "v_max3_f32 v232, v232, v171, v172\n\t"
    "v_max3_f32 v232, v232, v173, v174\n\t"
    "v_max_f32 v232, v232, v175\n\t"
    "v_mov_b32 v233, v232\n\t"
    "s_nop 1\n\t"
    "v_permlane32_swap_b32 v233, v232\n\t"
    "v_max_f32 v232, v232, v233\n\t"
    "v_mov_b32 v233, v232\n\t"
    "s_nop 1\n\t"
    "v_permlane16_swap_b32 v233, v232\n\t"
    "v_max_f32 v232, v232, v233\n\t"
    "v_max_f32 v232, s74, v232\n\t"
    "v_sub_f32 v234, 0, v232\n\t"
    "v_min_f32 v234, 0x42c80000, v234\n\t"
    "v_exp_f32 v234, v234\n\t"
    "v_sub_f32 v40, v40, v232\n\t"
    "v_sub_f32 v41, v41, v232\n\t"
    "v_sub_f32 v42, v42, v232\n\t"
    "v_sub_f32 v43, v43, v232\n\t"
    "v_mul_f32 v52, v52, v234\n\t"
    "v_sub_f32 v168, v168, v232\n\t"
    "v_sub_f32 v169, v169, v232\n\t"
    "v_sub_f32 v170, v170, v232\n\t"
    "v_sub_f32 v171, v171, v232\n\t"
    "v_sub_f32 v172, v172, v232\n\t"
    "v_sub_f32 v173, v173, v232\n\t"
    "v_sub_f32 v174, v174, v232\n\t"
    "v_sub_f32 v175, v175, v232\n\t"
    "v_exp_f32 v236, v168\n\t"
    "v_exp_f32 v237, v169\n\t"
    "v_exp_f32 v238, v170\n\t"
    "v_exp_f32 v239, v171\n\t"
    "v_exp_f32 v240, v172\n\t"
    "v_exp_f32 v241, v173\n\t"
    "v_exp_f32 v242, v174\n\t"
    "v_exp_f32 v243, v175\n\t"
    "v_cvt_pk_bf16_f32 v188, v236, v237\n\t"
    "v_cvt_pk_bf16_f32 v189, v238, v239\n\t"
    "v_cvt_pk_bf16_f32 v190, v240, v241\n\t"
    "v_cvt_pk_bf16_f32 v191, v242, v243\n\t"
    "s_nop 1\n\t"
    "v_mfma_f32_16x16x32_bf16 v[196:199], v[54:57], v[188:191], 0\n\t"
    "v_accvgpr_read_b32 v244, a128\n\t"
    "v_mul_f32 v244, v244, v234\n\t"
    "v_accvgpr_write_b32 a128, v244\n\t"
    "v_accvgpr_read_b32 v245, a129\n\t"
    "v_mul_f32 v245, v245, v234\n\t"
    "v_accvgpr_write_b32 a129, v245\n\t"
    "v_accvgpr_read_b32 v246, a130\n\t"
    "v_mul_f32 v246, v246, v234\n\t"
    "v_accvgpr_write_b32 a130, v246\n\t"
    "v_accvgpr_read_b32 v247, a131\n\t"
    "v_mul_f32 v247, v247, v234\n\t"
    "v_accvgpr_write_b32 a131, v247\n\t"
    "v_accvgpr_read_b32 v248, a132\n\t"
    "v_mul_f32 v248, v248, v234\n\t"
    "v_accvgpr_write_b32 a132, v248\n\t"
    "v_accvgpr_read_b32 v249, a133\n\t"
    "v_mul_f32 v249, v249, v234\n\t"
    "v_accvgpr_write_b32 a133, v249\n\t"
    "v_accvgpr_read_b32 v250, a134\n\t"
    "v_mul_f32 v250, v250, v234\n\t"
    "v_accvgpr_write_b32 a134, v250\n\t"
    "v_accvgpr_read_b32 v251, a135\n\t"
    "v_mul_f32 v251, v251, v234\n\t"
    "v_accvgpr_write_b32 a135, v251\n\t"
    "v_accvgpr_read_b32 v244, a136\n\t"
    "v_mul_f32 v244, v244, v234\n\t"
    "v_accvgpr_write_b32 a136, v244\n\t"
    "v_accvgpr_read_b32 v245, a137\n\t"
    "v_mul_f32 v245, v245, v234\n\t"
    "v_accvgpr_write_b32 a137, v245\n\t"
    "v_accvgpr_read_b32 v246, a138\n\t"
    "v_mul_f32 v246, v246, v234\n\t"
    "v_accvgpr_write_b32 a138, v246\n\t"
    "v_accvgpr_read_b32 v247, a139\n\t"
    "v_mul_f32 v247, v247, v234\n\t"
    "v_accvgpr_write_b32 a139, v247\n\t"
    "v_accvgpr_read_b32 v248, a140\n\t"
    "v_mul_f32 v248, v248, v234\n\t"
    "v_accvgpr_write_b32 a140, v248\n\t"
    "v_accvgpr_read_b32 v249, a141\n\t"
    "v_mul_f32 v249, v249, v234\n\t"
    "v_accvgpr_write_b32 a141, v249\n\t"
    "v_accvgpr_read_b32 v250, a142\n\t"
    "v_mul_f32 v250, v250, v234\n\t"
    "v_accvgpr_write_b32 a142, v250\n\t"
    "v_accvgpr_read_b32 v251, a143\n\t"
    "v_mul_f32 v251, v251, v234\n\t"
    "v_accvgpr_write_b32 a143, v251\n\t"
    "v_accvgpr_read_b32 v244, a144\n\t"
    "v_mul_f32 v244, v244, v234\n\t"
    "v_accvgpr_write_b32 a144, v244\n\t"
    "v_accvgpr_read_b32 v245, a145\n\t"
    "v_mul_f32 v245, v245, v234\n\t"
    "v_accvgpr_write_b32 a145, v245\n\t"
    "v_accvgpr_read_b32 v246, a146\n\t"
    "v_mul_f32 v246, v246, v234\n\t"
    "v_accvgpr_write_b32 a146, v246\n\t"
    "v_accvgpr_read_b32 v247, a147\n\t"
    "v_mul_f32 v247, v247, v234\n\t"
    "v_accvgpr_write_b32 a147, v247\n\t"
    "v_accvgpr_read_b32 v248, a148\n\t"
    "v_mul_f32 v248, v248, v234\n\t"
    "v_accvgpr_write_b32 a148, v248\n\t"
    "v_accvgpr_read_b32 v249, a149\n\t"
    "v_mul_f32 v249, v249, v234\n\t"
    "v_accvgpr_write_b32 a149, v249\n\t"
    "v_accvgpr_read_b32 v250, a150\n\t"
    "v_mul_f32 v250, v250, v234\n\t"
    "v_accvgpr_write_b32 a150, v250\n\t"
    "v_accvgpr_read_b32 v251, a151\n\t"
    "v_mul_f32 v251, v251, v234\n\t"
    "v_accvgpr_write_b32 a151, v251\n\t"
    "v_accvgpr_read_b32 v244, a152\n\t"
    "v_mul_f32 v244, v244, v234\n\t"
    "v_accvgpr_write_b32 a152, v244\n\t"
    "v_accvgpr_read_b32 v245, a153\n\t"
    "v_mul_f32 v245, v245, v234\n\t"
    "v_accvgpr_write_b32 a153, v245\n\t"
    "v_accvgpr_read_b32 v246, a154\n\t"
    "v_mul_f32 v246, v246, v234\n\t"
    "v_accvgpr_write_b32 a154, v246\n\t"
    "v_accvgpr_read_b32 v247, a155\n\t"
    "v_mul_f32 v247, v247, v234\n\t"
    "v_accvgpr_write_b32 a155, v247\n\t"
    "v_accvgpr_read_b32 v248, a156\n\t"
    "v_mul_f32 v248, v248, v234\n\t"
    "v_accvgpr_write_b32 a156, v248\n\t"
    "v_accvgpr_read_b32 v249, a157\n\t"
    "v_mul_f32 v249, v249, v234\n\t"
    "v_accvgpr_write_b32 a157, v249\n\t"
    "v_accvgpr_read_b32 v250, a158\n\t"
    "v_mul_f32 v250, v250, v234\n\t"
    "v_accvgpr_write_b32 a158, v250\n\t"
    "v_accvgpr_read_b32 v251, a159\n\t"
    "v_mul_f32 v251, v251, v234\n\t"
    "v_accvgpr_write_b32 a159, v251\n\t"
    "s_nop 3\n\t"
    "s_setpc_b64 s[76:77]\n\t"
    "Lrare5_%=:\n\t"
    "s_nop 7\n\t"
    "v_max3_f32 v232, v176, v177, v178\n\t"
    "v_max3_f32 v232, v232, v179, v180\n\t"
    "v_max3_f32 v232, v232, v181, v182\n\t"
    "v_max_f32 v232, v232, v183\n\t"
    "v_mov_b32 v233, v232\n\t"
    "s_nop 1\n\t"
    "v_permlane32_swap_b32 v233, v232\n\t"
    "v_max_f32 v232, v232, v233\n\t"
    "v_mov_b32 v233, v232\n\t"
    "s_nop 1\n\t"
    "v_permlane16_swap_b32 v233, v232\n\t"
    "v_max_f32 v232, v232, v233\n\t"
    "v_max_f32 v232, s74, v232\n\t"
    "v_sub_f32 v234, 0, v232\n\t"
    "v_min_f32 v234, 0x42c80000, v234\n\t"
    "v_exp_f32 v234, v234\n\t"
    "v_sub_f32 v44, v44, v232\n\t"
    "v_sub_f32 v45, v45, v232\n\t"
    "v_sub_f32 v46, v46, v232\n\t"
    "v_sub_f32 v47, v47, v232\n\t"
    "v_mul_f32 v53, v53, v234\n\t"
    "v_sub_f32 v176, v176, v232\n\t"
    "v_sub_f32 v177, v177, v232\n\t"
    "v_sub_f32 v178, v178, v232\n\t"
    "v_sub_f32 v179, v179, v232\n\t"
    "v_sub_f32 v180, v180, v232\n\t"
    "v_sub_f32 v181, v181, v232\n\t"
    "v_sub_f32 v182, v182, v232\n\t"
    "v_sub_f32 v183, v183, v232\n\t"
    "v_exp_f32 v236, v176\n\t"
    "v_exp_f32 v237, v177\n\t"
    "v_exp_f32 v238, v178\n\t"
    "v_exp_f32 v239, v179\n\t"
    "v_exp_f32 v240, v180\n\t"
    "v_exp_f32 v241, v181\n\t"
    "v_exp_f32 v242, v182\n\t"
    "v_exp_f32 v243, v183\n\t"
    "v_cvt_pk_bf16_f32 v192, v236, v237\n\t"
    "v_cvt_pk_bf16_f32 v193, v238, v239\n\t"
    "v_cvt_pk_bf16_f32 v194, v240, v241\n\t"
    "v_cvt_pk_bf16_f32 v195, v242, v243\n\t"
    "s_nop 1\n\t"
    "v_mfma_f32_16x16x32_bf16 v[196:199], v[54:57], v[192:195], 0\n\t"
    "v_accvgpr_read_b32 v244, a160\n\t"
    "v_mul_f32 v244, v244, v234\n\t"
    "v_accvgpr_write_b32 a160, v244\n\t"
    "v_accvgpr_read_b32 v245, a161\n\t"
    "v_mul_f32 v245, v245, v234\n\t"
    "v_accvgpr_write_b32 a161, v245\n\t"
    "v_accvgpr_read_b32 v246, a162\n\t"
    "v_mul_f32 v246, v246, v234\n\t"
    "v_accvgpr_write_b32 a162, v246\n\t"
    "v_accvgpr_read_b32 v247, a163\n\t"
    "v_mul_f32 v247, v247, v234\n\t"
    "v_accvgpr_write_b32 a163, v247\n\t"
    "v_accvgpr_read_b32 v248, a164\n\t"
    "v_mul_f32 v248, v248, v234\n\t"
    "v_accvgpr_write_b32 a164, v248\n\t"
    "v_accvgpr_read_b32 v249, a165\n\t"
    "v_mul_f32 v249, v249, v234\n\t"
    "v_accvgpr_write_b32 a165, v249\n\t"
    "v_accvgpr_read_b32 v250, a166\n\t"
    "v_mul_f32 v250, v250, v234\n\t"
    "v_accvgpr_write_b32 a166, v250\n\t"
    "v_accvgpr_read_b32 v251, a167\n\t"
    "v_mul_f32 v251, v251, v234\n\t"
    "v_accvgpr_write_b32 a167, v251\n\t"
    "v_accvgpr_read_b32 v244, a168\n\t"
    "v_mul_f32 v244, v244, v234\n\t"
    "v_accvgpr_write_b32 a168, v244\n\t"
    "v_accvgpr_read_b32 v245, a169\n\t"
    "v_mul_f32 v245, v245, v234\n\t"
    "v_accvgpr_write_b32 a169, v245\n\t"
    "v_accvgpr_read_b32 v246, a170\n\t"
    "v_mul_f32 v246, v246, v234\n\t"
    "v_accvgpr_write_b32 a170, v246\n\t"
    "v_accvgpr_read_b32 v247, a171\n\t"
    "v_mul_f32 v247, v247, v234\n\t"
    "v_accvgpr_write_b32 a171, v247\n\t"
    "v_accvgpr_read_b32 v248, a172\n\t"
    "v_mul_f32 v248, v248, v234\n\t"
    "v_accvgpr_write_b32 a172, v248\n\t"
    "v_accvgpr_read_b32 v249, a173\n\t"
    "v_mul_f32 v249, v249, v234\n\t"
    "v_accvgpr_write_b32 a173, v249\n\t"
    "v_accvgpr_read_b32 v250, a174\n\t"
    "v_mul_f32 v250, v250, v234\n\t"
    "v_accvgpr_write_b32 a174, v250\n\t"
    "v_accvgpr_read_b32 v251, a175\n\t"
    "v_mul_f32 v251, v251, v234\n\t"
    "v_accvgpr_write_b32 a175, v251\n\t"
    "v_accvgpr_read_b32 v244, a176\n\t"
    "v_mul_f32 v244, v244, v234\n\t"
    "v_accvgpr_write_b32 a176, v244\n\t"
    "v_accvgpr_read_b32 v245, a177\n\t"
    "v_mul_f32 v245, v245, v234\n\t"
    "v_accvgpr_write_b32 a177, v245\n\t"
    "v_accvgpr_read_b32 v246, a178\n\t"
    "v_mul_f32 v246, v246, v234\n\t"
    "v_accvgpr_write_b32 a178, v246\n\t"
    "v_accvgpr_read_b32 v247, a179\n\t"
    "v_mul_f32 v247, v247, v234\n\t"
    "v_accvgpr_write_b32 a179, v247\n\t"
    "v_accvgpr_read_b32 v248, a180\n\t"
    "v_mul_f32 v248, v248, v234\n\t"
    "v_accvgpr_write_b32 a180, v248\n\t"
    "v_accvgpr_read_b32 v249, a181\n\t"
    "v_mul_f32 v249, v249, v234\n\t"
    "v_accvgpr_write_b32 a181, v249\n\t"
    "v_accvgpr_read_b32 v250, a182\n\t"
    "v_mul_f32 v250, v250, v234\n\t"
    "v_accvgpr_write_b32 a182, v250\n\t"
    "v_accvgpr_read_b32 v251, a183\n\t"
    "v_mul_f32 v251, v251, v234\n\t"
    "v_accvgpr_write_b32 a183, v251\n\t"
    "v_accvgpr_read_b32 v244, a184\n\t"
    "v_mul_f32 v244, v244, v234\n\t"
    "v_accvgpr_write_b32 a184, v244\n\t"
    "v_accvgpr_read_b32 v245, a185\n\t"
    "v_mul_f32 v245, v245, v234\n\t"
    "v_accvgpr_write_b32 a185, v245\n\t"
    "v_accvgpr_read_b32 v246, a186\n\t"
    "v_mul_f32 v246, v246, v234\n\t"
    "v_accvgpr_write_b32 a186, v246\n\t"
    "v_accvgpr_read_b32 v247, a187\n\t"
    "v_mul_f32 v247, v247, v234\n\t"
    "v_accvgpr_write_b32 a187, v247\n\t"
    "v_accvgpr_read_b32 v248, a188\n\t"
    "v_mul_f32 v248, v248, v234\n\t"
    "v_accvgpr_write_b32 a188, v248\n\t"
    "v_accvgpr_read_b32 v249, a189\n\t"
    "v_mul_f32 v249, v249, v234\n\t"
    "v_accvgpr_write_b32 a189, v249\n\t"
    "v_accvgpr_read_b32 v250, a190\n\t"
    "v_mul_f32 v250, v250, v234\n\t"
    "v_accvgpr_write_b32 a190, v250\n\t"
    "v_accvgpr_read_b32 v251, a191\n\t"
    "v_mul_f32 v251, v251, v234\n\t"
    "v_accvgpr_write_b32 a191, v251\n\t"
    "s_nop 3\n\t"
    "s_setpc_b64 s[76:77]\n\t"
    "Lend_%=:\n\t"
    :: "s"(__builtin_amdgcn_kernarg_segment_ptr()), "s"(blockIdx.x), "s"(__builtin_amdgcn_readfirstlane(threadIdx.x >> 6))
    : "v0","v1","v2","v3","v4","v5","v6","v7","v8","v9","v10","v11","v12","v13","v14","v15","v16","v17","v18","v19","v20","v21","v22","v23","v24","v25","v26","v27","v28","v29","v30","v31","v32","v33","v34","v35","v36","v37","v38","v39","v40","v41","v42","v43","v44","v45","v46","v47","v48","v49","v50","v51","v52","v53","v54","v55","v56","v57","v58","v59","v60","v61","v62","v63","v64","v65","v66","v67","v68","v69","v70","v71","v72","v73","v74","v75","v76","v77","v78","v79","v80","v81","v82","v83","v84","v85","v86","v87","v88","v89","v90","v91","v92","v93","v94","v95","v96","v97","v98","v99","v100","v101","v102","v103","v104","v105","v106","v107","v108","v109","v110","v111","v112","v113","v114","v115","v116","v117","v118","v119","v120","v121","v122","v123","v124","v125","v126","v127","v128","v129","v130","v131","v132","v133","v134","v135","v136","v137","v138","v139","v140","v141","v142","v143","v144","v145","v146","v147","v148","v149","v150","v151","v152","v153","v154","v155","v156","v157","v158","v159","v160","v161","v162","v163","v164","v165","v166","v167","v168","v169","v170","v171","v172","v173","v174","v175","v176","v177","v178","v179","v180","v181","v182","v183","v184","v185","v186","v187","v188","v189","v190","v191","v192","v193","v194","v195","v196","v197","v198","v199","v200","v201","v202","v203","v204","v205","v206","v207","v208","v209","v210","v211","v212","v213","v214","v215","v216","v217","v218","v219","v220","v221","v222","v223","v224","v225","v226","v227","v228","v229","v230","v231","v232","v233","v234","v235","v236","v237","v238","v239","v240","v241","v242","v243","v244","v245","v246","v247","v248","v249","v250","v251","v252","v253","v254","v255","a0","a1","a2","a3","a4","a5","a6","a7","a8","a9","a10","a11","a12","a13","a14","a15","a16","a17","a18","a19","a20","a21","a22","a23","a24","a25","a26","a27","a28","a29","a30","a31","a32","a33","a34","a35","a36","a37","a38","a39","a40","a41","a42","a43","a44","a45","a46","a47","a48","a49","a50","a51","a52","a53","a54","a55","a56","a57","a58","a59","a60","a61","a62","a63","a64","a65","a66","a67","a68","a69","a70","a71","a72","a73","a74","a75","a76","a77","a78","a79","a80","a81","a82","a83","a84","a85","a86","a87","a88","a89","a90","a91","a92","a93","a94","a95","a96","a97","a98","a99","a100","a101","a102","a103","a104","a105","a106","a107","a108","a109","a110","a111","a112","a113","a114","a115","a116","a117","a118","a119","a120","a121","a122","a123","a124","a125","a126","a127","a128","a129","a130","a131","a132","a133","a134","a135","a136","a137","a138","a139","a140","a141","a142","a143","a144","a145","a146","a147","a148","a149","a150","a151","a152","a153","a154","a155","a156","a157","a158","a159","a160","a161","a162","a163","a164","a165","a166","a167","a168","a169","a170","a171","a172","a173","a174","a175","a176","a177","a178","a179","a180","a181","a182","a183","a184","a185","a186","a187","a188","a189","a190","a191","a192","a193","a194","a195","a196","a197","a198","a199","a200","a201","a202","a203","a204","a205","a206","a207","a208","a209","a210","a211","a212","a213","a214","a215","a216","a217","a218","a219","a220","a221","a222","a223","a224","a225","a226","a227","a228","a229","a230","a231","a232","a233","a234","a235","a236","a237","a238","a239","a240","a241","a242","a243","a244","a245","a246","a247","a248","a249","a250","a251","a252","a253","a254","a255","s4","s5","s6","s7","s8","s9","s10","s11","s12","s13","s14","s15","s16","s17","s18","s19","s20","s21","s22","s23","s24","s25","s26","s27","s28","s29","s30","s31","s32","s33","s34","s35","s36","s37","s38","s39","s40","s41","s42","s43","s44","s45","s46","s47","s48","s49","s50","s51","s52","s53","s54","s55","s56","s57","s58","s59","s60","s61","s62","s63","s64","s65","s66","s67","s68","s69","s70","s71","s72","s73","s74","s75","s76","s77","s78","s79","s80","s81","s82","s83","s84","s85","s86","s87","s88","s89","s90","s91","s92","s93","s94","s95","s96","s97","s98","s99","s100","s101","vcc","scc","memory");
}

extern "C" void kernel_launch(void* const* d_in, const int* in_sizes, int n_in, void* d_out, int out_size, void* d_ws, size_t ws_size, hipStream_t stream) {
  k_attn<<<256, 256, 0, stream>>>(d_in[0], d_in[1], d_in[2], d_in[3], d_ws, d_in[4], d_out, d_ws);
}
```
